```python
import torch

def _softmax(x, axis=-1):
    return torch.softmax(x, dim=axis)
def _gelu(x):
    return torch.nn.functional.gelu(x, approximate="tanh")
def _silu(x):
    return torch.nn.functional.silu(x)
def _sigmoid(x):
    return torch.sigmoid(x)
def _layernorm(x, axis=-1, eps=1e-5):
    m = x.mean(dim=axis, keepdim=True)
    v = x.var(dim=axis, keepdim=True, unbiased=False)
    return (x - m) / torch.sqrt(v + eps)
def _scatter_add(tgt, idx, src, axis=0):
    return tgt.clone().index_add_(axis, idx, src)
def _segment_sum(src, seg, n):
    out = torch.zeros((n,) + src.shape[1:], dtype=src.dtype)
    return out.index_add_(0, seg, src)
def _pad(x, pw):
    flat = tuple(v for pair in reversed(pw) for v in pair)
    return torch.nn.functional.pad(x, flat)

kernel_name = 'interleave_layers__sliding_block__sliding_local_dilated__prompt'

def setup_inputs():

    _w = (lambda a: (lambda t: t.to(torch.bfloat16) if t.is_floating_point() else t)(torch.as_tensor(a)))
    def _A_setup():
        B, S, D = 1, 2048, 1024
        return {
            "x": _w((lambda *s: torch.randn(*s, dtype=torch.bfloat16) * 0.05)(B, S, D)),
            "wqkv": _w((lambda *s: torch.randn(*s, dtype=torch.bfloat16) * 0.05)(D, 3 * D)),
            "wo": _w((lambda *s: torch.randn(*s, dtype=torch.bfloat16) * 0.05)(D, D)),
        }
    _A_in = _A_setup()

    def _B_setup():
        B, S, D = 1, 2048, 1024
        return {
            "x": _w((lambda *s: torch.randn(*s, dtype=torch.bfloat16) * 0.05)(B, S, D)),
            "wqkv": _w((lambda *s: torch.randn(*s, dtype=torch.bfloat16) * 0.05)(D, 3 * D)),
            "wo": _w((lambda *s: torch.randn(*s, dtype=torch.bfloat16) * 0.05)(D, D)),
        }
    _B_in = _B_setup()

    return {
        "A_x": _A_in["x"],
        "A_wqkv": _A_in["wqkv"],
        "A_wo": _A_in["wo"],
        "B_x": _B_in["x"],
        "B_wqkv": _B_in["wqkv"],
        "B_wo": _B_in["wo"],
    }

def reference(A_x, A_wqkv, A_wo, B_x, B_wqkv, B_wo):
    def _A_ref(x, wqkv, wo):
        B, S, D = x.shape
        H = 32; Dh = D // H; W = 256
        qkv = (lambda x, *d: x.permute(*d))((x @ wqkv).reshape(B, S, 3, H, Dh), 2, 0, 3, 1, 4)
        q, k, v = qkv[0], qkv[1], qkv[2]
        scores = torch.einsum("bhqd,bhkd->bhqk", q, k) * (Dh ** -0.5)
        idx = torch.arange(S)
        mask = (idx[:, None] - idx[None, :] >= 0) & (idx[:, None] - idx[None, :] < W)
        scores = torch.where(mask[None, None], scores, -1e9)
        p = _softmax(scores, axis=-1)
        o = torch.einsum("bhqk,bhkd->bhqd", p, v)
        return (lambda x, *d: x.permute(*d))(o, 0, 2, 1, 3).reshape(B, S, D) @ wo

    def _B_ref(x, wqkv, wo):
        B, S, D = x.shape
        H = 32; Dh = D // H
        W, dil = 8, 2
        qkv = (lambda x, *d: x.permute(*d))((x @ wqkv).reshape(B, S, 3, H, Dh), 2, 0, 3, 1, 4)
        q, k, v = qkv[0], qkv[1], qkv[2]
        scale = Dh ** -0.5
        pos = torch.arange(S)
        off = torch.arange(W) * dil
        idx = pos[:, None] - off[None, :]
        mask = idx >= 0
        idx_c = torch.where(mask, idx, 0).reshape(-1)
        kg = k[:, :, idx_c, :].reshape(B, H, S, W, Dh)
        vg = v[:, :, idx_c, :].reshape(B, H, S, W, Dh)
        logits = torch.einsum("bhsd,bhswd->bhsw", q, kg) * scale
        logits = torch.where(mask[None, None, :, :], logits, -1e9)
        attn = _softmax(logits, axis=-1)
        o = torch.einsum("bhsw,bhswd->bhsd", attn, vg)
        return (lambda x, *d: x.permute(*d))(o, 0, 2, 1, 3).reshape(B, S, D) @ wo

    x = A_x
    x = x + _A_ref(_layernorm(x, axis=-1), A_wqkv, A_wo)
    x = x + _B_ref(_layernorm(x, axis=-1), B_wqkv, B_wo)
    x = x + _A_ref(_layernorm(x, axis=-1), A_wqkv, A_wo)
    x = x + _B_ref(_layernorm(x, axis=-1), B_wqkv, B_wo)
    return x
```

```cpp
#define NOR 0
#include <hip/hip_runtime.h>
#include <stdint.h>
#include <type_traits>

typedef unsigned short bf16_t;
typedef short bf16x8 __attribute__((ext_vector_type(8)));
typedef short s16x4 __attribute__((ext_vector_type(4)));
typedef float f32x4 __attribute__((ext_vector_type(4)));
typedef float f32x2 __attribute__((ext_vector_type(2)));
typedef float f32x16 __attribute__((ext_vector_type(16)));
typedef unsigned u32x4 __attribute__((ext_vector_type(4)));
typedef unsigned u32x2 __attribute__((ext_vector_type(2)));
#define DI __device__ __forceinline__
#define LAS __attribute__((address_space(3)))
#define GAS __attribute__((address_space(1)))
#define STAMP 0

constexpr int S_ = 2048, D_ = 1024, KT = 16;
constexpr float LN_EPS = 1e-5f;
constexpr float QSCALE = 0.17677669529663687f * 1.4426950408889634f;

#define FQO 0
#define XBO 524288
#define HX 0
#define HO 0
#define HK 0
#define HS 0
#define HM 1
constexpr size_t WS_FLAGQ = FQO;
constexpr size_t WS_FLAGW = 98304;
constexpr size_t WS_EPOCH = 65536;
constexpr size_t WS_CSUM = 131072;
constexpr size_t WS_XBOX = XBO;
constexpr size_t WS_OBOX = 655360;
constexpr size_t WS_STATS = 1u << 20;
constexpr size_t WS_X = 4u << 20;
constexpr size_t WS_O = 96u << 20;
constexpr size_t WS_KV = 28u << 20;
constexpr size_t WS_STAMP = 64u << 20;

template <int H> DI constexpr int hp(int c) { return !H ? c * 1024 : (c >> 2) * 8192 + (HM == 1 ? (c & 3) * 256 : (c & 1) * 128 + (c & 2) * 256); }
template <int H> DI constexpr int hl(int u) { return !H ? u : (HM == 1 ? u + (u & ~127) : u + (u & ~255)); }
template <int H> DI constexpr int hv(int lane) { return !H ? lane * 16 : (lane >> 3) * 1024 + (lane & 7) * 16; }
template <int H> DI constexpr int xrow(int p, int r) { return H == 0 ? (p * 128 + r) * 2048 : (H == 1 ? (p * 128 + r) * 4096 : (H == 2 ? p * 524288 + r * 256 : p * 262144 + r * 128)); }
template <int H> DI constexpr int xcol(int t) { return H == 0 ? t * 128 : (H == 1 ? hl<1>(t * 128) : (H == 2 ? t * 32768 : t * 16384)); }
#define WTC 2
#define WT0 1
#define SPR 1
#define SL 4
#define R4X 0
#define R5X 0
#define TOPJ 0x2000
#define WV0 1
#define C2X 13
#define FQS 256
#define SNK 1
#define WBE 1
#define WAE 1
#define EXF 0
#define RW0 6
#define KSP 0
#define K0E 1
#define TOPS 4
#define BPR 1
#define RLD 0
#define RCK 1
#define CPF 1
#define WN1 20
#define WLO 3
#define FPJ 2
#define WPJ 2
#define WB14 0
#define WB15 5
#define RESP 1
#define WT13 2
#define RPOS 2
#define CPOLL 1
#define ENT 1
#define MBOX 1
#define OBS 128
#define WTCH 0
#define WTDA 0
#define WTDB 4
#define WTG 0
#define PQX 8
#define ALT 1
#define RESA 1
#define CKP 1
#define ALN 1
#define EPH 2
#define NQA_ 3
#define NQW_ 4
#define WNP 8
#define LPRIO 3
#define TOUCH 1
#define TSTEP 13
#define XS_ 9
#define WROT 0
#define WROTD 1
#define JQX 0
#define ROTM 0
#define ROT0 0
#define WPRE 3
#define PSLEEP 1
#define XPRE 2
#define WE0 1
#define EPJ 3
#define TW0 5
#define TW1 99
#define KPJ 1
#define PKSUB 0
#define APRIO 1
#define QIL 0
#define QXM 8
#define WNQ 5
#define WRG 2
#define WMG 1
#define WB2 1
#define WBS 1
#define WEP 1
#define WAL 2
#define WL1 0
#define RS0 27
#define RN0 6
#define RN1 6
#define AH0 0
#define RSL 1
#define WXM 4
#define CPOS 5
#define LSL 0
#define SAFE_SUM 0x1p60f
#define AB2 0
#define W30S 8
#define STC 0
#define NRH 0
#define XE1 0
#define XE2 0
#define OTB 0
#define OTJ 8
#define OTA 0
constexpr int QA_SLOT = 18432, NQA = NQA_, QW_SLOT = 24576, NQW = NQW_, QA_OFF = 3 * QW_SLOT, QW_TOP = 131072;
DI constexpr int qw_slot(int s) { return s == 3 ? QW_TOP : s * QW_SLOT; }
constexpr int AT_N = 90112, AT_OWN = AT_N + 32768, AT_Q = 73728;
constexpr int MISC = 155648, M_MEAN = MISC, M_RSTD = MISC + 640, M_CS = MISC + 1280, M_CNT = MISC + 2816, M_EP = MISC + 2832, M_TCH = MISC + 3072, M_TAB = MISC + 4096, M_XCH = 131072;
constexpr int LDS_TOTAL = 163840;
static_assert(QA_OFF + NQA * QA_SLOT <= QW_TOP && QW_TOP + QW_SLOT <= MISC && AT_N + 65536 <= MISC && AT_Q + 16384 <= AT_N, "lds");

DI unsigned cvt_pk_bf16(float lo, float hi) {
    typedef __bf16 bf2 __attribute__((ext_vector_type(2)));
    bf2 r; r[0] = (__bf16)lo; r[1] = (__bf16)hi;
    return __builtin_bit_cast(unsigned, r);
}
DI u32x2 pack4(const f32x4& v) { return (u32x2){cvt_pk_bf16(v[0], v[1]), cvt_pk_bf16(v[2], v[3])}; }
DI bf16x8 join(const u32x2& a, const u32x2& b) { return __builtin_bit_cast(bf16x8, (u32x4){a[0], a[1], b[0], b[1]}); }
DI float bf_lo(unsigned u) { return __uint_as_float(u << 16); }
DI float bf_hi(unsigned u) { return __uint_as_float(u & 0xffff0000u); }
DI float red_max4(float x) {
    auto a = __builtin_amdgcn_permlane16_swap(__float_as_uint(x), __float_as_uint(x), false, false);
    x = fmaxf(__uint_as_float(a[0]), __uint_as_float(a[1]));
    auto b = __builtin_amdgcn_permlane32_swap(__float_as_uint(x), __float_as_uint(x), false, false);
    return fmaxf(__uint_as_float(b[0]), __uint_as_float(b[1]));
}
DI float red_sum4(float x) {
    auto a = __builtin_amdgcn_permlane16_swap(__float_as_uint(x), __float_as_uint(x), false, false);
    x = __uint_as_float(a[0]) + __uint_as_float(a[1]);
    auto b = __builtin_amdgcn_permlane32_swap(__float_as_uint(x), __float_as_uint(x), false, false);
    return __uint_as_float(b[0]) + __uint_as_float(b[1]);
}
DI u32x4 widen(const u32x2& a, const u32x2& b) {
    auto r0 = __builtin_amdgcn_permlane16_swap(a[0], b[0], false, false);
    auto r1 = __builtin_amdgcn_permlane16_swap(a[1], b[1], false, false);
    return (u32x4){r0[0], r1[0], r0[1], r1[1]};
}
#define MFMA(a, b, c) __builtin_amdgcn_mfma_f32_16x16x32_bf16((a), (b), (c), 0, 0, 0)
#define WAIT_L0() __builtin_amdgcn_s_waitcnt(0xC07F)
#define WAIT_V0() asm volatile("s_waitcnt vmcnt(0)" ::: "memory")
#define BAR() __builtin_amdgcn_s_barrier()
#define SCHED() __builtin_amdgcn_sched_barrier(0)
template <int N> DI void wait_v() { asm volatile("s_waitcnt vmcnt(%0)" ::"i"(N) : "memory"); }
template <int I, int N, class F> DI void static_for(F&& f) { if constexpr (I < N) { f(std::integral_constant<int, I>{}); static_for<I + 1, N>(f); } }

template <bool SC1> DI void dma16(unsigned lds, unsigned voff, const char* sbase) {
    if (SC1) asm volatile("s_mov_b32 m0, %0\n\ts_nop 3\n\tglobal_load_lds_dwordx4 %1, %2 sc1" ::"s"(lds), "v"(voff), "s"(sbase) : "memory");
    else asm volatile("s_mov_b32 m0, %0\n\ts_nop 3\n\tglobal_load_lds_dwordx4 %1, %2" ::"s"(lds), "v"(voff), "s"(sbase) : "memory");
}
DI void touch(unsigned lds, unsigned voff, const char* sbase) {
    asm volatile("s_mov_b32 m0, %0\n\ts_nop 3\n\tglobal_load_lds_dword %1, %2 sc1" ::"s"(lds), "v"(voff), "s"(sbase) : "memory");
}
DI s16x4 tr_read(const LAS char* p) {
    typedef short v4i16_t __attribute__((ext_vector_type(4)));
    return __builtin_bit_cast(s16x4, __builtin_amdgcn_ds_read_tr16_b64_v4i16((LAS v4i16_t*)p));
}
DI LAS char* lds(unsigned off) { return (LAS char*)(uintptr_t)off; }
DI unsigned long long rstamp() { unsigned long long t; asm volatile("s_memrealtime %0\n\ts_waitcnt lgkmcnt(0)" : "=s"(t)::"memory"); return t; }

template <int NAI, int NWI, bool FIRST> struct Sched {
    int w[KT];
    constexpr Sched() : w{} {
        int endA[KT] = {}, endW[KT] = {};
        int n = 0;
        if (FIRST) {
            if (ENT == 0) { n += 2; endA[0] = n; n += 3; endW[0] = n; }
            n += NAI; endA[1] = n; n += NWI; endW[1] = n;
        } else {
            for (int s = 0; s < WPRE; ++s) { n += NWI; endW[s] = n; }
            for (int s = 0; s < 2; ++s) { n += NAI; endA[s] = n; }
        }
        for (int kt = 0; kt < KT; ++kt) {
            const int need = endA[kt] > endW[kt] ? endA[kt] : endW[kt];
            w[kt] = (n - need) > 63 ? 63 : (n - need);
            if (kt + 2 < KT) { n += NAI; endA[kt + 2] = n; }
            if (FIRST && kt < 5) {
                if (kt < 4) { n += NWI; endW[kt + 2] = n; }
                if (kt >= 3) n += NWI / 2;
                if (kt == 4) { endW[6] = n; n += NWI; endW[7] = n; }
                continue;
            }
            if (!FIRST && WPRE == 2 && kt < 2) { n += NWI; endW[kt + 2] = n; }
            if (!FIRST && WPRE == 3 && kt == 0) { n += NWI; endW[3] = n; }
            if (kt >= 1 && kt + 3 < KT) { n += NWI; endW[kt + 3] = n; }
            if (kt == 13) n += WT13;
            if (kt == 14) n += 12 - WT13 + (NAI == 5 ? WB14 : 0);
        }
    }
};

struct Params {
    const bf16_t* x0;
    const bf16_t* wqkv0;
    char* ws;
    const bf16_t* wqkv1;
    const bf16_t* wo0;
    const bf16_t* wo1;
    bf16_t* out;
};
typedef const __attribute__((address_space(4))) Params* KP;
DI int rot_first(int pq, int jq) {
    const int pr = 2 * (pq & 7) + (pq >> 3);
    return (ROT0 == 0 ? pq + jq : (ROT0 == 1 ? pr + jq : (ROT0 == 2 ? pr + 4 * (jq & 3) + (jq >> 2) : pq + 4 * (jq & 3) + (jq >> 2)))) & 15;
}

typedef unsigned long long u64;
DI u64 lds_epoch(LAS char* smem) { return *(const LAS u64*)(smem + M_EP); }
DI bool reached(u64 v, u64 ep, unsigned target) { return (v - ep - target) < (u64)(64u - target); }
DI void poll_flags(const char* ws, u64 ep, unsigned off, bool active, unsigned target) {
    const GAS u64* f = (const GAS u64*)(ws + off);
#pragma unroll 1
    for (unsigned spins = 0; spins < (1u << 22); ++spins) {
        const u64 v = __hip_atomic_load(f, __ATOMIC_RELAXED, __HIP_MEMORY_SCOPE_AGENT);
        if (__all(!active || reached(v, ep, target))) break;
        __builtin_amdgcn_s_sleep(PSLEEP);
    }
    asm volatile("" ::: "memory");
}
template <int NW, int CNT, int NV = 0> DI void publish(LAS char* smem, int lane, char* ws, unsigned off, bool act, unsigned val) {
    const u64 ep = lds_epoch(smem) + val;
    unsigned old = 0;
    asm volatile("" :: "v"(ep), "v"(off));
    wait_v<NV>();
    if (lane == 0) asm volatile("ds_add_rtn_u32 %0, %1, %2\n\ts_waitcnt lgkmcnt(0)" : "+v"(old) : "v"(M_CNT + CNT * 4), "v"(1u) : "memory");
    if ((__builtin_amdgcn_readfirstlane(old) & (NW - 1)) == NW - 1) {
        if (act) __hip_atomic_store((GAS u64*)(ws + off), ep, __ATOMIC_RELAXED, __HIP_MEMORY_SCOPE_AGENT);
    }
}
typedef __bf16 bf16x2_t __attribute__((ext_vector_type(2)));
DI float dot2(unsigned a, unsigned b, float c) { return __builtin_amdgcn_fdot2_f32_bf16(__builtin_bit_cast(bf16x2_t, a), __builtin_bit_cast(bf16x2_t, b), c, false); }
DI u32x2 tile_stats(const u32x4& a, const u32x4& b) {
    float s = 0.f, q = 0.f;
#pragma unroll
    for (int j = 0; j < 4; ++j) { s = dot2(a[j], 0x3f803f80u, s); q = dot2(a[j], a[j], q); }
#pragma unroll
    for (int j = 0; j < 4; ++j) { s = dot2(b[j], 0x3f803f80u, s); q = dot2(b[j], b[j], q); }
    return (u32x2){__float_as_uint(red_sum4(s)), __float_as_uint(red_sum4(q))};
}

struct Ctx {
    LAS char* smem;
    char* ws;
    int lane, wid;
    int pq, jq;
    int pw, jw;
    int bid;
};
DI Ctx fresh(const Ctx& c0) {
    Ctx c = c0;
    asm volatile("" : "+v"(c.lane));
    asm volatile("" : "+s"(c.wid), "+s"(c.pq), "+s"(c.jq), "+s"(c.pw), "+s"(c.jw), "+s"(c.ws));
    return c;
}
DI unsigned o_post(const Ctx& c) { return (MBOX & 2) ? (unsigned)WS_OBOX + (c.pq * 16 + (c.lane & 15)) * OBS + c.jq * 8 : (unsigned)WS_FLAGQ + (c.pq * 16 + c.jq) * FQS; }
DI bool o_act(const Ctx& c) { return c.lane < ((MBOX & 2) ? 16 : 1); }
DI unsigned o_find(const Ctx& c) { return (MBOX & 2) ? (unsigned)WS_OBOX + (c.pw * 16 + c.jw) * OBS + (c.lane & 15) * 8 : (unsigned)WS_FLAGQ + (c.pw * 16 + (c.lane & 15)) * FQS; }
DI unsigned x_post(const Ctx& c) { return (MBOX & 1) ? (unsigned)WS_XBOX + (c.pw * 16 + (c.lane & 31)) * (2 * FQS) + (c.lane & 16) * (FQS / 16) + c.jw * 8 : (unsigned)WS_FLAGW + (c.pw * 16 + c.jw) * 128; }
DI bool x_act(const Ctx& c, bool halo) { return c.lane < ((MBOX & 1) ? (halo && c.pw < 15 ? 32 : 16) : 1); }
DI unsigned x_find(const Ctx& c) {
    const int pp = c.pq - (c.lane >> 4);
    return (MBOX & 1) ? (unsigned)WS_XBOX + (c.pq * 16 + c.jq) * (2 * FQS) + (c.lane & 16) * (FQS / 16) + (c.lane & 15) * 8 : (unsigned)WS_FLAGW + ((pp < 0 ? 0 : pp) * 16 + (c.lane & 15)) * 128;
}
#define STAMP_AT(slot) do { } while (0)

#define LSTAMP(i) do { } while (0)
#define LSTORE(ph) do { } while (0)
#define LMARK() do { } while (0)
#define LPUT(i) do { } while (0)
#define ASTORE() do { } while (0)
#define QSTAMP(i) do { } while (0)
#define QSTORE() do { } while (0)
#define FENCE() asm volatile("" ::: "memory")
#define ALIGN8() do { if (ALN) { asm volatile(".p2align 3"); SCHED(); } } while (0)
DI float addf(float a, float b) { float r; asm("v_add_f32 %0, %1, %2" : "=v"(r) : "v"(a), "v"(b)); return r; }
DI f32x4 add4x4(const u32x4& a, const u32x4& b, const u32x4& c, const u32x4& d) {
    f32x4 r;
#pragma unroll
    for (int i = 0; i < 4; ++i) r[i] = addf(addf(__uint_as_float(a[i]), __uint_as_float(b[i])), addf(__uint_as_float(c[i]), __uint_as_float(d[i])));
    asm volatile("s_nop 1" : "+v"(r));
    return r;
}
DI float fold16(float a, float b) {
    auto r = __builtin_amdgcn_permlane16_swap(__float_as_uint(a), __float_as_uint(b), false, false);
    float x = __uint_as_float(r[0]) + __uint_as_float(r[1]);
    asm volatile("" : "+v"(x));
    return x;
}
DI float fold32(float a, float b) {
    auto r = __builtin_amdgcn_permlane32_swap(__float_as_uint(a), __float_as_uint(b), false, false);
    float x = __uint_as_float(r[0]) + __uint_as_float(r[1]);
    asm volatile("" : "+v"(x));
    return x;
}
DI void aload4(u32x4& d, unsigned voff, const char* sb) { asm volatile("s_nop 4\n\tglobal_load_dwordx4 %0, %1, %2" : "=v"(d) : "v"(voff), "s"(sb) : "memory"); }
DI void aload4c(u32x4& d, unsigned voff, const char* sb) { asm volatile("s_nop 4\n\tglobal_load_dwordx4 %0, %1, %2 sc1" : "=v"(d) : "v"(voff), "s"(sb) : "memory"); }
DI void aload2c(u32x2& d, unsigned voff, const char* sb) { asm volatile("s_nop 4\n\tglobal_load_dwordx2 %0, %1, %2 sc1" : "=v"(d) : "v"(voff), "s"(sb) : "memory"); }
DI unsigned wo_w_off(const Ctx& c) { return (unsigned)((8 * (c.wid & 3) + (c.lane >> 3)) * (D_ * 2) + c.jw * 128 + (((c.lane & 7) ^ ((c.lane >> 3) & 6)) << 4)); }
template <int S0, int S1> DI void wo_w_issue(const Ctx& c, const char* W, unsigned vw) {
    const int lw = c.wid & 3;
#pragma unroll
    for (int s = S0; s < S1; ++s) dma16<false>(s * 4096 + lw * 1024, vw, W + (size_t)((2 * (s & 15) + (s >> 4)) * 32) * (D_ * 2));
}

template <bool HALO, bool FIRST> DI void qkv_load(const Ctx& c0, const char* xin, const char* W, const char* Wo, int l, const char* Wb = nullptr, const char* Wo2 = nullptr, const char* outp = nullptr) {
    const Ctx c = fresh(c0);
    asm volatile("" : "+s"(xin), "+s"(W), "+s"(Wo));
    LAS char* smem = c.smem;
    constexpr int NAI = HALO ? 5 : 4, NWI = 6, XS = XS_, NR = HALO ? 144 : 128;
    constexpr Sched<NAI, NWI, FIRST> SC{};
    static_assert(NQA == 3 && NQW == 4, "schedule");
    const int lw = c.wid - 4, lane = c.lane, lr = lane >> 3, lc = lane & 7, fr = lane & 15, fq = lane >> 4;
    const int row0 = c.pq * 128;
    const int pr = (ROTM == 1 || (ROTM == 2 && (c.jq & 7) == 3)) ? 2 * (c.pq & 7) + (c.pq >> 3) : (ROTM == 3 ? (c.pq & 7) + ((c.pq & 7) >> 1) + (c.pq & 8) : c.pq);
    const bool nor = !FIRST && l > 1 && (NOR == 1 || (NOR == 2 && (c.jq & 7) == 3) || (NOR == 3 && (c.jq & 7) != 3));
    const int rot = FIRST ? rot_first(c.pq, c.jq) : (nor ? (c.pq & 8) * NRH : pr & 15);
    unsigned va[5], vw[NWI];
#pragma unroll
    for (int j = 0; j < 4; ++j) va[j] = (unsigned)(FIRST ? xrow<0>(c.pq, 8 * (j * 4 + lw) + lr) + ((lc ^ lr) << 4) : c.pq * 262144 + (j * 4 + lw) * 1024 + lane * 16);
    if (HALO) va[4] = (unsigned)((c.pq > 0 ? (c.pq - 1) * 262144 + 14336 : 0) + lw * 512 + lane * 16);
#pragma unroll
    for (int j = 0; j < NWI; ++j) {
        const int ib = j * 4 + lw, seg = ib >> 3, ii = ib & 7;
        const int f = (lr & 2) | ((ii & 1) << 2);
        vw[j] = (unsigned)((8 * ii + lr) * 6144 + (seg * 1024 + c.jq * 64) * 2 + ((lc ^ f) << 4));
    }
    auto issueA = [&](int s, int slot) {
        const char* b = xin + ((s + rot) & 15) * (FIRST ? 128 : 16384);
#pragma unroll
        for (int j = 0; j < 4; ++j) dma16<true>(QA_OFF + slot * QA_SLOT + (j * 4 + lw) * 1024, va[j], b);
        if (HALO) { if (lane < 32) dma16<true>(QA_OFF + slot * QA_SLOT + 16384 + lw * 512, va[4], b); }
    };
    auto issueW = [&](int s, int slot, int j0 = 0, int j1 = NWI) {
        const char* b = W + (size_t)((s + rot) & 15) * (64 * 6144);
#pragma unroll
        for (int j = j0; j < j1; ++j) dma16<false>(qw_slot(slot) + (j * 4 + lw) * 1024, vw[j], b);
    };
    auto ahead = [&]() {
        const int r3 = (lane * 43) >> 7;
        const unsigned o = (unsigned)((((rot - (c.pq & 7) + 10) * 64 + (c.pq & 7) * 48 + lw * 12 + r3) & 1023) * 6144 + (lane - 3 * r3) * 2048 + c.jq * 128);
        if (lane < 36 && !nor && (WTC != 2 || (c.jq & 7) == 3)) asm volatile("s_mov_b32 m0, %0\n\ts_nop 3\n\tglobal_load_lds_dword %1, %2" ::"s"(M_TCH), "v"(o), "s"(W) : "memory");
    };
    int tv = 0; unsigned long long lt = 0;
    if (FIRST) {
        LSTAMP(55);
    } else {
        auto some = [&](int n0, int n1) {
#pragma unroll
            for (int s = 0; s < WPRE; ++s) {
                const int a = n0 - s * NWI < 0 ? 0 : n0 - s * NWI, b = n1 - s * NWI > NWI ? NWI : n1 - s * NWI;
                if (a < b) issueW(s, s, a, b);
            }
        };
        if (WTC && AH0 == 3) ahead();
        if (RS0) {
            BAR();
            if (WTC && AH0 == 1) ahead();
            if (!RSL || nor || (c.jq & 7) != 3) some(0, RN0);
        }
        if (!RS0 || RN1 > RN0) BAR();
        some(RS0 ? RN0 : 0, RS0 ? RN1 : RW0);
        BAR();
        if (WTC && AH0 == 2) ahead();
        static_assert(!RSL || (RS0 && RN0 == RN1), "order");
        if (RSL && !nor && (c.jq & 7) == 3) some(0, RN0);
        some(RS0 ? RN1 : RW0, WPRE * NWI);
        if (WTC && AH0 == 0) ahead();
    }
    const bool docs = FIRST || (HALO && l == 1);
    const int t = lw * 64 + lane;
    static_assert(MBOX & 1, "residual flag");
    unsigned foff = lane < 32 ? (unsigned)WS_FLAGQ + (lane < 16 ? c.pq * 16 + lane : fr * 16 + c.jq) * FQS : (unsigned)WS_XBOX + (c.pw * 16 + c.jw) * (2 * FQS) + c.jw * 8;
    unsigned ftg = lane < 32 ? (FIRST ? 1u : 5u) : (unsigned)(l + 1);
    asm volatile("" : "+v"(ftg));
    unsigned so = (unsigned)hl<HS>((c.pq * 16 + fq * 4) * 1024 + (lw * 32 + fr * 2) * 8);
    unsigned ho = (unsigned)hl<HS>(((c.pq > 0 ? c.pq - 1 : 0) * 16 + (lane >> 2)) * 1024 + ((c.pq > 0 ? 112 : 0) + lw * 4 + (lane & 3)) * 8);
    const int cq = lw * 12 + (fr < 12 ? fr : 11);
    unsigned co = (unsigned)((fq * 4 * 3072 + (cq >> 4) * 1024 + c.jq * 64 + (cq & 15) * 4) * 4);
    unsigned l3 = (unsigned)(M_CS + (HALO ? 768 : 0) + (cq * 4 + fq) * 4), vwo = wo_w_off(c);
    unsigned l4 = (unsigned)(M_MEAN + (lane < 32 ? 16 + lw * 32 + fr * 2 + fq : lw * 4 + (lane & 3)) * 4);
    asm volatile("" : "+v"(vwo));
    unsigned wt[3], cso, xt, xt1, xso;
    {
        const int q4 = fr >> 2, p4 = lane & 3, fb = (q4 & 2) | ((fq & 1) << 2);
#pragma unroll
        for (int i = 0; i < 3; ++i) {
            const int T = 3 * lw + i;
            wt[i] = (unsigned)((T >> 2) * 8192 + (8 * fq + q4) * 128 + (((2 * (T & 3) + (p4 >> 1)) ^ fb) << 4) + (p4 & 1) * 8);
        }
        const int T = 3 * lw + fq;
        cso = (unsigned)((((HALO ? 16 : 0) + rot) * 3072 + (T >> 2) * 1024 + c.jq * 64 + (T & 3) * 16 + fr) * 4);
        xt = (unsigned)(QA_OFF + (lw * 32 + fr) * 128 + ((fq ^ (fr & 7)) << 4));
        xt1 = xt ^ 64;
        xso = (unsigned)hl<HS>((c.pq * 16 + rot) * 1024 + (lw * 32 + fr) * 8);
    }
    asm volatile("" : "+v"(foff), "+v"(so), "+v"(ho), "+v"(co), "+v"(l3), "+v"(l4));
    asm volatile("" : "+v"(wt[0]), "+v"(wt[1]), "+v"(wt[2]), "+v"(cso), "+v"(xt), "+v"(xt1), "+v"(xso));
    unsigned tp = (unsigned)((lane < 30 ? (c.bid >> 3) * 30 + lane : (c.bid >> 3) * 16 + lane - 30 + (int)(WS_O >> 16)) * 65536 + 65536 - 128);
    unsigned tl = (unsigned)(c.bid * 8192 + lane * 128);
    if (FIRST) asm volatile("" : "+v"(tp), "+v"(tl));
    u32x4 on = {0x3f803f80u, 0x3f803f80u, 0x3f803f80u, 0x3f803f80u};
    asm volatile("" : "+v"(on));
    if (!FIRST && !CPOLL) {
        if (lw == 0) {
            const int pp = c.pq - fq;
            poll_flags(c.ws, lds_epoch(smem), x_find(c), lane < (HALO ? 32 : 16) && pp >= 0, (unsigned)(l + 1));
            STAMP_AT(32 + l * 8 + 0);
        }
    }
    unsigned xfo = x_find(c);
    u64 xep = 0;
    u32x2 xfl;
    const bool xact = lane < (HALO ? 32 : 16) && c.pq - fq >= 0;
    if (!FIRST && EXF) { xep = lds_epoch(smem); asm volatile("" : "+v"(xfo), "+v"(xep)); }
    BAR();
    __builtin_amdgcn_s_setprio(LPRIO);
    if (!FIRST) { issueA(0, 0); if (EXF) aload2c(xfl, xfo, c.ws); issueA(1, 1); }
    else { LSTAMP(57); issueA(1, 1); issueW(1, 1); LSTAMP(58); }
    u32x4 st[4], cs[4]; u32x2 sh, fl; u64 fv = 0; float rs_, rq_, hs;
    const bool fact = FIRST ? lane < 32 : (lane >= (docs ? 16 : 32) && lane <= 32);
    static_assert(XS + 3 <= 12, "aux schedule");
    auto side = [&](auto ktc) {
        constexpr int kt = decltype(ktc)::value, S0 = SPR && !FIRST ? SL : XS;
        {
            constexpr int R0 = !SPR ? 12 : S0 + 2, R1 = !SPR ? 12 : S0 + 3, R2 = !SPR ? 12 : (FIRST ? 12 : S0 + 4), R3 = !SPR ? 12 : (FIRST ? 12 : S0 + 5), R4 = !SPR ? 12 : (FIRST ? 13 : (R4X ? R4X : S0 + 6));
            constexpr int C0 = !SPR ? 13 : (FIRST ? 12 : 11), C1 = !SPR ? 13 : (FIRST ? 13 : 12), C2 = SPR && !FIRST ? C2X : 13;
            auto acc4 = [](u32x4& a, const u32x4& b) {
#pragma unroll
                for (int i = 0; i < 4; ++i) a[i] = __float_as_uint(addf(__uint_as_float(a[i]), __uint_as_float(b[i])));
                asm volatile("" : "+v"(a));
            };
            if constexpr (kt == R0) {
                asm volatile("" : "+v"(st[0]), "+v"(st[1]));
                acc4(st[0], st[1]);
                if (SPR && FIRST) { asm volatile("" : "+v"(st[2])); acc4(st[0], st[2]); }
                if (HALO) {
                    asm volatile("" : "+v"(sh));
                    hs = fold16(__uint_as_float(sh[0]), __uint_as_float(sh[1]));
                    hs = fold32(hs, hs);
                }
            }
            if constexpr (kt == R1) {
                asm volatile("" : "+v"(st[2]), "+v"(st[3]));
                if (!(SPR && FIRST)) acc4(st[3], st[2]);
                acc4(st[0], st[3]);
            }
            if constexpr (kt == C0) {
                if (docs) { asm volatile("" : "+v"(cs[0]), "+v"(cs[1])); acc4(cs[0], cs[1]); }
            }
            if constexpr (kt == R2) {
                asm volatile("s_nop 1" : "+v"(st[0]));
                rs_ = fold16(__uint_as_float(st[0][0]), __uint_as_float(st[0][2])); rq_ = fold16(__uint_as_float(st[0][1]), __uint_as_float(st[0][3]));
                if (HALO) {
                    hs += __uint_as_float(__builtin_amdgcn_update_dpp(0u, __float_as_uint(hs), 0x124, 0xf, 0xf, false));
                    hs += __uint_as_float(__builtin_amdgcn_update_dpp(0u, __float_as_uint(hs), 0x128, 0xf, 0xf, false));
                    asm volatile("" : "+v"(hs));
                }
            }
            if constexpr (kt == R3) {
                rs_ = fold32(rs_, rs_); rq_ = fold32(rq_, rq_);
                if (HALO) {
                    auto hh = __builtin_amdgcn_permlane16_swap(__float_as_uint(hs), __float_as_uint(hs), false, false);
                    if (lane >= 32) { rs_ = __uint_as_float(hh[0]); rq_ = __uint_as_float(hh[1]); }
                    asm volatile("" : "+v"(rs_), "+v"(rq_));
                }
            }
            if constexpr (kt == R4) {
                rs_ *= -1.f / 1024.f;
                asm volatile("" : "+v"(rs_));
                rq_ = __builtin_amdgcn_rsqf(fmaxf(rq_ * (1.f / 1024.f) - rs_ * rs_, 0.f) + LN_EPS);
                asm volatile("" : "+v"(rq_));
            }
            if constexpr (kt == C1) {
                if (docs) { asm volatile("" : "+v"(cs[2]), "+v"(cs[3])); acc4(cs[2], cs[3]); acc4(cs[0], cs[2]); }
            }
            if constexpr (kt == C2) {
                if (docs) {
                    asm volatile("s_nop 1" : "+v"(cs[0]));
                    hs = fold32(fold16(__uint_as_float(cs[0][0]), __uint_as_float(cs[0][1])), fold16(__uint_as_float(cs[0][2]), __uint_as_float(cs[0][3])));
                }
            }
            if constexpr (kt == 13) {
                if (WV0) WAIT_V0();
                if (lane < (HALO ? 36 : 32)) {
                    *(LAS float*)lds(l4) = rs_;
                    *(LAS float*)(lds(l4) + (M_RSTD - M_MEAN)) = rq_;
                }
                if (docs) *(LAS float*)lds(l3) = hs;
                FENCE();
            }
        }
    };
    static_for<0, KT>([&](auto ktc) {
        constexpr int kt = decltype(ktc)::value;
        LSTAMP(3 * kt);
        wait_v<SC.w[kt]>();
        LSTAMP(3 * kt + 1);
        if constexpr (kt == XS || (SPR && !FIRST && kt == 15)) {
            if ((SPR ? docs == (kt == XS) : (RCK || docs)) && lw == 0) {
                asm volatile("" : "+v"(fl));
                const u64 ep = lds_epoch(smem);
                if (!__all(!fact || reached(__builtin_bit_cast(u64, fl), ep, ftg))) poll_flags(c.ws, ep, foff, fact, ftg);
            }
        }
        if constexpr (kt == 14) asm volatile("s_waitcnt lgkmcnt(0)" ::: "memory");
        BAR();
        FENCE();
        LMARK();
        LSTAMP(3 * kt + 2);
        if constexpr (TOPJ & (1 << kt)) side(ktc);
        if constexpr (kt == 3) {
            if (docs && lw == 0 && lane == 0) __hip_atomic_store((GAS u64*)(c.ws + WS_FLAGQ + (c.pq * 16 + c.jq) * FQS), fv, __ATOMIC_RELAXED, __HIP_MEMORY_SCOPE_AGENT);
        }
        if constexpr (kt == XS + 1 - NQA) {
            if ((RCK || docs) && lw == 0) { if (fact) aload2c(fl, foff, c.ws); }
        }
        constexpr int S0 = SPR && !FIRST ? SL : XS;
        {
            const char* sb = c.ws + WS_STATS + (size_t)l * 524288;
            const char* cb = c.ws + WS_CSUM + (HALO ? 16 * 3072 * 4 : 0);
            constexpr int sa = !SPR || !FIRST ? 2 * (kt - S0) : 3 * (kt - S0), sn = !SPR || !FIRST ? 2 : 3;
            constexpr int ca = !SPR ? (kt == XS + 1 ? 0 : (kt == XS + 2 ? 1 : 9)) : (FIRST ? 2 * (kt - XS - 1) : 2 * (kt - XS)), cn = !SPR ? (kt == XS + 1 ? 1 : 3) : 2;
#pragma unroll
            for (int i = sa; i < sa + sn; ++i) if (i >= 0 && i < 4) aload4c(st[i], so, sb + hl<HS>(i * 1024));
            if (HALO && kt == S0) aload2c(sh, ho, sb);
            if constexpr (ca >= 0 && ca < 4) {
                if (docs) {
#pragma unroll
                    for (int i = ca; i < ca + cn; ++i) aload4c(cs[i], co, cb + i * 12288);
                }
            }
        }
        f32x4 d[3], sm[2], g[2];
        bf16x8 wf[3][2], xf[2][2];
        if constexpr (kt == 0) {
            if (docs) {
#pragma unroll
                for (int i = 0; i < 3; ++i)
#pragma unroll
                    for (int ks = 0; ks < 2; ++ks) {
                        const s16x4 lo = tr_read(lds(wt[i]) + ks * 4096), hi = tr_read(lds(wt[i]) + ks * 4096 + 512);
                        wf[i][ks] = __builtin_shufflevector(lo, hi, 0, 1, 2, 3, 4, 5, 6, 7);
                    }
                if (FIRST) {
#pragma unroll
                    for (int mt = 0; mt < 2; ++mt)
#pragma unroll
                        for (int ks = 0; ks < 2; ++ks) xf[mt][ks] = *(const LAS bf16x8*)(lds(ks ? xt1 : xt) + mt * 2048);
                }
            }
            SCHED();
        }
        if constexpr (kt == 0 && !FIRST && EXF) {
            asm volatile("" : "+v"(xfl));
            if (!__all(!xact || reached(__builtin_bit_cast(u64, xfl), xep, (unsigned)(l + 1)))) poll_flags(c.ws, xep, xfo, xact, (unsigned)(l + 1));
        }
        if constexpr (kt + 2 < KT) issueA(kt + 2, (kt + 2) % NQA);
        LPUT(3 * kt + 2);
        if constexpr (kt == 0) {
            SCHED();
            if (docs) {
                const bf16x8 ones = __builtin_bit_cast(bf16x8, on);
                const f32x4 zero = {0.f, 0.f, 0.f, 0.f};
#pragma unroll
                for (int ks = 0; ks < 2; ++ks) {
#pragma unroll
                    for (int i = 0; i < 3; ++i) d[i] = MFMA(ones, wf[i][ks], ks ? d[i] : zero);
                    if (FIRST) {
#pragma unroll
                        for (int mt = 0; mt < 2; ++mt) { sm[mt] = MFMA(ones, xf[mt][ks], ks ? sm[mt] : zero); g[mt] = MFMA(xf[mt][ks], xf[mt][ks], ks ? g[mt] : zero); }
                    }
                }
            }
            SCHED();
        }
        if constexpr (FIRST && kt < 5) {
            if constexpr (kt < 4) issueW(kt + 2, (kt + 2) % NQW);
            if constexpr (kt == 3) issueW(6, 2, 0, NWI / 2);
            if constexpr (kt == 4) { issueW(6, 2, NWI / 2, NWI); issueW(7, 3); }
        } else {
            if constexpr (WPRE == 2 && kt < 2) issueW(kt + 2, kt + 2);
            if constexpr (WPRE == 3 && kt == 0) issueW(3, 3);
            if constexpr (kt >= 1 && kt + 3 < KT) issueW(kt + 3, (kt + 3) % NQW);
        }
        if constexpr (FIRST && kt == WT0) ahead();
        if constexpr (FIRST && kt == TSTEP) {
            if (lw == 1 && lane < 46) touch(M_TCH, tp, c.ws);
        }
        if constexpr (FIRST && kt == TW0) {
            if (lw == 0) touch(M_TCH, tl, Wo);
            else if (lw == 1) { if (lane < 2) touch(M_TCH, (unsigned)(((c.bid >> 3) * 2 + lane) * 65536), outp); }
        }
        if constexpr (FIRST && kt == TW1 && TW1 < KT) {
            touch(M_TCH, tl, lw < 3 ? Wb + (size_t)lw * (2u << 20) : Wo2);
        }
        if constexpr (kt == 0) {
            SCHED();
            if (docs) {
                if (FIRST) {
                    auto rst = __builtin_amdgcn_make_buffer_rsrc(c.ws + WS_STATS, 0, 524288, 0x00020000);
                    const bool b0 = fr & 1, b1 = fr & 2;
                    u32x2 sv[2];
#pragma unroll
                    for (int mt = 0; mt < 2; ++mt) {
                        float qa = b0 ? g[mt][1] : g[mt][0], qb = b0 ? g[mt][3] : g[mt][2];
                        asm volatile("" : "+v"(qa), "+v"(qb));
                        sv[mt] = (u32x2){__float_as_uint(sm[mt][0]), __float_as_uint(b1 ? qb : qa)};
                    }
                    if (fq == (fr >> 2)) {
                        __builtin_amdgcn_raw_buffer_store_b64(sv[0], rst, xso, 0, 16);
                        __builtin_amdgcn_raw_buffer_store_b64(sv[1], rst, xso + hl<HS>(128), 0, 16);
                    }
                }
                const float v = fq == 0 ? d[0][0] : (fq == 1 ? d[1][0] : d[2][0]);
                auto rc = __builtin_amdgcn_make_buffer_rsrc(c.ws + WS_CSUM, 0, 2 * 16 * 3072 * 4, 0x00020000);
                if (lane < 48) __builtin_amdgcn_raw_buffer_store_b32(__float_as_uint(v), rc, cso, 0, 16);
                asm volatile("s_waitcnt lgkmcnt(0)" ::: "memory");
            }
        }
        if constexpr (kt == 1) {
            if (docs && lw == 0) {
                const u32x2 e = *(const LAS u32x2*)(smem + M_EP);
                const u64 es = ((u64)(unsigned)__builtin_amdgcn_readfirstlane(e[1]) << 32) | (unsigned)__builtin_amdgcn_readfirstlane(e[0]);
                fv = es + (FIRST ? 1u : 5u);
                asm volatile("" : "+s"(fv));
            }
        }
        if constexpr (!(TOPJ & (1 << kt))) side(ktc);
        if constexpr (kt == 13) wo_w_issue<0, WT13>(c, Wo, vwo);
        if constexpr (kt == 14) { wo_w_issue<WT13, 12>(c, Wo, vwo); if (HALO) wo_w_issue<23, 23 + WB14>(c, Wo, vwo); }
        if constexpr (kt == 15) {
            if (HALO ? WBE < 2 : WAE < 2) wo_w_issue<12, 18>(c, Wo, vwo);
            if (HALO) wo_w_issue<23 + WB14, 23 + WB14 + WB15>(c, Wo, vwo);
        }
    });
    __builtin_amdgcn_s_setprio(0);
    BAR();
    LMARK(); LPUT(48);
    LSTAMP(48);
    LSTORE(0);
    if (HALO ? WBE < 1 : WAE < 1) wo_w_issue<18, HALO ? 23 : 20>(c, Wo, vwo);
    if (HALO) {
        static_assert(!WBS || (WBE == 1 && WB14 == 0 && WRG <= 12), "Wo weights of the first twelve steps are older than the last 6 + WB15");
        if (WBS) wait_v<6 + WB15>();
        BAR();
        if (WBE) wo_w_issue<WBE < 2 ? 18 : 12, 23>(c, Wo, vwo);
        wo_w_issue<23 + WB14 + WB15, 32>(c, Wo, vwo);
    }
}

DI float max3(float a, float b, float c) { return fmaxf(fmaxf(a, b), c); }
#define SGB(m, n) __builtin_amdgcn_sched_group_barrier(m, n, 0)
template <bool HALO> struct Tail {
    int f[26], n[4], t[4][26], d[4][27], v[4][27], h3[32], s3[32], o3[33], q3[112];
    constexpr Tail() : f{}, n{}, t{}, d{}, v{}, h3{}, s3{}, o3{}, q3{} {
        const int FB[26] = {0, 1, 2, 3, 4, 5, 6, 7, 8, 9, 10, 11, 12, 13, 14, 15, 24, 25, 19, 23, 18, 22, 17, 21, 16, 20};
        const int FA[24] = {0, 1, 2, 3, 4, 5, 6, 7, 8, 9, 10, 11, 12, 13, 14, 15, 16, 17, 20, 21, 18, 19, 22, 23};
        const int TA[4][26] = {{0, 1, 2, 3, 7, 6, 5, 4, 8, 9, 10, 11, 15, 14, 13, 12, 16, 17, 18, 19, 23, 22, 21, 20, 0, 0}, {4, 8, 9, 10, 11, 15, 14, 13, 12, 16, 17, 18, 19, 23, 22, 21, 20, 0, 1, 2, 3, 7, 6, 5, 0, 0}, {16, 17, 18, 19, 23, 22, 21, 20, 0, 1, 2, 3, 7, 6, 5, 4, 8, 9, 10, 11, 15, 14, 13, 12, 0, 0}, {17, 18, 19, 23, 22, 21, 20, 8, 9, 10, 11, 15, 14, 13, 12, 16, 0, 0, 0, 0, 0, 0, 0, 0, 0, 0}};
        const int DA[4][26] = {{1, 1, 1, 1, 1, 1, 1, 1, 1, 1, 1, 1, 1, 1, 1, 1, 1, 1, 1, 1, 1, 1, 0, 0, 0, 0}, {1, 1, 1, 1, 1, 1, 1, 1, 1, 1, 1, 1, 1, 1, 1, 1, 1, 1, 1, 1, 1, 1, 0, 0, 0, 0}, {1, 1, 1, 1, 1, 1, 1, 1, 1, 1, 1, 1, 1, 1, 1, 1, 0, 0, 0, 0, 0, 0, 0, 0, 0, 0}, {2, 2, 2, 2, 1, 1, 1, 1, 1, 1, 1, 1, 0, 0, 0, 0, 0, 0, 0, 0, 0, 0, 0, 0, 0, 0}};
        const int VA[4][26] = {{1, 1, 0, 0, 0, 0, 1, 1, 1, 1, 1, 1, 1, 1, 1, 1, 1, 1, 1, 1, 1, 1, 2, 2, 0, 0}, {1, 1, 1, 1, 1, 1, 1, 1, 1, 1, 1, 1, 1, 1, 1, 1, 1, 1, 1, 1, 1, 1, 2, 2, 0, 0}, {1, 1, 1, 1, 1, 1, 1, 1, 1, 1, 1, 1, 1, 1, 1, 1, 2, 2, 2, 2, 2, 3, 3, 2, 0, 0}, {0, 0, 0, 0, 1, 1, 1, 1, 1, 1, 1, 1, 2, 2, 2, 2, 0, 0, 0, 0, 0, 0, 0, 0, 0, 0}};
        const int TB[4][26] = {{0, 1, 2, 3, 7, 6, 5, 4, 24, 8, 9, 10, 11, 15, 14, 13, 12, 25, 16, 17, 18, 19, 23, 22, 21, 20}, {24, 8, 9, 10, 11, 15, 14, 13, 12, 25, 16, 17, 18, 19, 23, 22, 21, 20, 0, 1, 2, 3, 7, 6, 5, 4}, {25, 16, 17, 18, 19, 23, 22, 21, 20, 0, 1, 2, 3, 7, 6, 5, 4, 24, 8, 9, 10, 11, 15, 14, 13, 12}, {0, 1, 2, 3, 7, 6, 5, 4, 24, 8, 9, 10, 11, 15, 14, 13, 12, 25, 19, 23, 0, 0, 0, 0, 0, 0}};
        const int DB[4][26] = {{1, 1, 1, 1, 1, 1, 1, 1, 1, 1, 1, 1, 1, 1, 1, 1, 1, 1, 1, 1, 1, 1, 1, 0, 0, 0}, {1, 1, 1, 1, 1, 1, 1, 1, 1, 1, 1, 1, 1, 1, 1, 1, 1, 1, 1, 1, 1, 1, 0, 0, 0, 0}, {1, 1, 1, 1, 1, 1, 1, 1, 1, 1, 1, 1, 1, 1, 1, 1, 1, 1, 1, 1, 1, 1, 1, 0, 0, 0}, {2, 2, 1, 1, 1, 1, 1, 1, 1, 1, 1, 1, 1, 1, 1, 1, 1, 1, 1, 0, 0, 0, 0, 0, 0, 0}};
        const int VB[4][26] = {{1, 1, 1, 1, 1, 1, 1, 1, 1, 1, 1, 1, 1, 1, 1, 1, 1, 1, 1, 1, 1, 1, 1, 2, 2, 2}, {1, 1, 1, 1, 1, 1, 1, 1, 1, 1, 1, 1, 1, 1, 1, 1, 1, 1, 1, 1, 1, 1, 3, 3, 3, 2}, {1, 1, 1, 1, 1, 1, 1, 1, 1, 1, 1, 1, 1, 1, 1, 1, 1, 1, 1, 1, 1, 1, 1, 3, 3, 2}, {0, 0, 1, 1, 1, 1, 1, 1, 1, 1, 1, 1, 1, 1, 1, 1, 1, 1, 1, 2, 0, 0, 0, 0, 0, 0}};
        const int G[32] = {8, 12, 9, 13, 10, 14, 11, 15, 16, 17, 20, 21, 18, 19, 22, 23, 0, 4, 1, 5, 2, 6, 3, 7, 0, 4, 1, 5, 2, 6, 3, 7};
        const int NM = HALO ? 26 : 24;
        n[0] = n[1] = n[2] = NM; n[3] = HALO ? 20 : 16;
        for (int i = 0; i < NM; ++i) f[i] = HALO ? FB[i] : FA[i];
        int vv = 0;
        for (int h = 0; h < 4; ++h) {
            v[h][0] = vv;
            for (int i = 0; i < n[h]; ++i) {
                t[h][i] = HALO ? TB[h][i] : TA[h][i];
                d[h][i + 1] = d[h][i] + (HALO ? DB[h][i] : DA[h][i]);
                vv += HALO ? VB[h][i] : VA[h][i];
                v[h][i + 1] = vv;
            }
        }
        for (int i = 0; i < 32; ++i) { h3[i] = G[i]; s3[i] = i >= 16 && i < 24 ? 0 : 1; }
        for (int i = 3; i < 32; ++i) o3[i + 1] = o3[i] + TOPS;
        int nv = 0, ns = 0;
        for (int k = 0; k < 8 * 14; ++k) {
            const bool top = k % TOPS < 2 || nv == 96;
            if (top && ns < 16 && nv >= 12 * (ns / 2 + 1) && (nv == 96 || k % TOPS == (ns & 1))) { q3[k] = (ns / 2) * 14 + 13 - (ns & 1); ++ns; }
            else { q3[k] = (nv / 12) * 14 + nv % 12; ++nv; }
        }
    }
};
template <bool HALO> DI void qkv_consume(const Ctx& c0, int l) {
    const Ctx c = fresh(c0);
    LAS char* smem = c.smem;
    const int lane = c.lane, wr = c.wid >> 1, wc = c.wid & 1, fr = lane & 15, fq = lane >> 4;
    const int sw = HALO ? wr : 0;
    f32x4 acc[4][6], ah[2];
#pragma unroll
    for (int mt = 0; mt < 4; ++mt)
#pragma unroll
        for (int nt = 0; nt < 6; ++nt) { acc[mt][nt] = (f32x4){0.f, 0.f, 0.f, 0.f}; asm volatile("" : "+v"(acc[mt][nt])); }
    ah[0] = ah[1] = (f32x4){0.f, 0.f, 0.f, 0.f};
    if (HALO) asm volatile("" : "+v"(ah[0]), "+v"(ah[1]));
    const unsigned aoff0 = (unsigned)(QA_OFF + wr * 8192 + (HALO || l ? lane * 16 : fr * 128 + ((fq ^ (fr & 7)) << 4)));
    const unsigned hoff0 = (unsigned)(QA_OFF + 16384 + lane * 16);
    unsigned xm = HALO || l ? 1024u : 64u;
    asm volatile("" : "+s"(xm));
    static_assert(((QA_OFF | QA_SLOT) & 2047) == 0, "halves");
    const int q4 = fr >> 2, p4 = lane & 3;
    const int fb = (q4 & 2) | ((fq & 1) << 2);
    unsigned boff[2];
#pragma unroll
    for (int cc = 0; cc < 2; ++cc)
        boff[cc] = (unsigned)((8 * fq + q4) * 128 + (((2 * (2 * wc + (cc ^ sw)) + (p4 >> 1)) ^ fb) << 4) + (p4 & 1) * 8);
    bf16x8 xa[2][4], xh[2], wb[2][6];
    auto ldfrag = [&](int set, unsigned pa, unsigned ph, unsigned pb0, unsigned pb1, int ks) {
#pragma unroll
        for (int mt = 0; mt < 4; ++mt) xa[set][mt] = *(const LAS bf16x8*)(lds(pa) + mt * 2048);
        if (HALO) xh[set] = *(const LAS bf16x8*)lds(ph);
#pragma unroll
        for (int nt = 0; nt < 6; ++nt) {
            const LAS char* pb = lds((nt & 1) ? pb1 : pb0) + (nt >> 1) * 8192 + ks * 4096;
            const s16x4 lo = tr_read(pb), hi = tr_read(pb + 512);
            wb[set][nt] = __builtin_shufflevector(lo, hi, 0, 1, 2, 3, 4, 5, 6, 7);
        }
    };
    constexpr int NM = HALO ? 26 : 24, NH = HALO ? 17 : 16;
    auto mm = [&](int set, int i0, int i1) {
#pragma unroll
        for (int ii = i0; ii < i1; ++ii) {
            const int i = !HALO || ii < 8 ? ii : (!SNK ? (ii < 10 ? ii + 16 : ii - 2) : (ii == 8 ? 24 : (ii < 17 ? ii - 1 : (ii == 17 ? 25 : ii - 2))));
            const int nt = i >> 2, mt = SNK && (nt & 1) ? 3 - (i & 3) : i & 3;
            if (i < 16) acc[mt][nt] = MFMA(wb[set][nt], xa[set][mt], acc[mt][nt]);
            else if (i < 24) acc[mt][nt] = MFMA(xa[set][mt], wb[set][nt], acc[mt][nt]);
            else if (i == 24) ah[0] = MFMA(wb[set][2], xh[set], ah[0]);
            else ah[1] = MFMA(xh[set], wb[set][4], ah[1]);
        }
    };
    auto interleave = [&]() {
#pragma unroll
        for (int i = 0; i < NH; ++i) { SGB(0x008, 1); SGB(0x100, 1); }
        SCHED();
    };
    unsigned sa = 0, sb = 0;
    unsigned pa0 = aoff0, pa1 = aoff0 ^ xm, ph0 = hoff0, ph1 = hoff0 ^ xm, pb0 = boff[0], pb1 = boff[1];
    asm volatile("" : "+v"(pa0), "+v"(pa1), "+v"(pb0), "+v"(pb1));
    if (HALO) asm volatile("" : "+v"(ph0), "+v"(ph1));
    constexpr int M_CSH = M_CS + (HALO ? 768 : 0);
    unsigned sa0 = (unsigned)(M_MEAN + 64 + (wr * 64 + fr) * 4), sa1 = (unsigned)(M_MEAN + 64 + wr * 256 + fq * 16);
    unsigned sa2 = (unsigned)(M_CSH + wc * 128 + sw * 64 + fq * 16), sa6 = (unsigned)(M_CSH + 512 + wc * 128 + sw * 64 + fr * 4);
    unsigned sa3 = sa2 ^ 64, sa7 = sa6 ^ 64, sa4 = (unsigned)(M_MEAN + fr * 4), sa5 = (unsigned)(M_MEAN + fq * 16);
    asm volatile("" : "+v"(sa0), "+v"(sa1), "+v"(sa2), "+v"(sa6), "+v"(sa3), "+v"(sa7));
    if (HALO) asm volatile("" : "+v"(sa4), "+v"(sa5));
    const int head = c.jq * 2 + wc;
    auto rso = __builtin_amdgcn_make_buffer_rsrc(HALO ? c.ws + WS_O + (size_t)l * (8u << 20) + (size_t)((head * 16 + c.pq) * 8 + wr * 4) * (1024 << HO)
                                                      : c.ws + WS_KV + (size_t)(l >> 1) * (16u << 20) + (size_t)((c.pq * 32 + head) * 4 + wr) * (4096 << HK), 0, HALO ? 4096 << HO : 12288 << HK, 0x00020000);
    unsigned xrs = (unsigned)(M_XCH + wc * 6144 + wr * 2048), xhs = (unsigned)(M_XCH + wc * 6144 + 8 - wr * 8);
    unsigned b0m = (c.pq == 0 && wr == 0) ? 15u * 16u : 0u;
    asm volatile("" : "+s"(rso));
    if (HALO) asm volatile("" : "+s"(xrs), "+s"(xhs), "+s"(b0m));
    if (CPOLL && l > 0 && c.wid == CPOLL - 1) {
        const int pp = c.pq - fq;
        static_assert(!EXF || ROTM == 0, "first tiles");
        poll_flags(c.ws, lds_epoch(smem), x_find(c), lane < (HALO ? 32 : 16) && pp >= 0 && (!EXF || ((fr - c.pq) & 15) < 2), (unsigned)(l + 1));
        STAMP_AT(32 + l * 8 + 0);
    }
    BAR();
    if (!HALO) WAIT_V0();
    BAR();
    if (c.wid == 0) STAMP_AT(l * 8 + 0);
    ldfrag(0, pa0, ph0, pb0, pb1, 0);
    SCHED();
    if (ALN) WAIT_L0();
    SCHED();
    int cnt = KT - 3, tv = 0;
    QSTAMP(0);
    auto half0 = [&]() {
        ldfrag(1, pa1, ph1, pb0, pb1, 1);
        mm(0, 0, NH);
        interleave();
        mm(0, NH, NH + 1); SCHED();
        asm volatile("s_addk_i32 %0, %2\n\ts_addk_i32 %1, %3" : "+s"(sa), "+s"(sb) : "i"(QA_SLOT), "i"(QW_SLOT) : "scc"); SCHED();
        mm(0, NH + 1, NH + 2); SCHED();
        asm volatile("s_cmpk_lg_u32 %0, %2\n\ts_cselect_b32 %0, %0, 0\n\ts_cmp_lg_u32 %1, %3\n\ts_cselect_b32 %1, %1, %4" : "+s"(sa), "+s"(sb) : "i"(NQA * QA_SLOT), "i"(3 * QW_SLOT), "i"(QW_TOP) : "scc"); SCHED();
        mm(0, NH + 2, NH + 3); SCHED();
        asm volatile("s_cmp_lg_u32 %0, %2\n\ts_cselect_b32 %0, %0, 0\n\ts_addk_i32 %1, -1" : "+s"(sb), "+s"(cnt) : "i"(QW_TOP + QW_SLOT) : "scc"); SCHED();
        mm(0, NH + 3, NH + 4); SCHED();
        pa0 = aoff0 + sa; pb0 = boff[0] + sb;
        asm volatile("" : "+v"(pa0), "+v"(pb0)); SCHED();
        mm(0, NH + 4, NH + 5); SCHED();
        pb1 = boff[1] + sb; pa1 = pa0 ^ xm;
        asm volatile("" : "+v"(pa1), "+v"(pb1)); SCHED();
        mm(0, NH + 5, NH + 6); SCHED();
        if (HALO) { ph0 = hoff0 + sa; ph1 = ph0 ^ xm; asm volatile("" : "+v"(ph0), "+v"(ph1)); SCHED(); }
        mm(0, NH + 6, NM);
        SCHED();
        WAIT_L0();
        BAR();
        SCHED();
    };
    asm volatile("" : "+s"(sa), "+s"(sb), "+s"(cnt));
    ALIGN8();
    do {
        half0();
        ldfrag(0, pa0, ph0, pb0, pb1, 0);
        mm(1, 0, NH);
        interleave();
        mm(1, NH, NM - 1);
        SCHED();
        WAIT_L0();
        asm volatile("s_nop 0");
        SCHED();
        mm(1, NM - 1, NM);
        SCHED();
    } while (cnt != 0);
    QSTAMP(1);
    FENCE();
    SCHED();

    f32x4 csq[4], m4[4], mh4; float csv[2], mr[4], rr[4], mh;
    auto r1 = [&](unsigned b, int off) { return *(const LAS float*)(lds(b) + off); };
    auto r4_ = [&](unsigned b, int off) { return *(const LAS f32x4*)(lds(b) + off); };
    auto mf = [&](int set, int i) {
        const int nt = i >> 2, mt = i & 3;
        if (i < 16) acc[mt][nt] = MFMA(wb[set][nt], xa[set][mt], acc[mt][nt]);
        else if (i < 24) acc[mt][nt] = MFMA(xa[set][mt], wb[set][nt], acc[mt][nt]);
        else if (i == 24) ah[0] = MFMA(wb[set][2], xh[set], ah[0]);
        else ah[1] = MFMA(xh[set], wb[set][4], ah[1]);
        SCHED();
    };
    auto fold = [&](int i, int e) {
        const int nt = i >> 2, mt = i & 3;
        float t;
        if (i < 16) t = __builtin_fmaf(mr[mt], csq[nt][e], acc[mt][nt][e]);
        else if (i < 24) t = __builtin_fmaf(m4[mt][e], csv[nt - 4], acc[mt][nt][e]);
        else if (i == 24) t = __builtin_fmaf(mh, csq[2][e], ah[0][e]);
        else t = __builtin_fmaf(mh4[e], csv[0], ah[1][e]);
        asm volatile("" : "+v"(t));
        if (i < 24) acc[mt][nt][e] = t; else ah[i - 24][e] = t;
    };
    constexpr Tail<HALO> TL{};
    constexpr int NP = HALO ? 6 : 2, FN = 4 * NM;
    static_assert(TL.v[3][TL.n[3]] == NP + FN + (HALO ? 2 : 0) && KT % NQA == 1 && KT % NQW == 0, "tail");
    s16x4 wl[6], wh[6];
    auto frd = [&](int set, int k, int ao) {
        if (k < 4) xa[set][k] = *(const LAS bf16x8*)(lds(set ? pa1 : pa0) + ao + k * 2048);
        else if (k == 16) xh[set] = *(const LAS bf16x8*)(lds(set ? ph1 : ph0) + ao);
        else {
            const int nt = (k - 4) >> 1;
            const LAS char* pb = lds((nt & 1) ? pb1 : pb0) + (nt >> 1) * 8192 + set * 4096;
            if (k & 1) { wh[nt] = tr_read(pb + 512); wb[set][nt] = __builtin_shufflevector(wl[nt], wh[nt], 0, 1, 2, 3, 4, 5, 6, 7); }
            else wl[nt] = tr_read(pb);
        }
    };
    constexpr int RS = M_RSTD - M_MEAN;
    unsigned vl, vg, tb, xr, xh_, b0a, qb0, qb1;
    f32x4 bprev, bcur, bprev0; u32x4 ps; u32x2 ko; float ninf;
    auto rdop = [&](int h, int k) {
        const int j = k - NH;
        if (h == 0) {
            if (k < 2) { mr[2 * k] = r1(sa0, k * 128); mr[2 * k + 1] = r1(sa0, k * 128 + 64); }
            else if (k < 4) csq[k - 2] = r4_((k & 1) ? sa3 : sa2, 0);
            else if (j < 4) frd(0, k - 4, 2 * QA_SLOT);
            else csq[j - 2] = r4_((j & 1) ? sa3 : sa2, 256);
        } else if (j < 0) frd(h & 1, k, h == 1 ? 2 * QA_SLOT : 0);
        else if (h == 1) {
            if (j < 2) csv[j] = r1(j ? sa7 : sa6, 0);
            else if (HALO && j == 2) mh = r1(sa4, 0);
            else if (HALO && j == 3) mh4 = r4_(sa5, 0);
            else { const int mt = HALO ? 7 - j : j - 2; m4[mt] = r4_(sa1, mt * 64); }
        } else if (h == 2) {
            if (j == 0) m4[2] = r4_(sa1, 128);
            if (j == 1) ko = *(const LAS u32x2*)(lds(tb) + 3072);
            if (j == 2) { rr[0] = r1(sa0, RS); rr[1] = r1(sa0, 64 + RS); }
            if (j == 3) { rr[2] = r1(sa0, 128 + RS); rr[3] = r1(sa0, 192 + RS); }
            if (j >= 4) m4[5 - j] = r4_(sa1, (5 - j) * 64);
        } else {
            if (j == 0) bprev = r4_(tb, 0);
            if (j == 1) bcur = r4_(tb, 1024);
            if (j == 2) ps = *(const LAS u32x4*)(lds(tb) + 2048);
            if (j == 3) bprev0 = r4_(b0a, 0);
        }
    };
    auto vop = [&](int k) {
        if (k == 0) { asm volatile("" : "+v"(boff[0])); qb0 = boff[0] + QW_TOP; asm volatile("" : "+v"(qb0)); }
        else if (k == 1) { asm volatile("" : "+v"(boff[1])); qb1 = boff[1] + QW_TOP; asm volatile("" : "+v"(qb1)); }
        else if (k < NP) {
            if (k == 2) { vl = (unsigned)(lane * 16); asm volatile("" : "+v"(vl)); }
            if (k == 3) { tb = vl + M_TAB; asm volatile("" : "+v"(tb)); }
            if (k == 4) { b0a = tb | b0m; asm volatile("" : "+v"(b0a)); }
            if (k == 5) { xr = xrs + vl; asm volatile("" : "+v"(xr)); }
        } else if (k < NP + FN) fold(TL.f[(k - NP) >> 2], (k - NP) & 3);
        else {
            if (k == NP + FN) { xh_ = xhs + vl; asm volatile("" : "+v"(xh_)); }
            if (k == NP + FN + 1) { ninf = -INFINITY; asm volatile("" : "+v"(ninf)); }
        }
    };
    unsigned wa, wq;
    int wr2 = wr, wc2 = wc;
#pragma unroll
    for (int i = 0; i < NM; ++i) {
        mf(0, TL.t[0][i]);
        if (TL.t[0][i] < 24) asm volatile("" : "+v"(acc[TL.t[0][i] & 3][TL.t[0][i] >> 2])); else asm volatile("" : "+v"(ah[TL.t[0][i] & 1]));
        FENCE();
        if (i < NH) frd(1, i, 0);
        if (!HALO && i == NH + 3) { asm volatile("" : "+s"(wr2), "+s"(wc2)); vl = (unsigned)(lane * 16); wq = (unsigned)(AT_Q + (wc2 * 8 + wr2 * 4) * 1024) + vl; asm volatile("" : "+v"(vl), "+v"(wq)); }
        if (!HALO && i == NH + 4) { asm volatile("" : "+s"(wr2), "+s"(wc2)); wa = (unsigned)(AT_OWN + wc2 * 16384 + wr2 * 4096) + vl; asm volatile("" : "+v"(wa)); }
        if (i == NH + 1) { asm volatile("" : "+v"(boff[0]), "+v"(boff[1])); pb0 = boff[0] + 2 * QW_SLOT; pb1 = boff[1] + 2 * QW_SLOT; asm volatile("" : "+v"(pb0), "+v"(pb1)); }
        if (i == NH + 2) { pa0 = aoff0; pa1 = aoff0 ^ xm; asm volatile("" : "+v"(pa1)); if (HALO) { ph0 = hoff0; ph1 = hoff0 ^ xm; asm volatile("" : "+v"(ph1)); } }
        SCHED();
    }
    WAIT_L0();
    BAR();
    FENCE();
    SCHED();
    auto frags = [&](int h) {
        static_assert(TL.d[0][NM] == NH + 6 && TL.d[1][NM] == NH + (HALO ? 5 : 6) && TL.d[2][NM] == NH + (HALO ? 6 : 0) && TL.d[3][TL.n[3]] == NH + (HALO ? 4 : 0), "reads");
        if (h == 0) __builtin_amdgcn_s_waitcnt(0xC27F);
        if (h == 1) __builtin_amdgcn_s_waitcnt(HALO ? 0xC57F : 0xC67F);
        if (h == 2) __builtin_amdgcn_s_waitcnt(HALO ? 0xC67F : 0xC07F);
        if (h == 3) __builtin_amdgcn_s_waitcnt(HALO ? 0xC47F : 0xC07F);
    };
#pragma unroll
    for (int h = 0; h < 4; ++h) {
        if (h == 2) { pb0 = qb0; pb1 = qb1; }
#pragma unroll
        for (int i = 0; i < TL.n[h]; ++i) {
            if (!(h & 1) && i == TL.n[h] - 1) { frags(h); SCHED(); if (h) QSTAMP(4); }
            mf(~h & 1, TL.t[h][i]);
#pragma unroll
            for (int k = TL.d[h][i]; k < TL.d[h][i + 1]; ++k) { FENCE(); rdop(h, k); }
            SCHED();
#pragma unroll
            for (int k = TL.v[h][i]; k < TL.v[h][i + 1]; ++k) vop(k);
            SCHED();
        }
        if (h & 1) {
            frags(h);
            if (h == 1) QSTAMP(2); else QSTAMP(5);
            BAR();
            FENCE();
            SCHED();
            if (h == 1) QSTAMP(3); else QSTAMP(6);
        }
    }
    if (!HALO) {
        f32x4 r4[4];
        mf(1, 8);
#pragma unroll
        for (int mt = 0; mt < 4; ++mt) rr[mt] = r1(sa0, mt * 64 + RS);
        SCHED();
        mf(1, 12);
#pragma unroll
        for (int mt = 0; mt < 4; ++mt) r4[mt] = r4_(sa1, mt * 64 + RS);
        SCHED();
        mf(1, 9);
        vg = HK ? (unsigned)hv<HK>(lane) : vl;
        if (HK) asm volatile("" : "+v"(vg));
        SCHED();
        u32x4 pk[8];
        auto op = [&](int n) {
            if (n >= 8 * 14) return;
            const int g = TL.q3[n] / 14, k = TL.q3[n] % 14;
            const int ta = g < 4 ? g : (g - 4) & 2, tb = g < 4 ? g : ta + 1, na = g < 4 ? 2 : 4 + (g & 1), nb = g < 4 ? 3 : na;
            f32x4& A = acc[ta][na]; f32x4& Bv = acc[tb][nb];
            if (k < 12) {
                const int h = k / 6, kk = k % 6;
                f32x4& X = h ? Bv : A;
                const int tx = h ? tb : ta;
                if (kk < 4) { float t = X[kk] * (g < 4 ? rr[tx] : r4[tx][kk]); asm volatile("" : "+v"(t)); X[kk] = t; }
                else { unsigned t = cvt_pk_bf16(X[2 * (kk - 4)], X[2 * (kk - 4) + 1]); asm volatile("" : "+v"(t)); pk[g][2 * h + kk - 4] = t; }
            } else if (k == 12) __builtin_amdgcn_raw_buffer_store_b128(pk[g], rso, vg + hp<HK>(g & 3), (g >> 2) * (8192 << HK), 16);
            else *(LAS u32x4*)(lds(wa) + (g >> 2) * 8192 + (g & 3) * 1024) = pk[g];
        };
        constexpr int NOP = 8 * 14;
#pragma unroll
        for (int i = 3; i < 32; ++i) {
            mf(TL.s3[i], TL.h3[i]);
#pragma unroll
            for (int n = TL.o3[i]; n < TL.o3[i + 1]; ++n) { op(n); SCHED(); }
        }
        QSTAMP(7);
        if (c.wid == 0) STAMP_AT(l * 8 + 1);
#pragma unroll
        for (int n = TL.o3[32]; n < NOP; ++n) op(n);
        SCHED();
        QSTAMP(8);
#pragma unroll
        for (int mt = 0; mt < 4; ++mt) {
            const u32x2 q0 = pack4(acc[mt][0] * (rr[mt] * QSCALE)), q1 = pack4(acc[mt][1] * (rr[mt] * QSCALE));
            *(LAS u32x4*)(lds(wq) + mt * 1024) = (u32x4){q0[0], q0[1], q1[0], q1[1]};
        }
        QSTAMP(9);
        QSTORE();
        if (K0E) {
            SCHED();
            WAIT_L0();
            BAR();
            FENCE();
            SCHED();
            if (KSP == 4) {
                publish<4, 0>(smem, lane, c.ws, (unsigned)WS_FLAGQ + (c.pq * 16 + c.jq) * FQS, lane == 0, (unsigned)(4 * l + 2));
                if (c.wid == 0) STAMP_AT(l * 8 + 2);
                SCHED();
            }
        }
    } else {
        typedef unsigned u32x10 __attribute__((ext_vector_type(10)));
        u32x4 kp[4], qp[4];
        u32x10 vw[2];
        float ks[2][4], cs[2][4];
        auto pk8 = [&](const f32x4& x, const f32x4& y) { const u32x2 a = pack4(x), b = pack4(y); u32x4 r = {a[0], a[1], b[0], b[1]}; asm volatile("" : "+v"(r)); SCHED(); return r; };
        auto pkv = [&](int mt, int cc) { const u32x2 a = pack4(acc[mt][4 + cc]); vw[cc][2 * mt + 2] = a[0]; vw[cc][2 * mt + 3] = a[1]; };
        auto fr8 = [](const u32x4& x) { return __builtin_bit_cast(bf16x8, x); };
        mf(1, 24);
        unsigned ka0 = ko[0] + wr * 256, ka1 = ko[1] + wr * 256;
        asm volatile("" : "+v"(ka0), "+v"(ka1));
        SCHED();
        mf(1, 25);
#pragma unroll
        for (int mt = 0; mt < 4; ++mt) ks[0][mt] = r1(ka0, mt * 64);
        rr[0] *= QSCALE; asm volatile("" : "+v"(rr[0]));
        SCHED();
        mf(1, 11);
#pragma unroll
        for (int mt = 0; mt < 4; ++mt) ks[1][mt] = r1(ka1, mt * 64);
        rr[1] *= QSCALE; asm volatile("" : "+v"(rr[1]));
        SCHED();
        mf(1, 15);
        rr[2] *= QSCALE; asm volatile("" : "+v"(rr[2]));
        rr[3] *= QSCALE; asm volatile("" : "+v"(rr[3]));
        SCHED();
        mf(1, 19);
        const u32x4 hh = pk8(ah[0], ah[1]);
        mf(1, 23);
        *(LAS u32x2*)lds(xh_) = (u32x2){hh[0], hh[1]}; *(LAS u32x2*)(lds(xh_) + 1024) = (u32x2){hh[2], hh[3]};
        SCHED();
        mf(1, 10);
        kp[3] = pk8(acc[3][2], acc[3][3]);
        mf(1, 14);
        *(LAS u32x4*)(lds(xr) + 2048) = kp[3];
        pkv(3, 0); asm volatile("" : "+v"(vw[0]));
        *(LAS u32x2*)(lds(xr) + 3072) = (u32x2){vw[0][8], vw[0][9]};
        SCHED();
        mf(1, 3);
        pkv(3, 1); asm volatile("" : "+v"(vw[1]));
        *(LAS u32x2*)(lds(xr) + 3072 + 8) = (u32x2){vw[1][8], vw[1][9]};
        SCHED();
        mf(1, 7);
        mf(1, 9);
        kp[2] = pk8(acc[2][2], acc[2][3]);
        mf(1, 13);
        qp[3] = pk8(acc[3][0], acc[3][1]);
        mf(1, 2);
        f32x4 s0[4], s1[4];
        u32x4 pk;
        auto sc = [&](int mt, int h) {
            if (h) { s1[mt] = MFMA(fr8(kp[mt]), fr8(qp[mt]), bcur); asm volatile("" : "+v"(s1[mt])); }
            else { s0[mt] = MFMA(fr8(mt ? kp[mt - (mt > 0)] : pk), fr8(qp[mt]), mt == 0 ? bprev0 : bprev); asm volatile("" : "+v"(s0[mt])); }
            SCHED();
        };
        auto csm = [&](int mt) { cs[0][mt] = ks[0][mt] * rr[mt]; asm volatile("" : "+v"(cs[0][mt])); cs[1][mt] = ks[1][mt] * rr[mt]; asm volatile("" : "+v"(cs[1][mt])); };
        float u[2][4], mx[4], iv[4];
        auto sv = [](float x) { asm volatile("" : "+v"(x)); return x; };
        auto mx2 = [&](float a, float b) { return max3(ninf, a, b); };
        auto pick = [&](int mt, int h) { u[h][mt] = sv(max3(mx2(s0[mt][2 * h], s0[mt][2 * h + 1]), s1[mt][2 * h], s1[mt][2 * h + 1]) * cs[h][mt]); };
        auto vf = [&](int i) { const int mt = 2 - (i >> 2), nt = 4 + (i & 1), set = (i >> 1) & 1; SCHED(); acc[mt][nt] = MFMA(xa[set][mt], wb[set][nt], acc[mt][nt]); asm volatile("" : "+v"(acc[mt][nt])); SCHED(); };
        mf(1, 6);
        kp[1] = pk8(acc[1][2], acc[1][3]);
        mf(1, 8);
        WAIT_L0();
        QSTAMP(7);
        BAR();
        FENCE();
        SCHED();
        QSTAMP(8);
        mf(1, 12);
        {
            const u32x2 k0 = *(const LAS u32x2*)(lds(xr) + 8);
            FENCE(); SCHED();
            const u32x2 k1 = *(const LAS u32x2*)lds(xr);
            FENCE(); SCHED();
            const u32x2 a = *(const LAS u32x2*)(lds(xr) + 1024 + 8);
            FENCE(); SCHED();
            const u32x2 b = *(const LAS u32x2*)(lds(xr) + 1024);
            pk = (u32x4){k0[0], k0[1], k1[0], k1[1]};
            vw[0][0] = a[0]; vw[0][1] = a[1]; vw[1][0] = b[0]; vw[1][1] = b[1];
        }
        SCHED();
        auto sw16 = [](float& a, float& b) { auto r = __builtin_amdgcn_permlane16_swap(__float_as_uint(a), __float_as_uint(b), false, false); a = __uint_as_float(r[0]); b = __uint_as_float(r[1]); };
        auto sw32 = [](float& a, float& b) { auto r = __builtin_amdgcn_permlane32_swap(__float_as_uint(a), __float_as_uint(b), false, false); a = __uint_as_float(r[0]); b = __uint_as_float(r[1]); };
        auto ldr = [&](int i) { };
        auto top = [&](int mt) { mx[mt] = sv(mx2(u[0][mt], u[1][mt])); };
        auto pv1 = [&](int mt, int k) {
            SCHED();
            vw[k >> 1][2 * mt + 2 + (k & 1)] = cvt_pk_bf16(acc[mt][4 + (k >> 1)][2 * (k & 1)], acc[mt][4 + (k >> 1)][2 * (k & 1) + 1]);
            asm volatile("" : "+v"(vw[k >> 1]));
            SCHED();
        };
        auto uk = [&](int mt) { SCHED(); u[0][mt] = sv(u[0][mt] * ks[0][mt]); u[1][mt] = sv(u[1][mt] * ks[1][mt]); SCHED(); };
        mf(1, 1);
        qp[2] = pk8(acc[2][0], acc[2][1]);
        mf(1, 5);
        csm(3); SCHED();
        mf(1, 0);
        kp[0] = pk8(acc[0][2], acc[0][3]);
        mf(1, 4);
        csm(2); SCHED();
        sc(3, 1);
        qp[1] = pk8(acc[1][0], acc[1][1]);
        sc(3, 0);
        csm(1); ldr(0); SCHED();
        sc(2, 1);
        qp[0] = pk8(acc[0][0], acc[0][1]);
        sc(2, 0);
        csm(0); pick(3, 0); SCHED();
        sc(1, 1);
        pick(3, 1); ldr(1); SCHED();
        sc(1, 0);
        pick(2, 0); top(3); SCHED();
        sc(0, 1);
        pick(2, 1); ldr(2); SCHED();
        sc(0, 0);
        if (c.wid == 0) STAMP_AT(l * 8 + 1);
        pick(1, 0); top(2);
        vf(0);
        pick(1, 1); ldr(3);
        vf(1);
        sw16(mx[2], mx[3]);
        pick(0, 0);
        vf(2);
        pick(0, 1); top(1);
        vf(3);
        top(0); mx[2] = sv(mx2(mx[2], mx[3]));
        vf(4);
        sw16(mx[0], mx[1]); mx[0] = sv(mx2(mx[0], mx[1]));
        vf(5); pv1(2, 0);
        sw32(mx[0], mx[2]);
        { const float a = mx[0], b = mx[2]; mx[0] = sv(mx2(a, b)); mx[2] = sv(mx2(b, a)); }
        vf(6); pv1(2, 1);
        sw32(mx[0], mx[2]);
        mx[3] = sv(mx[2]); mx[1] = sv(mx[0]);
        vf(7);
        sw16(mx[2], mx[3]); sw16(mx[0], mx[1]);
        SCHED();
#pragma unroll
        for (int mm = 0; mm < 4; ++mm) { const int mt = 3 - mm; u[0][mt] = sv(u[0][mt] - mx[mt]); u[1][mt] = sv(u[1][mt] - mx[mt]); if (mm == 1) vf(8); }
        vf(9);
#pragma unroll
        for (int mm = 0; mm < 4; ++mm) {
            const int mt = 3 - mm;
            u[0][mt] = sv(__builtin_amdgcn_exp2f(u[0][mt])); u[1][mt] = sv(__builtin_amdgcn_exp2f(u[1][mt]));
            if (mm > 0) iv[mt + 1] = sv(u[0][mt + 1] + u[1][mt + 1]);
            if (mm < 2) vf(10 + mm);
        }
        SCHED();
        sw16(iv[2], iv[3]);
        iv[0] = sv(u[0][0] + u[1][0]);
        iv[2] = sv(iv[2] + iv[3]);
        pv1(2, 2);
        sw16(iv[0], iv[1]); iv[0] = sv(iv[0] + iv[1]);
        uk(3);
        sw32(iv[0], iv[2]);
        iv[0] = sv(iv[0] + iv[2]);
        pv1(2, 3);
        iv[0] = sv(__builtin_amdgcn_rcpf(iv[0]));
        pv1(1, 0);
        iv[2] = sv(iv[0]);
        uk(2);
        sw32(iv[0], iv[2]);
        iv[3] = sv(iv[2]); iv[1] = sv(iv[0]);
        pv1(1, 1);
        sw16(iv[2], iv[3]); sw16(iv[0], iv[1]);
        SCHED();
        f32x4 o[4][2];
        u32x4 pf[4];
        auto nrm = [&](int mt) { u[0][mt] = sv(u[0][mt] * iv[mt]); u[1][mt] = sv(u[1][mt] * iv[mt]); SCHED(); };
        auto prb = [&](int mt) {
            const unsigned p = cvt_pk_bf16(u[0][mt], u[1][mt]);
            pf[mt] = (u32x4){__builtin_amdgcn_perm(0u, p, ps[0]), __builtin_amdgcn_perm(0u, p, ps[1]), __builtin_amdgcn_perm(0u, p, ps[2]), __builtin_amdgcn_perm(0u, p, ps[3])};
            asm volatile("" : "+v"(pf[mt]));
            SCHED();
        };
        auto pvm = [&](int mt, int cc) {
            o[mt][cc] = MFMA(fr8((u32x4){vw[cc][2 * mt], vw[cc][2 * mt + 1], vw[cc][2 * mt + 2], vw[cc][2 * mt + 3]}), fr8(pf[mt]), ((f32x4){0.f, 0.f, 0.f, 0.f}));
            asm volatile("" : "+v"(o[mt][cc]));
            SCHED();
        };
        auto out = [&](int mt) {
            const u32x2 lo = pack4(o[mt][0]), hi = pack4(o[mt][1]);
            __builtin_amdgcn_raw_buffer_store_b128((u32x4){lo[0], lo[1], hi[0], hi[1]}, rso, vg + hp<HO>(mt), 0, 16);
            SCHED();
        };
        vg = (unsigned)hv<HO>(lane);
        asm volatile("" : "+v"(vg));
        nrm(3); nrm(2); prb(3);
        pv1(1, 2); pv1(1, 3);
        pvm(3, 0); prb(2); pvm(3, 1);
        uk(1); uk(0);
        pvm(2, 0); nrm(1); nrm(0); pvm(2, 1);
        prb(1); pv1(0, 0); pv1(0, 1);
        pvm(1, 0); pv1(0, 2); pv1(0, 3); prb(0);
        pvm(1, 1); out(3); pvm(0, 0); out(2); pvm(0, 1); out(1); out(0);
        SCHED();
        QSTAMP(9);
        if (c.wid == 0) STAMP_AT(l * 8 + 2);
        publish<4, 0>(smem, lane, c.ws, o_post(c), o_act(c), (unsigned)(4 * l + 4));
        QSTAMP(10);
        QSTORE();
        if (c.wid == 0) STAMP_AT(l * 8 + 4);
    }
}

#define MFMA32(a, b, c) __builtin_amdgcn_mfma_f32_32x32x16_bf16(__builtin_bit_cast(bf16x8, a), __builtin_bit_cast(bf16x8, b), (c), 0, 0, 0)
DI float max16(const f32x16& s) {
    float m = max3(s[0], s[1], s[2]), n = max3(s[3], s[4], s[5]);
    m = max3(m, s[6], s[7]); n = max3(n, s[8], s[9]);
    m = max3(m, s[10], s[11]); n = max3(n, s[12], s[13]);
    m = max3(max3(m, s[14], s[15]), n, n);
    auto b = __builtin_amdgcn_permlane32_swap(__float_as_uint(m), __float_as_uint(m), false, false);
    return __builtin_amdgcn_fmed3f(__uint_as_float(b[0]), __uint_as_float(b[1]), INFINITY);
}
template <bool HI> DI void attn_a(const Ctx& c0, int l, const char* Wo, KP kp) {
    const Ctx c = fresh(c0);
    asm volatile("" : "+s"(Wo));
    LAS char* smem = c.smem;
    const int lane = c.lane, w = c.wid;
    constexpr int H = HI ? 0 : 1;
    int qt = HI ? w - 4 : 3 - w;
    asm volatile("" : "+s"(qt));
    const float NEG = -INFINITY;
    int zq = (lane & 31) - 4 * (lane >> 5);
    unsigned lb = (unsigned)(((lane >> 4) & 1) * 1024 + ((lane >> 5) * 16 + (lane & 15)) * 16);
    unsigned qa = (unsigned)(AT_Q + H * 8192 + qt * 2048) + lb, ta = (unsigned)(AT_OWN + H * 16384 - 3 * 2048 + qt * 2048) + lb;
    asm volatile("" : "+v"(lb), "+v"(qa), "+v"(ta));
    auto rd = [&](unsigned base, int off) { return *(const LAS u32x4*)(lds(base) + off); };
    f32x16 ce;
    u32x4 sel;
    int zb = (int)((0xfffffffeu << (lane & 31)) >> (4 * (lane >> 5)));
    asm volatile("" : "+v"(zb));
    auto masks = [&]() {
#pragma unroll
        for (int r = 0; r < 16; ++r) ce[r] = __int_as_float(__builtin_amdgcn_sbfe(zb, 8 * (r >> 2) + (r & 3), 1) << 23);
        const unsigned s1 = ((lane >> 4) ^ (lane >> 2)) & 1 ? 0u : 0x3f803f80u;
        sel = (u32x4){s1, s1, s1, s1};
        asm volatile("" : "+v"(ce), "+v"(sel));
    };
    u64 ep;
    char* myflag;
    unsigned cnta = M_CNT, one = 1u;
    char* ob;
    const char *kvb, *s1_, *s2_;
    unsigned vk, dst, fo;
    auto setup = [&]() {
        const u32x2 e = *(const LAS u32x2*)(smem + M_EP);
        ep = ((u64)(unsigned)__builtin_amdgcn_readfirstlane(e[1]) << 32) | (unsigned)__builtin_amdgcn_readfirstlane(e[0]);
        myflag = c.ws + WS_FLAGQ + (c.pq * 16 + c.jq) * FQS;
        ob = c.ws + WS_O + (size_t)l * (8u << 20) + (size_t)(((c.jq * 2 + H) * 16 + c.pq) * 8192 + qt * 2048);
        kvb = c.ws + WS_KV + (size_t)(l >> 1) * (16u << 20) + (size_t)(c.jq * 32768);
        asm volatile("" : "+s"(ep), "+s"(myflag), "+s"(ob), "+s"(kvb));
        asm volatile("" : "+v"(cnta), "+v"(one));
    };
    auto setup2 = [&]() {
        const int p1 = c.pq >= 1 ? c.pq - 1 : c.pq, p2 = c.pq >= 2 ? c.pq - 2 : c.pq;
        fo = (unsigned)(WS_FLAGQ + (((lane & 1) ? p2 : p1) * 16 + c.jq) * FQS);
        s1_ = kvb + (size_t)p1 * 524288;
        s2_ = kvb + (size_t)p2 * 524288;
        vk = (unsigned)((w & 3) * 1024 + lane * 16); dst = (unsigned)(AT_N + (w & 3) * 1024);
        asm volatile("" : "+s"(s1_), "+s"(dst));
        if (!HI) { asm volatile("" : "+s"(s2_)); asm volatile("" : "+v"(fo)); }
        asm volatile("" : "+v"(vk));
    };
    auto arrive = [&](auto cc, unsigned msk, unsigned val) {
        unsigned old = 0;
        if (lane == 0) asm volatile("ds_add_rtn_u32 %0, %1, %2 offset:%3\n\ts_waitcnt lgkmcnt(0)" : "+v"(old) : "v"(cnta), "v"(one), "i"(decltype(cc)::value * 4) : "memory");
        if ((old & msk) == msk) __hip_atomic_store((GAS u64*)myflag, ep + (4 * l + val), __ATOMIC_RELAXED, __HIP_MEMORY_SCOPE_AGENT);
    };
    static_assert(!(MBOX & 2) && !HO && !HK, "layout");
    int tv = 0;
    auto kvs = [&]() {
        if (!HI) {
            SCHED();
            setup2();
            SCHED();
            LSTAMP(2);
            WAIT_V0();
            arrive(std::integral_constant<int, 0>{}, 3u, 2u);
            if (w == 0) STAMP_AT(l * 8 + 2);
            LSTAMP(3);
            SCHED();
        }
    };
    if (HI) { masks(); setup(); setup2(); }
    LSTAMP(0);
    if (HI) BAR();
    FENCE();
    SCHED();
    LSTAMP(1);
    u32x4 q0, q1, k0, k1, va, vp, pa, pb;
    f32x16 cm, s0, s1, o;
    f32x4 ls;
    auto stage = [&](f32x16& sc, f32x16& sn, const f32x16& cn, bool pend, bool zero, unsigned kb, int ko, unsigned vb, int vo1, unsigned nb, int vo0) {
        auto ex = [&](int i) { float x = __builtin_amdgcn_exp2f(sc[i]); asm volatile("" : "+v"(x)); sc[i] = x; SCHED(); };
        auto cv = [&](u32x4& p, int j, int i) { unsigned x = cvt_pk_bf16(sc[i], sc[i + 1]); asm volatile("" : "+v"(x)); p[j] = x; SCHED(); };
        sn = MFMA32(k0, q0, cn); SCHED();
        k0 = rd(kb, ko); SCHED();
        ex(0); ex(1); ex(2);
        if (pend) { o = MFMA32(vp, pb, o); SCHED(); }
        ex(3); ex(8); ex(9);
        sn = MFMA32(k1, q1, sn); SCHED();
        k1 = rd(kb, ko + 512); SCHED();
        ex(10); ex(11); cv(pa, 0, 0); cv(pa, 1, 2);
        if (pend) { ls = MFMA(__builtin_bit_cast(bf16x8, sel), __builtin_bit_cast(bf16x8, pb), ls); SCHED(); }
        vp = rd(vb, 8192 + vo1 + 512); SCHED();
        cv(pa, 2, 8); cv(pa, 3, 10); ex(4); ex(5);
        o = MFMA32(va, pa, zero ? (f32x16)(0.f) : o); SCHED();
        va = rd(nb, 8192 + vo0); SCHED();
        ex(6); ex(7); ex(12);
        ls = MFMA(__builtin_bit_cast(bf16x8, sel), __builtin_bit_cast(bf16x8, pa), zero ? (f32x4)(0.f) : ls); SCHED();
        ex(13); ex(14); ex(15); cv(pb, 0, 4); cv(pb, 1, 6); cv(pb, 2, 12); cv(pb, 3, 14);
    };
    auto flush = [&]() {
        o = MFMA32(vp, pb, o); SCHED();
        ls = MFMA(__builtin_bit_cast(bf16x8, sel), __builtin_bit_cast(bf16x8, pb), ls); SCHED();
    };
    float nm;
    {
        q0 = rd(qa, 0); q1 = rd(qa, 512);
        const u32x4 d0 = rd(ta, 6144), d1 = rd(ta, 6144 + 512);
        k0 = rd(ta, 4096); k1 = rd(ta, 4096 + 512); va = rd(ta, 8192 + 6144);
        SCHED();
        if (!HI) { masks(); SCHED(); }
        s0 = MFMA32(d0, q0, ce);
        s0 = MFMA32(d1, q1, s0);
        SCHED();
        if (!HI) { setup(); SCHED(); }
        if (KSP == 0) kvs();
        nm = -max16(s0);
        asm volatile("" : "+v"(nm));
#pragma unroll
        for (int r = 0; r < 16; ++r) { cm[r] = nm; s0[r] = addf(s0[r], nm); }
        asm volatile("" : "+v"(cm), "+v"(s0));
        SCHED();
        if (KSP == 1) kvs();
        stage(s0, s1, cm, false, true, ta, 2048, ta, 6144, ta, 4096);
        if (KSP == 2) kvs();
        if (qt > 0) {
            stage(s1, s0, cm, true, false, ta, 0, ta, 4096, ta, 2048);
            if (KSP == 3) kvs();
            if (qt > 1) {
                stage(s0, s1, cm, true, false, ta, 0, ta, 2048, ta, 0);
                if (qt > 2) stage(s1, s0, cm, true, false, ta, 0, ta, 0, ta, 0);
            }
        } else if (KSP == 3) kvs();
        flush();
    }
    LSTAMP(4);
    f32x16 c2;
    unsigned na, nb;
    auto starts = [&]() {
        const float n1 = c.pq >= 1 ? nm : NEG, n2 = c.pq >= 2 ? nm : NEG;
        asm volatile("" : "+v"(zq));
#pragma unroll
        for (int r = 0; r < 16; ++r) { cm[r] = n1; c2[r] = n2; ce[r] = 8 * (r >> 2) + (r & 3) <= zq ? NEG : n2; }
        na = (unsigned)(AT_N + H * 32768) + lb; nb = na + (unsigned)(16384 + qt * 2048);
        asm volatile("" : "+v"(cm), "+v"(c2), "+v"(ce), "+v"(na), "+v"(nb));
        SCHED();
    };
    {
        auto piece = [&](int b, int i) { dma16<true>(dst + b * 16384 + i * 4096, vk, ((b & 1) ? s2_ : s1_) + (b >> 1) * 16384 + i * 4096); };
        auto block = [&](int b) {
#pragma unroll
            for (int i = 0; i < 4; ++i) piece(b, i);
        };
        if (!HI && w == 3) {
            poll_flags(c.ws, ep, fo, true, (unsigned)(4 * l + 2));
            STAMP_AT(32 + l * 8 + 1);
        }
        WAIT_L0();
        if (HI) WAIT_V0();
        LSTAMP(5);
        BAR();
        FENCE();
        LSTAMP(6);
        piece(0, H); piece(0, 2 + H);
        if (HI) {
            starts();
            if (APRIO) __builtin_amdgcn_s_setprio(APRIO);
            WAIT_V0();
            LSTAMP(7);
            BAR();
            FENCE();
            if (w == 4) STAMP_AT(l * 8 + 3);
        } else {
            if (AB2 >= 0) block(1);
            if (AB2 > 0) block(2);
            starts();
            unsigned vw = wo_w_off(c), vt = (unsigned)(l == 0 ? c.bid * 8192 + lane * 128 : 0);
            asm volatile("" : "+s"(kp));
            const char* tw = w < 3 ? (const char*)kp->wqkv1 + (size_t)w * (2u << 20) : (const char*)kp->wo1;
            asm volatile("" : "+v"(vw), "+v"(vt));
            asm volatile("" : "+s"(tw));
            SCHED();
            wait_v<4 * AB2 + 4>();
            LSTAMP(7);
            BAR();
            if (AB2 < 0) block(1);
            if (AB2 <= 0) block(2);
            block(3);
            wait_v<8>();
            BAR();
            wo_w_issue<18, 22>(c, Wo, vw);
            if (TOUCH) touch(M_TCH, vt, tw);
            wait_v<4 + TOUCH>();
            LSTAMP(9);
            BAR();
            FENCE();
            wo_w_issue<22, 26>(c, Wo, vw);
            if (LSL) { for (int i = 0; i < qt; ++i) __builtin_amdgcn_s_sleep(LSL); }
        }
    }
    SCHED();
    LSTAMP(8);
    {
        const u32x4 d0 = rd(na, 0), d1 = rd(na, 512);
        k0 = rd(na, 2048); k1 = rd(na, 2048 + 512); va = rd(na, 8192);
        SCHED();
        s0 = MFMA32(d0, q0, cm);
        s0 = MFMA32(d1, q1, s0);
        SCHED();
        stage(s0, s1, cm, false, false, na, 4096, na, 0, na, 2048);
        stage(s1, s0, cm, true, false, na, 6144, na, 2048, na, 4096);
        if (HI) { BAR(); FENCE(); SCHED(); }
        stage(s0, s1, cm, true, false, nb, 0, na, 4096, na, 6144);
        stage(s1, s0, ce, true, false, nb, 2048, na, 6144, nb, 0);
        if (HI && CPOS == 4) { WAIT_L0(); BAR(); SCHED(); }
        stage(s0, s1, c2, true, false, nb, 4096, nb, 0, nb, 2048);
        if (HI && CPOS == 5) { BAR(); SCHED(); }
        if (qt < 3) {
            stage(s1, s0, c2, true, false, nb, 6144, nb, 2048, nb, 4096);
            if (qt < 2) {
                stage(s0, s1, c2, true, false, nb, 0, nb, 4096, nb, 6144);
                if (qt < 1) stage(s1, s0, c2, true, false, nb, 0, nb, 6144, nb, 0);
            }
        }
        flush();
    }
    LSTAMP(11);
    if (__builtin_expect(__any(!(ls[0] < SAFE_SUM)), 0)) {
        auto rk = __builtin_amdgcn_make_buffer_rsrc((void*)(kvb + H * 16384), 0, 16u << 20, 0x00020000);
        const int g0 = c.pq * 4 + qt - 8;
        float m = NEG;
#pragma unroll 1
        for (int ps = 0; ps < 2; ++ps) {
            o = (f32x16)(0.f); ls = (f32x4)(0.f);
#pragma unroll 1
            for (int j = g0 < 0 ? -g0 : 0; j < 9; ++j) {
                const int so = ((g0 + j) >> 2) * 524288 + ((g0 + j) & 3) * 2048;
                u32x4 f[4];
#pragma unroll
                for (int i = 0; i < 4; ++i) f[i] = __builtin_amdgcn_raw_buffer_load_b128(rk, lb + (i & 1) * 512, so + (i >> 1) * 8192, 16);
                f32x16 s;
                const float sb = ps ? -m : 0.f;
                float sa = j == 0 ? NEG : sb, sz = j == 8 ? NEG : sb;
                asm volatile("" : "+v"(zq), "+v"(sa), "+v"(sz));
#pragma unroll
                for (int r = 0; r < 16; ++r) s[r] = 8 * (r >> 2) + (r & 3) <= zq ? sa : sz;
                s = MFMA32(f[0], q0, s);
                s = MFMA32(f[1], q1, s);
                if (ps == 0) m = fmaxf(m, max16(s));
                else {
                    u32x4 p[2];
#pragma unroll
                    for (int r = 0; r < 16; r += 2) p[(r >> 2) & 1][(r >> 3) * 2 + ((r >> 1) & 1)] = cvt_pk_bf16(__builtin_amdgcn_exp2f(s[r]), __builtin_amdgcn_exp2f(s[r + 1]));
#pragma unroll
                    for (int u = 0; u < 2; ++u) {
                        o = MFMA32(f[2 + u], p[u], o);
                        ls = MFMA(__builtin_bit_cast(bf16x8, sel), __builtin_bit_cast(bf16x8, p[u]), ls);
                    }
                }
            }
        }
    }
    {
        auto rs = __builtin_amdgcn_make_buffer_rsrc(ob, 0, 2048, 0x00020000);
        const float iv = __builtin_amdgcn_rcpf(ls[0]);
#pragma unroll
        for (int u = 0; u < 2; ++u) {
            u32x4 t;
#pragma unroll
            for (int i = 0; i < 4; ++i) { const int r = 4 * u + (i >> 1) * 8 + (i & 1) * 2; t[i] = cvt_pk_bf16(o[r] * iv, o[r + 1] * iv); }
            __builtin_amdgcn_raw_buffer_store_b128(t, rs, lb + u * 512, 0, 16);
        }
    }
    SCHED();
    LSTAMP(12);
    if (HI) {
        WAIT_V0();
        arrive(std::integral_constant<int, 1>{}, 3u, 3u);
        LSTAMP(13);
        if (APRIO) __builtin_amdgcn_s_setprio(0);
        if (lane == 0) asm volatile("ds_add_u32 %0, %1 offset:12" :: "v"(cnta), "v"(one) : "memory");
        WAIT_V0();
        arrive(std::integral_constant<int, 2>{}, 7u, 4u);
        if (w == 5 || w == 6) {
            Ctx c2 = c;
            c2.wid = 2 * w - 10;
            unsigned vw = wo_w_off(c2);
            asm volatile("" : "+v"(vw));
#pragma unroll 1
            for (unsigned spins = 0; spins < (1u << 22); ++spins) {
                if (__hip_atomic_load((LAS unsigned*)(smem + M_CNT + 12), __ATOMIC_RELAXED, __HIP_MEMORY_SCOPE_WORKGROUP) == (unsigned)(2 * l + 4)) break;
                __builtin_amdgcn_s_sleep(1);
            }
            FENCE();
            wo_w_issue<26, 30>(c2, Wo, vw);
            c2.wid += 1;
            wo_w_issue<26, 30>(c2, Wo + 8 * D_ * 2, vw);
        }
    }
    LSTAMP(23);
    ASTORE();
}

DI void wo_load(const Ctx& c0, int l, const char* Wn, const char* Wo) {
    const Ctx c = fresh(c0);
    asm volatile("" : "+s"(Wn), "+s"(Wo));
    const u64 ep = lds_epoch(c.smem);
    const unsigned fo = o_find(c);
    if (STC && l < 3 && (c.wid == 5 || c.wid == 6) && (STC > 1 || !(c.jq & 4))) {
        const int r3 = (c.lane * 43) >> 7;
        const unsigned o = (unsigned)(((c.bid >> 3) * 32 + (c.wid - 5) * 16 + r3) * 6144 + (c.lane - 3 * r3) * 2048 + (c.jq | 3) * 128);
        if (l & 1) {
#pragma unroll 1
            for (unsigned spins = 0; spins < (1u << 22); ++spins) {
                if (__hip_atomic_load((LAS unsigned*)(c.smem + M_CNT), __ATOMIC_RELAXED, __HIP_MEMORY_SCOPE_WORKGROUP) == (unsigned)(8 * l + 4)) break;
                __builtin_amdgcn_s_sleep(2);
            }
            __builtin_amdgcn_s_sleep(2);
            WAIT_V0();
        }
        if (c.lane < 48) {
            asm volatile("s_mov_b32 m0, %0\n\ts_nop 3\n\tglobal_load_lds_dword %1, %2" ::"s"(M_TCH), "v"(o), "s"(Wn) : "memory");
            if (STC > 1) {
                asm volatile("s_mov_b32 m0, %0\n\ts_nop 3\n\tglobal_load_lds_dword %1, %2 offset:-128" ::"s"(M_TCH + 128), "v"(o), "s"(Wn) : "memory");
                asm volatile("s_mov_b32 m0, %0\n\ts_nop 3\n\tglobal_load_lds_dword %1, %2 offset:-256" ::"s"(M_TCH + 256), "v"(o), "s"(Wn) : "memory");
                asm volatile("s_mov_b32 m0, %0\n\ts_nop 3\n\tglobal_load_lds_dword %1, %2 offset:-384" ::"s"(M_TCH + 384), "v"(o), "s"(Wn) : "memory");
            }
        }
        if (l & 1) { BAR(); goto done; }
    }
    if (!(l & 1)) {
        if (c.wid == 7) {
            poll_flags(c.ws, ep, fo, c.lane < 16, (unsigned)(4 * l + 3));
            STAMP_AT(32 + l * 8 + 2);
        }
        unsigned vw = wo_w_off(c);
        asm volatile("" : "+v"(vw));
        BAR();
        __builtin_amdgcn_s_sleep(W30S);
        wo_w_issue<30, 32>(c, Wo, vw);
    }
    if (c.wid == 4) {
        poll_flags(c.ws, ep, fo, c.lane < 16, (unsigned)(4 * l + 4));
        STAMP_AT(32 + l * 8 + 2 + !(l & 1));
    }
    WAIT_V0();
    BAR();
done:
    if (l == 3 && c.bid == 0 && c.wid == 7) {
#pragma unroll 1
        for (int i = 0; i < 4; ++i) poll_flags(c.ws, ep, (unsigned)WS_FLAGQ + (i * 64 + c.lane) * FQS, true, 1u);
        if (c.lane == 0) __hip_atomic_store((GAS u64*)(c.ws + WS_EPOCH), ep + 64, __ATOMIC_RELAXED, __HIP_MEMORY_SCOPE_AGENT);
    }
    if (RS0 && l == 3) BAR();
}

DI void wo_consume(const bool B, const Ctx& c0, const bf16_t* xin, bf16_t* xout, int l) {
    u32x4 res[2][2];
    const Ctx c = fresh(c0);
    LAS char* smem = c.smem;
    const int lane = c.lane, w = c.wid, fr = lane & 15, fq = lane >> 4;
    const char* obase = c.ws + WS_O + (size_t)l * (8u << 20);
    int ovo = (c.pw * 8 + w * 2) * 1024 + lane * 16;
    asm volatile("" : "+v"(ovo));
    constexpr int NP = WNQ, R = 3;
    static_assert(!HO && !HX && !HS && !RLD && BPR == 1, "layout");
    auto ro = __builtin_amdgcn_make_buffer_rsrc((void*)obase, 0, 4u << 20, 0x00020000);
    asm volatile("" : "+s"(ro));
    u32x4 xa[NP][2];
    s16x4 wf[R][8];
    f32x4 acc[2][4];
    unsigned bw[8];
    int coff, spr;
    u32x4 on = {0x3f803f80u, 0x3f803f80u, 0x3f803f80u, 0x3f803f80u};
    unsigned sto, xpo, coff1, cnta = M_CNT, one = 1;
    u64 xv, b0, b1, am;
    auto rs = __builtin_amdgcn_make_buffer_rsrc(xout, 0, S_ * D_ * 2, 0x00020000);
    auto rst = __builtin_amdgcn_make_buffer_rsrc(c.ws + WS_STATS + (size_t)((l + 1) & 3) * 524288, 0, 524288, 0x00020000);
    auto prep2 = [&]() {
        sto = fq == (fr >> 2) && l < 3 ? (unsigned)((c.pw * 16 + c.jw) * 1024 + (w * 32 + fr) * 8) : 0x80000000u;
        xpo = x_post(c);
        if (l == 3) coff = xrow<0>(c.pw, w * 32 + fr) + xcol<0>(c.jw) + (fq & 1) * 32 + (fq >> 1) * 16;
        coff1 = coff + (l == 3 ? xrow<0>(0, 16) : 2048);
        spr = l == 3 ? 64 : 1024;
        asm volatile("" : "+s"(spr));
        xv = lds_epoch(smem) + (unsigned)(l + 2);
        b0 = __builtin_amdgcn_ballot_w64(fr & 1); b1 = __builtin_amdgcn_ballot_w64(fr & 2); am = __builtin_amdgcn_ballot_w64(x_act(c, !B));
        asm volatile("" : "+v"(on), "+v"(sto), "+v"(xpo), "+v"(coff), "+v"(coff1), "+v"(xv), "+v"(cnta), "+v"(one));
        asm volatile("" : "+s"(rs), "+s"(rst), "+s"(b0), "+s"(b1), "+s"(am));
    };
    auto prep = [&]() {
        int ln = lane;
        asm volatile("" : "+v"(ln));
        const int fr = ln & 15, fq = ln >> 4, q4 = fr >> 2, p4 = ln & 3;
        coff = (c.pw * 16 + c.jw) * 16384 + w * 4096 + (((fq & 1) * 2 + (fq >> 1)) * 16 + fr) * 16;
        asm volatile("" : "+v"(coff));
        const int fb = (q4 & 2) | ((fq & 1) << 2);
        const unsigned swp = (B && w >= 2) ? 2048u : 0u;
#pragma unroll
        for (int cc = 0; cc < 4; ++cc) {
            const unsigned b = (unsigned)((4 * fq + q4) * 128 + (((2 * cc + (p4 >> 1)) ^ fb) << 4) + (p4 & 1) * 8);
            bw[2 * cc] = b + swp; bw[2 * cc + 1] = b + (swp ^ 2048u);
            asm volatile("" : "+v"(bw[2 * cc]), "+v"(bw[2 * cc + 1]));
        }
    };
    auto start = [&]() {
#pragma unroll
        for (int mt = 0; mt < 2; ++mt)
#pragma unroll
            for (int pr = 0; pr < 2; ++pr) {
                auto r0 = __builtin_amdgcn_permlane16_swap(res[mt][pr][0], res[mt][pr][2], false, false);
                auto r1 = __builtin_amdgcn_permlane16_swap(res[mt][pr][1], res[mt][pr][3], false, false);
#pragma unroll
                for (int h = 0; h < 2; ++h) {
                    acc[mt][2 * pr + h] = (f32x4){bf_lo(r0[h]), bf_hi(r0[h]), bf_lo(r1[h]), bf_hi(r1[h])};
                    asm volatile("" : "+v"(acc[mt][2 * pr + h]));
                }
            }
    };
    auto rdw = [&](auto ic) {
        constexpr int i = decltype(ic)::value, h = i >> 3, k = i & 7;
        wf[h % R][k] = tr_read(lds(bw[k]) + (h & 15) * 4096);
        if constexpr (h == 15) { bw[k] += 65536; asm volatile("" : "+v"(bw[k])); }
    };
    auto first = [&]() {
        static_for<0, 16>([&](auto ic) { rdw(ic); SCHED(); });
        start();
        SCHED();
    };
    {
        prep();
        auto rx = __builtin_amdgcn_make_buffer_rsrc((void*)xin, 0, S_ * D_ * 2, 0x00020000);
        const int roff = l ? coff : xrow<0>(c.pw, w * 32 + fr) + xcol<0>(c.jw) + (fq & 1) * 32 + (fq >> 1) * 16;
        const int smt = __builtin_amdgcn_readfirstlane(l ? 2048 : xrow<0>(0, 16)), spx = __builtin_amdgcn_readfirstlane(l ? 1024 : 64);
#pragma unroll
        for (int mt = 0; mt < 2; ++mt)
#pragma unroll
            for (int pr = 0; pr < 2; ++pr) res[mt][pr] = __builtin_amdgcn_raw_buffer_load_b128(rx, roff, mt * smt + pr * spx, 16);
        SCHED();
        prep2();
        SCHED();
        if (!B) {
            publish<8, 2, 4>(smem, lane, c.ws, o_post(c), o_act(c), (unsigned)(4 * l + 4));
            if (w == 0) STAMP_AT(l * 8 + 4);
            SCHED();
        }
        first();
        BAR();
    }
    FENCE();
    SCHED();
    if (w == 0) STAMP_AT(l * 8 + 5);
    int so = 0;
    auto next = [&](int j) { so += j == 15 ? 131072 - 15 * 262144 : 262144; asm volatile("" : "+s"(so)); };
    auto ldo = [&](int j, int mt) { xa[j % NP][mt] = __builtin_amdgcn_raw_buffer_load_b128(ro, ovo + mt * 1024, so, 16); };
#pragma unroll
    for (int h = 0; h < NP; ++h) {
        ldo(h, 0); ldo(h, 1);
        if (!WAL || h + 1 < NP) next(h);
        SCHED();
    }
    int d0 = 262144, d1 = 131072 - 15 * 262144;
    asm volatile("" : "+s"(d0), "+s"(d1));
    static_assert(WEP || !WAL, "last step");
    static_for<0, WEP ? 31 : 32>([&](auto jc) {
        constexpr int j = decltype(jc)::value;
        constexpr int c0 = j < 30 ? 16 + 8 * j : 256, c1 = j < 30 ? c0 + 8 : 256;
        if constexpr (j + NP == 16) {
            static_assert(c0 <= 8 * 20, "weights that landed before the attention");
            if (!B) BAR();
            FENCE();
            asm volatile("" : "+v"(ovo));
            SCHED();
        }
        if constexpr (j == 0 || j + NP == 16) ALIGN8();
        constexpr int nv = WAL == 1 || j == 0 || j + NP > 32 ? 2 * (NP - 1 < 31 - j ? NP - 1 : 31 - j) : 2 * NP - 3;
        __builtin_amdgcn_s_waitcnt(0x0870 | nv);
        const int sp = so;
        static_assert(!RS0 || (RS0 + NP >= 32 && RS0 < 31 && WPRE * QW_SLOT <= (RS0 - 1) * 4096), "R0");
        if constexpr (RS0 && j == RS0) BAR();
        else asm volatile("s_add_i32 %0, %1, %2" : "=&s"(so) : "s"(sp), "s"(j + NP == 16 ? d1 : d0) : "scc");
        SCHED();
        static_for<0, 8>([&](auto mc) {
            constexpr int m = decltype(mc)::value, mt = m >> 2, nt = mt ? 3 - (m & 3) : m & 3;
            const bf16x8 wv = __builtin_shufflevector(wf[j % R][2 * nt], wf[j % R][2 * nt + 1], 0, 1, 2, 3, 4, 5, 6, 7);
            if constexpr (j < 31) asm volatile("v_mfma_f32_16x16x32_bf16 %0, %1, %2, %0" : "+v"(acc[mt][nt]) : "v"(wv), "v"(xa[j % NP][mt]));
            else acc[mt][nt] = MFMA(wv, __builtin_bit_cast(bf16x8, xa[j % NP][mt]), acc[mt][nt]);
            SCHED();
            if constexpr (c0 < c1 && m < 7) { rdw(std::integral_constant<int, c0 + m>{}); SCHED(); }
            if constexpr (c0 < c1 && m == 6) rdw(std::integral_constant<int, c0 + 7>{});
            if constexpr (m == WL1 && j > 0 && j - 1 + NP < 32) xa[(j + NP - 1) % NP][1] = __builtin_amdgcn_raw_buffer_load_b128(ro, ovo + 1024, sp, 16);
            if constexpr (m == WL1 + 4 && j + NP < 32) ldo(j, 0);
            SCHED();
        });
    });
    const bf16x8 ones = __builtin_bit_cast(bf16x8, on);
    u32x4 o[2][2];
    f32x4 sm[2], g[2];
    const f32x4 zero = {0.f, 0.f, 0.f, 0.f};
    auto sel = [&](float a, float b, u64 m) { return __builtin_amdgcn_inverse_ballot_w64(m) ? b : a; };
    auto stats = [&](int mt) {
        float qa = sel(g[mt][0], g[mt][1], b0), qb = sel(g[mt][2], g[mt][3], b0);
        asm volatile("" : "+v"(qa), "+v"(qb));
        SCHED();
        if (WEP) asm volatile("" :: "v"(sm[1][1]), "v"(sm[1][2]), "v"(sm[1][3]), "v"(o[1][0]), "v"(o[1][1]), "v"(on));
        __builtin_amdgcn_raw_buffer_store_b64((u32x2){__float_as_uint(sm[mt][0]), __float_as_uint(sel(qa, qb, b1))}, rst, sto + mt * 128, 0, 16);
        SCHED();
    };
    u32x2 pk[2];
    auto last = [&](int mt, int nt) {
        acc[mt][nt] = MFMA(__builtin_shufflevector(wf[31 % R][2 * nt], wf[31 % R][2 * nt + 1], 0, 1, 2, 3, 4, 5, 6, 7), __builtin_bit_cast(bf16x8, xa[31 % NP][mt]), acc[mt][nt]);
        SCHED();
    };
    auto cv = [&](int mt, int pr) {
        pk[0] = pack4(acc[mt][2 * pr]); pk[1] = pack4(acc[mt][2 * pr + 1]);
        asm volatile("" : "+v"(pk[0]), "+v"(pk[1]));
        SCHED();
    };
    auto st = [&](int mt, int pr) {
        o[mt][pr] = widen(pk[0], pk[1]);
        __builtin_amdgcn_raw_buffer_store_b128(o[mt][pr], rs, mt ? coff1 : coff, pr ? spr : 0, 16);
        SCHED();
    };
    auto gq = [&](int mt, int pr, bool f) { const bf16x8 of = __builtin_bit_cast(bf16x8, o[mt][pr]); g[mt] = MFMA(of, of, f ? zero : g[mt]); SCHED(); };
    auto gs = [&](int mt, int pr, bool f) { sm[mt] = MFMA(ones, __builtin_bit_cast(bf16x8, o[mt][pr]), f ? zero : sm[mt]); SCHED(); };
    WAIT_L0();
    last(0, 0); last(0, 1); last(0, 2); last(0, 3);
    last(1, 3); cv(0, 0);
    last(1, 2); st(0, 0);
    last(1, 1); cv(0, 1);
    last(1, 0); st(0, 1);
    if (w == 0) STAMP_AT(l * 8 + 6);
    gq(0, 0, true); cv(1, 1);
    gs(0, 0, true); st(1, 1);
    gq(0, 1, false); cv(1, 0);
    gs(0, 1, false); st(1, 0);
    gq(1, 1, true);
    gs(1, 1, true);
    gq(1, 0, false);
    gs(1, 0, false);
    stats(0);
    stats(1);
    if (l < 3) {
        unsigned old = 0;
        if (!RS0 || RN1 > RN0) BAR();
        wait_v<XE2>();
        if (lane == 0) asm volatile("ds_add_rtn_u32 %0, %1, %2\n\ts_waitcnt lgkmcnt(0)" : "+v"(old) : "v"(cnta), "v"(one) : "memory");
        if ((__builtin_amdgcn_readfirstlane(old) & 3) == 3) {
            if (__builtin_amdgcn_inverse_ballot_w64(am)) __hip_atomic_store((GAS u64*)(c.ws + xpo), xv, __ATOMIC_RELAXED, __HIP_MEMORY_SCOPE_AGENT);
        }
        BAR();
    }
    if (w == 0) STAMP_AT(l * 8 + 7);
}

__global__ __launch_bounds__(512, 2) void k_main(Params P) {
    extern __shared__ __attribute__((aligned(16))) char smem_[];
    KP kp = (KP)__builtin_amdgcn_kernarg_segment_ptr();
    const char *x0, *w0;
    Ctx c;
    asm volatile("s_load_dwordx2 %0, %3, 0x0\n\ts_load_dwordx2 %1, %3, 0x8\n\ts_load_dwordx2 %2, %3, 0x10" : "=&s"(x0), "=&s"(w0), "=&s"(c.ws) : "s"(kp));
    c.smem = (LAS char*)smem_;
    c.lane = threadIdx.x & 63;
    c.wid = __builtin_amdgcn_readfirstlane(threadIdx.x >> 6);
    c.bid = blockIdx.x;
    const int xcd = c.bid & 7, idx = c.bid >> 3;
    c.pq = ((xcd % (16 / QXM)) * QXM + (idx % QXM)) ^ PQX; c.jq = ((xcd / (16 / QXM)) * (32 / QXM) + (idx / QXM)) ^ JQX;
    const bool loader = c.wid >= 4;
    if (!loader) {
        const char* pc; int len;
        asm volatile("s_getpc_b64 %0\n.Lcpf_a:\n\ts_mov_b32 %1, .Lcpf_end-.Lcpf_a-4" : "=s"(pc), "=s"(len));
#pragma unroll
        for (int i = 0; i < 2; ++i) {
            const int o = min(((i * 4 + c.wid) * 64 + c.lane) * 128, len);
            asm volatile("s_mov_b32 m0, %0\n\ts_nop 3\n\tglobal_load_lds_dword %1, %2" ::"s"(M_TCH), "v"(o), "s"(pc) : "memory");
        }
    }
    {
        const int w = c.wid, lr = c.lane >> 3, lc = c.lane & 7, rot = rot_first(c.pq, c.jq);
        unsigned va = (unsigned)((c.pq * 128 + 8 * (w & 3) + lr) * (D_ * 2) + ((lc ^ lr) << 4));
        unsigned vw = (unsigned)((8 * (w & 3) + lr) * 6144 + c.jq * 128 + ((lc ^ ((lr & 2) | ((w & 1) << 2))) << 4));
        asm volatile("" : "+v"(va), "+v"(vw));
        asm volatile("s_waitcnt lgkmcnt(0)" : "+s"(x0), "+s"(w0), "+s"(c.ws));
        auto stage = [&](int s, int h0, int h1, bool a) {
            const char* xb = x0 + ((s + rot) & 15) * 128;
            const char* wb = w0 + (size_t)((s + rot) & 15) * (64 * 6144);
            for (int h = h0; h <= h1; ++h) {
                if (a) {
#pragma unroll
                    for (int j = 0; j < 2; ++j) dma16<false>(QA_OFF + s * QA_SLOT + (j * 8 + h * 4 + (w & 3)) * 1024, va, xb + (j * 64 + h * 32) * (D_ * 2));
                }
#pragma unroll
                for (int j = 0; j < 3; ++j) dma16<false>(qw_slot(s) + (j * 8 + h * 4 + (w & 3)) * 1024, vw, wb + h * (32 * 6144) + j * 2048);
            }
        };
        if (ENT == 0) stage(0, w >> 2, w >> 2, true);
        else if (!loader) stage(0, 0, 1, true);
        if (w == 0 && c.lane < 2) touch(M_EP, (unsigned)(WS_EPOCH + c.lane * 4), c.ws);
        if (w == 1 && c.lane < 4) *(LAS unsigned*)(c.smem + M_CNT + c.lane * 4) = 0u;
        if (w == 2) {
            const int fr = c.lane & 15, od = fr & 1, j0 = 4 * (c.lane >> 4) + od;
            const bool p0 = j0 > fr, p1 = j0 + 2 > fr;
            const unsigned N = 0xff800000u, Z = 0x0c0c0c0cu, lo = od ? 0x01000c0cu : 0x0c0c0100u, hi = od ? 0x03020c0cu : 0x0c0c0302u;
            LAS u32x4* t = (LAS u32x4*)(c.smem + M_TAB + c.lane * 16);
            t[0] = (u32x4){p0 && !od ? 0u : N, p0 && od ? 0u : N, p1 && !od ? 0u : N, p1 && od ? 0u : N};
            t[64] = (u32x4){!p0 && !od ? 0u : N, !p0 && od ? 0u : N, !p1 && !od ? 0u : N, !p1 && od ? 0u : N};
            t[128] = (u32x4){p0 ? lo : Z, p1 ? hi : Z, p0 ? Z : lo, p1 ? Z : hi};
            t[192] = (u32x4){(unsigned)(M_RSTD + j0 * 4 + (p0 ? 0 : 64)), (unsigned)(M_RSTD + j0 * 4 + 8 + (p1 ? 0 : 64)), 0u, 0u};
        }
    }
    asm volatile("" : "+s"(kp), "+s"(c.bid), "+s"(c.ws));
    c.pw = ((c.bid & 7) % (16 / WXM)) * WXM + ((c.bid >> 3) % WXM); c.jw = ((c.bid & 7) / (16 / WXM)) * (32 / WXM) + ((c.bid >> 3) / WXM);
#pragma unroll 1
    for (int l = 0; l < 4; ++l) {
        asm volatile("" : "+s"(kp));
        if (ALT) {
            if (l > 0 && (ALT == 1 || l != ALT)) c.pq ^= 8;
            const int nh = (ALT != 1 && l + 1 == ALT) ? (c.pq & 8) : (c.pq & 8) ^ 8;
            static_assert(WXM == 4 || WXM == 2, "map");
            c.pw = nh + ((c.bid >> 1) & (8 / WXM - 1)) * WXM + ((c.bid >> 3) & (WXM - 1));
        }
        const bf16_t* xi = l == 0 ? kp->x0 : (const bf16_t*)(c.ws + WS_X + (size_t)(l - 1) * (8u << 20));
        bf16_t* xo = l == 3 ? kp->out : (bf16_t*)(c.ws + WS_X + (size_t)l * (8u << 20));
        const char* wo = (const char*)((l & 1) ? kp->wo1 : kp->wo0);
        const char* wb = (const char*)kp->wqkv1;
        const char* wo2 = (const char*)kp->wo1;
        if (loader) {
            if (l == 0) qkv_load<false, true>(c, (const char*)xi, (const char*)kp->wqkv0, wo, l, wb, wo2, (const char*)kp->out);
            else if (l == 2) qkv_load<false, false>(c, (const char*)xi, (const char*)kp->wqkv0, wo, l);
            else qkv_load<true, false>(c, (const char*)xi, wb, wo, l);
            if (!(l & 1)) attn_a<true>(c, l, wo, kp);
            wo_load(c, l, (const char*)((l & 1) ? kp->wqkv0 : kp->wqkv1), wo);
        } else {
            if (l & 1) qkv_consume<true>(c, l);
            else {
                qkv_consume<false>(c, l);
                attn_a<false>(c, l, wo, kp);
            }
            wo_consume(l & 1, c, xi, xo, l);
        }
    }
    asm volatile(".Lcpf_end:");
}

extern "C" void kernel_launch(void* const* d_in, const int* in_sizes, int n_in, void* d_out, int out_size, void* d_ws, size_t ws_size,
                              hipStream_t stream) {
    Params P;
    P.x0 = (const bf16_t*)d_in[0];
    P.wqkv0 = (const bf16_t*)d_in[1];
    P.wo0 = (const bf16_t*)d_in[2];
    P.wqkv1 = (const bf16_t*)d_in[4];
    P.wo1 = (const bf16_t*)d_in[5];
    P.out = (bf16_t*)d_out;
    P.ws = (char*)d_ws;
    (void)hipFuncSetAttribute((const void*)k_main, hipFuncAttributeMaxDynamicSharedMemorySize, LDS_TOTAL);
    k_main<<<256, 512, LDS_TOTAL, stream>>>(P);
}
```

```cpp
#include <hip/hip_runtime.h>
#include <stdint.h>
#include <type_traits>

typedef unsigned short bf16_t;
typedef short bf16x8 __attribute__((ext_vector_type(8)));
typedef short s16x4 __attribute__((ext_vector_type(4)));
typedef float f32x4 __attribute__((ext_vector_type(4)));
typedef float f32x2 __attribute__((ext_vector_type(2)));
typedef float f32x16 __attribute__((ext_vector_type(16)));
typedef unsigned u32x4 __attribute__((ext_vector_type(4)));
typedef unsigned u32x2 __attribute__((ext_vector_type(2)));
#define DI __device__ __forceinline__
#define LAS __attribute__((address_space(3)))
#define GAS __attribute__((address_space(1)))
#define STAMP 0

constexpr int S_ = 2048, D_ = 1024, KT = 16;
constexpr float LN_EPS = 1e-5f;
constexpr float QSCALE = 0.17677669529663687f * 1.4426950408889634f;

#define FQO 0
#define XBO 524288
#define HX 0
#define HO 0
#define HK 0
#define HS 0
#define HM 1
constexpr size_t WS_FLAGQ = FQO;
constexpr size_t WS_FLAGW = 98304;
constexpr size_t WS_EPOCH = 65536;
constexpr size_t WS_CSUM = 131072;
constexpr size_t WS_XBOX = XBO;
constexpr size_t WS_OBOX = 655360;
constexpr size_t WS_KBOX = 720896;
constexpr size_t WS_STATS = 1u << 20;
constexpr size_t WS_X = 4u << 20;
constexpr size_t WS_O = 96u << 20;
constexpr size_t WS_KV = 28u << 20;
constexpr size_t WS_CQ = 36u << 20;
constexpr size_t WS_CO = 42u << 20;
constexpr size_t WS_STAMP = 64u << 20;

template <int H> DI constexpr int hp(int c) { return !H ? c * 1024 : (c >> 2) * 8192 + (HM == 1 ? (c & 3) * 256 : (c & 1) * 128 + (c & 2) * 256); }
template <int H> DI constexpr int hl(int u) { return !H ? u : (HM == 1 ? u + (u & ~127) : u + (u & ~255)); }
template <int H> DI constexpr int hv(int lane) { return !H ? lane * 16 : (lane >> 3) * 1024 + (lane & 7) * 16; }
template <int H> DI constexpr int xrow(int p, int r) { return H == 0 ? (p * 128 + r) * 2048 : (H == 1 ? (p * 128 + r) * 4096 : (H == 2 ? p * 524288 + r * 256 : p * 262144 + r * 128)); }
template <int H> DI constexpr int xcol(int t) { return H == 0 ? t * 128 : (H == 1 ? hl<1>(t * 128) : (H == 2 ? t * 32768 : t * 16384)); }
#define WTC 2
#define WT0 1
#define SPR 1
#define SL 4
#define R4X 0
#define R5X 0
#define TOPJ 0x2000
#define WV0 1
#define C2X 13
#define FQS 256
#define SNK 1
#define WBE 1
#define WAE 1
#define EXF 0
#define RW0 6
#define KSP 0
#define K0E 1
#define TOPS 4
#define BPR 1
#define RLD 0
#define RCK 1
#define CPF 1
#define WN1 20
#define WLO 3
#define FPJ 2
#define WPJ 2
#define WB14 0
#define WB15 5
#define RESP 1
#define WT13 2
#define RPOS 2
#define CPOLL 1
#define ENT 1
#define MBOX 3
#define OBS 256
#define WTCH 0
#define WTDA 0
#define WTDB 4
#define WTG 0
#define PQX 8
#define ALT 1
#define RESA 1
#define CKP 1
#define ALN 1
#define EPH 2
#define NQA_ 3
#define NQW_ 4
#define WNP 8
#define LPRIO 3
#define TOUCH 1
#define TSTEP 13
#define XS_ 9
#define WROT 0
#define WROTD 1
#define JQX 0
#define ROTM 0
#define ROT0 0
#define WPRE 3
#define PSLEEP 1
#define XPRE 2
#define WE0 1
#define EPJ 3
#define TW0 5
#define TW1 99
#define KPJ 1
#define PKSUB 0
#define APRIO 1
#define QIL 0
#define QXM 8
#define WNQ 5
#define WRG 2
#define WMG 1
#define WB2 1
#define WBS 1
#define WEP 1
#define WAL 2
#define WL1 0
#define RS0 27
#define RN0 6
#define RN1 6
#define AH0 0
#define RSL 1
#define WXM 4
#define CPOS 5
#define LSL 0
#define SAFE_SUM 0x1p60f
#define AB2 0
#define W30S 8
#define STC 0
#define NOR 1
#define NRH 0
#define CPY 1
#define AGG 1
#define HT0 0
#define DEF 0
#define XST 2
#define CKW 4
#define XSP 1
#define EPOLL 1
#define ORD 2
#define RSLP 1
#define APOLL 1
#define APOST 1
#define ASLP 2
#define APIPE 4
#define XE1 0
#define XE2 0
#define OTB 0
#define OTJ 8
#define OTA 0
constexpr int QA_SLOT = 18432, NQA = NQA_, QW_SLOT = 24576, NQW = NQW_, QA_OFF = 3 * QW_SLOT, QW_TOP = 131072;
DI constexpr int qw_slot(int s) { return s == 3 ? QW_TOP : s * QW_SLOT; }
constexpr int AT_N = 90112, AT_OWN = AT_N + 32768, AT_Q = 73728;
constexpr int MISC = 155648, M_MEAN = MISC, M_RSTD = MISC + 640, M_CS = MISC + 1280, M_CNT = MISC + 2816, M_EP = MISC + 2832, M_GO = MISC + 2848, M_TCH = MISC + 3072, M_TAB = MISC + 4096, M_XCH = 131072;
constexpr int LDS_TOTAL = 163840;
static_assert(QA_OFF + NQA * QA_SLOT <= QW_TOP && QW_TOP + QW_SLOT <= MISC && AT_N + 65536 <= MISC && AT_Q + 16384 <= AT_N, "lds");

DI unsigned cvt_pk_bf16(float lo, float hi) {
    typedef __bf16 bf2 __attribute__((ext_vector_type(2)));
    bf2 r; r[0] = (__bf16)lo; r[1] = (__bf16)hi;
    return __builtin_bit_cast(unsigned, r);
}
DI u32x2 pack4(const f32x4& v) { return (u32x2){cvt_pk_bf16(v[0], v[1]), cvt_pk_bf16(v[2], v[3])}; }
DI bf16x8 join(const u32x2& a, const u32x2& b) { return __builtin_bit_cast(bf16x8, (u32x4){a[0], a[1], b[0], b[1]}); }
DI float bf_lo(unsigned u) { return __uint_as_float(u << 16); }
DI float bf_hi(unsigned u) { return __uint_as_float(u & 0xffff0000u); }
DI float red_max4(float x) {
    auto a = __builtin_amdgcn_permlane16_swap(__float_as_uint(x), __float_as_uint(x), false, false);
    x = fmaxf(__uint_as_float(a[0]), __uint_as_float(a[1]));
    auto b = __builtin_amdgcn_permlane32_swap(__float_as_uint(x), __float_as_uint(x), false, false);
    return fmaxf(__uint_as_float(b[0]), __uint_as_float(b[1]));
}
DI float red_sum4(float x) {
    auto a = __builtin_amdgcn_permlane16_swap(__float_as_uint(x), __float_as_uint(x), false, false);
    x = __uint_as_float(a[0]) + __uint_as_float(a[1]);
    auto b = __builtin_amdgcn_permlane32_swap(__float_as_uint(x), __float_as_uint(x), false, false);
    return __uint_as_float(b[0]) + __uint_as_float(b[1]);
}
DI u32x4 widen(const u32x2& a, const u32x2& b) {
    auto r0 = __builtin_amdgcn_permlane16_swap(a[0], b[0], false, false);
    auto r1 = __builtin_amdgcn_permlane16_swap(a[1], b[1], false, false);
    return (u32x4){r0[0], r1[0], r0[1], r1[1]};
}
#define MFMA(a, b, c) __builtin_amdgcn_mfma_f32_16x16x32_bf16((a), (b), (c), 0, 0, 0)
#define WAIT_L0() __builtin_amdgcn_s_waitcnt(0xC07F)
#define WAIT_V0() asm volatile("s_waitcnt vmcnt(0)" ::: "memory")
#define BAR() __builtin_amdgcn_s_barrier()
#define SCHED() __builtin_amdgcn_sched_barrier(0)
template <int N> DI void wait_v() { asm volatile("s_waitcnt vmcnt(%0)" ::"i"(N) : "memory"); }
template <int I, int N, class F> DI void static_for(F&& f) { if constexpr (I < N) { f(std::integral_constant<int, I>{}); static_for<I + 1, N>(f); } }

template <bool SC1> DI void dma16(unsigned lds, unsigned voff, const char* sbase) {
    if (SC1) asm volatile("s_mov_b32 m0, %0\n\ts_nop 3\n\tglobal_load_lds_dwordx4 %1, %2 sc1" ::"s"(lds), "v"(voff), "s"(sbase) : "memory");
    else asm volatile("s_mov_b32 m0, %0\n\ts_nop 3\n\tglobal_load_lds_dwordx4 %1, %2" ::"s"(lds), "v"(voff), "s"(sbase) : "memory");
}
DI void touch(unsigned lds, unsigned voff, const char* sbase) {
    asm volatile("s_mov_b32 m0, %0\n\ts_nop 3\n\tglobal_load_lds_dword %1, %2 sc1" ::"s"(lds), "v"(voff), "s"(sbase) : "memory");
}
DI s16x4 tr_read(const LAS char* p) {
    typedef short v4i16_t __attribute__((ext_vector_type(4)));
    return __builtin_bit_cast(s16x4, __builtin_amdgcn_ds_read_tr16_b64_v4i16((LAS v4i16_t*)p));
}
DI LAS char* lds(unsigned off) { return (LAS char*)(uintptr_t)off; }
DI unsigned long long rstamp() { unsigned long long t; asm volatile("s_memrealtime %0\n\ts_waitcnt lgkmcnt(0)" : "=s"(t)::"memory"); return t; }

template <int NAI, int NWI, bool FIRST> struct Sched {
    int w[KT];
    constexpr Sched() : w{} {
        int endA[KT] = {}, endW[KT] = {};
        int n = 0;
        if (FIRST) {
            if (ENT == 0) { n += 2; endA[0] = n; n += 3; endW[0] = n; }
            n += NAI; endA[1] = n; n += NWI; endW[1] = n;
        } else {
            for (int s = 0; s < WPRE; ++s) { n += NWI; endW[s] = n; }
            for (int s = 0; s < 2; ++s) { n += NAI; endA[s] = n; }
        }
        for (int kt = 0; kt < KT; ++kt) {
            const int need = endA[kt] > endW[kt] ? endA[kt] : endW[kt];
            w[kt] = (n - need) > 63 ? 63 : (n - need);
            if (kt + 2 < KT) { n += NAI; endA[kt + 2] = n; }
            if (FIRST && kt < 5) {
                if (kt < 4) { n += NWI; endW[kt + 2] = n; }
                if (kt >= 3) n += NWI / 2;
                if (kt == 4) { endW[6] = n; n += NWI; endW[7] = n; }
                continue;
            }
            if (!FIRST && WPRE == 2 && kt < 2) { n += NWI; endW[kt + 2] = n; }
            if (!FIRST && WPRE == 3 && kt == 0) { n += NWI; endW[3] = n; }
            if (kt >= 1 && kt + 3 < KT) { n += NWI; endW[kt + 3] = n; }
            if (kt == 13) n += WT13;
            if (kt == 14) n += 12 - WT13 + (NAI == 5 ? WB14 : 0);
        }
    }
};

struct Params {
    const bf16_t* x0;
    const bf16_t* wqkv0;
    char* ws;
    const bf16_t* wqkv1;
    const bf16_t* wo0;
    const bf16_t* wo1;
    bf16_t* out;
};
typedef const __attribute__((address_space(4))) Params* KP;
DI int rot_first(int pq, int jq) {
    const int pr = 2 * (pq & 7) + (pq >> 3);
    if (ROT0 == 4) return (pq + (jq ^ 3)) & 15;
    if (ROT0 == 5) return (pr + (jq ^ 3)) & 15;
    return (ROT0 == 0 ? pq + jq : (ROT0 == 1 ? pr + jq : (ROT0 == 2 ? pr + 4 * (jq & 3) + (jq >> 2) : pq + 4 * (jq & 3) + (jq >> 2)))) & 15;
}

typedef unsigned long long u64;
DI u64 lds_epoch(LAS char* smem) { return *(const LAS u64*)(smem + M_EP); }
DI bool reached(u64 v, u64 ep, unsigned target) { return (v - ep - target) < (u64)(64u - target); }
DI void poll_flags(const char* ws, u64 ep, unsigned off, bool active, unsigned target) {
    const GAS u64* f = (const GAS u64*)(ws + off);
#pragma unroll 1
    for (unsigned spins = 0; spins < (1u << 22); ++spins) {
        const u64 v = __hip_atomic_load(f, __ATOMIC_RELAXED, __HIP_MEMORY_SCOPE_AGENT);
        if (__all(!active || reached(v, ep, target))) break;
        __builtin_amdgcn_s_sleep(PSLEEP);
    }
    asm volatile("" ::: "memory");
}
struct Ask { u64 v[4]; };
DI void keep(Ask& t) { asm volatile("" : "+v"(t.v[0]), "+v"(t.v[1]), "+v"(t.v[2]), "+v"(t.v[3])); }
#define ASK(r) "global_atomic_or_x2 " r ", %[off], %[z], %[ws] sc0 sc1\n\t"
#define CHK(r) "s_waitcnt vmcnt(%[n])\n\tv_lshl_add_u64 %[d], " r ", 0, %[nb]\n\tv_cmp_gt_u64 vcc, %[span], %[d]\n\ts_cmp_eq_u64 vcc, exec\n\ts_cbranch_scc1 .Lpe%=\n\ts_sleep %[sl]\n\t"
DI void poll_rmw(Ask& t, const char* ws, unsigned off, bool act, u64 base, unsigned span) {
    u64 z, d, nb = 0 - base, sp = span;
    unsigned cnt;
    asm volatile("" : "=v"(t.v[0]), "=v"(t.v[1]), "=v"(t.v[2]), "=v"(t.v[3]));
    if (act) {
        if (APIPE == 4)
            asm volatile("v_mov_b64 %[z], 0\n\ts_mov_b32 %[cnt], 0x80000\n\ts_nop 3\n\t" ASK("%[a]") "s_sleep %[sl]\n\t" ASK("%[b]") "s_sleep %[sl]\n\t" ASK("%[c]") "s_sleep %[sl]\n"
                         ".Lpl%=:\n\t" ASK("%[e]") CHK("%[a]") ASK("%[a]") CHK("%[b]") ASK("%[b]") CHK("%[c]") ASK("%[c]") CHK("%[e]")
                         "s_sub_u32 %[cnt], %[cnt], 1\n\ts_cbranch_scc0 .Lpl%=\n.Lpe%=:"
                         : [a] "+v"(t.v[0]), [b] "+v"(t.v[1]), [c] "+v"(t.v[2]), [e] "+v"(t.v[3]), [d] "=&v"(d), [z] "=&v"(z), [cnt] "=&s"(cnt)
                         : [off] "v"(off), [ws] "s"(ws), [nb] "v"(nb), [span] "s"(sp), [sl] "i"(ASLP), [n] "i"(3) : "vcc", "scc", "memory");
        else if (APIPE == 2)
            asm volatile("v_mov_b64 %[z], 0\n\ts_mov_b32 %[cnt], 0x100000\n\ts_nop 3\n\t" ASK("%[a]") "s_sleep %[sl]\n"
                         ".Lpl%=:\n\t" ASK("%[b]") CHK("%[a]") ASK("%[a]") CHK("%[b]")
                         "s_sub_u32 %[cnt], %[cnt], 1\n\ts_cbranch_scc0 .Lpl%=\n.Lpe%=:"
                         : [a] "+v"(t.v[0]), [b] "+v"(t.v[1]), [d] "=&v"(d), [z] "=&v"(z), [cnt] "=&s"(cnt)
                         : [off] "v"(off), [ws] "s"(ws), [nb] "v"(nb), [span] "s"(sp), [sl] "i"(ASLP), [n] "i"(1) : "vcc", "scc", "memory");
        else
            asm volatile("v_mov_b64 %[z], 0\n\ts_mov_b32 %[cnt], 0x200000\n\ts_nop 3\n"
                         ".Lpl%=:\n\t" ASK("%[a]") CHK("%[a]")
                         "s_sub_u32 %[cnt], %[cnt], 1\n\ts_cbranch_scc0 .Lpl%=\n.Lpe%=:"
                         : [a] "+v"(t.v[0]), [d] "=&v"(d), [z] "=&v"(z), [cnt] "=&s"(cnt)
                         : [off] "v"(off), [ws] "s"(ws), [nb] "v"(nb), [span] "s"(sp), [sl] "i"(ASLP), [n] "i"(0) : "vcc", "scc", "memory");
    }
}
DI void post(char* ws, unsigned off, u64 v) {
    if (APOST) asm volatile("s_nop 4\n\tglobal_atomic_swap_x2 %0, %1, %2 sc1" :: "v"(off), "v"(v), "s"(ws) : "memory");
    else __hip_atomic_store((GAS u64*)(ws + off), v, __ATOMIC_RELAXED, __HIP_MEMORY_SCOPE_AGENT);
}
template <int NW, int CNT, int NV = 0> DI void publish(LAS char* smem, int lane, char* ws, unsigned off, bool act, unsigned val) {
    const u64 ep = lds_epoch(smem) + val;
    unsigned old = 0;
    asm volatile("" :: "v"(ep), "v"(off));
    wait_v<NV>();
    if (lane == 0) asm volatile("ds_add_rtn_u32 %0, %1, %2\n\ts_waitcnt lgkmcnt(0)" : "+v"(old) : "v"(M_CNT + CNT * 4), "v"(1u) : "memory");
    if ((__builtin_amdgcn_readfirstlane(old) & (NW - 1)) == NW - 1) {
        if (act) post(ws, off, ep);
    }
}
typedef __bf16 bf16x2_t __attribute__((ext_vector_type(2)));
DI float dot2(unsigned a, unsigned b, float c) { return __builtin_amdgcn_fdot2_f32_bf16(__builtin_bit_cast(bf16x2_t, a), __builtin_bit_cast(bf16x2_t, b), c, false); }
DI u32x2 tile_stats(const u32x4& a, const u32x4& b) {
    float s = 0.f, q = 0.f;
#pragma unroll
    for (int j = 0; j < 4; ++j) { s = dot2(a[j], 0x3f803f80u, s); q = dot2(a[j], a[j], q); }
#pragma unroll
    for (int j = 0; j < 4; ++j) { s = dot2(b[j], 0x3f803f80u, s); q = dot2(b[j], b[j], q); }
    return (u32x2){__float_as_uint(red_sum4(s)), __float_as_uint(red_sum4(q))};
}

struct Ctx {
    LAS char* smem;
    char* ws;
    int lane, wid;
    int pq, jq;
    int pw, jw;
    int bid;
    int cw;
};
DI Ctx fresh(const Ctx& c0) {
    Ctx c = c0;
    asm volatile("" : "+v"(c.lane));
    asm volatile("" : "+s"(c.wid), "+s"(c.pq), "+s"(c.jq), "+s"(c.pw), "+s"(c.jw), "+s"(c.ws), "+s"(c.cw));
    return c;
}
DI unsigned o_post(const Ctx& c) { return (MBOX & 2) ? (unsigned)WS_OBOX + (c.pq * 16 + (c.lane & 15)) * OBS + c.jq * 8 : (unsigned)WS_FLAGQ + (c.pq * 16 + c.jq) * FQS; }
DI bool o_act(const Ctx& c) { return c.lane < ((MBOX & 2) ? 16 : 1); }
DI unsigned o_find(const Ctx& c) { return (MBOX & 2) ? (unsigned)WS_OBOX + (c.pw * 16 + c.jw) * OBS + (c.lane & 15) * 8 : (unsigned)WS_FLAGQ + (c.pw * 16 + (c.lane & 15)) * FQS; }
DI unsigned x_post(const Ctx& c) { return (MBOX & 1) ? (unsigned)WS_XBOX + (c.pw * 16 + (c.lane & 31)) * (2 * FQS) + (c.lane & 16) * (FQS / 16) + c.jw * 8 : (unsigned)WS_FLAGW + (c.pw * 16 + c.jw) * 128; }
DI bool x_act(const Ctx& c, bool halo) { return c.lane < ((MBOX & 1) ? (halo && c.pw < 15 ? 32 : 16) : 1); }
DI unsigned x_find(const Ctx& c) {
    const int pp = c.pq - (c.lane >> 4);
    return (MBOX & 1) ? (unsigned)WS_XBOX + (c.pq * 16 + c.jq) * (2 * FQS) + (c.lane & 16) * (FQS / 16) + (c.lane & 15) * 8 : (unsigned)WS_FLAGW + ((pp < 0 ? 0 : pp) * 16 + (c.lane & 15)) * 128;
}
#define STAMP_AT(slot) do { } while (0)

#define LSTAMP(i) do { } while (0)
#define LSTORE(ph) do { } while (0)
#define LMARK() do { } while (0)
#define LPUT(i) do { } while (0)
#define ASTORE() do { } while (0)
#define QSTAMP(i) do { } while (0)
#define QSTORE() do { } while (0)
#define FENCE() asm volatile("" ::: "memory")
#define ALIGN8() do { if (ALN) { asm volatile(".p2align 3"); SCHED(); } } while (0)
DI float addf(float a, float b) { float r; asm("v_add_f32 %0, %1, %2" : "=v"(r) : "v"(a), "v"(b)); return r; }
DI f32x4 add4x4(const u32x4& a, const u32x4& b, const u32x4& c, const u32x4& d) {
    f32x4 r;
#pragma unroll
    for (int i = 0; i < 4; ++i) r[i] = addf(addf(__uint_as_float(a[i]), __uint_as_float(b[i])), addf(__uint_as_float(c[i]), __uint_as_float(d[i])));
    asm volatile("s_nop 1" : "+v"(r));
    return r;
}
DI float fold16(float a, float b) {
    auto r = __builtin_amdgcn_permlane16_swap(__float_as_uint(a), __float_as_uint(b), false, false);
    float x = __uint_as_float(r[0]) + __uint_as_float(r[1]);
    asm volatile("" : "+v"(x));
    return x;
}
DI float fold32(float a, float b) {
    auto r = __builtin_amdgcn_permlane32_swap(__float_as_uint(a), __float_as_uint(b), false, false);
    float x = __uint_as_float(r[0]) + __uint_as_float(r[1]);
    asm volatile("" : "+v"(x));
    return x;
}
DI void aload4(u32x4& d, unsigned voff, const char* sb) { asm volatile("s_nop 4\n\tglobal_load_dwordx4 %0, %1, %2" : "=v"(d) : "v"(voff), "s"(sb) : "memory"); }
DI void aload4p(u32x4& d, unsigned voff, const char* sb) { asm volatile("s_nop 4\n\tglobal_load_dwordx4 %0, %1, %2" : "+v"(d) : "v"(voff), "s"(sb) : "memory"); }
DI void aload4c(u32x4& d, unsigned voff, const char* sb) { asm volatile("s_nop 4\n\tglobal_load_dwordx4 %0, %1, %2 sc1" : "=v"(d) : "v"(voff), "s"(sb) : "memory"); }
DI void aload2c(u32x2& d, unsigned voff, const char* sb) { asm volatile("s_nop 4\n\tglobal_load_dwordx2 %0, %1, %2 sc1" : "=v"(d) : "v"(voff), "s"(sb) : "memory"); }
DI unsigned wo_src(int q, int lane, int jw) { return (unsigned)((8 * q + (lane >> 3)) * (D_ * 2) + jw * 128 + (((lane & 7) ^ ((lane >> 3) & 6)) << 4)); }
DI unsigned pick(bool f, unsigned a, unsigned b) { asm volatile("" : "+v"(a), "+v"(b)); return f ? a : b; }
DI unsigned wo_w_off(const Ctx& c) { return pick(c.cw == 3, (unsigned)((c.wid & 3) * 1024 + c.lane * 16), wo_src(c.wid & 3, c.lane, c.jw)); }
DI unsigned wq_src(int ib, int lane, int jq) {
    const int seg = ib >> 3, ii = ib & 7, lr = lane >> 3;
    return (unsigned)((8 * ii + lr) * 6144 + seg * 2048 + jq * 128 + (((lane & 7) ^ ((lr & 2) | ((ii & 1) << 2))) << 4));
}
template <int S0, int S1> DI void wo_w_issue(const Ctx& c, const char* W, unsigned vw) {
    const int lw = c.wid & 3;
#pragma unroll
    for (int s = S0; s < S1; ++s) dma16<CPY != 0>(s * 4096 + lw * 1024, vw, W + (size_t)((2 * (s & 15) + (s >> 4)) * 32) * (D_ * 2));
}

template <bool HALO, bool FIRST> DI void qkv_load(const Ctx& c0, const char* xin, const char* W, const char* Wo, int l, const char* Wb = nullptr, const char* Wo2 = nullptr, const char* outp = nullptr) {
    const Ctx c = fresh(c0);
    asm volatile("" : "+s"(xin), "+s"(W), "+s"(Wo));
    LAS char* smem = c.smem;
    constexpr int NAI = HALO ? 5 : 4, NWI = 6, XS = XS_, NR = HALO ? 144 : 128;
    constexpr Sched<NAI, NWI, FIRST> SC{};
    static_assert(NQA == 3 && NQW == 4, "schedule");
    const int lw = c.wid - 4, lane = c.lane, lr = lane >> 3, lc = lane & 7, fr = lane & 15, fq = lane >> 4;
    const int row0 = c.pq * 128;
    const int pr = (ROTM == 1 || (ROTM == 2 && (c.jq & 7) == 3)) ? 2 * (c.pq & 7) + (c.pq >> 3) : (ROTM == 3 ? (c.pq & 7) + ((c.pq & 7) >> 1) + (c.pq & 8) : c.pq);
    const bool cq_ = CPY && !FIRST && (c.jq & 7) == 3;
    if (cq_) W = c.ws + WS_CQ + ((HALO ? 0 : 2) + (c.jq >> 3)) * 24576;
    asm volatile("" : "+s"(W));
    const bool nor = !FIRST && l > 1 && (NOR == 1 || (NOR == 2 && (c.jq & 7) == 3) || (NOR == 3 && (c.jq & 7) != 3));
    const int rot = FIRST ? rot_first(c.pq, c.jq) : (nor ? (c.pq & 8) * NRH : pr & 15);
    unsigned va[5], vw[NWI];
#pragma unroll
    for (int j = 0; j < 4; ++j) va[j] = (unsigned)(FIRST ? xrow<0>(c.pq, 8 * (j * 4 + lw) + lr) + ((lc ^ lr) << 4) : c.pq * 262144 + (j * 4 + lw) * 1024 + lane * 16);
    if (HALO) va[4] = (unsigned)((c.pq > 0 ? (c.pq - 1) * 262144 + 14336 : 0) + lw * 512 + lane * 16);
#pragma unroll
    for (int j = 0; j < NWI; ++j) vw[j] = FIRST ? wq_src(j * 4 + lw, lane, c.jq) : pick(cq_, (unsigned)((j * 4 + lw) * 1024 + lane * 16), wq_src(j * 4 + lw, lane, c.jq));
    auto issueA = [&](int s, int slot) {
        const char* b = xin + ((s + rot) & 15) * (FIRST ? 128 : 16384);
#pragma unroll
        for (int j = 0; j < 4; ++j) dma16<true>(QA_OFF + slot * QA_SLOT + (j * 4 + lw) * 1024, va[j], b);
        if (HALO) { if (lane < 32) dma16<true>(QA_OFF + slot * QA_SLOT + 16384 + lw * 512, va[4], b); }
    };
    auto issueW = [&](int s, int slot, int j0 = 0, int j1 = NWI) {
        const char* b = W + (size_t)((s + rot) & 15) * (64 * 6144);
#pragma unroll
        for (int j = j0; j < j1; ++j) dma16<CPY && !FIRST>(qw_slot(slot) + (j * 4 + lw) * 1024, vw[j], b);
    };
    auto ahead = [&]() {
        const int r3 = (lane * 43) >> 7;
        const unsigned o = (unsigned)((((rot - (c.pq & 7) + 10) * 64 + (c.pq & 7) * 48 + lw * 12 + r3) & 1023) * 6144 + (lane - 3 * r3) * 2048 + c.jq * 128);
        if (lane < 36 && !nor && !cq_ && (WTC != 2 || (c.jq & 7) == 3)) asm volatile("s_mov_b32 m0, %0\n\ts_nop 3\n\tglobal_load_lds_dword %1, %2" ::"s"(M_TCH), "v"(o), "s"(W) : "memory");
    };
    const bool docs = FIRST || (HALO && l == 1);
    const int t = lw * 64 + lane;
    static_assert(MBOX & 1, "residual flag");
    unsigned foff = lane < 32 ? (unsigned)WS_FLAGQ + (lane < 16 ? c.pq * 16 + lane : fr * 16 + c.jq) * FQS : (unsigned)WS_XBOX + (c.pw * 16 + c.jw) * (2 * FQS) + c.jw * 8;
    unsigned ftg = lane < 32 ? (FIRST ? 1u : 5u) : (unsigned)(l + 1);
    asm volatile("" : "+v"(ftg));
    unsigned so = (unsigned)hl<HS>((c.pq * 16 + fq * 4) * 1024 + (lw * 32 + fr * 2) * 8);
    unsigned ho = (unsigned)hl<HS>(((c.pq > 0 ? c.pq - 1 : 0) * 16 + (lane >> 2)) * 1024 + ((c.pq > 0 ? 112 : 0) + lw * 4 + (lane & 3)) * 8);
    const int cq = lw * 12 + (fr < 12 ? fr : 11);
    unsigned co = (unsigned)((fq * 4 * 3072 + (cq >> 4) * 1024 + c.jq * 64 + (cq & 15) * 4) * 4);
    unsigned l3 = (unsigned)(M_CS + (HALO ? 768 : 0) + (cq * 4 + fq) * 4), vwo = wo_w_off(c);
    unsigned l4 = (unsigned)(M_MEAN + (lane < 32 ? 16 + lw * 32 + fr * 2 + fq : lw * 4 + (lane & 3)) * 4);
    asm volatile("" : "+v"(vwo));
    unsigned wt[3], cso, xt, xt1, xso;
    {
        const int q4 = fr >> 2, p4 = lane & 3, fb = (q4 & 2) | ((fq & 1) << 2);
#pragma unroll
        for (int i = 0; i < 3; ++i) {
            const int T = 3 * lw + i;
            wt[i] = (unsigned)((T >> 2) * 8192 + (8 * fq + q4) * 128 + (((2 * (T & 3) + (p4 >> 1)) ^ fb) << 4) + (p4 & 1) * 8);
        }
        const int T = 3 * lw + fq;
        cso = (unsigned)((((HALO ? 16 : 0) + rot) * 3072 + (T >> 2) * 1024 + c.jq * 64 + (T & 3) * 16 + fr) * 4);
        xt = (unsigned)(QA_OFF + (lw * 32 + fr) * 128 + ((fq ^ (fr & 7)) << 4));
        xt1 = xt ^ 64;
        xso = (unsigned)hl<HS>((c.pq * 16 + rot) * 1024 + (lw * 32 + fr) * 8);
    }
    asm volatile("" : "+v"(foff), "+v"(so), "+v"(ho), "+v"(co), "+v"(l3), "+v"(l4));
    asm volatile("" : "+v"(wt[0]), "+v"(wt[1]), "+v"(wt[2]), "+v"(cso), "+v"(xt), "+v"(xt1), "+v"(xso));
    unsigned tp = (unsigned)((lane < 30 ? (c.bid >> 3) * 30 + lane : (c.bid >> 3) * 16 + lane - 30 + (int)(WS_O >> 16)) * 65536 + 65536 - 128);
    unsigned tl = (unsigned)(c.bid * 8192 + lane * 128);
    if (FIRST) asm volatile("" : "+v"(tp), "+v"(tl));
    u32x4 on = {0x3f803f80u, 0x3f803f80u, 0x3f803f80u, 0x3f803f80u};
    asm volatile("" : "+v"(on));
#pragma unroll
    for (int j = 0; j < NAI; ++j) asm volatile("" : "+v"(va[j]));
#pragma unroll
    for (int j = 0; j < NWI; ++j) asm volatile("" : "+v"(vw[j]));
    SCHED();
    int tv = 0; unsigned long long lt = 0;
    if (FIRST) {
        LSTAMP(55);
    } else {
        static_assert(CPY == 1 && RS0 > 0 && RN0 == NWI && WPRE == 3 && (FIRST || SC.w[0] == NAI), "order");
        unsigned vx[2], xfo = (unsigned)WS_XBOX + (c.pq * 16 + c.jq) * (2 * FQS) + (HALO && c.pq > 0 ? (lane & 16) * (FQS / 16) : 0) + fr * 8, go = M_GO;
        u64 xv = lds_epoch(smem) + (unsigned)(l + 1);
#pragma unroll
        for (int i = 0; i < 2; ++i) vx[i] = pick(cq_, (unsigned)(((2 * lw + i) * 4 + 3) * 1024 + lane * 16), wq_src((2 * lw + i) * 4 + 3, lane, c.jq));
        asm volatile("" : "+v"(vx[0]), "+v"(vx[1]), "+v"(xfo), "+v"(xv), "+v"(go));
        BAR();
        issueW(0, 0);
        auto word = [&](int off) { return __builtin_amdgcn_readfirstlane(*(volatile LAS int*)(lds(go) + off)); };
        auto until = [&](int off, int v) {
#pragma unroll 1
            for (unsigned spins = 0; spins < (1u << 24); ++spins) { if (word(off) == v) break; }
            FENCE();
        };
        auto stage = [&](int s) {
            issueW(s, s);
            const char* b = W + (size_t)((s + rot) & 15) * (64 * 6144);
#pragma unroll
            for (int i = 0; i < 2; ++i) dma16<true>(qw_slot(s) + ((2 * lw + i) * 4 + 3) * 1024, vx[i], b);
        };
        auto first2 = [&]() { __builtin_amdgcn_s_setprio(LPRIO); issueA(0, 0); issueA(1, 1); };
        if (lw == 3) {
            if (!EPOLL) until(M_CNT - M_GO, 8 * l);
            Ask t;
            poll_rmw(t, c.ws, xfo, lane < (HALO && c.pq > 0 ? 32 : 16), xv, 63 - l);
            *(volatile LAS int*)lds(go) = l;
            until(M_CNT - M_GO, 8 * l);
            first2();
            STAMP_AT(32 + l * 8 + 0);
            wait_v<NAI>();
            keep(t);
        } else {
            until(M_CNT - M_GO, 8 * l);
            if (RSLP) __builtin_amdgcn_s_sleep(RSLP);
            if (ORD > 0 && word(0) == l) {
                first2(); stage(1); stage(2);
                wait_v<NAI + 16>();
            } else {
                stage(1);
                if (ORD > 1 && word(0) == l) {
                    first2(); stage(2);
                    wait_v<NAI + 8>();
                } else {
                    stage(2);
                    if (lw == 1) STAMP_AT(l == 1 ? 44 : (l == 2 ? 57 : 62));
                    until(0, l);
                    first2();
                    wait_v<NAI>();
                }
            }
        }
        FENCE();
    }
    if (FIRST) BAR();
    __builtin_amdgcn_s_setprio(LPRIO);
    if (FIRST) { LSTAMP(57); issueA(1, 1); issueW(1, 1); LSTAMP(58); }
    u32x4 st[4], cs[4]; u32x2 sh, fl; u64 fv = 0; float rs_, rq_, hs;
    const bool fact = FIRST ? lane < 32 : (lane >= (docs ? 16 : 32) && lane <= 32);
    static_assert(XS + 3 <= 12, "aux schedule");
    auto side = [&](auto ktc) {
        constexpr int kt = decltype(ktc)::value, S0 = SPR && !FIRST ? SL : XS;
        {
            constexpr int R0 = !SPR ? 12 : S0 + 2, R1 = !SPR ? 12 : S0 + 3, R2 = !SPR ? 12 : (FIRST ? 12 : S0 + 4), R3 = !SPR ? 12 : (FIRST ? 12 : S0 + 5), R4 = !SPR ? 12 : (FIRST ? 13 : (R4X ? R4X : S0 + 6));
            constexpr int C0 = !SPR ? 13 : (FIRST ? 12 : 11), C1 = !SPR ? 13 : (FIRST ? 13 : 12), C2 = SPR && !FIRST ? C2X : 13;
            auto acc4 = [](u32x4& a, const u32x4& b) {
#pragma unroll
                for (int i = 0; i < 4; ++i) a[i] = __float_as_uint(addf(__uint_as_float(a[i]), __uint_as_float(b[i])));
                asm volatile("" : "+v"(a));
            };
            if constexpr (kt == R0) {
                asm volatile("" : "+v"(st[0]), "+v"(st[1]));
                acc4(st[0], st[1]);
                if (SPR && FIRST) { asm volatile("" : "+v"(st[2])); acc4(st[0], st[2]); }
                if (HALO) {
                    asm volatile("" : "+v"(sh));
                    hs = fold16(__uint_as_float(sh[0]), __uint_as_float(sh[1]));
                    hs = fold32(hs, hs);
                }
            }
            if constexpr (kt == R1) {
                asm volatile("" : "+v"(st[2]), "+v"(st[3]));
                if (!(SPR && FIRST)) acc4(st[3], st[2]);
                acc4(st[0], st[3]);
            }
            if constexpr (kt == C0) {
                if (docs) { asm volatile("" : "+v"(cs[0]), "+v"(cs[1])); acc4(cs[0], cs[1]); }
            }
            if constexpr (kt == R2) {
                asm volatile("s_nop 1" : "+v"(st[0]));
                rs_ = fold16(__uint_as_float(st[0][0]), __uint_as_float(st[0][2])); rq_ = fold16(__uint_as_float(st[0][1]), __uint_as_float(st[0][3]));
                if (HALO) {
                    hs += __uint_as_float(__builtin_amdgcn_update_dpp(0u, __float_as_uint(hs), 0x124, 0xf, 0xf, false));
                    hs += __uint_as_float(__builtin_amdgcn_update_dpp(0u, __float_as_uint(hs), 0x128, 0xf, 0xf, false));
                    asm volatile("" : "+v"(hs));
                }
            }
            if constexpr (kt == R3) {
                rs_ = fold32(rs_, rs_); rq_ = fold32(rq_, rq_);
                if (HALO) {
                    auto hh = __builtin_amdgcn_permlane16_swap(__float_as_uint(hs), __float_as_uint(hs), false, false);
                    if (lane >= 32) { rs_ = __uint_as_float(hh[0]); rq_ = __uint_as_float(hh[1]); }
                    asm volatile("" : "+v"(rs_), "+v"(rq_));
                }
            }
            if constexpr (kt == R4) {
                rs_ *= -1.f / 1024.f;
                asm volatile("" : "+v"(rs_));
                rq_ = __builtin_amdgcn_rsqf(fmaxf(rq_ * (1.f / 1024.f) - rs_ * rs_, 0.f) + LN_EPS);
                asm volatile("" : "+v"(rq_));
            }
            if constexpr (kt == C1) {
                if (docs) { asm volatile("" : "+v"(cs[2]), "+v"(cs[3])); acc4(cs[2], cs[3]); acc4(cs[0], cs[2]); }
            }
            if constexpr (kt == C2) {
                if (docs) {
                    asm volatile("s_nop 1" : "+v"(cs[0]));
                    hs = fold32(fold16(__uint_as_float(cs[0][0]), __uint_as_float(cs[0][1])), fold16(__uint_as_float(cs[0][2]), __uint_as_float(cs[0][3])));
                }
            }
            if constexpr (kt == 13) {
                if (WV0) WAIT_V0();
                if (lane < (HALO ? 36 : 32)) {
                    *(LAS float*)lds(l4) = rs_;
                    *(LAS float*)(lds(l4) + (M_RSTD - M_MEAN)) = rq_;
                }
                if (docs) *(LAS float*)lds(l3) = hs;
                FENCE();
            }
        }
    };
    static_for<0, KT>([&](auto ktc) {
        constexpr int kt = decltype(ktc)::value;
        LSTAMP(3 * kt);
        if constexpr (FIRST || kt > 0) wait_v<SC.w[kt]>();
        LSTAMP(3 * kt + 1);
        if constexpr (kt == XS || (SPR && !FIRST && kt == 15)) {
            if ((SPR ? docs == (kt == XS) : (RCK || docs)) && lw == 0) {
                asm volatile("" : "+v"(fl));
                const u64 ep = lds_epoch(smem);
                if (!__all(!fact || reached(__builtin_bit_cast(u64, fl), ep, ftg))) poll_flags(c.ws, ep, foff, fact, ftg);
            }
        }
        if constexpr (kt == 14) asm volatile("s_waitcnt lgkmcnt(0)" ::: "memory");
        BAR();
        FENCE();
        LMARK();
        LSTAMP(3 * kt + 2);
        if constexpr (TOPJ & (1 << kt)) side(ktc);
        if constexpr (kt == 3) {
            if (docs && lw == 0 && lane == 0) __hip_atomic_store((GAS u64*)(c.ws + WS_FLAGQ + (c.pq * 16 + c.jq) * FQS), fv, __ATOMIC_RELAXED, __HIP_MEMORY_SCOPE_AGENT);
        }
        if constexpr (kt == XS + 1 - NQA) {
            if ((RCK || docs) && lw == 0) { if (fact) aload2c(fl, foff, c.ws); }
        }
        constexpr int S0 = SPR && !FIRST ? SL : XS;
        {
            const char* sb = c.ws + WS_STATS + (size_t)l * 524288;
            const char* cb = c.ws + WS_CSUM + (HALO ? 16 * 3072 * 4 : 0);
            constexpr int sa = !SPR || !FIRST ? 2 * (kt - S0) : 3 * (kt - S0), sn = !SPR || !FIRST ? 2 : 3;
            constexpr int ca = !SPR ? (kt == XS + 1 ? 0 : (kt == XS + 2 ? 1 : 9)) : (FIRST ? 2 * (kt - XS - 1) : 2 * (kt - XS)), cn = !SPR ? (kt == XS + 1 ? 1 : 3) : 2;
#pragma unroll
            for (int i = sa; i < sa + sn; ++i) if (i >= 0 && i < 4) aload4c(st[i], so, sb + hl<HS>(i * 1024));
            if (HALO && kt == S0) aload2c(sh, ho, sb);
            if constexpr (ca >= 0 && ca < 4) {
                if (docs) {
#pragma unroll
                    for (int i = ca; i < ca + cn; ++i) aload4c(cs[i], co, cb + i * 12288);
                }
            }
        }
        f32x4 d[3], sm[2], g[2];
        bf16x8 wf[3][2], xf[2][2];
        if constexpr (kt == 0) {
            if (docs) {
#pragma unroll
                for (int i = 0; i < 3; ++i)
#pragma unroll
                    for (int ks = 0; ks < 2; ++ks) {
                        const s16x4 lo = tr_read(lds(wt[i]) + ks * 4096), hi = tr_read(lds(wt[i]) + ks * 4096 + 512);
                        wf[i][ks] = __builtin_shufflevector(lo, hi, 0, 1, 2, 3, 4, 5, 6, 7);
                    }
                if (FIRST) {
#pragma unroll
                    for (int mt = 0; mt < 2; ++mt)
#pragma unroll
                        for (int ks = 0; ks < 2; ++ks) xf[mt][ks] = *(const LAS bf16x8*)(lds(ks ? xt1 : xt) + mt * 2048);
                }
            }
            SCHED();
        }
        if constexpr (kt + 2 < KT) issueA(kt + 2, (kt + 2) % NQA);
        LPUT(3 * kt + 2);
        if constexpr (kt == 0) {
            SCHED();
            if (docs) {
                const bf16x8 ones = __builtin_bit_cast(bf16x8, on);
                const f32x4 zero = {0.f, 0.f, 0.f, 0.f};
#pragma unroll
                for (int ks = 0; ks < 2; ++ks) {
#pragma unroll
                    for (int i = 0; i < 3; ++i) d[i] = MFMA(ones, wf[i][ks], ks ? d[i] : zero);
                    if (FIRST) {
#pragma unroll
                        for (int mt = 0; mt < 2; ++mt) { sm[mt] = MFMA(ones, xf[mt][ks], ks ? sm[mt] : zero); g[mt] = MFMA(xf[mt][ks], xf[mt][ks], ks ? g[mt] : zero); }
                    }
                }
            }
            SCHED();
        }
        if constexpr (FIRST && kt < 5) {
            if constexpr (kt < 4) issueW(kt + 2, (kt + 2) % NQW);
            if constexpr (kt == 3) issueW(6, 2, 0, NWI / 2);
            if constexpr (kt == 4) { issueW(6, 2, NWI / 2, NWI); issueW(7, 3); }
        } else {
            if constexpr (WPRE == 2 && kt < 2) issueW(kt + 2, kt + 2);
            if constexpr (WPRE == 3 && kt == 0) issueW(3, 3);
            if constexpr (kt >= 1 && kt + 3 < KT) issueW(kt + 3, (kt + 3) % NQW);
        }
        if constexpr (FIRST && kt == WT0) ahead();
        if constexpr (FIRST && kt == TSTEP) {
            if (lw == 1 && lane < 46) touch(M_TCH, tp, c.ws);
        }
        if constexpr (FIRST && kt == TW0) {
            if (lw == 0) touch(M_TCH, tl, Wo);
            else if (lw == 1) { if (lane < 2) touch(M_TCH, (unsigned)(((c.bid >> 3) * 2 + lane) * 65536), outp); }
        }
        if constexpr (FIRST && kt == TW1 && TW1 < KT) {
            touch(M_TCH, tl, lw < 3 ? Wb + (size_t)lw * (2u << 20) : Wo2);
        }
        if constexpr (kt == 0) {
            SCHED();
            if (docs) {
                if (FIRST) {
                    auto rst = __builtin_amdgcn_make_buffer_rsrc(c.ws + WS_STATS, 0, 524288, 0x00020000);
                    const bool b0 = fr & 1, b1 = fr & 2;
                    u32x2 sv[2];
#pragma unroll
                    for (int mt = 0; mt < 2; ++mt) {
                        float qa = b0 ? g[mt][1] : g[mt][0], qb = b0 ? g[mt][3] : g[mt][2];
                        asm volatile("" : "+v"(qa), "+v"(qb));
                        sv[mt] = (u32x2){__float_as_uint(sm[mt][0]), __float_as_uint(b1 ? qb : qa)};
                    }
                    if (fq == (fr >> 2)) {
                        __builtin_amdgcn_raw_buffer_store_b64(sv[0], rst, xso, 0, 16);
                        __builtin_amdgcn_raw_buffer_store_b64(sv[1], rst, xso + hl<HS>(128), 0, 16);
                    }
                }
                const float v = fq == 0 ? d[0][0] : (fq == 1 ? d[1][0] : d[2][0]);
                auto rc = __builtin_amdgcn_make_buffer_rsrc(c.ws + WS_CSUM, 0, 2 * 16 * 3072 * 4, 0x00020000);
                if (lane < 48) __builtin_amdgcn_raw_buffer_store_b32(__float_as_uint(v), rc, cso, 0, 16);
                asm volatile("s_waitcnt lgkmcnt(0)" ::: "memory");
            }
        }
        if constexpr (kt == 1) {
            if (docs && lw == 0) {
                const u32x2 e = *(const LAS u32x2*)(smem + M_EP);
                const u64 es = ((u64)(unsigned)__builtin_amdgcn_readfirstlane(e[1]) << 32) | (unsigned)__builtin_amdgcn_readfirstlane(e[0]);
                fv = es + (FIRST ? 1u : 5u);
                asm volatile("" : "+s"(fv));
            }
        }
        if constexpr (!(TOPJ & (1 << kt))) side(ktc);
        if constexpr (kt == 13) wo_w_issue<0, WT13>(c, Wo, vwo);
        if constexpr (kt == 14) { wo_w_issue<WT13, 12>(c, Wo, vwo); if (HALO) wo_w_issue<23, 23 + WB14>(c, Wo, vwo); }
        if constexpr (kt == 15) {
            if (HALO ? WBE < 2 : WAE < 2) wo_w_issue<12, 18>(c, Wo, vwo);
            if (HALO) wo_w_issue<23 + WB14, 23 + WB14 + WB15>(c, Wo, vwo);
        }
    });
    __builtin_amdgcn_s_setprio(0);
    BAR();
    LMARK(); LPUT(48);
    LSTAMP(48);
    LSTORE(0);
    if (HALO ? WBE < 1 : WAE < 1) wo_w_issue<18, HALO ? 23 : 20>(c, Wo, vwo);
    if (HALO) {
        static_assert(!WBS || (WBE == 1 && WB14 == 0 && WRG <= 12), "Wo weights of the first twelve steps are older than the last 6 + WB15");
        if (WBS) wait_v<6 + WB15>();
        BAR();
        if (WBE) wo_w_issue<WBE < 2 ? 18 : 12, 23>(c, Wo, vwo);
        wo_w_issue<23 + WB14 + WB15, 32>(c, Wo, vwo);
    }
}

DI float max3(float a, float b, float c) { return fmaxf(fmaxf(a, b), c); }
#define SGB(m, n) __builtin_amdgcn_sched_group_barrier(m, n, 0)
template <bool HALO> struct Tail {
    int f[26], n[4], t[4][26], d[4][27], v[4][27], h3[32], s3[32], o3[33], q3[112];
    constexpr Tail() : f{}, n{}, t{}, d{}, v{}, h3{}, s3{}, o3{}, q3{} {
        const int FB[26] = {0, 1, 2, 3, 4, 5, 6, 7, 8, 9, 10, 11, 12, 13, 14, 15, 24, 25, 19, 23, 18, 22, 17, 21, 16, 20};
        const int FA[24] = {0, 1, 2, 3, 4, 5, 6, 7, 8, 9, 10, 11, 12, 13, 14, 15, 16, 17, 20, 21, 18, 19, 22, 23};
        const int TA[4][26] = {{0, 1, 2, 3, 7, 6, 5, 4, 8, 9, 10, 11, 15, 14, 13, 12, 16, 17, 18, 19, 23, 22, 21, 20, 0, 0}, {4, 8, 9, 10, 11, 15, 14, 13, 12, 16, 17, 18, 19, 23, 22, 21, 20, 0, 1, 2, 3, 7, 6, 5, 0, 0}, {16, 17, 18, 19, 23, 22, 21, 20, 0, 1, 2, 3, 7, 6, 5, 4, 8, 9, 10, 11, 15, 14, 13, 12, 0, 0}, {17, 18, 19, 23, 22, 21, 20, 8, 9, 10, 11, 15, 14, 13, 12, 16, 0, 0, 0, 0, 0, 0, 0, 0, 0, 0}};
        const int DA[4][26] = {{1, 1, 1, 1, 1, 1, 1, 1, 1, 1, 1, 1, 1, 1, 1, 1, 1, 1, 1, 1, 1, 1, 0, 0, 0, 0}, {1, 1, 1, 1, 1, 1, 1, 1, 1, 1, 1, 1, 1, 1, 1, 1, 1, 1, 1, 1, 1, 1, 0, 0, 0, 0}, {1, 1, 1, 1, 1, 1, 1, 1, 1, 1, 1, 1, 1, 1, 1, 1, 0, 0, 0, 0, 0, 0, 0, 0, 0, 0}, {2, 2, 2, 2, 1, 1, 1, 1, 1, 1, 1, 1, 0, 0, 0, 0, 0, 0, 0, 0, 0, 0, 0, 0, 0, 0}};
        const int VA[4][26] = {{1, 1, 0, 0, 0, 0, 1, 1, 1, 1, 1, 1, 1, 1, 1, 1, 1, 1, 1, 1, 1, 1, 2, 2, 0, 0}, {1, 1, 1, 1, 1, 1, 1, 1, 1, 1, 1, 1, 1, 1, 1, 1, 1, 1, 1, 1, 1, 1, 2, 2, 0, 0}, {1, 1, 1, 1, 1, 1, 1, 1, 1, 1, 1, 1, 1, 1, 1, 1, 2, 2, 2, 2, 2, 3, 3, 2, 0, 0}, {0, 0, 0, 0, 1, 1, 1, 1, 1, 1, 1, 1, 2, 2, 2, 2, 0, 0, 0, 0, 0, 0, 0, 0, 0, 0}};
        const int TB[4][26] = {{0, 1, 2, 3, 7, 6, 5, 4, 24, 8, 9, 10, 11, 15, 14, 13, 12, 25, 16, 17, 18, 19, 23, 22, 21, 20}, {24, 8, 9, 10, 11, 15, 14, 13, 12, 25, 16, 17, 18, 19, 23, 22, 21, 20, 0, 1, 2, 3, 7, 6, 5, 4}, {25, 16, 17, 18, 19, 23, 22, 21, 20, 0, 1, 2, 3, 7, 6, 5, 4, 24, 8, 9, 10, 11, 15, 14, 13, 12}, {0, 1, 2, 3, 7, 6, 5, 4, 24, 8, 9, 10, 11, 15, 14, 13, 12, 25, 19, 23, 0, 0, 0, 0, 0, 0}};
        const int DB[4][26] = {{1, 1, 1, 1, 1, 1, 1, 1, 1, 1, 1, 1, 1, 1, 1, 1, 1, 1, 1, 1, 1, 1, 1, 0, 0, 0}, {1, 1, 1, 1, 1, 1, 1, 1, 1, 1, 1, 1, 1, 1, 1, 1, 1, 1, 1, 1, 1, 1, 0, 0, 0, 0}, {1, 1, 1, 1, 1, 1, 1, 1, 1, 1, 1, 1, 1, 1, 1, 1, 1, 1, 1, 1, 1, 1, 1, 0, 0, 0}, {2, 2, 1, 1, 1, 1, 1, 1, 1, 1, 1, 1, 1, 1, 1, 1, 1, 1, 1, 0, 0, 0, 0, 0, 0, 0}};
        const int VB[4][26] = {{1, 1, 1, 1, 1, 1, 1, 1, 1, 1, 1, 1, 1, 1, 1, 1, 1, 1, 1, 1, 1, 1, 1, 2, 2, 2}, {1, 1, 1, 1, 1, 1, 1, 1, 1, 1, 1, 1, 1, 1, 1, 1, 1, 1, 1, 1, 1, 1, 3, 3, 3, 2}, {1, 1, 1, 1, 1, 1, 1, 1, 1, 1, 1, 1, 1, 1, 1, 1, 1, 1, 1, 1, 1, 1, 1, 3, 3, 2}, {0, 0, 1, 1, 1, 1, 1, 1, 1, 1, 1, 1, 1, 1, 1, 1, 1, 1, 1, 2, 0, 0, 0, 0, 0, 0}};
        const int G[32] = {8, 12, 9, 13, 10, 14, 11, 15, 16, 17, 20, 21, 18, 19, 22, 23, 0, 4, 1, 5, 2, 6, 3, 7, 0, 4, 1, 5, 2, 6, 3, 7};
        const int NM = HALO ? 26 : 24;
        n[0] = n[1] = n[2] = NM; n[3] = HALO ? 20 : 16;
        for (int i = 0; i < NM; ++i) f[i] = HALO ? FB[i] : FA[i];
        int vv = 0;
        for (int h = 0; h < 4; ++h) {
            v[h][0] = vv;
            for (int i = 0; i < n[h]; ++i) {
                t[h][i] = HALO ? TB[h][i] : TA[h][i];
                d[h][i + 1] = d[h][i] + (HALO ? DB[h][i] : DA[h][i]);
                vv += HALO ? VB[h][i] : VA[h][i];
                v[h][i + 1] = vv;
            }
        }
        for (int i = 0; i < 32; ++i) { h3[i] = G[i]; s3[i] = i >= 16 && i < 24 ? 0 : 1; }
        for (int i = 3; i < 32; ++i) o3[i + 1] = o3[i] + TOPS;
        int nv = 0, ns = 0;
        for (int k = 0; k < 8 * 14; ++k) {
            const bool top = k % TOPS < 2 || nv == 96;
            if (top && ns < 16 && nv >= 12 * (ns / 2 + 1) && (nv == 96 || k % TOPS == (ns & 1))) { q3[k] = (ns / 2) * 14 + 13 - (ns & 1); ++ns; }
            else { q3[k] = (nv / 12) * 14 + nv % 12; ++nv; }
        }
    }
};
template <bool HALO> DI void qkv_consume(const Ctx& c0, int l, KP kp) {
    const Ctx c = fresh(c0);
    LAS char* smem = c.smem;
    const int lane = c.lane, wr = c.wid >> 1, wc = c.wid & 1, fr = lane & 15, fq = lane >> 4;
    const int sw = HALO ? wr : 0;
    f32x4 acc[4][6], ah[2];
#pragma unroll
    for (int mt = 0; mt < 4; ++mt)
#pragma unroll
        for (int nt = 0; nt < 6; ++nt) { acc[mt][nt] = (f32x4){0.f, 0.f, 0.f, 0.f}; asm volatile("" : "+v"(acc[mt][nt])); }
    ah[0] = ah[1] = (f32x4){0.f, 0.f, 0.f, 0.f};
    if (HALO) asm volatile("" : "+v"(ah[0]), "+v"(ah[1]));
    const unsigned aoff0 = (unsigned)(QA_OFF + wr * 8192 + (HALO || l ? lane * 16 : fr * 128 + ((fq ^ (fr & 7)) << 4)));
    const unsigned hoff0 = (unsigned)(QA_OFF + 16384 + lane * 16);
    unsigned xm = HALO || l ? 1024u : 64u;
    asm volatile("" : "+s"(xm));
    static_assert(((QA_OFF | QA_SLOT) & 2047) == 0, "halves");
    const int q4 = fr >> 2, p4 = lane & 3;
    const int fb = (q4 & 2) | ((fq & 1) << 2);
    unsigned boff[2];
#pragma unroll
    for (int cc = 0; cc < 2; ++cc)
        boff[cc] = (unsigned)((8 * fq + q4) * 128 + (((2 * (2 * wc + (cc ^ sw)) + (p4 >> 1)) ^ fb) << 4) + (p4 & 1) * 8);
    bf16x8 xa[2][4], xh[2], wb[2][6];
    auto ldfrag = [&](int set, unsigned pa, unsigned ph, unsigned pb0, unsigned pb1, int ks) {
#pragma unroll
        for (int mt = 0; mt < 4; ++mt) xa[set][mt] = *(const LAS bf16x8*)(lds(pa) + mt * 2048);
        if (HALO) xh[set] = *(const LAS bf16x8*)lds(ph);
#pragma unroll
        for (int nt = 0; nt < 6; ++nt) {
            const LAS char* pb = lds((nt & 1) ? pb1 : pb0) + (nt >> 1) * 8192 + ks * 4096;
            const s16x4 lo = tr_read(pb), hi = tr_read(pb + 512);
            wb[set][nt] = __builtin_shufflevector(lo, hi, 0, 1, 2, 3, 4, 5, 6, 7);
        }
    };
    constexpr int NM = HALO ? 26 : 24, NH = HALO ? 17 : 16;
    auto mm = [&](int set, int i0, int i1) {
#pragma unroll
        for (int ii = i0; ii < i1; ++ii) {
            const int i = !HALO || ii < 8 ? ii : (!SNK ? (ii < 10 ? ii + 16 : ii - 2) : (ii == 8 ? 24 : (ii < 17 ? ii - 1 : (ii == 17 ? 25 : ii - 2))));
            const int nt = i >> 2, mt = SNK && (nt & 1) ? 3 - (i & 3) : i & 3;
            if (i < 16) acc[mt][nt] = MFMA(wb[set][nt], xa[set][mt], acc[mt][nt]);
            else if (i < 24) acc[mt][nt] = MFMA(xa[set][mt], wb[set][nt], acc[mt][nt]);
            else if (i == 24) ah[0] = MFMA(wb[set][2], xh[set], ah[0]);
            else ah[1] = MFMA(xh[set], wb[set][4], ah[1]);
        }
    };
    auto interleave = [&]() {
#pragma unroll
        for (int i = 0; i < NH; ++i) { SGB(0x008, 1); SGB(0x100, 1); }
        SCHED();
    };
    unsigned sa = 0, sb = 0;
    unsigned pa0 = aoff0, pa1 = aoff0 ^ xm, ph0 = hoff0, ph1 = hoff0 ^ xm, pb0 = boff[0], pb1 = boff[1];
    asm volatile("" : "+v"(pa0), "+v"(pa1), "+v"(pb0), "+v"(pb1));
    if (HALO) asm volatile("" : "+v"(ph0), "+v"(ph1));
    constexpr int M_CSH = M_CS + (HALO ? 768 : 0);
    unsigned sa0 = (unsigned)(M_MEAN + 64 + (wr * 64 + fr) * 4), sa1 = (unsigned)(M_MEAN + 64 + wr * 256 + fq * 16);
    unsigned sa2 = (unsigned)(M_CSH + wc * 128 + sw * 64 + fq * 16), sa6 = (unsigned)(M_CSH + 512 + wc * 128 + sw * 64 + fr * 4);
    unsigned sa3 = sa2 ^ 64, sa7 = sa6 ^ 64, sa4 = (unsigned)(M_MEAN + fr * 4), sa5 = (unsigned)(M_MEAN + fq * 16);
    asm volatile("" : "+v"(sa0), "+v"(sa1), "+v"(sa2), "+v"(sa6), "+v"(sa3), "+v"(sa7));
    if (HALO) asm volatile("" : "+v"(sa4), "+v"(sa5));
    const int head = c.jq * 2 + wc;
    auto rso = __builtin_amdgcn_make_buffer_rsrc(HALO ? c.ws + WS_O + (size_t)l * (8u << 20) + (size_t)((head * 16 + c.pq) * 8 + wr * 4) * (1024 << HO)
                                                      : c.ws + WS_KV + (size_t)(l >> 1) * (16u << 20) + (size_t)((c.pq * 32 + head) * 4 + wr) * (4096 << HK), 0, HALO ? 4096 << HO : 12288 << HK, 0x00020000);
    unsigned xrs = (unsigned)(M_XCH + wc * 6144 + wr * 2048), xhs = (unsigned)(M_XCH + wc * 6144 + 8 - wr * 8);
    unsigned b0m = (c.pq == 0 && wr == 0) ? 15u * 16u : 0u;
    asm volatile("" : "+s"(rso));
    if (HALO) asm volatile("" : "+s"(xrs), "+s"(xhs), "+s"(b0m));
    u32x4 cpy[3];
    unsigned cdst, cv[3], vt;
    const char *cs_, *tw;
    int more = !HALO && l == 0 ? KT - 3 - XST - (XSP == 1 ? c.pq & 7 : (XSP == 2 ? ((c.pq & 1) * 4 + c.wid) : (XSP == 3 ? (c.pq + 2 * c.wid) & 7 : 0))) : 0, three = c.jq & 4;
    if (!HALO) {
        static_assert(CPY + TOUCH == 2, "extra");
        asm volatile("" : "+s"(kp));
        const int g = c.jq >> 2, n = (c.pq * 4 + (c.jq & 3)) * 4 + c.wid, m = n >> 7;
        const int kt = (n >> 3) & 15, h = (n >> 2) & 31;
        const char* pq_ = (const char*)(m ? kp->wqkv0 : kp->wqkv1);
        const char* po_ = (const char*)(m ? kp->wo0 : kp->wo1);
        const char* t0_ = (const char*)kp->wqkv1;
        const char* t1_ = (const char*)kp->wo1;
        asm volatile("" : "+s"(pq_), "+s"(po_), "+s"(t0_), "+s"(t1_));
        cs_ = three ? pq_ + (size_t)kt * (64 * 6144) : po_ + (size_t)h * 65536;
        tw = c.wid < 3 ? t0_ + (size_t)c.wid * (2u << 20) : t1_;
#pragma unroll
        for (int i = 0; i < 3; ++i) cv[i] = wq_src((n & 7) * 3 + i, lane, 3 + 4 * (g & 2));
        cv[0] = pick(three, cv[0], wo_src(n & 3, lane, 11 - 4 * g));
        int so = three ? (int)WS_CQ + kt * (64 * 6144) + (m * 2 + (g >> 1)) * 24576 + (n & 7) * 3072 : (int)WS_CO + h * 65536 + (m * 2 + 1 - (g >> 1)) * 4096 + (n & 3) * 1024;
        asm volatile("" : "+s"(so));
        cdst = (unsigned)(so + lane * 16);
        vt = (unsigned)(c.bid * 8192 + ((lane & 7) == 3 ? lane - 1 : lane) * 128);
        asm volatile("" : "+s"(more), "+s"(three), "+s"(cs_), "+s"(tw));
        asm volatile("" : "+v"(cv[0]), "+v"(cv[1]), "+v"(cv[2]), "+v"(cdst), "+v"(vt));
        asm volatile("" : "=v"(cpy[0]), "=v"(cpy[1]), "=v"(cpy[2]));
    }
    unsigned hto = HT0 == 2 ? 0u : 0x80000000u;
    const char* W0 = (const char*)kp->wqkv0;
    if (!HALO && HT0) {
        const int n = ((c.pq & 7) * 3 + (c.jq & 3)) * 64 + c.wid * 16 + fr, r3 = (n * 43691) >> 17;
        const int t0 = rot_first(c.pq & 8, c.jq | 3) + 8;
        if (l == 0 && !(c.jq & 4) && (c.jq & 3) != 3 && lane < 16) hto = (unsigned)(((t0 * 64 + r3) & 1023) * 6144 + (n - 3 * r3) * 2048 + (c.jq | 3) * 128);
        asm volatile("" : "+v"(hto));
        asm volatile("" : "+s"(W0));
    }
    if (l > 0 && c.wid < 2) STAMP_AT(l == 1 ? 41 + 2 * c.wid : (l == 2 ? 46 + c.wid : 59 + c.wid));
    if (!HALO && HT0 == 2) { asm volatile("s_mov_b32 m0, %0\n\ts_nop 3\n\tglobal_load_lds_dword %1, %2" ::"s"(M_TCH), "v"(hto), "s"(W0) : "memory"); }
    if (!HALO && l == 0) BAR();
    if (!HALO) { if (HT0 == 2) wait_v<1>(); else WAIT_V0(); }
    BAR();
    if (!HALO && HT0 == 1) { if (hto < 0x80000000u) asm volatile("s_mov_b32 m0, %0\n\ts_nop 3\n\tglobal_load_lds_dword %1, %2" ::"s"(M_TCH), "v"(hto), "s"(W0) : "memory"); }
    if (c.wid == 0) STAMP_AT(l * 8 + 0);
    ldfrag(0, pa0, ph0, pb0, pb1, 0);
    SCHED();
    if (ALN) WAIT_L0();
    SCHED();
    int cnt = KT - 3 - more, tv = 0;
    QSTAMP(0);
    auto half0 = [&]() {
        ldfrag(1, pa1, ph1, pb0, pb1, 1);
        mm(0, 0, NH);
        interleave();
        mm(0, NH, NH + 1); SCHED();
        asm volatile("s_addk_i32 %0, %2\n\ts_addk_i32 %1, %3" : "+s"(sa), "+s"(sb) : "i"(QA_SLOT), "i"(QW_SLOT) : "scc"); SCHED();
        mm(0, NH + 1, NH + 2); SCHED();
        asm volatile("s_cmpk_lg_u32 %0, %2\n\ts_cselect_b32 %0, %0, 0\n\ts_cmp_lg_u32 %1, %3\n\ts_cselect_b32 %1, %1, %4" : "+s"(sa), "+s"(sb) : "i"(NQA * QA_SLOT), "i"(3 * QW_SLOT), "i"(QW_TOP) : "scc"); SCHED();
        mm(0, NH + 2, NH + 3); SCHED();
        asm volatile("s_cmp_lg_u32 %0, %2\n\ts_cselect_b32 %0, %0, 0\n\ts_addk_i32 %1, -1" : "+s"(sb), "+s"(cnt) : "i"(QW_TOP + QW_SLOT) : "scc"); SCHED();
        mm(0, NH + 3, NH + 4); SCHED();
        pa0 = aoff0 + sa; pb0 = boff[0] + sb;
        asm volatile("" : "+v"(pa0), "+v"(pb0)); SCHED();
        mm(0, NH + 4, NH + 5); SCHED();
        pb1 = boff[1] + sb; pa1 = pa0 ^ xm;
        asm volatile("" : "+v"(pa1), "+v"(pb1)); SCHED();
        mm(0, NH + 5, NH + 6); SCHED();
        if (HALO) { ph0 = hoff0 + sa; ph1 = ph0 ^ xm; asm volatile("" : "+v"(ph0), "+v"(ph1)); SCHED(); }
        mm(0, NH + 6, NM);
        SCHED();
        WAIT_L0();
        BAR();
        SCHED();
    };
    asm volatile("" : "+s"(sa), "+s"(sb), "+s"(cnt));
    do {
    ALIGN8();
    do {
        half0();
        ldfrag(0, pa0, ph0, pb0, pb1, 0);
        mm(1, 0, NH);
        interleave();
        mm(1, NH, NM - 1);
        SCHED();
        WAIT_L0();
        asm volatile("s_nop 0");
        SCHED();
        mm(1, NM - 1, NM);
        SCHED();
    } while (cnt != 0);
    if (!HALO) {
        asm volatile("s_cmp_eq_u32 %[more], 0\n\ts_cbranch_scc1 .Lex%=\n\ts_mov_b32 m0, %[tch]\n\ts_nop 3\n\tglobal_load_lds_dword %[vt], %[tw] sc1\n\t"
                     "global_load_dwordx4 %[c0], %[v0], %[cs]\n\ts_cmp_eq_u32 %[three], 0\n\ts_cbranch_scc1 .Lex%=\n\t"
                     "global_load_dwordx4 %[c1], %[v1], %[cs]\n\tglobal_load_dwordx4 %[c2], %[v2], %[cs]\n.Lex%=:"
                     : [c0] "+v"(cpy[0]), [c1] "+v"(cpy[1]), [c2] "+v"(cpy[2])
                     : [more] "s"(more), [tch] "i"(M_TCH), [vt] "v"(vt), [tw] "s"(tw), [v0] "v"(cv[0]), [v1] "v"(cv[1]), [v2] "v"(cv[2]), [cs] "s"(cs_), [three] "s"(three) : "scc", "memory");
        cnt = more; more = 0;
        asm volatile("" : "+s"(cnt), "+s"(more));
        SCHED();
    }
    } while (cnt != 0);
    if (!HALO) {
        asm volatile("s_cmp_lg_u32 %[l], 0\n\ts_cbranch_scc1 .Lex%=\n\ts_waitcnt vmcnt(0)\n\ts_nop 3\n\tglobal_store_dwordx4 %[d], %[c0], %[ws] sc1\n\ts_cmp_eq_u32 %[three], 0\n\ts_cbranch_scc1 .Lex%=\n\t"
                     "global_store_dwordx4 %[d], %[c1], %[ws] offset:1024 sc1\n\tglobal_store_dwordx4 %[d], %[c2], %[ws] offset:2048 sc1\n.Lex%=:\n\ts_nop 1"
                     :: [c0] "v"(cpy[0]), [c1] "v"(cpy[1]), [c2] "v"(cpy[2]), [l] "s"(l), [d] "v"(cdst), [ws] "s"(c.ws), [three] "s"(three) : "scc", "memory");
    }
    QSTAMP(1);
    FENCE();
    SCHED();

    f32x4 csq[4], m4[4], mh4; float csv[2], mr[4], rr[4], mh;
    auto r1 = [&](unsigned b, int off) { return *(const LAS float*)(lds(b) + off); };
    auto r4_ = [&](unsigned b, int off) { return *(const LAS f32x4*)(lds(b) + off); };
    auto mf = [&](int set, int i) {
        const int nt = i >> 2, mt = i & 3;
        if (i < 16) acc[mt][nt] = MFMA(wb[set][nt], xa[set][mt], acc[mt][nt]);
        else if (i < 24) acc[mt][nt] = MFMA(xa[set][mt], wb[set][nt], acc[mt][nt]);
        else if (i == 24) ah[0] = MFMA(wb[set][2], xh[set], ah[0]);
        else ah[1] = MFMA(xh[set], wb[set][4], ah[1]);
        SCHED();
    };
    auto fold = [&](int i, int e) {
        const int nt = i >> 2, mt = i & 3;
        float t;
        if (i < 16) t = __builtin_fmaf(mr[mt], csq[nt][e], acc[mt][nt][e]);
        else if (i < 24) t = __builtin_fmaf(m4[mt][e], csv[nt - 4], acc[mt][nt][e]);
        else if (i == 24) t = __builtin_fmaf(mh, csq[2][e], ah[0][e]);
        else t = __builtin_fmaf(mh4[e], csv[0], ah[1][e]);
        asm volatile("" : "+v"(t));
        if (i < 24) acc[mt][nt][e] = t; else ah[i - 24][e] = t;
    };
    constexpr Tail<HALO> TL{};
    constexpr int NP = HALO ? 6 : 2, FN = 4 * NM;
    static_assert(TL.v[3][TL.n[3]] == NP + FN + (HALO ? 2 : 0) && KT % NQA == 1 && KT % NQW == 0, "tail");
    s16x4 wl[6], wh[6];
    auto frd = [&](int set, int k, int ao) {
        if (k < 4) xa[set][k] = *(const LAS bf16x8*)(lds(set ? pa1 : pa0) + ao + k * 2048);
        else if (k == 16) xh[set] = *(const LAS bf16x8*)(lds(set ? ph1 : ph0) + ao);
        else {
            const int nt = (k - 4) >> 1;
            const LAS char* pb = lds((nt & 1) ? pb1 : pb0) + (nt >> 1) * 8192 + set * 4096;
            if (k & 1) { wh[nt] = tr_read(pb + 512); wb[set][nt] = __builtin_shufflevector(wl[nt], wh[nt], 0, 1, 2, 3, 4, 5, 6, 7); }
            else wl[nt] = tr_read(pb);
        }
    };
    constexpr int RS = M_RSTD - M_MEAN;
    unsigned vl, vg, tb, xr, xh_, b0a, qb0, qb1;
    f32x4 bprev, bcur, bprev0; u32x4 ps; u32x2 ko; float ninf;
    auto rdop = [&](int h, int k) {
        const int j = k - NH;
        if (h == 0) {
            if (k < 2) { mr[2 * k] = r1(sa0, k * 128); mr[2 * k + 1] = r1(sa0, k * 128 + 64); }
            else if (k < 4) csq[k - 2] = r4_((k & 1) ? sa3 : sa2, 0);
            else if (j < 4) frd(0, k - 4, 2 * QA_SLOT);
            else csq[j - 2] = r4_((j & 1) ? sa3 : sa2, 256);
        } else if (j < 0) frd(h & 1, k, h == 1 ? 2 * QA_SLOT : 0);
        else if (h == 1) {
            if (j < 2) csv[j] = r1(j ? sa7 : sa6, 0);
            else if (HALO && j == 2) mh = r1(sa4, 0);
            else if (HALO && j == 3) mh4 = r4_(sa5, 0);
            else { const int mt = HALO ? 7 - j : j - 2; m4[mt] = r4_(sa1, mt * 64); }
        } else if (h == 2) {
            if (j == 0) m4[2] = r4_(sa1, 128);
            if (j == 1) ko = *(const LAS u32x2*)(lds(tb) + 3072);
            if (j == 2) { rr[0] = r1(sa0, RS); rr[1] = r1(sa0, 64 + RS); }
            if (j == 3) { rr[2] = r1(sa0, 128 + RS); rr[3] = r1(sa0, 192 + RS); }
            if (j >= 4) m4[5 - j] = r4_(sa1, (5 - j) * 64);
        } else {
            if (j == 0) bprev = r4_(tb, 0);
            if (j == 1) bcur = r4_(tb, 1024);
            if (j == 2) ps = *(const LAS u32x4*)(lds(tb) + 2048);
            if (j == 3) bprev0 = r4_(b0a, 0);
        }
    };
    auto vop = [&](int k) {
        if (k == 0) { asm volatile("" : "+v"(boff[0])); qb0 = boff[0] + QW_TOP; asm volatile("" : "+v"(qb0)); }
        else if (k == 1) { asm volatile("" : "+v"(boff[1])); qb1 = boff[1] + QW_TOP; asm volatile("" : "+v"(qb1)); }
        else if (k < NP) {
            if (k == 2) { vl = (unsigned)(lane * 16); asm volatile("" : "+v"(vl)); }
            if (k == 3) { tb = vl + M_TAB; asm volatile("" : "+v"(tb)); }
            if (k == 4) { b0a = tb | b0m; asm volatile("" : "+v"(b0a)); }
            if (k == 5) { xr = xrs + vl; asm volatile("" : "+v"(xr)); }
        } else if (k < NP + FN) fold(TL.f[(k - NP) >> 2], (k - NP) & 3);
        else {
            if (k == NP + FN) { xh_ = xhs + vl; asm volatile("" : "+v"(xh_)); }
            if (k == NP + FN + 1) { ninf = -INFINITY; asm volatile("" : "+v"(ninf)); }
        }
    };
    unsigned wa, wq;
    int wr2 = wr, wc2 = wc;
#pragma unroll
    for (int i = 0; i < NM; ++i) {
        mf(0, TL.t[0][i]);
        if (TL.t[0][i] < 24) asm volatile("" : "+v"(acc[TL.t[0][i] & 3][TL.t[0][i] >> 2])); else asm volatile("" : "+v"(ah[TL.t[0][i] & 1]));
        FENCE();
        if (i < NH) frd(1, i, 0);
        if (!HALO && i == NH + 3) { asm volatile("" : "+s"(wr2), "+s"(wc2)); vl = (unsigned)(lane * 16); wq = (unsigned)(AT_Q + (wc2 * 8 + wr2 * 4) * 1024) + vl; asm volatile("" : "+v"(vl), "+v"(wq)); }
        if (!HALO && i == NH + 4) { asm volatile("" : "+s"(wr2), "+s"(wc2)); wa = (unsigned)(AT_OWN + wc2 * 16384 + wr2 * 4096) + vl; asm volatile("" : "+v"(wa)); }
        if (i == NH + 1) { asm volatile("" : "+v"(boff[0]), "+v"(boff[1])); pb0 = boff[0] + 2 * QW_SLOT; pb1 = boff[1] + 2 * QW_SLOT; asm volatile("" : "+v"(pb0), "+v"(pb1)); }
        if (i == NH + 2) { pa0 = aoff0; pa1 = aoff0 ^ xm; asm volatile("" : "+v"(pa1)); if (HALO) { ph0 = hoff0; ph1 = hoff0 ^ xm; asm volatile("" : "+v"(ph1)); } }
        SCHED();
    }
    WAIT_L0();
    BAR();
    FENCE();
    SCHED();
    auto frags = [&](int h) {
        static_assert(TL.d[0][NM] == NH + 6 && TL.d[1][NM] == NH + (HALO ? 5 : 6) && TL.d[2][NM] == NH + (HALO ? 6 : 0) && TL.d[3][TL.n[3]] == NH + (HALO ? 4 : 0), "reads");
        if (h == 0) __builtin_amdgcn_s_waitcnt(0xC27F);
        if (h == 1) __builtin_amdgcn_s_waitcnt(HALO ? 0xC57F : 0xC67F);
        if (h == 2) __builtin_amdgcn_s_waitcnt(HALO ? 0xC67F : 0xC07F);
        if (h == 3) __builtin_amdgcn_s_waitcnt(HALO ? 0xC47F : 0xC07F);
    };
#pragma unroll
    for (int h = 0; h < 4; ++h) {
        if (h == 2) { pb0 = qb0; pb1 = qb1; }
#pragma unroll
        for (int i = 0; i < TL.n[h]; ++i) {
            if (!(h & 1) && i == TL.n[h] - 1) { frags(h); SCHED(); if (h) QSTAMP(4); }
            mf(~h & 1, TL.t[h][i]);
#pragma unroll
            for (int k = TL.d[h][i]; k < TL.d[h][i + 1]; ++k) { FENCE(); rdop(h, k); }
            SCHED();
#pragma unroll
            for (int k = TL.v[h][i]; k < TL.v[h][i + 1]; ++k) vop(k);
            SCHED();
        }
        if (h & 1) {
            frags(h);
            if (h == 1) QSTAMP(2); else QSTAMP(5);
            BAR();
            FENCE();
            SCHED();
            if (h == 1) QSTAMP(3); else QSTAMP(6);
        }
    }
    if (!HALO) {
        f32x4 r4[4];
        mf(1, 8);
#pragma unroll
        for (int mt = 0; mt < 4; ++mt) rr[mt] = r1(sa0, mt * 64 + RS);
        SCHED();
        mf(1, 12);
#pragma unroll
        for (int mt = 0; mt < 4; ++mt) r4[mt] = r4_(sa1, mt * 64 + RS);
        SCHED();
        mf(1, 9);
        vg = HK ? (unsigned)hv<HK>(lane) : vl;
        if (HK) asm volatile("" : "+v"(vg));
        SCHED();
        u32x4 pk[8];
        auto op = [&](int n) {
            if (n >= 8 * 14) return;
            const int g = TL.q3[n] / 14, k = TL.q3[n] % 14;
            const int ta = g < 4 ? g : (g - 4) & 2, tb = g < 4 ? g : ta + 1, na = g < 4 ? 2 : 4 + (g & 1), nb = g < 4 ? 3 : na;
            f32x4& A = acc[ta][na]; f32x4& Bv = acc[tb][nb];
            if (k < 12) {
                const int h = k / 6, kk = k % 6;
                f32x4& X = h ? Bv : A;
                const int tx = h ? tb : ta;
                if (kk < 4) { float t = X[kk] * (g < 4 ? rr[tx] : r4[tx][kk]); asm volatile("" : "+v"(t)); X[kk] = t; }
                else { unsigned t = cvt_pk_bf16(X[2 * (kk - 4)], X[2 * (kk - 4) + 1]); asm volatile("" : "+v"(t)); pk[g][2 * h + kk - 4] = t; }
            } else if (k == 12) __builtin_amdgcn_raw_buffer_store_b128(pk[g], rso, vg + hp<HK>(g & 3), (g >> 2) * (8192 << HK), 16);
            else *(LAS u32x4*)(lds(wa) + (g >> 2) * 8192 + (g & 3) * 1024) = pk[g];
        };
        constexpr int NOP = 8 * 14;
#pragma unroll
        for (int i = 3; i < 32; ++i) {
            mf(TL.s3[i], TL.h3[i]);
#pragma unroll
            for (int n = TL.o3[i]; n < TL.o3[i + 1]; ++n) { op(n); SCHED(); }
        }
        QSTAMP(7);
        if (c.wid == 0) STAMP_AT(l * 8 + 1);
#pragma unroll
        for (int n = TL.o3[32]; n < NOP; ++n) op(n);
        SCHED();
        QSTAMP(8);
#pragma unroll
        for (int mt = 0; mt < 4; ++mt) {
            const u32x2 q0 = pack4(acc[mt][0] * (rr[mt] * QSCALE)), q1 = pack4(acc[mt][1] * (rr[mt] * QSCALE));
            *(LAS u32x4*)(lds(wq) + mt * 1024) = (u32x4){q0[0], q0[1], q1[0], q1[1]};
        }
        QSTAMP(9);
        QSTORE();
        if (K0E) {
            SCHED();
            WAIT_L0();
            BAR();
            FENCE();
            SCHED();
            if (KSP == 4) {
                publish<4, 0>(smem, lane, c.ws, (unsigned)WS_FLAGQ + (c.pq * 16 + c.jq) * FQS, lane == 0, (unsigned)(4 * l + 2));
                if (c.wid == 0) STAMP_AT(l * 8 + 2);
                SCHED();
            }
        }
    } else {
        typedef unsigned u32x10 __attribute__((ext_vector_type(10)));
        u32x4 kp[4], qp[4];
        u32x10 vw[2];
        float ks[2][4], cs[2][4];
        auto pk8 = [&](const f32x4& x, const f32x4& y) { const u32x2 a = pack4(x), b = pack4(y); u32x4 r = {a[0], a[1], b[0], b[1]}; asm volatile("" : "+v"(r)); SCHED(); return r; };
        auto pkv = [&](int mt, int cc) { const u32x2 a = pack4(acc[mt][4 + cc]); vw[cc][2 * mt + 2] = a[0]; vw[cc][2 * mt + 3] = a[1]; };
        auto fr8 = [](const u32x4& x) { return __builtin_bit_cast(bf16x8, x); };
        mf(1, 24);
        unsigned ka0 = ko[0] + wr * 256, ka1 = ko[1] + wr * 256;
        asm volatile("" : "+v"(ka0), "+v"(ka1));
        SCHED();
        mf(1, 25);
#pragma unroll
        for (int mt = 0; mt < 4; ++mt) ks[0][mt] = r1(ka0, mt * 64);
        rr[0] *= QSCALE; asm volatile("" : "+v"(rr[0]));
        SCHED();
        mf(1, 11);
#pragma unroll
        for (int mt = 0; mt < 4; ++mt) ks[1][mt] = r1(ka1, mt * 64);
        rr[1] *= QSCALE; asm volatile("" : "+v"(rr[1]));
        SCHED();
        mf(1, 15);
        rr[2] *= QSCALE; asm volatile("" : "+v"(rr[2]));
        rr[3] *= QSCALE; asm volatile("" : "+v"(rr[3]));
        SCHED();
        mf(1, 19);
        const u32x4 hh = pk8(ah[0], ah[1]);
        mf(1, 23);
        *(LAS u32x2*)lds(xh_) = (u32x2){hh[0], hh[1]}; *(LAS u32x2*)(lds(xh_) + 1024) = (u32x2){hh[2], hh[3]};
        SCHED();
        mf(1, 10);
        kp[3] = pk8(acc[3][2], acc[3][3]);
        mf(1, 14);
        *(LAS u32x4*)(lds(xr) + 2048) = kp[3];
        pkv(3, 0); asm volatile("" : "+v"(vw[0]));
        *(LAS u32x2*)(lds(xr) + 3072) = (u32x2){vw[0][8], vw[0][9]};
        SCHED();
        mf(1, 3);
        pkv(3, 1); asm volatile("" : "+v"(vw[1]));
        *(LAS u32x2*)(lds(xr) + 3072 + 8) = (u32x2){vw[1][8], vw[1][9]};
        SCHED();
        mf(1, 7);
        mf(1, 9);
        kp[2] = pk8(acc[2][2], acc[2][3]);
        mf(1, 13);
        qp[3] = pk8(acc[3][0], acc[3][1]);
        mf(1, 2);
        f32x4 s0[4], s1[4];
        u32x4 pk;
        auto sc = [&](int mt, int h) {
            if (h) { s1[mt] = MFMA(fr8(kp[mt]), fr8(qp[mt]), bcur); asm volatile("" : "+v"(s1[mt])); }
            else { s0[mt] = MFMA(fr8(mt ? kp[mt - (mt > 0)] : pk), fr8(qp[mt]), mt == 0 ? bprev0 : bprev); asm volatile("" : "+v"(s0[mt])); }
            SCHED();
        };
        auto csm = [&](int mt) { cs[0][mt] = ks[0][mt] * rr[mt]; asm volatile("" : "+v"(cs[0][mt])); cs[1][mt] = ks[1][mt] * rr[mt]; asm volatile("" : "+v"(cs[1][mt])); };
        float u[2][4], mx[4], iv[4];
        auto sv = [](float x) { asm volatile("" : "+v"(x)); return x; };
        auto mx2 = [&](float a, float b) { return max3(ninf, a, b); };
        auto pick = [&](int mt, int h) { u[h][mt] = sv(max3(mx2(s0[mt][2 * h], s0[mt][2 * h + 1]), s1[mt][2 * h], s1[mt][2 * h + 1]) * cs[h][mt]); };
        auto vf = [&](int i) { const int mt = 2 - (i >> 2), nt = 4 + (i & 1), set = (i >> 1) & 1; SCHED(); acc[mt][nt] = MFMA(xa[set][mt], wb[set][nt], acc[mt][nt]); asm volatile("" : "+v"(acc[mt][nt])); SCHED(); };
        mf(1, 6);
        kp[1] = pk8(acc[1][2], acc[1][3]);
        mf(1, 8);
        WAIT_L0();
        QSTAMP(7);
        BAR();
        FENCE();
        SCHED();
        QSTAMP(8);
        mf(1, 12);
        {
            const u32x2 k0 = *(const LAS u32x2*)(lds(xr) + 8);
            FENCE(); SCHED();
            const u32x2 k1 = *(const LAS u32x2*)lds(xr);
            FENCE(); SCHED();
            const u32x2 a = *(const LAS u32x2*)(lds(xr) + 1024 + 8);
            FENCE(); SCHED();
            const u32x2 b = *(const LAS u32x2*)(lds(xr) + 1024);
            pk = (u32x4){k0[0], k0[1], k1[0], k1[1]};
            vw[0][0] = a[0]; vw[0][1] = a[1]; vw[1][0] = b[0]; vw[1][1] = b[1];
        }
        SCHED();
        auto sw16 = [](float& a, float& b) { auto r = __builtin_amdgcn_permlane16_swap(__float_as_uint(a), __float_as_uint(b), false, false); a = __uint_as_float(r[0]); b = __uint_as_float(r[1]); };
        auto sw32 = [](float& a, float& b) { auto r = __builtin_amdgcn_permlane32_swap(__float_as_uint(a), __float_as_uint(b), false, false); a = __uint_as_float(r[0]); b = __uint_as_float(r[1]); };
        auto ldr = [&](int i) { };
        auto top = [&](int mt) { mx[mt] = sv(mx2(u[0][mt], u[1][mt])); };
        auto pv1 = [&](int mt, int k) {
            SCHED();
            vw[k >> 1][2 * mt + 2 + (k & 1)] = cvt_pk_bf16(acc[mt][4 + (k >> 1)][2 * (k & 1)], acc[mt][4 + (k >> 1)][2 * (k & 1) + 1]);
            asm volatile("" : "+v"(vw[k >> 1]));
            SCHED();
        };
        auto uk = [&](int mt) { SCHED(); u[0][mt] = sv(u[0][mt] * ks[0][mt]); u[1][mt] = sv(u[1][mt] * ks[1][mt]); SCHED(); };
        mf(1, 1);
        qp[2] = pk8(acc[2][0], acc[2][1]);
        mf(1, 5);
        csm(3); SCHED();
        mf(1, 0);
        kp[0] = pk8(acc[0][2], acc[0][3]);
        mf(1, 4);
        csm(2); SCHED();
        sc(3, 1);
        qp[1] = pk8(acc[1][0], acc[1][1]);
        sc(3, 0);
        csm(1); ldr(0); SCHED();
        sc(2, 1);
        qp[0] = pk8(acc[0][0], acc[0][1]);
        sc(2, 0);
        csm(0); pick(3, 0); SCHED();
        sc(1, 1);
        pick(3, 1); ldr(1); SCHED();
        sc(1, 0);
        pick(2, 0); top(3); SCHED();
        sc(0, 1);
        pick(2, 1); ldr(2); SCHED();
        sc(0, 0);
        if (c.wid == 0) STAMP_AT(l * 8 + 1);
        pick(1, 0); top(2);
        vf(0);
        pick(1, 1); ldr(3);
        vf(1);
        sw16(mx[2], mx[3]);
        pick(0, 0);
        vf(2);
        pick(0, 1); top(1);
        vf(3);
        top(0); mx[2] = sv(mx2(mx[2], mx[3]));
        vf(4);
        sw16(mx[0], mx[1]); mx[0] = sv(mx2(mx[0], mx[1]));
        vf(5); pv1(2, 0);
        sw32(mx[0], mx[2]);
        { const float a = mx[0], b = mx[2]; mx[0] = sv(mx2(a, b)); mx[2] = sv(mx2(b, a)); }
        vf(6); pv1(2, 1);
        sw32(mx[0], mx[2]);
        mx[3] = sv(mx[2]); mx[1] = sv(mx[0]);
        vf(7);
        sw16(mx[2], mx[3]); sw16(mx[0], mx[1]);
        SCHED();
#pragma unroll
        for (int mm = 0; mm < 4; ++mm) { const int mt = 3 - mm; u[0][mt] = sv(u[0][mt] - mx[mt]); u[1][mt] = sv(u[1][mt] - mx[mt]); if (mm == 1) vf(8); }
        vf(9);
#pragma unroll
        for (int mm = 0; mm < 4; ++mm) {
            const int mt = 3 - mm;
            u[0][mt] = sv(__builtin_amdgcn_exp2f(u[0][mt])); u[1][mt] = sv(__builtin_amdgcn_exp2f(u[1][mt]));
            if (mm > 0) iv[mt + 1] = sv(u[0][mt + 1] + u[1][mt + 1]);
            if (mm < 2) vf(10 + mm);
        }
        SCHED();
        sw16(iv[2], iv[3]);
        iv[0] = sv(u[0][0] + u[1][0]);
        iv[2] = sv(iv[2] + iv[3]);
        pv1(2, 2);
        sw16(iv[0], iv[1]); iv[0] = sv(iv[0] + iv[1]);
        uk(3);
        sw32(iv[0], iv[2]);
        iv[0] = sv(iv[0] + iv[2]);
        pv1(2, 3);
        iv[0] = sv(__builtin_amdgcn_rcpf(iv[0]));
        pv1(1, 0);
        iv[2] = sv(iv[0]);
        uk(2);
        sw32(iv[0], iv[2]);
        iv[3] = sv(iv[2]); iv[1] = sv(iv[0]);
        pv1(1, 1);
        sw16(iv[2], iv[3]); sw16(iv[0], iv[1]);
        SCHED();
        f32x4 o[4][2];
        u32x4 pf[4];
        auto nrm = [&](int mt) { u[0][mt] = sv(u[0][mt] * iv[mt]); u[1][mt] = sv(u[1][mt] * iv[mt]); SCHED(); };
        auto prb = [&](int mt) {
            const unsigned p = cvt_pk_bf16(u[0][mt], u[1][mt]);
            pf[mt] = (u32x4){__builtin_amdgcn_perm(0u, p, ps[0]), __builtin_amdgcn_perm(0u, p, ps[1]), __builtin_amdgcn_perm(0u, p, ps[2]), __builtin_amdgcn_perm(0u, p, ps[3])};
            asm volatile("" : "+v"(pf[mt]));
            SCHED();
        };
        auto pvm = [&](int mt, int cc) {
            o[mt][cc] = MFMA(fr8((u32x4){vw[cc][2 * mt], vw[cc][2 * mt + 1], vw[cc][2 * mt + 2], vw[cc][2 * mt + 3]}), fr8(pf[mt]), ((f32x4){0.f, 0.f, 0.f, 0.f}));
            asm volatile("" : "+v"(o[mt][cc]));
            SCHED();
        };
        auto out = [&](int mt) {
            const u32x2 lo = pack4(o[mt][0]), hi = pack4(o[mt][1]);
            __builtin_amdgcn_raw_buffer_store_b128((u32x4){lo[0], lo[1], hi[0], hi[1]}, rso, vg + hp<HO>(mt), 0, 16);
            SCHED();
        };
        vg = (unsigned)hv<HO>(lane);
        asm volatile("" : "+v"(vg));
        nrm(3); nrm(2); prb(3);
        pv1(1, 2); pv1(1, 3);
        pvm(3, 0); prb(2); pvm(3, 1);
        uk(1); uk(0);
        pvm(2, 0); nrm(1); nrm(0); pvm(2, 1);
        prb(1); pv1(0, 0); pv1(0, 1);
        pvm(1, 0); pv1(0, 2); pv1(0, 3); prb(0);
        pvm(1, 1); out(3); pvm(0, 0); out(2); pvm(0, 1); out(1); out(0);
        SCHED();
        QSTAMP(9);
        if (c.wid == 0) STAMP_AT(l * 8 + 2);
        publish<4, 0>(smem, lane, c.ws, o_post(c), o_act(c), (unsigned)(4 * l + 4));
        QSTAMP(10);
        QSTORE();
        if (c.wid == 0) STAMP_AT(l * 8 + 4);
    }
}

#define MFMA32(a, b, c) __builtin_amdgcn_mfma_f32_32x32x16_bf16(__builtin_bit_cast(bf16x8, a), __builtin_bit_cast(bf16x8, b), (c), 0, 0, 0)
DI float max16(const f32x16& s) {
    float m = max3(s[0], s[1], s[2]), n = max3(s[3], s[4], s[5]);
    m = max3(m, s[6], s[7]); n = max3(n, s[8], s[9]);
    m = max3(m, s[10], s[11]); n = max3(n, s[12], s[13]);
    m = max3(max3(m, s[14], s[15]), n, n);
    auto b = __builtin_amdgcn_permlane32_swap(__float_as_uint(m), __float_as_uint(m), false, false);
    return __builtin_amdgcn_fmed3f(__uint_as_float(b[0]), __uint_as_float(b[1]), INFINITY);
}
template <bool HI> DI void attn_a(const Ctx& c0, int l, const char* Wo, KP kp, const char* xin, u32x4 (&res)[2][2]) {
    const Ctx c = fresh(c0);
    asm volatile("" : "+s"(Wo));
    LAS char* smem = c.smem;
    const int lane = c.lane, w = c.wid;
    constexpr int H = HI ? 0 : 1;
    int qt = HI ? w - 4 : 3 - w;
    asm volatile("" : "+s"(qt));
    const float NEG = -INFINITY;
    int zq = (lane & 31) - 4 * (lane >> 5);
    unsigned lb = (unsigned)(((lane >> 4) & 1) * 1024 + ((lane >> 5) * 16 + (lane & 15)) * 16);
    unsigned qa = (unsigned)(AT_Q + H * 8192 + qt * 2048) + lb, ta = (unsigned)(AT_OWN + H * 16384 - 3 * 2048 + qt * 2048) + lb;
    asm volatile("" : "+v"(lb), "+v"(qa), "+v"(ta));
    auto rd = [&](unsigned base, int off) { return *(const LAS u32x4*)(lds(base) + off); };
    f32x16 ce;
    u32x4 sel;
    int zb = (int)((0xfffffffeu << (lane & 31)) >> (4 * (lane >> 5)));
    asm volatile("" : "+v"(zb));
    auto masks = [&]() {
#pragma unroll
        for (int r = 0; r < 16; ++r) ce[r] = __int_as_float(__builtin_amdgcn_sbfe(zb, 8 * (r >> 2) + (r & 3), 1) << 23);
        const unsigned s1 = ((lane >> 4) ^ (lane >> 2)) & 1 ? 0u : 0x3f803f80u;
        sel = (u32x4){s1, s1, s1, s1};
        asm volatile("" : "+v"(ce), "+v"(sel));
    };
    u64 ep;
    unsigned po;
    u64 pa_;
    unsigned cnta = M_CNT, one = 1u;
    char* ob;
    const char *kvb, *s1_, *s2_;
    unsigned vk, dst, fo;
    auto setup = [&]() {
        const u32x2 e = *(const LAS u32x2*)(smem + M_EP);
        ep = ((u64)(unsigned)__builtin_amdgcn_readfirstlane(e[1]) << 32) | (unsigned)__builtin_amdgcn_readfirstlane(e[0]);
        po = HI ? o_post(c) : (unsigned)(lane < 2 ? WS_KBOX + ((c.pq + 1 + lane) * 16 + c.jq) * 256 + lane * 8 : WS_FLAGQ + (c.pq * 16 + c.jq) * FQS);
        pa_ = HI ? 0xffffull : (c.pq < 14 ? 7ull : (c.pq < 15 ? 5ull : 4ull));
        asm volatile("" : "+v"(po));
        ob = c.ws + WS_O + (size_t)l * (8u << 20) + (size_t)(((c.jq * 2 + H) * 16 + c.pq) * 8192 + qt * 2048);
        kvb = c.ws + WS_KV + (size_t)(l >> 1) * (16u << 20) + (size_t)(c.jq * 32768);
        asm volatile("" : "+s"(ep), "+s"(pa_), "+s"(ob), "+s"(kvb));
        asm volatile("" : "+v"(cnta), "+v"(one));
    };
    auto setup2 = [&]() {
        const int p1 = c.pq >= 1 ? c.pq - 1 : c.pq, p2 = c.pq >= 2 ? c.pq - 2 : c.pq;
        fo = (unsigned)(WS_KBOX + (c.pq * 16 + c.jq) * 256 + lane * 8);
        s1_ = kvb + (size_t)p1 * 524288;
        s2_ = kvb + (size_t)p2 * 524288;
        vk = (unsigned)((w & 3) * 1024 + lane * 16); dst = (unsigned)(AT_N + (w & 3) * 1024);
        asm volatile("" : "+s"(s1_), "+s"(dst));
        if (!HI) { asm volatile("" : "+s"(s2_)); asm volatile("" : "+v"(fo)); }
        asm volatile("" : "+v"(vk));
    };
    auto arrive = [&](auto cc, unsigned msk, unsigned val, unsigned off, u64 act) {
        unsigned old = 0;
        if (lane == 0) asm volatile("ds_add_rtn_u32 %0, %1, %2 offset:%3\n\ts_waitcnt lgkmcnt(0)" : "+v"(old) : "v"(cnta), "v"(one), "i"(decltype(cc)::value * 4) : "memory");
        if ((__builtin_amdgcn_readfirstlane(old) & msk) == msk) { if (__builtin_amdgcn_inverse_ballot_w64(act)) post(c.ws, off, ep + (4 * l + val)); }
    };
    static_assert((MBOX & 2) && !HO && !HK, "layout");
    int tv = 0;
    auto kvs = [&]() {
        if (!HI) {
            SCHED();
            setup2();
            SCHED();
            LSTAMP(2);
            WAIT_V0();
            arrive(std::integral_constant<int, 0>{}, 3u, 2u, po, pa_);
            if (w == 0) STAMP_AT(l * 8 + 2);
            LSTAMP(3);
            SCHED();
        }
    };
    if (HI) { masks(); setup(); setup2(); }
    LSTAMP(0);
    if (HI) BAR();
    FENCE();
    SCHED();
    LSTAMP(1);
    u32x4 q0, q1, k0, k1, va, vp, pa, pb;
    f32x16 cm, s0, s1, o;
    f32x4 ls;
    auto stage = [&](f32x16& sc, f32x16& sn, const f32x16& cn, bool pend, bool zero, unsigned kb, int ko, unsigned vb, int vo1, unsigned nb, int vo0) {
        auto ex = [&](int i) { float x = __builtin_amdgcn_exp2f(sc[i]); asm volatile("" : "+v"(x)); sc[i] = x; SCHED(); };
        auto cv = [&](u32x4& p, int j, int i) { unsigned x = cvt_pk_bf16(sc[i], sc[i + 1]); asm volatile("" : "+v"(x)); p[j] = x; SCHED(); };
        sn = MFMA32(k0, q0, cn); SCHED();
        k0 = rd(kb, ko); SCHED();
        ex(0); ex(1); ex(2);
        if (pend) { o = MFMA32(vp, pb, o); SCHED(); }
        ex(3); ex(8); ex(9);
        sn = MFMA32(k1, q1, sn); SCHED();
        k1 = rd(kb, ko + 512); SCHED();
        ex(10); ex(11); cv(pa, 0, 0); cv(pa, 1, 2);
        if (pend) { ls = MFMA(__builtin_bit_cast(bf16x8, sel), __builtin_bit_cast(bf16x8, pb), ls); SCHED(); }
        vp = rd(vb, 8192 + vo1 + 512); SCHED();
        cv(pa, 2, 8); cv(pa, 3, 10); ex(4); ex(5);
        o = MFMA32(va, pa, zero ? (f32x16)(0.f) : o); SCHED();
        va = rd(nb, 8192 + vo0); SCHED();
        ex(6); ex(7); ex(12);
        ls = MFMA(__builtin_bit_cast(bf16x8, sel), __builtin_bit_cast(bf16x8, pa), zero ? (f32x4)(0.f) : ls); SCHED();
        ex(13); ex(14); ex(15); cv(pb, 0, 4); cv(pb, 1, 6); cv(pb, 2, 12); cv(pb, 3, 14);
    };
    auto flush = [&]() {
        o = MFMA32(vp, pb, o); SCHED();
        ls = MFMA(__builtin_bit_cast(bf16x8, sel), __builtin_bit_cast(bf16x8, pb), ls); SCHED();
    };
    float nm;
    {
        q0 = rd(qa, 0); q1 = rd(qa, 512);
        const u32x4 d0 = rd(ta, 6144), d1 = rd(ta, 6144 + 512);
        k0 = rd(ta, 4096); k1 = rd(ta, 4096 + 512); va = rd(ta, 8192 + 6144);
        SCHED();
        if (!HI) { masks(); SCHED(); }
        s0 = MFMA32(d0, q0, ce);
        s0 = MFMA32(d1, q1, s0);
        SCHED();
        if (!HI) { setup(); SCHED(); }
        if (KSP == 0) kvs();
        nm = -max16(s0);
        asm volatile("" : "+v"(nm));
#pragma unroll
        for (int r = 0; r < 16; ++r) { cm[r] = nm; s0[r] = addf(s0[r], nm); }
        asm volatile("" : "+v"(cm), "+v"(s0));
        SCHED();
        if (KSP == 1) kvs();
        stage(s0, s1, cm, false, true, ta, 2048, ta, 6144, ta, 4096);
        if (KSP == 2) kvs();
        if (qt > 0) {
            stage(s1, s0, cm, true, false, ta, 0, ta, 4096, ta, 2048);
            if (KSP == 3) kvs();
            if (qt > 1) {
                stage(s0, s1, cm, true, false, ta, 0, ta, 2048, ta, 0);
                if (qt > 2) stage(s1, s0, cm, true, false, ta, 0, ta, 0, ta, 0);
            }
        } else if (KSP == 3) kvs();
        flush();
    }
    LSTAMP(4);
    f32x16 c2;
    unsigned na, nb;
    auto starts = [&]() {
        const float n1 = c.pq >= 1 ? nm : NEG, n2 = c.pq >= 2 ? nm : NEG;
        asm volatile("" : "+v"(zq));
#pragma unroll
        for (int r = 0; r < 16; ++r) { cm[r] = n1; c2[r] = n2; ce[r] = 8 * (r >> 2) + (r & 3) <= zq ? NEG : n2; }
        na = (unsigned)(AT_N + H * 32768) + lb; nb = na + (unsigned)(16384 + qt * 2048);
        asm volatile("" : "+v"(cm), "+v"(c2), "+v"(ce), "+v"(na), "+v"(nb));
        SCHED();
    };
    {
        auto piece = [&](int b, int i) { dma16<true>(dst + b * 16384 + i * 4096, vk, ((b & 1) ? s2_ : s1_) + (b >> 1) * 16384 + i * 4096); };
        auto block = [&](int b) {
#pragma unroll
            for (int i = 0; i < 4; ++i) piece(b, i);
        };
        Ask t;
        if (!HI) {
            poll_rmw(t, c.ws, fo, w == 3 && lane < 2 && lane < c.pq, ep + (4 * l + 2), 62 - 4 * l);
            if (w == 3) STAMP_AT(32 + l * 8 + 1);
        }
        WAIT_L0();
        if (HI) WAIT_V0();
        LSTAMP(5);
        BAR();
        FENCE();
        LSTAMP(6);
        piece(0, H); piece(0, 2 + H);
        if (HI) {
            starts();
            if (APRIO) __builtin_amdgcn_s_setprio(APRIO);
            WAIT_V0();
            LSTAMP(7);
            BAR();
            FENCE();
            if (w == 4) STAMP_AT(l * 8 + 3);
        } else {
            if (AB2 >= 0) block(1);
            if (AB2 > 0) block(2);
            starts();
            unsigned vw = wo_w_off(c);
            asm volatile("" : "+v"(vw));
            unsigned ro = pick(l != 0, (unsigned)((c.pw * 16 + c.jw) * 16384 + w * 4096 + (lane & 15) * 16 + ((lane >> 4) & 1) * 512 + (lane >> 5) * 256),
                               (unsigned)(xrow<0>(c.pw, w * 32 + (lane & 15)) + xcol<0>(c.jw) + ((lane >> 4) & 1) * 32 + (lane >> 5) * 16));
            const char* xp[4];
#pragma unroll
            for (int i = 0; i < 4; ++i) { xp[i] = xin + (i >> 1) * (l ? 2048 : xrow<0>(0, 16)) + (i & 1) * (l ? 1024 : 64); asm volatile("" : "+s"(xp[i])); }
            asm volatile("" : "+v"(ro));
            SCHED();
            wait_v<4 * AB2 + 4>();
            keep(t);
            LSTAMP(7);
            BAR();
            if (AB2 < 0) block(1);
            if (AB2 <= 0) block(2);
            block(3);
            wait_v<8>();
            BAR();
            wo_w_issue<18, 22>(c, Wo, vw);
#pragma unroll
            for (int i = 0; i < 4; ++i) aload4c(res[i >> 1][i & 1], ro, xp[i]);
            wait_v<8>();
            LSTAMP(9);
            BAR();
            FENCE();
            wo_w_issue<22, 26>(c, Wo, vw);
            if (LSL) { for (int i = 0; i < qt; ++i) __builtin_amdgcn_s_sleep(LSL); }
        }
    }
    SCHED();
    LSTAMP(8);
    {
        const u32x4 d0 = rd(na, 0), d1 = rd(na, 512);
        k0 = rd(na, 2048); k1 = rd(na, 2048 + 512); va = rd(na, 8192);
        SCHED();
        s0 = MFMA32(d0, q0, cm);
        s0 = MFMA32(d1, q1, s0);
        SCHED();
        stage(s0, s1, cm, false, false, na, 4096, na, 0, na, 2048);
        stage(s1, s0, cm, true, false, na, 6144, na, 2048, na, 4096);
        if (HI) { BAR(); FENCE(); SCHED(); }
        stage(s0, s1, cm, true, false, nb, 0, na, 4096, na, 6144);
        stage(s1, s0, ce, true, false, nb, 2048, na, 6144, nb, 0);
        if (HI && CPOS == 4) { WAIT_L0(); BAR(); SCHED(); }
        stage(s0, s1, c2, true, false, nb, 4096, nb, 0, nb, 2048);
        if (HI && CPOS == 5) { BAR(); SCHED(); }
        if (qt < 3) {
            stage(s1, s0, c2, true, false, nb, 6144, nb, 2048, nb, 4096);
            if (qt < 2) {
                stage(s0, s1, c2, true, false, nb, 0, nb, 4096, nb, 6144);
                if (qt < 1) stage(s1, s0, c2, true, false, nb, 0, nb, 6144, nb, 0);
            }
        }
        flush();
    }
    LSTAMP(11);
    if (__builtin_expect(__any(!(ls[0] < SAFE_SUM)), 0)) {
        auto rk = __builtin_amdgcn_make_buffer_rsrc((void*)(kvb + H * 16384), 0, 16u << 20, 0x00020000);
        const int g0 = c.pq * 4 + qt - 8;
        float m = NEG;
#pragma unroll 1
        for (int ps = 0; ps < 2; ++ps) {
            o = (f32x16)(0.f); ls = (f32x4)(0.f);
#pragma unroll 1
            for (int j = g0 < 0 ? -g0 : 0; j < 9; ++j) {
                const int so = ((g0 + j) >> 2) * 524288 + ((g0 + j) & 3) * 2048;
                u32x4 f[4];
#pragma unroll
                for (int i = 0; i < 4; ++i) f[i] = __builtin_amdgcn_raw_buffer_load_b128(rk, lb + (i & 1) * 512, so + (i >> 1) * 8192, 16);
                f32x16 s;
                const float sb = ps ? -m : 0.f;
                float sa = j == 0 ? NEG : sb, sz = j == 8 ? NEG : sb;
                asm volatile("" : "+v"(zq), "+v"(sa), "+v"(sz));
#pragma unroll
                for (int r = 0; r < 16; ++r) s[r] = 8 * (r >> 2) + (r & 3) <= zq ? sa : sz;
                s = MFMA32(f[0], q0, s);
                s = MFMA32(f[1], q1, s);
                if (ps == 0) m = fmaxf(m, max16(s));
                else {
                    u32x4 p[2];
#pragma unroll
                    for (int r = 0; r < 16; r += 2) p[(r >> 2) & 1][(r >> 3) * 2 + ((r >> 1) & 1)] = cvt_pk_bf16(__builtin_amdgcn_exp2f(s[r]), __builtin_amdgcn_exp2f(s[r + 1]));
#pragma unroll
                    for (int u = 0; u < 2; ++u) {
                        o = MFMA32(f[2 + u], p[u], o);
                        ls = MFMA(__builtin_bit_cast(bf16x8, sel), __builtin_bit_cast(bf16x8, p[u]), ls);
                    }
                }
            }
        }
    }
    {
        auto rs = __builtin_amdgcn_make_buffer_rsrc(ob, 0, 2048, 0x00020000);
        const float iv = __builtin_amdgcn_rcpf(ls[0]);
#pragma unroll
        for (int u = 0; u < 2; ++u) {
            u32x4 t;
#pragma unroll
            for (int i = 0; i < 4; ++i) { const int r = 4 * u + (i >> 1) * 8 + (i & 1) * 2; t[i] = cvt_pk_bf16(o[r] * iv, o[r + 1] * iv); }
            __builtin_amdgcn_raw_buffer_store_b128(t, rs, lb + u * 512, 0, 16);
        }
    }
    if (!HI) {
        wait_v<2>();
        asm volatile("" : "+v"(res[0][0]), "+v"(res[0][1]), "+v"(res[1][0]), "+v"(res[1][1]));
    }
    SCHED();
    if (w == 3 || w == 4) STAMP_AT(32 + l * 8 + 3 + w);
    LSTAMP(12);
    if (HI) {
        WAIT_V0();
        arrive(std::integral_constant<int, 1>{}, 3u, 3u, po, pa_);
        if (w == 4) STAMP_AT(32 + l * 8 + 4);
        LSTAMP(13);
        if (APRIO) __builtin_amdgcn_s_setprio(0);
        if (lane == 0) asm volatile("ds_add_u32 %0, %1 offset:12" :: "v"(cnta), "v"(one) : "memory");
        WAIT_V0();
        arrive(std::integral_constant<int, 2>{}, 7u, 4u, po, pa_);
        if (w == 5 || w == 6) {
            Ctx c2 = c;
            c2.wid = 2 * w - 10;
            unsigned vw = wo_w_off(c2), vw2 = vw + (c.cw == 3 ? 1024u : 8u * D_ * 2);
            asm volatile("" : "+v"(vw), "+v"(vw2));
#pragma unroll 1
            for (unsigned spins = 0; spins < (1u << 22); ++spins) {
                if (__hip_atomic_load((LAS unsigned*)(smem + M_CNT + 12), __ATOMIC_RELAXED, __HIP_MEMORY_SCOPE_WORKGROUP) == (unsigned)(2 * l + 4)) break;
                __builtin_amdgcn_s_sleep(1);
            }
            FENCE();
            wo_w_issue<26, 30>(c2, Wo, vw);
            c2.wid += 1;
            wo_w_issue<26, 30>(c2, Wo, vw2);
        }
    }
    LSTAMP(23);
    ASTORE();
}

DI void wo_load(const Ctx& c0, int l, const char* Wn, const char* Wo) {
    const Ctx c = fresh(c0);
    asm volatile("" : "+s"(Wn), "+s"(Wo));
    const u64 ep = lds_epoch(c.smem);
    const unsigned fo = o_find(c);
    if (CPY && l == 0 && c.wid == CKW) {
#pragma unroll 1
        for (int i = 0; i < 2; ++i) {
            const int j = i ? c.jw : c.jq, g = i ? 2 - 2 * (j >> 3) : 1 + 2 * (j >> 3);
            if ((j & 7) == 3) poll_flags(c.ws, ep, (unsigned)WS_FLAGQ + ((c.lane >> 2) * 16 + 4 * g + (c.lane & 3)) * FQS, true, 2u);
        }
    }
    Ask t;
    if (!(l & 1)) {
        unsigned vw = wo_w_off(c);
        asm volatile("" : "+v"(vw));
        poll_rmw(t, c.ws, fo, c.wid == 7 && c.lane < 16, ep + (4 * l + 3), 61 - 4 * l);
        if (c.wid == 7) STAMP_AT(32 + l * 8 + 2);
        BAR();
        __builtin_amdgcn_s_sleep(W30S);
        wo_w_issue<30, 32>(c, Wo, vw);
        WAIT_V0();
        keep(t);
    }
    WAIT_V0();
    poll_rmw(t, c.ws, fo, c.wid == 4 && c.lane < 16, ep + (4 * l + 4), 60 - 4 * l);
    if (c.wid == 4) STAMP_AT(32 + l * 8 + 2 + !(l & 1));
    BAR();
    WAIT_V0();
    keep(t);
    if (l == 3 && c.bid == 0 && c.wid == 7) {
#pragma unroll 1
        for (int i = 0; i < 4; ++i) poll_flags(c.ws, ep, (unsigned)WS_FLAGQ + (i * 64 + c.lane) * FQS, true, 1u);
        if (c.lane == 0) __hip_atomic_store((GAS u64*)(c.ws + WS_EPOCH), ep + 64, __ATOMIC_RELAXED, __HIP_MEMORY_SCOPE_AGENT);
    }
    if (RS0 && l == 3) BAR();
}

DI void wo_consume(const bool B, const Ctx& c0, const bf16_t* xin, bf16_t* xout, int l, u32x4 (&res)[2][2]) {
    const Ctx c = fresh(c0);
    LAS char* smem = c.smem;
    const int lane = c.lane, w = c.wid, fr = lane & 15, fq = lane >> 4;
    const char* obase = c.ws + WS_O + (size_t)l * (8u << 20);
    int ovo = (c.pw * 8 + w * 2) * 1024 + lane * 16;
    asm volatile("" : "+v"(ovo));
    constexpr int NP = WNQ, R = 3;
    static_assert(!HO && !HX && !HS && !RLD && BPR == 1, "layout");
    auto ro = __builtin_amdgcn_make_buffer_rsrc((void*)obase, 0, 4u << 20, 0x00020000);
    asm volatile("" : "+s"(ro));
    u32x4 xa[NP][2];
    s16x4 wf[R][8];
    f32x4 acc[2][4];
    unsigned bw[8];
    int coff, spr;
    u32x4 on = {0x3f803f80u, 0x3f803f80u, 0x3f803f80u, 0x3f803f80u};
    unsigned sto, xpo, coff1, cnta = M_CNT, one = 1;
    u64 xv, b0, b1, am;
    auto rs = __builtin_amdgcn_make_buffer_rsrc(xout, 0, S_ * D_ * 2, 0x00020000);
    auto rst = __builtin_amdgcn_make_buffer_rsrc(c.ws + WS_STATS + (size_t)((l + 1) & 3) * 524288, 0, 524288, 0x00020000);
    auto prep2 = [&]() {
        sto = fq == (fr >> 2) && l < 3 ? (unsigned)((c.pw * 16 + c.jw) * 1024 + (w * 32 + fr) * 8) : 0x80000000u;
        xpo = x_post(c);
        if (l == 3) coff = xrow<0>(c.pw, w * 32 + fr) + xcol<0>(c.jw) + (fq & 1) * 32 + (fq >> 1) * 16;
        coff1 = coff + (l == 3 ? xrow<0>(0, 16) : 2048);
        spr = l == 3 ? 64 : 1024;
        asm volatile("" : "+s"(spr));
        xv = lds_epoch(smem) + (unsigned)(l + 2);
        b0 = __builtin_amdgcn_ballot_w64(fr & 1); b1 = __builtin_amdgcn_ballot_w64(fr & 2); am = __builtin_amdgcn_ballot_w64(x_act(c, !B));
        asm volatile("" : "+v"(on), "+v"(sto), "+v"(xpo), "+v"(coff), "+v"(coff1), "+v"(xv), "+v"(cnta), "+v"(one));
        asm volatile("" : "+s"(rs), "+s"(rst), "+s"(b0), "+s"(b1), "+s"(am));
    };
    auto prep = [&]() {
        int ln = lane;
        asm volatile("" : "+v"(ln));
        const int fr = ln & 15, fq = ln >> 4, q4 = fr >> 2, p4 = ln & 3;
        coff = (c.pw * 16 + c.jw) * 16384 + w * 4096 + (((fq & 1) * 2 + (fq >> 1)) * 16 + fr) * 16;
        asm volatile("" : "+v"(coff));
        const int fb = (q4 & 2) | ((fq & 1) << 2);
        const unsigned swp = (B && w >= 2) ? 2048u : 0u;
#pragma unroll
        for (int cc = 0; cc < 4; ++cc) {
            const unsigned b = (unsigned)((4 * fq + q4) * 128 + (((2 * cc + (p4 >> 1)) ^ fb) << 4) + (p4 & 1) * 8);
            bw[2 * cc] = b + swp; bw[2 * cc + 1] = b + (swp ^ 2048u);
            asm volatile("" : "+v"(bw[2 * cc]), "+v"(bw[2 * cc + 1]));
        }
    };
    auto start = [&]() {
#pragma unroll
        for (int mt = 0; mt < 2; ++mt)
#pragma unroll
            for (int pr = 0; pr < 2; ++pr) {
                auto r0 = __builtin_amdgcn_permlane16_swap(res[mt][pr][0], res[mt][pr][2], false, false);
                auto r1 = __builtin_amdgcn_permlane16_swap(res[mt][pr][1], res[mt][pr][3], false, false);
#pragma unroll
                for (int h = 0; h < 2; ++h) {
                    acc[mt][2 * pr + h] = (f32x4){bf_lo(r0[h]), bf_hi(r0[h]), bf_lo(r1[h]), bf_hi(r1[h])};
                    asm volatile("" : "+v"(acc[mt][2 * pr + h]));
                }
            }
    };
    auto rdw = [&](auto ic) {
        constexpr int i = decltype(ic)::value, h = i >> 3, k = i & 7;
        wf[h % R][k] = tr_read(lds(bw[k]) + (h & 15) * 4096);
        if constexpr (h == 15) { bw[k] += 65536; asm volatile("" : "+v"(bw[k])); }
    };
    auto first = [&]() {
        static_for<0, 16>([&](auto ic) { rdw(ic); SCHED(); });
        start();
        SCHED();
    };
    {
        prep();
        if (B) {
            auto rx = __builtin_amdgcn_make_buffer_rsrc((void*)xin, 0, S_ * D_ * 2, 0x00020000);
#pragma unroll
            for (int mt = 0; mt < 2; ++mt)
#pragma unroll
                for (int pr = 0; pr < 2; ++pr) res[mt][pr] = __builtin_amdgcn_raw_buffer_load_b128(rx, coff + mt * 2048 + pr * 1024, 0, 16);
            SCHED();
            asm volatile("" : "+v"(res[0][0]), "+v"(res[0][1]), "+v"(res[1][0]), "+v"(res[1][1]));
        }
        SCHED();
        prep2();
        SCHED();
        first();
        if (!B && !DEF) {
            publish<8, 2, 0>(smem, lane, c.ws, o_post(c), o_act(c), (unsigned)(4 * l + 4));
            if (w == 0) STAMP_AT(l * 8 + 4);
            SCHED();
        }
        if (w == 3) STAMP_AT(32 + l * 8 + 5);
        BAR();
    }
    FENCE();
    SCHED();
    if (w == 0) STAMP_AT(l * 8 + 5);
    int so = 0;
    auto next = [&](int j) { so += j == 15 ? 131072 - 15 * 262144 : 262144; asm volatile("" : "+s"(so)); };
    auto ldo = [&](int j, int mt) { xa[j % NP][mt] = __builtin_amdgcn_raw_buffer_load_b128(ro, ovo + mt * 1024, so, 16); };
#pragma unroll
    for (int h = 0; h < NP; ++h) {
        ldo(h, 0); ldo(h, 1);
        if (!WAL || h + 1 < NP) next(h);
        SCHED();
        if (DEF && h == WPJ) {
            if (!B) {
                publish<8, 2, 2 * WPJ + 2>(smem, lane, c.ws, o_post(c), o_act(c), (unsigned)(4 * l + 4));
                if (w == 0) STAMP_AT(l * 8 + 4);
            }
            asm volatile("" : "+v"(ovo));
            SCHED();
        }
    }
    int d0 = 262144, d1 = 131072 - 15 * 262144;
    asm volatile("" : "+s"(d0), "+s"(d1));
    static_assert(WEP || !WAL, "last step");
    static_for<0, WEP ? 31 : 32>([&](auto jc) {
        constexpr int j = decltype(jc)::value;
        constexpr int c0 = j < 30 ? 16 + 8 * j : 256, c1 = j < 30 ? c0 + 8 : 256;
        if constexpr (j + NP == 16) {
            static_assert(c0 <= 8 * 20, "weights that landed before the attention");
            if (!B) BAR();
            FENCE();
            asm volatile("" : "+v"(ovo));
            SCHED();
        }
        if constexpr (j == 0 || j + NP == 16) ALIGN8();
        constexpr int nv = WAL == 1 || j == 0 || j + NP > 32 ? 2 * (NP - 1 < 31 - j ? NP - 1 : 31 - j) : 2 * NP - 3;
        __builtin_amdgcn_s_waitcnt(0x0870 | nv);
        const int sp = so;
        static_assert(!RS0 || (RS0 + NP >= 32 && RS0 < 31 && WPRE * QW_SLOT <= (RS0 - 1) * 4096), "R0");
        if constexpr (RS0 && j == RS0) BAR();
        else asm volatile("s_add_i32 %0, %1, %2" : "=&s"(so) : "s"(sp), "s"(j + NP == 16 ? d1 : d0) : "scc");
        SCHED();
        static_for<0, 8>([&](auto mc) {
            constexpr int m = decltype(mc)::value, mt = m >> 2, nt = mt ? 3 - (m & 3) : m & 3;
            const bf16x8 wv = __builtin_shufflevector(wf[j % R][2 * nt], wf[j % R][2 * nt + 1], 0, 1, 2, 3, 4, 5, 6, 7);
            if constexpr (j < 31) asm volatile("v_mfma_f32_16x16x32_bf16 %0, %1, %2, %0" : "+v"(acc[mt][nt]) : "v"(wv), "v"(xa[j % NP][mt]));
            else acc[mt][nt] = MFMA(wv, __builtin_bit_cast(bf16x8, xa[j % NP][mt]), acc[mt][nt]);
            SCHED();
            if constexpr (c0 < c1 && m < 7) { rdw(std::integral_constant<int, c0 + m>{}); SCHED(); }
            if constexpr (c0 < c1 && m == 6) rdw(std::integral_constant<int, c0 + 7>{});
            if constexpr (m == WL1 && j > 0 && j - 1 + NP < 32) xa[(j + NP - 1) % NP][1] = __builtin_amdgcn_raw_buffer_load_b128(ro, ovo + 1024, sp, 16);
            if constexpr (m == WL1 + 4 && j + NP < 32) ldo(j, 0);
            SCHED();
        });
    });
    const bf16x8 ones = __builtin_bit_cast(bf16x8, on);
    u32x4 o[2][2];
    f32x4 sm[2], g[2];
    const f32x4 zero = {0.f, 0.f, 0.f, 0.f};
    auto sel = [&](float a, float b, u64 m) { return __builtin_amdgcn_inverse_ballot_w64(m) ? b : a; };
    auto stats = [&](int mt) {
        float qa = sel(g[mt][0], g[mt][1], b0), qb = sel(g[mt][2], g[mt][3], b0);
        asm volatile("" : "+v"(qa), "+v"(qb));
        SCHED();
        if (WEP) asm volatile("" :: "v"(sm[1][1]), "v"(sm[1][2]), "v"(sm[1][3]), "v"(o[1][0]), "v"(o[1][1]), "v"(on));
        __builtin_amdgcn_raw_buffer_store_b64((u32x2){__float_as_uint(sm[mt][0]), __float_as_uint(sel(qa, qb, b1))}, rst, sto + mt * 128, 0, 16);
        SCHED();
    };
    u32x2 pk[2];
    auto last = [&](int mt, int nt) {
        acc[mt][nt] = MFMA(__builtin_shufflevector(wf[31 % R][2 * nt], wf[31 % R][2 * nt + 1], 0, 1, 2, 3, 4, 5, 6, 7), __builtin_bit_cast(bf16x8, xa[31 % NP][mt]), acc[mt][nt]);
        SCHED();
    };
    auto cv = [&](int mt, int pr) {
        pk[0] = pack4(acc[mt][2 * pr]); pk[1] = pack4(acc[mt][2 * pr + 1]);
        asm volatile("" : "+v"(pk[0]), "+v"(pk[1]));
        SCHED();
    };
    auto st = [&](int mt, int pr) {
        o[mt][pr] = widen(pk[0], pk[1]);
        __builtin_amdgcn_raw_buffer_store_b128(o[mt][pr], rs, mt ? coff1 : coff, pr ? spr : 0, 16);
        SCHED();
    };
    auto gq = [&](int mt, int pr, bool f) { const bf16x8 of = __builtin_bit_cast(bf16x8, o[mt][pr]); g[mt] = MFMA(of, of, f ? zero : g[mt]); SCHED(); };
    auto gs = [&](int mt, int pr, bool f) { sm[mt] = MFMA(ones, __builtin_bit_cast(bf16x8, o[mt][pr]), f ? zero : sm[mt]); SCHED(); };
    WAIT_L0();
    last(0, 0); last(0, 1); last(0, 2); last(0, 3);
    last(1, 3); cv(0, 0);
    last(1, 2); st(0, 0);
    last(1, 1); cv(0, 1);
    last(1, 0); st(0, 1);
    if (w == 0) STAMP_AT(l * 8 + 6);
    gq(0, 0, true); cv(1, 1);
    gs(0, 0, true); st(1, 1);
    gq(0, 1, false); cv(1, 0);
    gs(0, 1, false); st(1, 0);
    gq(1, 1, true);
    gs(1, 1, true);
    gq(1, 0, false);
    gs(1, 0, false);
    stats(0);
    stats(1);
    if (l < 3) {
        unsigned old = 0;
        if (!RS0 || RN1 > RN0) BAR();
        wait_v<XE2>();
        if (lane == 0) asm volatile("ds_add_rtn_u32 %0, %1, %2\n\ts_waitcnt lgkmcnt(0)" : "+v"(old) : "v"(cnta), "v"(one) : "memory");
        if ((__builtin_amdgcn_readfirstlane(old) & 3) == 3) {
            if (__builtin_amdgcn_inverse_ballot_w64(am)) post(c.ws, xpo, xv);
        }
    }
    if (w == 0) STAMP_AT(l * 8 + 7);
}

__global__ __launch_bounds__(512, 2) void k_main(Params P) {
    extern __shared__ __attribute__((aligned(16))) char smem_[];
    KP kp = (KP)__builtin_amdgcn_kernarg_segment_ptr();
    const char *x0, *w0;
    Ctx c;
    asm volatile("s_load_dwordx2 %0, %3, 0x0\n\ts_load_dwordx2 %1, %3, 0x8\n\ts_load_dwordx2 %2, %3, 0x10" : "=&s"(x0), "=&s"(w0), "=&s"(c.ws) : "s"(kp));
    c.smem = (LAS char*)smem_;
    c.lane = threadIdx.x & 63;
    c.wid = __builtin_amdgcn_readfirstlane(threadIdx.x >> 6);
    c.bid = blockIdx.x;
    const int xcd = c.bid & 7, idx = c.bid >> 3;
    c.pq = ((xcd % (16 / QXM)) * QXM + (idx % QXM)) ^ PQX; c.jq = ((xcd / (16 / QXM)) * (32 / QXM) + (idx / QXM)) ^ JQX;
    const bool loader = c.wid >= 4;
    if (!loader) {
        const char* pc; int len;
        asm volatile("s_getpc_b64 %0\n.Lcpf_a:\n\ts_mov_b32 %1, .Lcpf_end-.Lcpf_a-4" : "=s"(pc), "=s"(len));
#pragma unroll
        for (int i = 0; i < 2; ++i) {
            const int o = min(((i * 4 + c.wid) * 64 + c.lane) * 128, len);
            asm volatile("s_mov_b32 m0, %0\n\ts_nop 3\n\tglobal_load_lds_dword %1, %2" ::"s"(M_TCH), "v"(o), "s"(pc) : "memory");
        }
    }
    {
        const int w = c.wid, lr = c.lane >> 3, lc = c.lane & 7, rot = rot_first(c.pq, c.jq);
        unsigned va = (unsigned)((c.pq * 128 + 8 * (w & 3) + lr) * (D_ * 2) + ((lc ^ lr) << 4));
        unsigned vw = (unsigned)((8 * (w & 3) + lr) * 6144 + c.jq * 128 + ((lc ^ ((lr & 2) | ((w & 1) << 2))) << 4));
        asm volatile("" : "+v"(va), "+v"(vw));
        asm volatile("s_waitcnt lgkmcnt(0)" : "+s"(x0), "+s"(w0), "+s"(c.ws));
        auto stage = [&](int s, int h0, int h1, bool a) {
            const char* xb = x0 + ((s + rot) & 15) * 128;
            const char* wb = w0 + (size_t)((s + rot) & 15) * (64 * 6144);
            for (int h = h0; h <= h1; ++h) {
                if (a) {
#pragma unroll
                    for (int j = 0; j < 2; ++j) dma16<false>(QA_OFF + s * QA_SLOT + (j * 8 + h * 4 + (w & 3)) * 1024, va, xb + (j * 64 + h * 32) * (D_ * 2));
                }
#pragma unroll
                for (int j = 0; j < 3; ++j) dma16<false>(qw_slot(s) + (j * 8 + h * 4 + (w & 3)) * 1024, vw, wb + h * (32 * 6144) + j * 2048);
            }
        };
        if (ENT == 0) stage(0, w >> 2, w >> 2, true);
        else if (!loader) stage(0, 0, 1, true);
        if (w == 0 && c.lane < 2) touch(M_EP, (unsigned)(WS_EPOCH + c.lane * 4), c.ws);
        if (w == 1 && c.lane < 5) *(LAS unsigned*)(c.smem + (c.lane < 4 ? M_CNT + c.lane * 4 : M_GO)) = 0u;
        if (w == 2) {
            const int fr = c.lane & 15, od = fr & 1, j0 = 4 * (c.lane >> 4) + od;
            const bool p0 = j0 > fr, p1 = j0 + 2 > fr;
            const unsigned N = 0xff800000u, Z = 0x0c0c0c0cu, lo = od ? 0x01000c0cu : 0x0c0c0100u, hi = od ? 0x03020c0cu : 0x0c0c0302u;
            LAS u32x4* t = (LAS u32x4*)(c.smem + M_TAB + c.lane * 16);
            t[0] = (u32x4){p0 && !od ? 0u : N, p0 && od ? 0u : N, p1 && !od ? 0u : N, p1 && od ? 0u : N};
            t[64] = (u32x4){!p0 && !od ? 0u : N, !p0 && od ? 0u : N, !p1 && !od ? 0u : N, !p1 && od ? 0u : N};
            t[128] = (u32x4){p0 ? lo : Z, p1 ? hi : Z, p0 ? Z : lo, p1 ? Z : hi};
            t[192] = (u32x4){(unsigned)(M_RSTD + j0 * 4 + (p0 ? 0 : 64)), (unsigned)(M_RSTD + j0 * 4 + 8 + (p1 ? 0 : 64)), 0u, 0u};
        }
    }
    asm volatile("" : "+s"(kp), "+s"(c.bid), "+s"(c.ws));
    c.pw = ((c.bid & 7) % (16 / WXM)) * WXM + ((c.bid >> 3) % WXM); c.jw = ((c.bid & 7) / (16 / WXM)) * (32 / WXM) + ((c.bid >> 3) / WXM);
#pragma unroll 1
    for (int l = 0; l < 4; ++l) {
        asm volatile("" : "+s"(kp));
        if (ALT) {
            if (l > 0 && (ALT == 1 || l != ALT)) c.pq ^= 8;
            const int nh = (ALT != 1 && l + 1 == ALT) ? (c.pq & 8) : (c.pq & 8) ^ 8;
            static_assert(WXM == 4 || WXM == 2, "map");
            c.pw = nh + ((c.bid >> 1) & (8 / WXM - 1)) * WXM + ((c.bid >> 3) & (WXM - 1));
        }
        const bf16_t* xi = l == 0 ? kp->x0 : (const bf16_t*)(c.ws + WS_X + (size_t)(l - 1) * (8u << 20));
        bf16_t* xo = l == 3 ? kp->out : (bf16_t*)(c.ws + WS_X + (size_t)l * (8u << 20));
        const char* wo = (const char*)((l & 1) ? kp->wo1 : kp->wo0);
        c.cw = CPY && l > 0 ? c.jw & 7 : 0;
        if (c.cw == 3) wo = c.ws + WS_CO + (((l & 1) ? 0 : 2) + (c.jw >> 3)) * 4096;
        const char* wb = (const char*)kp->wqkv1;
        const char* wo2 = (const char*)kp->wo1;
        u32x4 res[2][2];
        if (loader) {
            if (l == 0) qkv_load<false, true>(c, (const char*)xi, (const char*)kp->wqkv0, wo, l, wb, wo2, (const char*)kp->out);
            else if (l == 2) qkv_load<false, false>(c, (const char*)xi, (const char*)kp->wqkv0, wo, l);
            else qkv_load<true, false>(c, (const char*)xi, wb, wo, l);
            if (!(l & 1)) attn_a<true>(c, l, wo, kp, (const char*)xi, res);
            wo_load(c, l, (const char*)((l & 1) ? kp->wqkv0 : kp->wqkv1), wo);
        } else {
            if (l & 1) {
                qkv_consume<true>(c, l, kp);
                asm volatile("" : "=v"(res[0][0]), "=v"(res[0][1]), "=v"(res[1][0]), "=v"(res[1][1]));
            } else {
                qkv_consume<false>(c, l, kp);
                attn_a<false>(c, l, wo, kp, (const char*)xi, res);
            }
            wo_consume(l & 1, c, xi, xo, l, res);
        }
    }
    asm volatile(".Lcpf_end:");
}

extern "C" void kernel_launch(void* const* d_in, const int* in_sizes, int n_in, void* d_out, int out_size, void* d_ws, size_t ws_size,
                              hipStream_t stream) {
    Params P;
    P.x0 = (const bf16_t*)d_in[0];
    P.wqkv0 = (const bf16_t*)d_in[1];
    P.wo0 = (const bf16_t*)d_in[2];
    P.wqkv1 = (const bf16_t*)d_in[4];
    P.wo1 = (const bf16_t*)d_in[5];
    P.out = (bf16_t*)d_out;
    P.ws = (char*)d_ws;
    (void)hipFuncSetAttribute((const void*)k_main, hipFuncAttributeMaxDynamicSharedMemorySize, LDS_TOTAL);
    k_main<<<256, 512, LDS_TOTAL, stream>>>(P);
}
```

```cpp
#include <hip/hip_runtime.h>
#include <stdint.h>
#include <type_traits>

typedef unsigned short bf16_t;
typedef short bf16x8 __attribute__((ext_vector_type(8)));
typedef short s16x4 __attribute__((ext_vector_type(4)));
typedef float f32x4 __attribute__((ext_vector_type(4)));
typedef float f32x2 __attribute__((ext_vector_type(2)));
typedef float f32x16 __attribute__((ext_vector_type(16)));
typedef unsigned u32x4 __attribute__((ext_vector_type(4)));
typedef unsigned u32x2 __attribute__((ext_vector_type(2)));
#define DI __device__ __forceinline__
#define LAS __attribute__((address_space(3)))
#define GAS __attribute__((address_space(1)))
#define STAMP 0

constexpr int S_ = 2048, D_ = 1024, KT = 16;
constexpr float LN_EPS = 1e-5f;
constexpr float QSCALE = 0.17677669529663687f * 1.4426950408889634f;

#define FQO 0
#define XBO 524288
#define HX 0
#define HO 0
#define HK 0
#define HS 0
#define HM 1
constexpr size_t WS_FLAGQ = FQO;
constexpr size_t WS_FLAGW = 98304;
constexpr size_t WS_EPOCH = 65536;
constexpr size_t WS_CSUM = 131072;
constexpr size_t WS_XBOX = XBO;
constexpr size_t WS_OBOX = 655360;
constexpr size_t WS_KBOX = 720896;
constexpr size_t WS_STATS = 1u << 20;
constexpr size_t WS_X = 4u << 20;
constexpr size_t WS_O = 96u << 20;
constexpr size_t WS_KV = 28u << 20;
constexpr size_t WS_CQ = 36u << 20;
constexpr size_t WS_CO = 42u << 20;
constexpr size_t WS_STAMP = 64u << 20;

template <int H> DI constexpr int hp(int c) { return !H ? c * 1024 : (c >> 2) * 8192 + (HM == 1 ? (c & 3) * 256 : (c & 1) * 128 + (c & 2) * 256); }
template <int H> DI constexpr int hl(int u) { return !H ? u : (HM == 1 ? u + (u & ~127) : u + (u & ~255)); }
template <int H> DI constexpr int hv(int lane) { return !H ? lane * 16 : (lane >> 3) * 1024 + (lane & 7) * 16; }
template <int H> DI constexpr int xrow(int p, int r) { return H == 0 ? (p * 128 + r) * 2048 : (H == 1 ? (p * 128 + r) * 4096 : (H == 2 ? p * 524288 + r * 256 : p * 262144 + r * 128)); }
template <int H> DI constexpr int xcol(int t) { return H == 0 ? t * 128 : (H == 1 ? hl<1>(t * 128) : (H == 2 ? t * 32768 : t * 16384)); }
#define WTC 2
#define WT0 1
#define SPR 1
#define SL 4
#define R4X 0
#define R5X 0
#define TOPJ 0x2000
#define WV0 1
#define C2X 13
#define FQS 256
#define SNK 1
#define WBE 1
#define WAE 1
#define EXF 0
#define RW0 6
#define KSP 0
#define K0E 1
#define TOPS 4
#define BPR 1
#define RLD 0
#define RCK 1
#define CPF 2
#define WN1 20
#define WLO 3
#define FPJ 2
#define WPJ 2
#define WB14 0
#define WB15 5
#define RESP 1
#define WT13 2
#define RPOS 2
#define CPOLL 1
#define ENT 1
#define MBOX 3
#define OBS 256
#define WTCH 0
#define WTDA 0
#define WTDB 4
#define WTG 0
#define PQX 8
#define ALT 1
#define RESA 1
#define CKP 1
#define ALN 1
#define EPH 2
#define NQA_ 3
#define NQW_ 4
#define WNP 8
#define LPRIO 3
#define TOUCH 1
#define TSTEP 13
#define XS_ 9
#define WROT 0
#define WROTD 1
#define JQX 0
#define ROTM 0
#define ROT0 0
#define WPRE 3
#define PSLEEP 1
#define XPRE 2
#define WE0 1
#define EPJ 3
#define TW0 5
#define TW1 99
#define KPJ 1
#define PKSUB 0
#define APRIO 1
#define QIL 0
#define QXM 8
#define WNQ 5
#define WRG 2
#define WMG 1
#define WB2 1
#define WBS 1
#define WEP 1
#define WAL 2
#define WL1 0
#define RS0 27
#define RN0 6
#define RN1 6
#define AH0 0
#define RSL 1
#define WXM 4
#define CPOS 5
#define LSL 0
#define SAFE_SUM 0x1p60f
#define AB2 0
#define W30S 8
#define STC 0
#define NOR 1
#define NRH 0
#define CPY 1
#define AGG 1
#define HT0 0
#define DEF 0
#define XST 2
#define CKW 4
#define XSP 1
#define EPOLL 1
#define ORD 2
#define RSLP 1
#define APOLL 1
#define APOST 1
#define ASLP 2
#define APIPE 4
#define RBP 0
#define P2A 1
#define SH0 2
#define SL1 0
#define KPW 4
#define LWA 7
#define WA0 12
#define WA1 22
#define KPP 1
#define HP4 1
#define TWQ 1
#define CP2 0
#define PADN -1
#define ERL 6
#define ESL 0
#define DM 0
#define DS 0
#define ELO 0
#define EN0 3
#define EN1 2
#define ST0 0
#define NC 2
#define NCM 1
#define NCB 4
#define AD1 3
#define AD2 1
#define AD3 4
#define WB0 3
#define WBN 6
#define XE1 0
#define XE2 0
#define OTB 0
#define OTJ 8
#define OTA 0
constexpr int QA_SLOT = 18432, NQA = NQA_, QW_SLOT = 24576, NQW = NQW_, QA_OFF = 3 * QW_SLOT, QW_TOP = 131072;
DI constexpr int qw_slot(int s) { return s == 3 ? QW_TOP : s * QW_SLOT; }
constexpr int AT_N = 90112, AT_OWN = AT_N + 32768, AT_Q = 73728;
constexpr int MISC = 155648, M_MEAN = MISC, M_RSTD = MISC + 640, M_CS = MISC + 1280, M_CNT = MISC + 2816, M_EP = MISC + 2832, M_GO = MISC + 2848, M_TCH = MISC + 3072, M_TAB = MISC + 4096, M_XCH = 131072;
constexpr int LDS_TOTAL = 163840;
static_assert(QA_OFF + NQA * QA_SLOT <= QW_TOP && QW_TOP + QW_SLOT <= MISC && AT_N + 65536 <= MISC && AT_Q + 16384 <= AT_N, "lds");

DI unsigned cvt_pk_bf16(float lo, float hi) {
    typedef __bf16 bf2 __attribute__((ext_vector_type(2)));
    bf2 r; r[0] = (__bf16)lo; r[1] = (__bf16)hi;
    return __builtin_bit_cast(unsigned, r);
}
DI u32x2 pack4(const f32x4& v) { return (u32x2){cvt_pk_bf16(v[0], v[1]), cvt_pk_bf16(v[2], v[3])}; }
DI bf16x8 join(const u32x2& a, const u32x2& b) { return __builtin_bit_cast(bf16x8, (u32x4){a[0], a[1], b[0], b[1]}); }
DI float bf_lo(unsigned u) { return __uint_as_float(u << 16); }
DI float bf_hi(unsigned u) { return __uint_as_float(u & 0xffff0000u); }
DI float red_max4(float x) {
    auto a = __builtin_amdgcn_permlane16_swap(__float_as_uint(x), __float_as_uint(x), false, false);
    x = fmaxf(__uint_as_float(a[0]), __uint_as_float(a[1]));
    auto b = __builtin_amdgcn_permlane32_swap(__float_as_uint(x), __float_as_uint(x), false, false);
    return fmaxf(__uint_as_float(b[0]), __uint_as_float(b[1]));
}
DI float red_sum4(float x) {
    auto a = __builtin_amdgcn_permlane16_swap(__float_as_uint(x), __float_as_uint(x), false, false);
    x = __uint_as_float(a[0]) + __uint_as_float(a[1]);
    auto b = __builtin_amdgcn_permlane32_swap(__float_as_uint(x), __float_as_uint(x), false, false);
    return __uint_as_float(b[0]) + __uint_as_float(b[1]);
}
DI u32x4 widen(const u32x2& a, const u32x2& b) {
    auto r0 = __builtin_amdgcn_permlane16_swap(a[0], b[0], false, false);
    auto r1 = __builtin_amdgcn_permlane16_swap(a[1], b[1], false, false);
    return (u32x4){r0[0], r1[0], r0[1], r1[1]};
}
#define MFMA(a, b, c) __builtin_amdgcn_mfma_f32_16x16x32_bf16((a), (b), (c), 0, 0, 0)
#define WAIT_L0() __builtin_amdgcn_s_waitcnt(0xC07F)
#define WAIT_V0() asm volatile("s_waitcnt vmcnt(0)" ::: "memory")
#define BAR() __builtin_amdgcn_s_barrier()
#define PADS "0"
#define PADPFX ".Lc"
#define PADC() asm volatile("PADC %=" ::)
#define MARK(n) asm volatile(PADPFX "m" n ":")
#define CALL(n) "s_call_b64 vcc, " PADPFX #n "\n\t"
#define NOCALL "s_nop 0\n\t"
#define WARM(list) asm volatile("s_mov_b64 exec, 0\n\t" list "s_mov_b64 exec, -1" ::: "vcc")
#define WARM_A5 NOCALL
#define WARM_A6 NOCALL
#define WARM_A7 NOCALL
#define WARM_B0(X) X(NOCALL, NOCALL, NOCALL, NOCALL)
#define WARM_B1(X) X(NOCALL, NOCALL, NOCALL, NOCALL)
#define WARM_B2(X) X(NOCALL, NOCALL, NOCALL, NOCALL)
#define WARM_B3(X) X(NOCALL, NOCALL, NOCALL, NOCALL)
#define WARM_B4(X) X(NOCALL, NOCALL, NOCALL, NOCALL)
#define WARM_B5(X) X(NOCALL, NOCALL, NOCALL, NOCALL)
#define WARM_B6(X) X(NOCALL, NOCALL, NOCALL, NOCALL)
#define WARM_B7(X) X(NOCALL, NOCALL, NOCALL, NOCALL)
#define WARM_C4 NOCALL NOCALL
#define WARM_C5 WARM_C4
#define WARM_C6 WARM_C4
#define WARM_C7 WARM_C4
template <int I> DI void warm_b(int lw) {
#define WB_(a0, a1, a2, a3) \
    asm volatile("s_mov_b64 exec, 0\n\ts_cmp_lg_u32 %0, 0\n\ts_cbranch_scc1 1\n\t" a0 "s_cmp_lg_u32 %0, 1\n\ts_cbranch_scc1 1\n\t" a1 \
                 "s_cmp_lg_u32 %0, 2\n\ts_cbranch_scc1 1\n\t" a2 "s_cmp_lg_u32 %0, 3\n\ts_cbranch_scc1 1\n\t" a3 "s_mov_b64 exec, -1" :: "s"(lw) : "vcc", "scc")
    if constexpr (I == 0) WARM_B0(WB_);
    if constexpr (I == 1) WARM_B1(WB_);
    if constexpr (I == 2) WARM_B2(WB_);
    if constexpr (I == 3) WARM_B3(WB_);
    if constexpr (I == 4) WARM_B4(WB_);
    if constexpr (I == 5) WARM_B5(WB_);
    if constexpr (I == 6) WARM_B6(WB_);
    if constexpr (I == 7) WARM_B7(WB_);
}
#define SCHED0() __builtin_amdgcn_sched_barrier(0)
#define SCHED() do { SCHED0(); PADC(); SCHED0(); } while (0)
template <int N> DI void wait_v() { asm volatile("PADC %=\n\ts_waitcnt vmcnt(%0)" ::"i"(N) : "memory"); }
template <int I, int N, class F> DI void static_for(F&& f) { if constexpr (I < N) { f(std::integral_constant<int, I>{}); static_for<I + 1, N>(f); } }

template <bool SC1> DI void dma16(unsigned lds, unsigned voff, const char* sbase) {
    if (SC1) asm volatile("PADC %=\n\ts_mov_b32 m0, %0\n\ts_nop 3\n\tglobal_load_lds_dwordx4 %1, %2 sc1" ::"s"(lds), "v"(voff), "s"(sbase) : "memory");
    else asm volatile("PADC %=\n\ts_mov_b32 m0, %0\n\ts_nop 3\n\tglobal_load_lds_dwordx4 %1, %2" ::"s"(lds), "v"(voff), "s"(sbase) : "memory");
}
DI void touch(unsigned lds, unsigned voff, const char* sbase) {
    asm volatile("s_mov_b32 m0, %0\n\ts_nop 3\n\tglobal_load_lds_dword %1, %2 sc1" ::"s"(lds), "v"(voff), "s"(sbase) : "memory");
}
DI s16x4 tr_read(const LAS char* p) {
    typedef short v4i16_t __attribute__((ext_vector_type(4)));
    return __builtin_bit_cast(s16x4, __builtin_amdgcn_ds_read_tr16_b64_v4i16((LAS v4i16_t*)p));
}
DI LAS char* lds(unsigned off) { return (LAS char*)(uintptr_t)off; }
DI unsigned long long rstamp() { unsigned long long t; asm volatile("s_memrealtime %0\n\ts_waitcnt lgkmcnt(0)" : "=s"(t)::"memory"); return t; }

template <int NAI, int NWI, bool FIRST> struct Sched {
    int w[KT];
    constexpr Sched() : w{} {
        int endA[KT] = {}, endW[KT] = {};
        int n = 0;
        if (FIRST) {
            if (ENT == 0) { n += 2; endA[0] = n; n += 3; endW[0] = n; }
            n += NAI; endA[1] = n; n += NWI; endW[1] = n;
            if (ERL >= 5) { n += NAI; endA[2] = n; }
            if (ERL == 2 || ERL == 3 || ERL == 5) { n += NWI; endW[2] = n; }
        } else {
            for (int s = 0; s < WPRE; ++s) { n += NWI; endW[s] = n; }
            for (int s = 0; s < 2; ++s) { n += NAI; endA[s] = n; }
        }
        for (int kt = 0; kt < KT; ++kt) {
            const int need = endA[kt] > endW[kt] ? endA[kt] : endW[kt];
            w[kt] = (n - need) > 63 ? 63 : (n - need);
            if (kt + 2 < KT && !(FIRST && ERL >= 5 && kt == 0)) { n += NAI; endA[kt + 2] = n; }
            if (FIRST && (ERL == 4 || ERL == 6) && kt == 0) { n += NWI; endW[2] = n; }
            if (FIRST && ERL < 2 && kt < 5) {
                if (kt < 4) { n += NWI; endW[kt + 2] = n; }
                if (kt >= 3) n += NWI / 2;
                if (kt == 4) { endW[6] = n; n += NWI; endW[7] = n; }
                continue;
            }
            if (!FIRST && WPRE == 2 && kt < 2) { n += NWI; endW[kt + 2] = n; }
            if (WPRE == 3 && kt == 0) { n += NWI; endW[3] = n; }
            if (kt >= 1 && kt + 3 < KT) { n += NWI; endW[kt + 3] = n; }
            if (kt == 13) n += WT13;
            if (kt == 14) n += (NAI == 5 || WA0 > 12 ? 12 : WA0) - WT13 + (NAI == 5 ? WB14 : 0);
        }
    }
};

struct Params {
    const bf16_t* x0;
    const bf16_t* wqkv0;
    char* ws;
    const bf16_t* wqkv1;
    const bf16_t* wo0;
    const bf16_t* wo1;
    bf16_t* out;
};
typedef const __attribute__((address_space(4))) Params* KP;
DI int rot_first(int pq, int jq) {
    const int pr = 2 * (pq & 7) + (pq >> 3);
    if (ROT0 == 6) return 0;
    if (ROT0 == 7) return (pq & 1) * 8;
    if (ROT0 == 8) return (pq & 3) * 4;
    if (ROT0 == 9) return pq;
    if (ROT0 == 4) return (pq + (jq ^ 3)) & 15;
    if (ROT0 == 5) return (pr + (jq ^ 3)) & 15;
    return (ROT0 == 0 ? pq + jq : (ROT0 == 1 ? pr + jq : (ROT0 == 2 ? pr + 4 * (jq & 3) + (jq >> 2) : pq + 4 * (jq & 3) + (jq >> 2)))) & 15;
}

struct Order { int base, r, re; };
DI Order first_order(int pq, int jq) { Order o; o.base = (pq & NCM) * (16 / (NCM + 1)) + NCB; o.r = (rot_first(pq, jq) - o.base) & 15; o.re = o.r > NC ? o.r : NC; return o; }
DI int first_tile(const Order& o, int s) {
    const int v = o.re + s - NC, u = s < NC ? (o.r == s ? NC : s) : (s == NC ? o.r : (v > 15 ? v - (16 - NC) : v));
    return (u + o.base) & 15;
}

typedef unsigned long long u64;
DI u64 lds_epoch(LAS char* smem) { return *(const LAS u64*)(smem + M_EP); }
DI bool reached(u64 v, u64 ep, unsigned target) { return (v - ep - target) < (u64)(64u - target); }
DI void poll_flags(const char* ws, u64 ep, unsigned off, bool active, unsigned target) {
    const GAS u64* f = (const GAS u64*)(ws + off);
#pragma unroll 1
    for (unsigned spins = 0; spins < (1u << 22); ++spins) {
        const u64 v = __hip_atomic_load(f, __ATOMIC_RELAXED, __HIP_MEMORY_SCOPE_AGENT);
        if (__all(!active || reached(v, ep, target))) break;
        __builtin_amdgcn_s_sleep(PSLEEP);
    }
    asm volatile("" ::: "memory");
}
struct Ask { u64 v[4]; };
DI void keep(Ask& t) { asm volatile("" : "+v"(t.v[0]), "+v"(t.v[1]), "+v"(t.v[2]), "+v"(t.v[3])); }
#define ASK(r) "global_atomic_or_x2 " r ", %[off], %[z], %[ws] sc0 sc1\n\t"
#define CHK(r) "s_waitcnt vmcnt(%[n])\n\tv_lshl_add_u64 %[d], " r ", 0, %[nb]\n\tv_cmp_gt_u64 vcc, %[span], %[d]\n\ts_cmp_eq_u64 vcc, exec\n\ts_cbranch_scc1 .Lpe%=\n\ts_sleep %[sl]\n\t"
template <bool NEG = false> DI void poll_rmw(Ask& t, const char* ws, unsigned off, bool act, u64 base, unsigned span) {
    u64 z, d, nb = NEG ? base : 0 - base, sp = span;
    unsigned cnt;
    asm volatile("" : "=v"(t.v[0]), "=v"(t.v[1]), "=v"(t.v[2]), "=v"(t.v[3]));
    if (act) {
        if (APIPE == 4)
            asm volatile("v_mov_b64 %[z], 0\n\ts_mov_b32 %[cnt], 0x80000\n\ts_nop 3\n\t" ASK("%[a]") "s_sleep %[sl]\n\t" ASK("%[b]") "s_sleep %[sl]\n\t" ASK("%[c]") "s_sleep %[sl]\n"
                         ".Lpl%=:\n\t" ASK("%[e]") CHK("%[a]") ASK("%[a]") CHK("%[b]") ASK("%[b]") CHK("%[c]") ASK("%[c]") CHK("%[e]")
                         "s_sub_u32 %[cnt], %[cnt], 1\n\ts_cbranch_scc0 .Lpl%=\n.Lpe%=:"
                         : [a] "+v"(t.v[0]), [b] "+v"(t.v[1]), [c] "+v"(t.v[2]), [e] "+v"(t.v[3]), [d] "=&v"(d), [z] "=&v"(z), [cnt] "=&s"(cnt)
                         : [off] "v"(off), [ws] "s"(ws), [nb] "v"(nb), [span] "s"(sp), [sl] "i"(ASLP), [n] "i"(3) : "vcc", "scc", "memory");
        else if (APIPE == 2)
            asm volatile("v_mov_b64 %[z], 0\n\ts_mov_b32 %[cnt], 0x100000\n\ts_nop 3\n\t" ASK("%[a]") "s_sleep %[sl]\n"
                         ".Lpl%=:\n\t" ASK("%[b]") CHK("%[a]") ASK("%[a]") CHK("%[b]")
                         "s_sub_u32 %[cnt], %[cnt], 1\n\ts_cbranch_scc0 .Lpl%=\n.Lpe%=:"
                         : [a] "+v"(t.v[0]), [b] "+v"(t.v[1]), [d] "=&v"(d), [z] "=&v"(z), [cnt] "=&s"(cnt)
                         : [off] "v"(off), [ws] "s"(ws), [nb] "v"(nb), [span] "s"(sp), [sl] "i"(ASLP), [n] "i"(1) : "vcc", "scc", "memory");
        else
            asm volatile("v_mov_b64 %[z], 0\n\ts_mov_b32 %[cnt], 0x200000\n\ts_nop 3\n"
                         ".Lpl%=:\n\t" ASK("%[a]") CHK("%[a]")
                         "s_sub_u32 %[cnt], %[cnt], 1\n\ts_cbranch_scc0 .Lpl%=\n.Lpe%=:"
                         : [a] "+v"(t.v[0]), [d] "=&v"(d), [z] "=&v"(z), [cnt] "=&s"(cnt)
                         : [off] "v"(off), [ws] "s"(ws), [nb] "v"(nb), [span] "s"(sp), [sl] "i"(ASLP), [n] "i"(0) : "vcc", "scc", "memory");
    }
}
DI void post(char* ws, unsigned off, u64 v) {
    if (APOST) asm volatile("s_nop 4\n\tglobal_atomic_swap_x2 %0, %1, %2 sc1" :: "v"(off), "v"(v), "s"(ws) : "memory");
    else __hip_atomic_store((GAS u64*)(ws + off), v, __ATOMIC_RELAXED, __HIP_MEMORY_SCOPE_AGENT);
}
template <int NW, int CNT, int NV = 0> DI void publish(LAS char* smem, int lane, char* ws, unsigned off, bool act, unsigned val) {
    const u64 ep = lds_epoch(smem) + val;
    unsigned old = 0;
    asm volatile("" :: "v"(ep), "v"(off));
    wait_v<NV>();
    if (lane == 0) asm volatile("ds_add_rtn_u32 %0, %1, %2\n\ts_waitcnt lgkmcnt(0)" : "+v"(old) : "v"(M_CNT + CNT * 4), "v"(1u) : "memory");
    if ((__builtin_amdgcn_readfirstlane(old) & (NW - 1)) == NW - 1) {
        if (act) post(ws, off, ep);
    }
}
typedef __bf16 bf16x2_t __attribute__((ext_vector_type(2)));
DI float dot2(unsigned a, unsigned b, float c) { return __builtin_amdgcn_fdot2_f32_bf16(__builtin_bit_cast(bf16x2_t, a), __builtin_bit_cast(bf16x2_t, b), c, false); }
DI u32x2 tile_stats(const u32x4& a, const u32x4& b) {
    float s = 0.f, q = 0.f;
#pragma unroll
    for (int j = 0; j < 4; ++j) { s = dot2(a[j], 0x3f803f80u, s); q = dot2(a[j], a[j], q); }
#pragma unroll
    for (int j = 0; j < 4; ++j) { s = dot2(b[j], 0x3f803f80u, s); q = dot2(b[j], b[j], q); }
    return (u32x2){__float_as_uint(red_sum4(s)), __float_as_uint(red_sum4(q))};
}

struct Ctx {
    LAS char* smem;
    char* ws;
    int lane, wid;
    int pq, jq;
    int pw, jw;
    int bid;
    int cw;
};
DI Ctx fresh(const Ctx& c0) {
    Ctx c = c0;
    PADC();
    asm volatile("" : "+v"(c.lane));
    asm volatile("" : "+s"(c.wid), "+s"(c.pq), "+s"(c.jq), "+s"(c.pw), "+s"(c.jw), "+s"(c.ws), "+s"(c.cw));
    return c;
}
DI unsigned o_post(const Ctx& c) { return (MBOX & 2) ? (unsigned)WS_OBOX + (c.pq * 16 + (c.lane & 15)) * OBS + c.jq * 8 : (unsigned)WS_FLAGQ + (c.pq * 16 + c.jq) * FQS; }
DI bool o_act(const Ctx& c) { return c.lane < ((MBOX & 2) ? 16 : 1); }
DI unsigned o_find(const Ctx& c) { return (MBOX & 2) ? (unsigned)WS_OBOX + (c.pw * 16 + c.jw) * OBS + (c.lane & 15) * 8 : (unsigned)WS_FLAGQ + (c.pw * 16 + (c.lane & 15)) * FQS; }
DI unsigned x_post(const Ctx& c) { return (MBOX & 1) ? (unsigned)WS_XBOX + (c.pw * 16 + (c.lane & 31)) * (2 * FQS) + (c.lane & 16) * (FQS / 16) + c.jw * 8 : (unsigned)WS_FLAGW + (c.pw * 16 + c.jw) * 128; }
DI bool x_act(const Ctx& c, bool halo) { return c.lane < ((MBOX & 1) ? (halo && c.pw < 15 ? 32 : 16) : 1); }
DI unsigned x_find(const Ctx& c) {
    const int pp = c.pq - (c.lane >> 4);
    return (MBOX & 1) ? (unsigned)WS_XBOX + (c.pq * 16 + c.jq) * (2 * FQS) + (c.lane & 16) * (FQS / 16) + (c.lane & 15) * 8 : (unsigned)WS_FLAGW + ((pp < 0 ? 0 : pp) * 16 + (c.lane & 15)) * 128;
}
#define STAMP_AT(slot) do { } while (0)

#define LSTAMP(i) do { } while (0)
#define LSTORE(ph) do { } while (0)
#define LMARK() do { } while (0)
#define LPUT(i) do { } while (0)
#define ASTORE() do { } while (0)
#define QSTAMP(i) do { } while (0)
#define QSTORE() do { } while (0)
#define FENCE() asm volatile("PADC %=" ::: "memory")
#define ALIGN8() do { if (ALN) { asm volatile(".p2align 3"); SCHED(); } } while (0)
DI float addf(float a, float b) { float r; asm("v_add_f32 %0, %1, %2" : "=v"(r) : "v"(a), "v"(b)); return r; }
DI f32x4 add4x4(const u32x4& a, const u32x4& b, const u32x4& c, const u32x4& d) {
    f32x4 r;
#pragma unroll
    for (int i = 0; i < 4; ++i) r[i] = addf(addf(__uint_as_float(a[i]), __uint_as_float(b[i])), addf(__uint_as_float(c[i]), __uint_as_float(d[i])));
    asm volatile("s_nop 1" : "+v"(r));
    return r;
}
DI float fold16(float a, float b) {
    auto r = __builtin_amdgcn_permlane16_swap(__float_as_uint(a), __float_as_uint(b), false, false);
    float x = __uint_as_float(r[0]) + __uint_as_float(r[1]);
    asm volatile("PADC %=" : "+v"(x));
    return x;
}
DI float fold32(float a, float b) {
    auto r = __builtin_amdgcn_permlane32_swap(__float_as_uint(a), __float_as_uint(b), false, false);
    float x = __uint_as_float(r[0]) + __uint_as_float(r[1]);
    asm volatile("PADC %=" : "+v"(x));
    return x;
}
DI void aload4(u32x4& d, unsigned voff, const char* sb) { asm volatile("s_nop 4\n\tglobal_load_dwordx4 %0, %1, %2" : "=v"(d) : "v"(voff), "s"(sb) : "memory"); }
DI void aload4p(u32x4& d, unsigned voff, const char* sb) { asm volatile("s_nop 4\n\tglobal_load_dwordx4 %0, %1, %2" : "+v"(d) : "v"(voff), "s"(sb) : "memory"); }
DI void aload4c(u32x4& d, unsigned voff, const char* sb) { asm volatile("s_nop 4\n\tglobal_load_dwordx4 %0, %1, %2 sc1" : "=v"(d) : "v"(voff), "s"(sb) : "memory"); }
DI void aload2c(u32x2& d, unsigned voff, const char* sb) { asm volatile("s_nop 4\n\tglobal_load_dwordx2 %0, %1, %2 sc1" : "=v"(d) : "v"(voff), "s"(sb) : "memory"); }
DI unsigned wo_src(int q, int lane, int jw) { return (unsigned)((8 * q + (lane >> 3)) * (D_ * 2) + jw * 128 + (((lane & 7) ^ ((lane >> 3) & 6)) << 4)); }
DI unsigned pick(bool f, unsigned a, unsigned b) { asm volatile("PADC %=" : "+v"(a), "+v"(b)); return f ? a : b; }
DI unsigned wo_w_off(const Ctx& c) { return pick(c.cw == 3, (unsigned)((c.wid & 3) * 1024 + c.lane * 16), wo_src(c.wid & 3, c.lane, c.jw)); }
DI unsigned wq_src(int ib, int lane, int jq) {
    const int seg = ib >> 3, ii = ib & 7, lr = lane >> 3;
    return (unsigned)((8 * ii + lr) * 6144 + seg * 2048 + jq * 128 + (((lane & 7) ^ ((lr & 2) | ((ii & 1) << 2))) << 4));
}
template <int S0, int S1> DI void wo_w_issue(const Ctx& c, const char* W, unsigned vw) {
    const int lw = c.wid & 3;
#pragma unroll
    for (int s = S0; s < S1; ++s) dma16<CPY != 0>(s * 4096 + lw * 1024, vw, W + (size_t)((2 * (s & 15) + (s >> 4)) * 32) * (D_ * 2));
}

DI void code_again(const Ctx& c) {
    const char* pc; int len, back;
    asm volatile("s_getpc_b64 %0\n.Lcpb%=:\n\ts_mov_b32 %2, .Lcpb%=-.Lcpf_a\n\ts_mov_b32 %1, .Lcpf_end-.Lcpf_a-4" : "=s"(pc), "=s"(len), "=s"(back));
    pc -= back;
    const int o = min((((c.bid >> 3) & 7) * 64 + c.lane) * 128, len);
    asm volatile("s_mov_b32 m0, %0\n\ts_nop 3\n\tglobal_load_lds_dword %1, %2" ::"s"(M_TCH), "v"(o), "s"(pc) : "memory");
}
template <bool HALO, bool FIRST> DI void qkv_load(const Ctx& c0, const char* xin, const char* W, const char* Wo, int l, KP kp = nullptr, unsigned va0 = 0, unsigned vw0 = 0) {
    const Ctx c = fresh(c0);
    asm volatile("" : "+s"(xin), "+s"(W), "+s"(Wo));
    LAS char* smem = c.smem;
    constexpr int NAI = HALO ? 5 : 4, NWI = 6, XS = XS_, NR = HALO ? 144 : 128;
    constexpr Sched<NAI, NWI, FIRST> SC{};
    static_assert(NQA == 3 && NQW == 4, "schedule");
    const int lw = c.wid - 4, lane = c.lane, lr = lane >> 3, lc = lane & 7, fr = lane & 15, fq = lane >> 4;
    const int row0 = c.pq * 128;
    const int pr = (ROTM == 1 || (ROTM == 2 && (c.jq & 7) == 3)) ? 2 * (c.pq & 7) + (c.pq >> 3) : (ROTM == 3 ? (c.pq & 7) + ((c.pq & 7) >> 1) + (c.pq & 8) : c.pq);
    const bool cq_ = CPY && !FIRST && (c.jq & 7) == 3;
    if (cq_) W = c.ws + WS_CQ + ((HALO ? 0 : 2) + (c.jq >> 3)) * 24576;
    asm volatile("" : "+s"(W));
    const bool nor = !FIRST && l > 1 && (NOR == 1 || (NOR == 2 && (c.jq & 7) == 3) || (NOR == 3 && (c.jq & 7) != 3));
    const int rot = FIRST ? rot_first(c.pq, c.jq) : (nor ? (c.pq & 8) * NRH : pr & 15);
    Order od = first_order(c.pq, c.jq);
    if (FIRST) asm volatile("" : "+s"(od.base), "+s"(od.r), "+s"(od.re));
    auto tile = [&](int s) { return FIRST ? first_tile(od, s) : (s + rot) & 15; };
    constexpr int SP = FIRST ? NC : 0;
    unsigned va[5], vw[NWI];
#pragma unroll
    for (int j = 0; j < 4; ++j) va[j] = (unsigned)(FIRST ? va0 + j * 32 * (D_ * 2) : c.pq * 262144 + (j * 4 + lw) * 1024 + lane * 16);
    if (HALO) va[4] = (unsigned)((c.pq > 0 ? (c.pq - 1) * 262144 + 14336 : 0) + lw * 512 + lane * 16);
#pragma unroll
    for (int j = 0; j < NWI; ++j) vw[j] = FIRST ? vw0 + (j & 1) * (32 * 6144) + (j >> 1) * 2048 : pick(cq_, (unsigned)((j * 4 + lw) * 1024 + lane * 16), wq_src(j * 4 + lw, lane, c.jq));
    auto issueA = [&](int s, int slot) {
        const char* b = xin + (unsigned)(tile(s) * (FIRST ? 128 : 16384));
#pragma unroll
        for (int j = 0; j < 4; ++j) dma16<true>(QA_OFF + slot * QA_SLOT + (j * 4 + lw) * 1024, va[j], b);
        if (HALO) { if (lane < 32) dma16<true>(QA_OFF + slot * QA_SLOT + 16384 + lw * 512, va[4], b); }
    };
    auto issueW = [&](int s, int slot, int j0 = 0, int j1 = NWI) {
        const char* b = W + (unsigned)(tile(s) * (64 * 6144));
#pragma unroll
        for (int j = j0; j < j1; ++j) dma16<CPY && !FIRST>(qw_slot(slot) + (j * 4 + lw) * 1024, vw[j], b);
    };
    auto ahead = [&]() {
        const int r3 = (lane * 43) >> 7;
        const unsigned o = (unsigned)((((rot - (c.pq & 7) + 10) * 64 + (c.pq & 7) * 48 + lw * 12 + r3) & 1023) * 6144 + (lane - 3 * r3) * 2048 + c.jq * 128);
        if (lane < 36 && !nor && !cq_ && (WTC != 2 || (c.jq & 7) == 3)) asm volatile("s_mov_b32 m0, %0\n\ts_nop 3\n\tglobal_load_lds_dword %1, %2" ::"s"(M_TCH), "v"(o), "s"(W) : "memory");
    };
    if (ST0 && FIRST && lw == 1) STAMP_AT(47);
    if (FIRST && ERL) {
        SCHED();
        if (ESL) __builtin_amdgcn_s_sleep(ESL);
        issueA(1, 1); issueW(1, 1);
        SCHED();
    }
    const bool docs = FIRST || (HALO && l == 1);
    const int t = lw * 64 + lane;
    static_assert(MBOX & 1, "residual flag");
    unsigned foff, ftg, so, ho, co, l3, l4, vwo, wt[3], cso, xt, xt1, xso, tp, tl;
    u32x4 on;
    auto addr1 = [&]() {
        int lane = c.lane;
        asm volatile("" : "+v"(lane));
        const int fr = lane & 15, fq = lane >> 4;
        foff = pick(lane < 32, (unsigned)WS_FLAGQ + pick(lane < 16, c.pq * 16 + lane, fr * 16 + c.jq) * FQS, (unsigned)WS_XBOX + (c.pw * 16 + c.jw) * (2 * FQS) + c.jw * 8);
        ftg = lane < 32 ? (FIRST ? 1u : 5u) : (unsigned)(l + 1);
        so = (unsigned)hl<HS>((c.pq * 16 + fq * 4) * 1024 + (lw * 32 + fr * 2) * 8);
        ho = (unsigned)hl<HS>(((c.pq > 0 ? c.pq - 1 : 0) * 16 + (lane >> 2)) * 1024 + ((c.pq > 0 ? 112 : 0) + lw * 4 + (lane & 3)) * 8);
        const int cq = lw * 12 + (fr < 12 ? fr : 11);
        co = (unsigned)((fq * 4 * 3072 + (cq >> 4) * 1024 + c.jq * 64 + (cq & 15) * 4) * 4);
        l3 = (unsigned)(M_CS + (HALO ? 768 : 0) + (cq * 4 + fq) * 4);
        l4 = (unsigned)(M_MEAN + pick(lane < 32, 16 + lw * 32 + fr * 2 + fq, lw * 4 + (lane & 3)) * 4);
        asm volatile("" : "+v"(foff), "+v"(ftg), "+v"(so), "+v"(co), "+v"(l3), "+v"(l4));
        if (HALO) asm volatile("" : "+v"(ho));
        SCHED();
    };
    auto addr2 = [&]() {
        int lane = c.lane;
        asm volatile("" : "+v"(lane));
        const int fr = lane & 15, fq = lane >> 4;
        const int q4 = fr >> 2, p4 = lane & 3, fb = (q4 & 2) | ((fq & 1) << 2);
#pragma unroll
        for (int i = 0; i < 3; ++i) {
            const int T = 3 * lw + i;
            wt[i] = (unsigned)(qw_slot(SP % NQW) + (T >> 2) * 8192 + (8 * fq + q4) * 128 + (((2 * (T & 3) + (p4 >> 1)) ^ fb) << 4) + (p4 & 1) * 8);
        }
        const int T = 3 * lw + fq;
        cso = (unsigned)((((HALO ? 16 : 0) + rot) * 3072 + (T >> 2) * 1024 + c.jq * 64 + (T & 3) * 16 + fr) * 4);
        xt = (unsigned)(QA_OFF + (SP % NQA) * QA_SLOT + (lw * 32 + fr) * 128 + ((fq ^ (fr & 7)) << 4));
        xt1 = xt ^ 64;
        xso = (unsigned)hl<HS>((c.pq * 16 + rot) * 1024 + (lw * 32 + fr) * 8);
        on = (u32x4){0x3f803f80u, 0x3f803f80u, 0x3f803f80u, 0x3f803f80u};
        if (FIRST || HALO) asm volatile("" : "+v"(wt[0]), "+v"(wt[1]), "+v"(wt[2]), "+v"(cso), "+v"(on));
        if (FIRST) asm volatile("" : "+v"(xt), "+v"(xt1), "+v"(xso));
        SCHED();
    };
    auto addr3 = [&]() {
        Ctx c2 = c;
        asm volatile("" : "+v"(c2.lane));
        asm volatile("" : "+s"(c2.bid));
        vwo = wo_w_off(c2);
        tp = (unsigned)(pick(c2.lane < 30, (c2.bid >> 3) * 30 + c2.lane, (c2.bid >> 3) * 16 + c2.lane - 30 + (int)(WS_O >> 16)) * 65536 + 65536 - 128);
        tl = (unsigned)(lw == 1 ? ((c2.bid >> 3) * 2 + c2.lane) * 65536 : c2.bid * 8192 + c2.lane * 128);
        asm volatile("" : "+v"(vwo));
        if (FIRST) asm volatile("" : "+v"(tp), "+v"(tl));
        SCHED();
    };
    if (!FIRST || AD1 < 0) addr1();
    if (FIRST && ERL >= 5) { issueA(2, 2); SCHED(); }
    if (FIRST && (ERL == 2 || ERL == 5)) { issueW(2, 2); SCHED(); }
    if (!FIRST || AD2 < 0) addr2();
    if (!FIRST || AD3 < 0) addr3();
#pragma unroll
    for (int j = 0; j < NAI; ++j) asm volatile("" : "+v"(va[j]));
#pragma unroll
    for (int j = 0; j < NWI; ++j) asm volatile("" : "+v"(vw[j]));
    SCHED();
    int tv = 0; unsigned long long lt = 0;
    if (FIRST) {
        LSTAMP(55);
    } else {
        static_assert(CPY == 1 && RS0 > 0 && RN0 == NWI && WPRE == 3 && (FIRST || SC.w[0] == NAI), "order");
        unsigned vx[2], xfo = (unsigned)WS_XBOX + (c.pq * 16 + c.jq) * (2 * FQS) + (HALO && c.pq > 0 ? (lane & 16) * (FQS / 16) : 0) + fr * 8, go = M_GO;
        u64 xv = lds_epoch(smem) + (unsigned)(l + 1);
#pragma unroll
        for (int i = 0; i < 2; ++i) vx[i] = pick(cq_, (unsigned)(((2 * lw + i) * 4 + 3) * 1024 + lane * 16), wq_src((2 * lw + i) * 4 + 3, lane, c.jq));
        asm volatile("" : "+v"(vx[0]), "+v"(vx[1]), "+v"(xfo), "+v"(xv), "+v"(go));
        BAR();
        issueW(0, 0);
        auto word = [&](int off) { return __builtin_amdgcn_readfirstlane(*(volatile LAS int*)(lds(go) + off)); };
        auto until = [&](int off, int v) {
#pragma unroll 1
            for (unsigned spins = 0; spins < (1u << 24); ++spins) { if (word(off) == v) break; }
            FENCE();
        };
        auto stage = [&](int s) {
            issueW(s, s);
            const char* b = W + (size_t)((s + rot) & 15) * (64 * 6144);
#pragma unroll
            for (int i = 0; i < 2; ++i) dma16<true>(qw_slot(s) + ((2 * lw + i) * 4 + 3) * 1024, vx[i], b);
        };
        auto first2 = [&]() { __builtin_amdgcn_s_setprio(LPRIO); issueA(0, 0); issueA(1, 1); };
        if (lw == 3) {
            if (!EPOLL) until(M_CNT - M_GO, 8 * l);
            Ask t;
            poll_rmw(t, c.ws, xfo, lane < (HALO && c.pq > 0 ? 32 : 16), xv, 63 - l);
            *(volatile LAS int*)lds(go) = l;
            until(M_CNT - M_GO, 8 * l);
            first2();
            STAMP_AT(32 + l * 8 + 0);
            wait_v<NAI>();
            keep(t);
        } else {
            until(M_CNT - M_GO, 8 * l);
            if (RSLP) __builtin_amdgcn_s_sleep(RSLP);
            if (ORD > 0 && word(0) == l) {
                first2(); stage(1); stage(2);
                wait_v<NAI + 16>();
            } else {
                stage(1);
                if (ORD > 1 && word(0) == l) {
                    first2(); stage(2);
                    wait_v<NAI + 8>();
                } else {
                    stage(2);
                    until(0, l);
                    first2();
                    wait_v<NAI>();
                }
            }
        }
        FENCE();
    }
    if (FIRST && ERL == 3) issueW(2, 2);
    if (ST0 && FIRST && lw == 2) STAMP_AT(57);
    if (FIRST && !ERL) BAR();
    __builtin_amdgcn_s_setprio(LPRIO);
    if (FIRST && !ERL) { LSTAMP(57); issueA(1, 1); issueW(1, 1); LSTAMP(58); }
    u32x4 st[4], cs[4]; u32x2 sh, fl; u64 fv = 0; float rs_, rq_, hs;
    f32x4 d[3], sm[2], g[2];
    bf16x8 wf[3][2], xf[2][2];
    const bool fact = FIRST ? lane < 32 : (lane >= (docs ? 16 : 32) && lane <= 32);
    static_assert(XS + 3 <= 12, "aux schedule");
    auto side = [&](auto ktc) {
        constexpr int kt = decltype(ktc)::value, S0 = SPR && !FIRST ? (SL1 ? SL1 + 4 : SL) : XS;
        {
            constexpr int R0 = !SPR ? 12 : S0 + 2, R1 = !SPR ? 12 : S0 + 3, R2 = !SPR ? 12 : (FIRST ? 12 : S0 + 4), R3 = !SPR ? 12 : (FIRST ? 12 : S0 + 5), R4 = !SPR ? 12 : (FIRST ? 13 : (R4X ? R4X : S0 + 6));
            constexpr int C0 = !SPR ? 13 : (FIRST ? 12 : 11), C1 = !SPR ? 13 : (FIRST ? 13 : 12), C2 = SPR && !FIRST ? C2X : 13;
            auto acc4 = [](u32x4& a, const u32x4& b) {
#pragma unroll
                for (int i = 0; i < 4; ++i) a[i] = __float_as_uint(addf(__uint_as_float(a[i]), __uint_as_float(b[i])));
                asm volatile("PADC %=" : "+v"(a));
            };
            if constexpr (kt == R0) {
                asm volatile("" : "+v"(st[0]), "+v"(st[1]));
                acc4(st[0], st[1]);
                if (SPR && FIRST) { asm volatile("" : "+v"(st[2])); acc4(st[0], st[2]); }
                if (HALO) {
                    asm volatile("" : "+v"(sh));
                    hs = fold16(__uint_as_float(sh[0]), __uint_as_float(sh[1]));
                    hs = fold32(hs, hs);
                }
            }
            if constexpr (kt == R1) {
                asm volatile("" : "+v"(st[2]), "+v"(st[3]));
                if (!(SPR && FIRST)) acc4(st[3], st[2]);
                acc4(st[0], st[3]);
            }
            if constexpr (kt == C0) {
                if (docs) { asm volatile("" : "+v"(cs[0]), "+v"(cs[1])); acc4(cs[0], cs[1]); }
            }
            if constexpr (kt == R2) {
                asm volatile("s_nop 1" : "+v"(st[0]));
                rs_ = fold16(__uint_as_float(st[0][0]), __uint_as_float(st[0][2])); rq_ = fold16(__uint_as_float(st[0][1]), __uint_as_float(st[0][3]));
                if (HALO) {
                    hs += __uint_as_float(__builtin_amdgcn_update_dpp(0u, __float_as_uint(hs), 0x124, 0xf, 0xf, false));
                    hs += __uint_as_float(__builtin_amdgcn_update_dpp(0u, __float_as_uint(hs), 0x128, 0xf, 0xf, false));
                    asm volatile("" : "+v"(hs));
                }
            }
            if constexpr (kt == R3) {
                rs_ = fold32(rs_, rs_); rq_ = fold32(rq_, rq_);
                if (HALO) {
                    auto hh = __builtin_amdgcn_permlane16_swap(__float_as_uint(hs), __float_as_uint(hs), false, false);
                    if (lane >= 32) { rs_ = __uint_as_float(hh[0]); rq_ = __uint_as_float(hh[1]); }
                    asm volatile("" : "+v"(rs_), "+v"(rq_));
                }
            }
            if constexpr (kt == R4) {
                rs_ *= -1.f / 1024.f;
                asm volatile("" : "+v"(rs_));
                rq_ = __builtin_amdgcn_rsqf(fmaxf(rq_ * (1.f / 1024.f) - rs_ * rs_, 0.f) + LN_EPS);
                asm volatile("" : "+v"(rq_));
            }
            if constexpr (kt == C1) {
                if (docs) { asm volatile("" : "+v"(cs[2]), "+v"(cs[3])); acc4(cs[2], cs[3]); acc4(cs[0], cs[2]); }
            }
            if constexpr (kt == C2) {
                if (docs) {
                    asm volatile("s_nop 1" : "+v"(cs[0]));
                    hs = fold32(fold16(__uint_as_float(cs[0][0]), __uint_as_float(cs[0][1])), fold16(__uint_as_float(cs[0][2]), __uint_as_float(cs[0][3])));
                }
            }
            if constexpr (kt == 13) {
                if (WV0) WAIT_V0();
                if (lane < (HALO ? 36 : 32)) {
                    *(LAS float*)lds(l4) = rs_;
                    *(LAS float*)(lds(l4) + (M_RSTD - M_MEAN)) = rq_;
                }
                if (docs) *(LAS float*)lds(l3) = hs;
                FENCE();
            }
        }
    };
    static_for<0, KT>([&](auto ktc) {
        constexpr int kt = decltype(ktc)::value;
        LSTAMP(3 * kt);
        if constexpr (FIRST || kt > 0) wait_v<SC.w[kt]>();
        LSTAMP(3 * kt + 1);
        if constexpr (kt == XS || (SPR && !FIRST && kt == 15)) {
            if ((SPR ? docs == (kt == XS) : (RCK || docs)) && lw == 0) {
                asm volatile("" : "+v"(fl));
                const u64 ep = lds_epoch(smem);
                if (!__all(!fact || reached(__builtin_bit_cast(u64, fl), ep, ftg))) poll_flags(c.ws, ep, foff, fact, ftg);
            }
        }
        if constexpr (kt == 14) asm volatile("s_waitcnt lgkmcnt(0)" ::: "memory");
        BAR();
        FENCE();
        LMARK();
        LSTAMP(3 * kt + 2);
        if constexpr (TOPJ & (1 << kt)) side(ktc);
        if constexpr (kt == SP + DS + 3) {
            if (docs && lw == 0 && lane == 0) __hip_atomic_store((GAS u64*)(c.ws + WS_FLAGQ + (c.pq * 16 + c.jq) * FQS), fv, __ATOMIC_RELAXED, __HIP_MEMORY_SCOPE_AGENT);
        }
        if constexpr (kt == XS + 1 - NQA) {
            if ((RCK || docs) && lw == 0) { if (fact) aload2c(fl, foff, c.ws); }
        }
        constexpr int S0 = SPR && !FIRST ? SL : XS;
        {
            const char* sb = c.ws + WS_STATS + (size_t)l * 524288;
            const char* cb = c.ws + WS_CSUM + (HALO ? 16 * 3072 * 4 : 0);
            constexpr int sa = !FIRST && SL1 ? kt - SL1 : (!SPR || !FIRST ? 2 * (kt - S0) : 3 * (kt - S0)), sn = !FIRST && SL1 ? 1 : (!SPR || !FIRST ? 2 : 3);
            constexpr int ca = !SPR ? (kt == XS + 1 ? 0 : (kt == XS + 2 ? 1 : 9)) : (FIRST ? 2 * (kt - XS - 1) : 2 * (kt - XS)), cn = !SPR ? (kt == XS + 1 ? 1 : 3) : 2;
#pragma unroll
            for (int i = sa; i < sa + sn; ++i) if (i >= 0 && i < 4) aload4c(st[i], so, sb + hl<HS>(i * 1024));
            if (HALO && kt == (SL1 ? SL1 + 4 : S0)) aload2c(sh, ho, sb);
            if constexpr (ca >= 0 && ca < 4) {
                if (docs) {
#pragma unroll
                    for (int i = ca; i < ca + cn; ++i) aload4c(cs[i], co, cb + i * 12288);
                }
            }
        }
        if constexpr (kt == SP) {
            if (docs) {
#pragma unroll
                for (int i = 0; i < 3; ++i)
#pragma unroll
                    for (int ks = 0; ks < 2; ++ks) {
                        const s16x4 lo = tr_read(lds(wt[i]) + ks * 4096), hi = tr_read(lds(wt[i]) + ks * 4096 + 512);
                        wf[i][ks] = __builtin_shufflevector(lo, hi, 0, 1, 2, 3, 4, 5, 6, 7);
                    }
                if (FIRST) {
#pragma unroll
                    for (int mt = 0; mt < 2; ++mt)
#pragma unroll
                        for (int ks = 0; ks < 2; ++ks) xf[mt][ks] = *(const LAS bf16x8*)(lds(ks ? xt1 : xt) + mt * 2048);
                }
            }
            SCHED();
        }
        if constexpr (kt + 2 < KT && !(FIRST && ERL >= 5 && kt == 0)) issueA(kt + 2, (kt + 2) % NQA);
        LPUT(3 * kt + 2);
        if constexpr (kt == SP + DM) {
            SCHED();
            if (docs) {
                const bf16x8 ones = __builtin_bit_cast(bf16x8, on);
                const f32x4 zero = {0.f, 0.f, 0.f, 0.f};
#pragma unroll
                for (int ks = 0; ks < 2; ++ks) {
#pragma unroll
                    for (int i = 0; i < 3; ++i) d[i] = MFMA(ones, wf[i][ks], ks ? d[i] : zero);
                    if (FIRST) {
#pragma unroll
                        for (int mt = 0; mt < 2; ++mt) { sm[mt] = MFMA(ones, xf[mt][ks], ks ? sm[mt] : zero); g[mt] = MFMA(xf[mt][ks], xf[mt][ks], ks ? g[mt] : zero); }
                    }
                }
            }
            SCHED();
        }
        if constexpr (FIRST && ERL < 2 && kt < 5) {
            if constexpr (kt < 4) issueW(kt + 2, (kt + 2) % NQW);
            if constexpr (kt == 3) issueW(6, 2, 0, NWI / 2);
            if constexpr (kt == 4) { issueW(6, 2, NWI / 2, NWI); issueW(7, 3); }
        } else {
            if constexpr (WPRE == 2 && kt < 2) issueW(kt + 2, kt + 2);
            if constexpr (FIRST && (ERL == 4 || ERL == 6) && kt == 0) issueW(2, 2);
            if constexpr (WPRE == 3 && kt == 0) issueW(3, 3);
            if constexpr (kt >= 1 && kt + 3 < KT) issueW(kt + 3, (kt + 3) % NQW);
        }
        if constexpr (FIRST && kt == WT0) ahead();
        if constexpr ((CP2 & 4) && FIRST && kt == 8) { if (lw == 2) code_again(c); }
        if constexpr ((CP2 & 8) && HALO && kt == 8) { if (lw == 2 && l == 1) code_again(c); }
        if constexpr (FIRST && kt == TSTEP) {
            if (lw == 1 && lane < 46) touch(M_TCH, tp, c.ws);
        }
        if constexpr (FIRST && kt == TW0) {
            if (lw == 0) touch(M_TCH, tl, Wo);
            else if (lw == 1) { asm volatile("" : "+s"(kp)); const char* outp = (const char*)kp->out; if (lane < 2) touch(M_TCH, tl, outp); }
        }
        if constexpr (kt == SP && DM > 0) {
            SCHED();
            if (docs) {
                asm volatile("" : "+v"(wf[0][0]), "+v"(wf[0][1]), "+v"(wf[1][0]), "+v"(wf[1][1]), "+v"(wf[2][0]), "+v"(wf[2][1]));
                if (FIRST) asm volatile("" : "+v"(xf[0][0]), "+v"(xf[0][1]), "+v"(xf[1][0]), "+v"(xf[1][1]));
            }
        }
        if constexpr (kt == SP + DS) {
            SCHED();
            if (docs) {
                if (FIRST) {
                    auto rst = __builtin_amdgcn_make_buffer_rsrc(c.ws + WS_STATS, 0, 524288, 0x00020000);
                    const bool b0 = fr & 1, b1 = fr & 2;
                    u32x2 sv[2];
#pragma unroll
                    for (int mt = 0; mt < 2; ++mt) {
                        float qa = b0 ? g[mt][1] : g[mt][0], qb = b0 ? g[mt][3] : g[mt][2];
                        asm volatile("PADC %=" : "+v"(qa), "+v"(qb));
                        sv[mt] = (u32x2){__float_as_uint(sm[mt][0]), __float_as_uint(b1 ? qb : qa)};
                    }
                    if (fq == (fr >> 2)) {
                        __builtin_amdgcn_raw_buffer_store_b64(sv[0], rst, xso, 0, 16);
                        __builtin_amdgcn_raw_buffer_store_b64(sv[1], rst, xso + hl<HS>(128), 0, 16);
                    }
                }
                float d0 = d[0][0], d1 = d[1][0], d2 = d[2][0];
                asm volatile("PADC %=" : "+v"(d0), "+v"(d1), "+v"(d2));
                const float v = fq == 0 ? d0 : (fq == 1 ? d1 : d2);
                auto rc = __builtin_amdgcn_make_buffer_rsrc(c.ws + WS_CSUM, 0, 2 * 16 * 3072 * 4, 0x00020000);
                if (lane < 48) __builtin_amdgcn_raw_buffer_store_b32(__float_as_uint(v), rc, cso, 0, 16);
                asm volatile("s_waitcnt lgkmcnt(0)" ::: "memory");
            }
        }
        if constexpr (kt == 1) {
            if (docs && lw == 0) {
                const u32x2 e = *(const LAS u32x2*)(smem + M_EP);
                const u64 es = ((u64)(unsigned)__builtin_amdgcn_readfirstlane(e[1]) << 32) | (unsigned)__builtin_amdgcn_readfirstlane(e[0]);
                fv = es + (FIRST ? 1u : 5u);
                asm volatile("" : "+s"(fv));
            }
        }
        if constexpr (FIRST && kt >= WB0 && kt < WB0 + WBN) warm_b<kt - WB0>(lw);
        if constexpr (FIRST && kt == AD1) addr1();
        if constexpr (FIRST && kt == AD2) addr2();
        if constexpr (FIRST && kt == AD3) addr3();
        if constexpr (!(TOPJ & (1 << kt))) side(ktc);
        if constexpr (kt == 13) wo_w_issue<0, WT13>(c, Wo, vwo);
        if constexpr (kt == 14) { wo_w_issue<WT13, (HALO || WA0 > 12) ? 12 : WA0>(c, Wo, vwo); if (HALO) wo_w_issue<23, 23 + WB14>(c, Wo, vwo); }
        if constexpr (kt == 15) {
            if (HALO ? WBE < 2 : WA0 > 12) wo_w_issue<12, HALO ? 18 : WA0>(c, Wo, vwo);
            if (HALO) wo_w_issue<23 + WB14, 23 + WB14 + WB15>(c, Wo, vwo);
        }
    });
    __builtin_amdgcn_s_setprio(0);
    BAR();
    LMARK(); LPUT(48);
    LSTAMP(48);
    LSTORE(0);
    if (HALO ? WBE < 1 : WAE < 1) wo_w_issue<18, HALO ? 23 : 20>(c, Wo, vwo);
    if (HALO) {
        static_assert(!WBS || (WBE == 1 && WB14 == 0 && WRG <= 12), "Wo weights of the first twelve steps are older than the last 6 + WB15");
        if (WBS) wait_v<6 + WB15>();
        BAR();
        if (WBE) wo_w_issue<WBE < 2 ? 18 : 12, 23>(c, Wo, vwo);
        wo_w_issue<23 + WB14 + WB15, 32>(c, Wo, vwo);
    }
}

DI float max3(float a, float b, float c) { return fmaxf(fmaxf(a, b), c); }
#define SGB(m, n) __builtin_amdgcn_sched_group_barrier(m, n, 0)
template <bool HALO> struct Tail {
    int f[26], n[4], t[4][26], d[4][27], v[4][27], h3[32], s3[32], o3[33], q3[112];
    constexpr Tail() : f{}, n{}, t{}, d{}, v{}, h3{}, s3{}, o3{}, q3{} {
        const int FB[26] = {0, 1, 2, 3, 4, 5, 6, 7, 8, 9, 10, 11, 12, 13, 14, 15, 24, 25, 19, 23, 18, 22, 17, 21, 16, 20};
        const int FA[24] = {0, 1, 2, 3, 4, 5, 6, 7, 8, 9, 10, 11, 12, 13, 14, 15, 16, 17, 20, 21, 18, 19, 22, 23};
        const int TA[4][26] = {{0, 1, 2, 3, 7, 6, 5, 4, 8, 9, 10, 11, 15, 14, 13, 12, 16, 17, 18, 19, 23, 22, 21, 20, 0, 0}, {4, 8, 9, 10, 11, 15, 14, 13, 12, 16, 17, 18, 19, 23, 22, 21, 20, 0, 1, 2, 3, 7, 6, 5, 0, 0}, {16, 17, 18, 19, 23, 22, 21, 20, 0, 1, 2, 3, 7, 6, 5, 4, 8, 9, 10, 11, 15, 14, 13, 12, 0, 0}, {17, 18, 19, 23, 22, 21, 20, 8, 9, 10, 11, 15, 14, 13, 12, 16, 0, 0, 0, 0, 0, 0, 0, 0, 0, 0}};
        const int DA[4][26] = {{1, 1, 1, 1, 1, 1, 1, 1, 1, 1, 1, 1, 1, 1, 1, 1, 1, 1, 1, 1, 1, 1, 0, 0, 0, 0}, {1, 1, 1, 1, 1, 1, 1, 1, 1, 1, 1, 1, 1, 1, 1, 1, 1, 1, 1, 1, 1, 1, 0, 0, 0, 0}, {1, 1, 1, 1, 1, 1, 1, 1, 1, 1, 1, 1, 1, 1, 1, 1, 0, 0, 0, 0, 0, 0, 0, 0, 0, 0}, {2, 2, 2, 2, 1, 1, 1, 1, 1, 1, 1, 1, 0, 0, 0, 0, 0, 0, 0, 0, 0, 0, 0, 0, 0, 0}};
        const int VA[4][26] = {{1, 1, 0, 0, 0, 0, 1, 1, 1, 1, 1, 1, 1, 1, 1, 1, 1, 1, 1, 1, 1, 1, 2, 2, 0, 0}, {1, 1, 1, 1, 1, 1, 1, 1, 1, 1, 1, 1, 1, 1, 1, 1, 1, 1, 1, 1, 1, 1, 2, 2, 0, 0}, {1, 1, 1, 1, 1, 1, 1, 1, 1, 1, 1, 1, 1, 1, 1, 1, 2, 2, 2, 2, 2, 3, 3, 2, 0, 0}, {0, 0, 0, 0, 1, 1, 1, 1, 1, 1, 1, 1, 2, 2, 2, 2, 0, 0, 0, 0, 0, 0, 0, 0, 0, 0}};
        const int TB[4][26] = {{0, 1, 2, 3, 7, 6, 5, 4, 24, 8, 9, 10, 11, 15, 14, 13, 12, 25, 16, 17, 18, 19, 23, 22, 21, 20}, {24, 8, 9, 10, 11, 15, 14, 13, 12, 25, 16, 17, 18, 19, 23, 22, 21, 20, 0, 1, 2, 3, 7, 6, 5, 4}, {25, 16, 17, 18, 19, 23, 22, 21, 20, 0, 1, 2, 3, 7, 6, 5, 4, 24, 8, 9, 10, 11, 15, 14, 13, 12}, {0, 1, 2, 3, 7, 6, 5, 4, 24, 8, 9, 10, 11, 15, 14, 13, 12, 25, 19, 23, 0, 0, 0, 0, 0, 0}};
        const int DB[4][26] = {{1, 1, 1, 1, 1, 1, 1, 1, 1, 1, 1, 1, 1, 1, 1, 1, 1, 1, 1, 1, 1, 1, 1, 0, 0, 0}, {1, 1, 1, 1, 1, 1, 1, 1, 1, 1, 1, 1, 1, 1, 1, 1, 1, 1, 1, 1, 1, 1, 0, 0, 0, 0}, {1, 1, 1, 1, 1, 1, 1, 1, 1, 1, 1, 1, 1, 1, 1, 1, 1, 1, 1, 1, 1, 1, 1, 0, 0, 0}, {2, 2, 1, 1, 1, 1, 1, 1, 1, 1, 1, 1, 1, 1, 1, 1, 1, 1, 1, 0, 0, 0, 0, 0, 0, 0}};
        const int VB[4][26] = {{1, 1, 1, 1, 1, 1, 1, 1, 1, 1, 1, 1, 1, 1, 1, 1, 1, 1, 1, 1, 1, 1, 1, 2, 2, 2}, {1, 1, 1, 1, 1, 1, 1, 1, 1, 1, 1, 1, 1, 1, 1, 1, 1, 1, 1, 1, 1, 1, 3, 3, 3, 2}, {1, 1, 1, 1, 1, 1, 1, 1, 1, 1, 1, 1, 1, 1, 1, 1, 1, 1, 1, 1, 1, 1, 1, 3, 3, 2}, {0, 0, 1, 1, 1, 1, 1, 1, 1, 1, 1, 1, 1, 1, 1, 1, 1, 1, 1, 2, 0, 0, 0, 0, 0, 0}};
        const int G[32] = {8, 12, 9, 13, 10, 14, 11, 15, 16, 17, 20, 21, 18, 19, 22, 23, 0, 4, 1, 5, 2, 6, 3, 7, 0, 4, 1, 5, 2, 6, 3, 7};
        const int NM = HALO ? 26 : 24;
        n[0] = n[1] = n[2] = NM; n[3] = HALO ? 20 : 16;
        for (int i = 0; i < NM; ++i) f[i] = HALO ? FB[i] : FA[i];
        int vv = 0;
        for (int h = 0; h < 4; ++h) {
            v[h][0] = vv;
            for (int i = 0; i < n[h]; ++i) {
                t[h][i] = HALO ? TB[h][i] : TA[h][i];
                d[h][i + 1] = d[h][i] + (HALO ? DB[h][i] : DA[h][i]);
                vv += HALO ? VB[h][i] : VA[h][i];
                v[h][i + 1] = vv;
            }
        }
        for (int i = 0; i < 32; ++i) { h3[i] = G[i]; s3[i] = i >= 16 && i < 24 ? 0 : 1; }
        for (int i = 3; i < 32; ++i) o3[i + 1] = o3[i] + TOPS;
        int nv = 0, ns = 0;
        for (int k = 0; k < 8 * 14; ++k) {
            const bool top = k % TOPS < 2 || nv == 96;
            if (top && ns < 16 && nv >= 12 * (ns / 2 + 1) && (nv == 96 || k % TOPS == (ns & 1))) { q3[k] = (ns / 2) * 14 + 13 - (ns & 1); ++ns; }
            else { q3[k] = (nv / 12) * 14 + nv % 12; ++nv; }
        }
    }
};
template <bool HALO> DI void qkv_consume(const Ctx& c0, int l, KP kp, unsigned va0 = 0, unsigned vw0 = 0) {
    const Ctx c = fresh(c0);
    if (ST0 && l == 0 && c.wid == 2) STAMP_AT(59);
    LAS char* smem = c.smem;
    const int lane = c.lane, wr = c.wid >> 1, wc = c.wid & 1, fr = lane & 15, fq = lane >> 4;
    const int sw = HALO ? wr : 0;
    f32x4 acc[4][6], ah[2];
#pragma unroll
    for (int mt = 0; mt < 4; ++mt)
#pragma unroll
        for (int nt = 0; nt < 6; ++nt) { acc[mt][nt] = (f32x4){0.f, 0.f, 0.f, 0.f}; asm volatile("" : "+v"(acc[mt][nt])); }
    ah[0] = ah[1] = (f32x4){0.f, 0.f, 0.f, 0.f};
    if (HALO) asm volatile("" : "+v"(ah[0]), "+v"(ah[1]));
    const unsigned aoff0 = (unsigned)(QA_OFF + wr * 8192 + (HALO || l ? lane * 16 : fr * 128 + ((fq ^ (fr & 7)) << 4)));
    const unsigned hoff0 = (unsigned)(QA_OFF + 16384 + lane * 16);
    unsigned xm = HALO || l ? 1024u : 64u;
    asm volatile("" : "+s"(xm));
    static_assert(((QA_OFF | QA_SLOT) & 2047) == 0, "halves");
    const int q4 = fr >> 2, p4 = lane & 3;
    const int fb = (q4 & 2) | ((fq & 1) << 2);
    unsigned boff[2];
#pragma unroll
    for (int cc = 0; cc < 2; ++cc)
        boff[cc] = (unsigned)((8 * fq + q4) * 128 + (((2 * (2 * wc + (cc ^ sw)) + (p4 >> 1)) ^ fb) << 4) + (p4 & 1) * 8);
    bf16x8 xa[2][4], xh[2], wb[2][6];
    auto ldfrag = [&](int set, unsigned pa, unsigned ph, unsigned pb0, unsigned pb1, int ks) {
#pragma unroll
        for (int mt = 0; mt < 4; ++mt) xa[set][mt] = *(const LAS bf16x8*)(lds(pa) + mt * 2048);
        if (HALO) xh[set] = *(const LAS bf16x8*)lds(ph);
#pragma unroll
        for (int nt = 0; nt < 6; ++nt) {
            const LAS char* pb = lds((nt & 1) ? pb1 : pb0) + (nt >> 1) * 8192 + ks * 4096;
            const s16x4 lo = tr_read(pb), hi = tr_read(pb + 512);
            wb[set][nt] = __builtin_shufflevector(lo, hi, 0, 1, 2, 3, 4, 5, 6, 7);
        }
    };
    constexpr int NM = HALO ? 26 : 24, NH = HALO ? 17 : 16;
    auto mm = [&](int set, int i0, int i1) {
#pragma unroll
        for (int ii = i0; ii < i1; ++ii) {
            const int i = !HALO || ii < 8 ? ii : (!SNK ? (ii < 10 ? ii + 16 : ii - 2) : (ii == 8 ? 24 : (ii < 17 ? ii - 1 : (ii == 17 ? 25 : ii - 2))));
            const int nt = i >> 2, mt = SNK && (nt & 1) ? 3 - (i & 3) : i & 3;
            if (i < 16) acc[mt][nt] = MFMA(wb[set][nt], xa[set][mt], acc[mt][nt]);
            else if (i < 24) acc[mt][nt] = MFMA(xa[set][mt], wb[set][nt], acc[mt][nt]);
            else if (i == 24) ah[0] = MFMA(wb[set][2], xh[set], ah[0]);
            else ah[1] = MFMA(xh[set], wb[set][4], ah[1]);
        }
    };
    auto interleave = [&]() {
#pragma unroll
        for (int i = 0; i < NH; ++i) { SGB(0x008, 1); SGB(0x100, 1); }
        SCHED();
    };
    unsigned sa = 0, sb = 0;
    unsigned pa0 = aoff0, pa1 = aoff0 ^ xm, ph0 = hoff0, ph1 = hoff0 ^ xm, pb0 = boff[0], pb1 = boff[1];
    asm volatile("" : "+v"(pa0), "+v"(pa1), "+v"(pb0), "+v"(pb1));
    if (HALO) asm volatile("" : "+v"(ph0), "+v"(ph1));
    constexpr int M_CSH = M_CS + (HALO ? 768 : 0);
    unsigned sa0 = (unsigned)(M_MEAN + 64 + (wr * 64 + fr) * 4), sa1 = (unsigned)(M_MEAN + 64 + wr * 256 + fq * 16);
    unsigned sa2 = (unsigned)(M_CSH + wc * 128 + sw * 64 + fq * 16), sa6 = (unsigned)(M_CSH + 512 + wc * 128 + sw * 64 + fr * 4);
    unsigned sa3 = sa2 ^ 64, sa7 = sa6 ^ 64, sa4 = (unsigned)(M_MEAN + fr * 4), sa5 = (unsigned)(M_MEAN + fq * 16);
    asm volatile("" : "+v"(sa0), "+v"(sa1), "+v"(sa2), "+v"(sa6), "+v"(sa3), "+v"(sa7));
    if (HALO) asm volatile("" : "+v"(sa4), "+v"(sa5));
    const int head = c.jq * 2 + wc;
    auto rso = __builtin_amdgcn_make_buffer_rsrc(HALO ? c.ws + WS_O + (size_t)l * (8u << 20) + (size_t)((head * 16 + c.pq) * 8 + wr * 4) * (1024 << HO)
                                                      : c.ws + WS_KV + (size_t)(l >> 1) * (16u << 20) + (size_t)((c.pq * 32 + head) * 4 + wr) * (4096 << HK), 0, HALO ? 4096 << HO : 12288 << HK, 0x00020000);
    unsigned xrs = (unsigned)(M_XCH + wc * 6144 + wr * 2048), xhs = (unsigned)(M_XCH + wc * 6144 + 8 - wr * 8);
    unsigned b0m = (c.pq == 0 && wr == 0) ? 15u * 16u : 0u;
    asm volatile("" : "+s"(rso));
    if (HALO) asm volatile("" : "+s"(xrs), "+s"(xhs), "+s"(b0m));
    u32x4 cpy[3];
    unsigned cv, vt, cdst;
    const char *cs_, *tw, *cd_;
    int more = !HALO && l == 0 ? KT - 3 - XST - (c.pq & 7) : 0, three = c.jq & 4;
    if (!HALO) {
        static_assert(CPY + TOUCH == 2 && XSP == 1, "extra");
        asm volatile("" : "+s"(kp));
        const int g = c.jq >> 2, n = c.pq * 4 + (c.jq & 3), m = n >> 5, kt = (n >> 1) & 15;
        const char* src = *(const char* const __attribute__((address_space(4)))*)((const __attribute__((address_space(4))) char*)kp + (three ? (m ? 8 : 24) : (m ? 32 : 40)));
        const char* tsr = *(const char* const __attribute__((address_space(4)))*)((const __attribute__((address_space(4))) char*)kp + (c.wid < 3 ? 24 : 40));
        int o1 = kt * (64 * 6144) + (n & 1) * (32 * 6144) + (3 + 4 * (g & 2) - c.jq) * 128 + 2048, o2 = (n & 31) * 65536 + (11 - 4 * g) * 128 - c.pq * 128 * (D_ * 2) + 2048;
        int d1 = (int)WS_CQ + kt * (64 * 6144) + (m * 2 + (g >> 1)) * 24576 + (n & 1) * 4096, d2 = (int)WS_CO + (n & 31) * 65536 + (m * 2 + 1 - (g >> 1)) * 4096;
        asm volatile("" : "+s"(o1), "+s"(o2), "+s"(d1), "+s"(d2));
        cs_ = src + (three ? o1 : o2);
        tw = tsr + (unsigned)((c.wid < 3 ? c.wid : 0) * (2u << 20) + c.bid * 8192);
        cd_ = c.ws + (unsigned)(three ? d1 : d2);
        cv = pick(three, vw0, va0 ^ ((lane & 8) << 1));
        cdst = (unsigned)(c.wid * 1024 + lane * 16);
        vt = (unsigned)(((lane & 7) == 3 ? lane - 1 : lane) * 128);
        asm volatile("" : "+s"(more), "+s"(three), "+s"(cs_), "+s"(tw), "+s"(cd_));
        asm volatile("" : "+v"(cv), "+v"(cdst), "+v"(vt));
        asm volatile("" : "=v"(cpy[0]), "=v"(cpy[1]), "=v"(cpy[2]));
    }
    unsigned hto = HT0 == 2 ? 0u : 0x80000000u;
    const char* W0 = (const char*)kp->wqkv0;
    if (!HALO && HT0) {
        const int n = ((c.pq & 7) * 3 + (c.jq & 3)) * 64 + c.wid * 16 + fr, r3 = (n * 43691) >> 17;
        const int t0 = rot_first(c.pq & 8, c.jq | 3) + 8;
        if (l == 0 && !(c.jq & 4) && (c.jq & 3) != 3 && lane < 16) hto = (unsigned)(((t0 * 64 + r3) & 1023) * 6144 + (n - 3 * r3) * 2048 + (c.jq | 3) * 128);
        asm volatile("" : "+v"(hto));
        asm volatile("" : "+s"(W0));
    }
    if (!HALO && HT0 == 2) { asm volatile("s_mov_b32 m0, %0\n\ts_nop 3\n\tglobal_load_lds_dword %1, %2" ::"s"(M_TCH), "v"(hto), "s"(W0) : "memory"); }
    if (!HALO && !ERL && l == 0) BAR();
    if (ST0 && l == 0 && c.wid == 1) STAMP_AT(44);
    if (!HALO) { if (HT0 == 2) wait_v<1>(); else WAIT_V0(); }
    if (ST0 && l == 0 && c.wid == 1) STAMP_AT(46);
    BAR();
    if (!HALO && HT0 == 1) { if (hto < 0x80000000u) asm volatile("s_mov_b32 m0, %0\n\ts_nop 3\n\tglobal_load_lds_dword %1, %2" ::"s"(M_TCH), "v"(hto), "s"(W0) : "memory"); }
    if (c.wid == 0) STAMP_AT(l * 8 + 0);
    ldfrag(0, pa0, ph0, pb0, pb1, 0);
    SCHED();
    if (ALN) WAIT_L0();
    SCHED();
    int cnt = KT - 3 - more, tv = 0;
    QSTAMP(0);
    auto half0 = [&]() {
        ldfrag(1, pa1, ph1, pb0, pb1, 1);
        mm(0, 0, NH);
        interleave();
        mm(0, NH, NH + 1); SCHED();
        asm volatile("s_addk_i32 %0, %2\n\ts_addk_i32 %1, %3" : "+s"(sa), "+s"(sb) : "i"(QA_SLOT), "i"(QW_SLOT) : "scc"); SCHED();
        mm(0, NH + 1, NH + 2); SCHED();
        asm volatile("s_cmpk_lg_u32 %0, %2\n\ts_cselect_b32 %0, %0, 0\n\ts_cmp_lg_u32 %1, %3\n\ts_cselect_b32 %1, %1, %4" : "+s"(sa), "+s"(sb) : "i"(NQA * QA_SLOT), "i"(3 * QW_SLOT), "i"(QW_TOP) : "scc"); SCHED();
        mm(0, NH + 2, NH + 3); SCHED();
        asm volatile("s_cmp_lg_u32 %0, %2\n\ts_cselect_b32 %0, %0, 0\n\ts_addk_i32 %1, -1" : "+s"(sb), "+s"(cnt) : "i"(QW_TOP + QW_SLOT) : "scc"); SCHED();
        mm(0, NH + 3, NH + 4); SCHED();
        pa0 = aoff0 + sa; pb0 = boff[0] + sb;
        asm volatile("" : "+v"(pa0), "+v"(pb0)); SCHED();
        mm(0, NH + 4, NH + 5); SCHED();
        pb1 = boff[1] + sb; pa1 = pa0 ^ xm;
        asm volatile("" : "+v"(pa1), "+v"(pb1)); SCHED();
        mm(0, NH + 5, NH + 6); SCHED();
        if (HALO) { ph0 = hoff0 + sa; ph1 = ph0 ^ xm; asm volatile("" : "+v"(ph0), "+v"(ph1)); SCHED(); }
        mm(0, NH + 6, NM);
        SCHED();
        WAIT_L0();
        BAR();
        SCHED();
    };
    asm volatile("" : "+s"(sa), "+s"(sb), "+s"(cnt));
    do {
    ALIGN8();
    do {
        half0();
        ldfrag(0, pa0, ph0, pb0, pb1, 0);
        mm(1, 0, NH);
        interleave();
        mm(1, NH, NM - 1);
        SCHED();
        WAIT_L0();
        asm volatile("s_nop 0");
        SCHED();
        mm(1, NM - 1, NM);
        SCHED();
    } while (cnt != 0);
    if (!HALO) {
        asm volatile("s_cmp_eq_u32 %[more], 0\n\ts_cbranch_scc1 .Lex%=\n\ts_mov_b32 m0, %[tch]\n\ts_nop 3\n\tglobal_load_lds_dword %[vt], %[tw] sc1\n\t"
                     "global_load_dwordx4 %[c0], %[v0], %[cs] offset:-2048\n\ts_cmp_eq_u32 %[three], 0\n\ts_cbranch_scc1 .Lex%=\n\t"
                     "global_load_dwordx4 %[c1], %[v0], %[cs]\n\tglobal_load_dwordx4 %[c2], %[v0], %[cs] offset:2048\n.Lex%=:"
                     : [c0] "+v"(cpy[0]), [c1] "+v"(cpy[1]), [c2] "+v"(cpy[2])
                     : [more] "s"(more), [tch] "i"(M_TCH), [vt] "v"(vt), [tw] "s"(tw), [v0] "v"(cv), [cs] "s"(cs_), [three] "s"(three) : "scc", "memory");
        cnt = more; more = 0;
        asm volatile("" : "+s"(cnt), "+s"(more));
        SCHED();
    }
    } while (cnt != 0);
    if (!HALO) {
        asm volatile("s_cmp_lg_u32 %[l], 0\n\ts_cbranch_scc1 .Lex%=\n\ts_waitcnt vmcnt(0)\n\ts_nop 3\n\tglobal_store_dwordx4 %[d], %[c0], %[ws] sc1\n\ts_cmp_eq_u32 %[three], 0\n\ts_cbranch_scc1 .Lex%=\n\t"
                     "v_add_u32 %[d], 0x2000, %[d]\n\tglobal_store_dwordx4 %[d], %[c1], %[ws] sc1\n\tv_add_u32 %[d], 0x2000, %[d]\n\tglobal_store_dwordx4 %[d], %[c2], %[ws] sc1\n.Lex%=:\n\ts_nop 1"
                     : [d] "+v"(cdst) : [c0] "v"(cpy[0]), [c1] "v"(cpy[1]), [c2] "v"(cpy[2]), [l] "s"(l), [ws] "s"(cd_), [three] "s"(three) : "scc", "memory");
    }
    QSTAMP(1);
    FENCE();
    SCHED();

    f32x4 csq[4], m4[4], mh4; float csv[2], mr[4], rr[4], mh;
    auto r1 = [&](unsigned b, int off) { return *(const LAS float*)(lds(b) + off); };
    auto r4_ = [&](unsigned b, int off) { return *(const LAS f32x4*)(lds(b) + off); };
    auto mf = [&](int set, int i) {
        const int nt = i >> 2, mt = i & 3;
        if (i < 16) acc[mt][nt] = MFMA(wb[set][nt], xa[set][mt], acc[mt][nt]);
        else if (i < 24) acc[mt][nt] = MFMA(xa[set][mt], wb[set][nt], acc[mt][nt]);
        else if (i == 24) ah[0] = MFMA(wb[set][2], xh[set], ah[0]);
        else ah[1] = MFMA(xh[set], wb[set][4], ah[1]);
        SCHED();
    };
    auto fold = [&](int i, int e) {
        const int nt = i >> 2, mt = i & 3;
        float t;
        if (i < 16) t = __builtin_fmaf(mr[mt], csq[nt][e], acc[mt][nt][e]);
        else if (i < 24) t = __builtin_fmaf(m4[mt][e], csv[nt - 4], acc[mt][nt][e]);
        else if (i == 24) t = __builtin_fmaf(mh, csq[2][e], ah[0][e]);
        else t = __builtin_fmaf(mh4[e], csv[0], ah[1][e]);
        asm volatile("" : "+v"(t));
        if (i < 24) acc[mt][nt][e] = t; else ah[i - 24][e] = t;
    };
    constexpr Tail<HALO> TL{};
    constexpr int NP = HALO ? 6 : 2, FN = 4 * NM;
    static_assert(TL.v[3][TL.n[3]] == NP + FN + (HALO ? 2 : 0) && KT % NQA == 1 && KT % NQW == 0, "tail");
    s16x4 wl[6], wh[6];
    auto frd = [&](int set, int k, int ao) {
        if (k < 4) xa[set][k] = *(const LAS bf16x8*)(lds(set ? pa1 : pa0) + ao + k * 2048);
        else if (k == 16) xh[set] = *(const LAS bf16x8*)(lds(set ? ph1 : ph0) + ao);
        else {
            const int nt = (k - 4) >> 1;
            const LAS char* pb = lds((nt & 1) ? pb1 : pb0) + (nt >> 1) * 8192 + set * 4096;
            if (k & 1) { wh[nt] = tr_read(pb + 512); wb[set][nt] = __builtin_shufflevector(wl[nt], wh[nt], 0, 1, 2, 3, 4, 5, 6, 7); }
            else wl[nt] = tr_read(pb);
        }
    };
    constexpr int RS = M_RSTD - M_MEAN;
    unsigned vl, vg, tb, xr, xh_, b0a, qb0, qb1;
    f32x4 bprev, bcur, bprev0; u32x4 ps; u32x2 ko; float ninf;
    auto rdop = [&](int h, int k) {
        const int j = k - NH;
        if (h == 0) {
            if (k < 2) { mr[2 * k] = r1(sa0, k * 128); mr[2 * k + 1] = r1(sa0, k * 128 + 64); }
            else if (k < 4) csq[k - 2] = r4_((k & 1) ? sa3 : sa2, 0);
            else if (j < 4) frd(0, k - 4, 2 * QA_SLOT);
            else csq[j - 2] = r4_((j & 1) ? sa3 : sa2, 256);
        } else if (j < 0) frd(h & 1, k, h == 1 ? 2 * QA_SLOT : 0);
        else if (h == 1) {
            if (j < 2) csv[j] = r1(j ? sa7 : sa6, 0);
            else if (HALO && j == 2) mh = r1(sa4, 0);
            else if (HALO && j == 3) mh4 = r4_(sa5, 0);
            else { const int mt = HALO ? 7 - j : j - 2; m4[mt] = r4_(sa1, mt * 64); }
        } else if (h == 2) {
            if (j == 0) m4[2] = r4_(sa1, 128);
            if (j == 1) ko = *(const LAS u32x2*)(lds(tb) + 3072);
            if (j == 2) { rr[0] = r1(sa0, RS); rr[1] = r1(sa0, 64 + RS); }
            if (j == 3) { rr[2] = r1(sa0, 128 + RS); rr[3] = r1(sa0, 192 + RS); }
            if (j >= 4) m4[5 - j] = r4_(sa1, (5 - j) * 64);
        } else {
            if (j == 0) bprev = r4_(tb, 0);
            if (j == 1) bcur = r4_(tb, 1024);
            if (j == 2) ps = *(const LAS u32x4*)(lds(tb) + 2048);
            if (j == 3) bprev0 = r4_(b0a, 0);
        }
    };
    auto vop = [&](int k) {
        if (k == 0) { asm volatile("" : "+v"(boff[0])); qb0 = boff[0] + QW_TOP; asm volatile("" : "+v"(qb0)); }
        else if (k == 1) { asm volatile("" : "+v"(boff[1])); qb1 = boff[1] + QW_TOP; asm volatile("" : "+v"(qb1)); }
        else if (k < NP) {
            if (k == 2) { vl = (unsigned)(lane * 16); asm volatile("" : "+v"(vl)); }
            if (k == 3) { tb = vl + M_TAB; asm volatile("" : "+v"(tb)); }
            if (k == 4) { b0a = tb | b0m; asm volatile("" : "+v"(b0a)); }
            if (k == 5) { xr = xrs + vl; asm volatile("" : "+v"(xr)); }
        } else if (k < NP + FN) fold(TL.f[(k - NP) >> 2], (k - NP) & 3);
        else {
            if (k == NP + FN) { xh_ = xhs + vl; asm volatile("" : "+v"(xh_)); }
            if (k == NP + FN + 1) { ninf = -INFINITY; asm volatile("" : "+v"(ninf)); }
        }
    };
    unsigned wa, wq;
    int wr2 = wr, wc2 = wc;
#pragma unroll
    for (int i = 0; i < NM; ++i) {
        mf(0, TL.t[0][i]);
        if (TL.t[0][i] < 24) asm volatile("" : "+v"(acc[TL.t[0][i] & 3][TL.t[0][i] >> 2])); else asm volatile("" : "+v"(ah[TL.t[0][i] & 1]));
        FENCE();
        if (i < NH) frd(1, i, 0);
        if (!HALO && i == NH + 3) { asm volatile("" : "+s"(wr2), "+s"(wc2)); vl = (unsigned)(lane * 16); wq = (unsigned)(AT_Q + (wc2 * 8 + wr2 * 4) * 1024) + vl; asm volatile("" : "+v"(vl), "+v"(wq)); }
        if (!HALO && i == NH + 4) { asm volatile("" : "+s"(wr2), "+s"(wc2)); wa = (unsigned)(AT_OWN + wc2 * 16384 + wr2 * 4096) + vl; asm volatile("" : "+v"(wa)); }
        if (i == NH + 1) { asm volatile("" : "+v"(boff[0]), "+v"(boff[1])); pb0 = boff[0] + 2 * QW_SLOT; pb1 = boff[1] + 2 * QW_SLOT; asm volatile("" : "+v"(pb0), "+v"(pb1)); }
        if (i == NH + 2) { pa0 = aoff0; pa1 = aoff0 ^ xm; asm volatile("" : "+v"(pa1)); if (HALO) { ph0 = hoff0; ph1 = hoff0 ^ xm; asm volatile("" : "+v"(ph1)); } }
        SCHED();
    }
    WAIT_L0();
    BAR();
    FENCE();
    SCHED();
    auto frags = [&](int h) {
        static_assert(TL.d[0][NM] == NH + 6 && TL.d[1][NM] == NH + (HALO ? 5 : 6) && TL.d[2][NM] == NH + (HALO ? 6 : 0) && TL.d[3][TL.n[3]] == NH + (HALO ? 4 : 0), "reads");
        if (h == 0) __builtin_amdgcn_s_waitcnt(0xC27F);
        if (h == 1) __builtin_amdgcn_s_waitcnt(HALO ? 0xC57F : 0xC67F);
        if (h == 2) __builtin_amdgcn_s_waitcnt(HALO ? 0xC67F : 0xC07F);
        if (h == 3) __builtin_amdgcn_s_waitcnt(HALO ? 0xC47F : 0xC07F);
    };
#pragma unroll
    for (int h = 0; h < 4; ++h) {
        if (h == 2) { pb0 = qb0; pb1 = qb1; }
#pragma unroll
        for (int i = 0; i < TL.n[h]; ++i) {
            if (!(h & 1) && i == TL.n[h] - 1) { frags(h); SCHED(); if (h) QSTAMP(4); }
            mf(~h & 1, TL.t[h][i]);
#pragma unroll
            for (int k = TL.d[h][i]; k < TL.d[h][i + 1]; ++k) { FENCE(); rdop(h, k); }
            SCHED();
#pragma unroll
            for (int k = TL.v[h][i]; k < TL.v[h][i + 1]; ++k) vop(k);
            SCHED();
        }
        if (h & 1) {
            frags(h);
            if (h == 1) QSTAMP(2); else QSTAMP(5);
            BAR();
            FENCE();
            SCHED();
            if (h == 1) QSTAMP(3); else QSTAMP(6);
        }
    }
    if (!HALO) {
        f32x4 r4[4];
        mf(1, 8);
#pragma unroll
        for (int mt = 0; mt < 4; ++mt) rr[mt] = r1(sa0, mt * 64 + RS);
        SCHED();
        mf(1, 12);
#pragma unroll
        for (int mt = 0; mt < 4; ++mt) r4[mt] = r4_(sa1, mt * 64 + RS);
        SCHED();
        mf(1, 9);
        vg = HK ? (unsigned)hv<HK>(lane) : vl;
        if (HK) asm volatile("" : "+v"(vg));
        SCHED();
        u32x4 pk[8];
        auto op = [&](int g, int k) {
            const int ta = g < 4 ? g : (g - 4) & 2, tb = g < 4 ? g : ta + 1, na = g < 4 ? 2 : 4 + (g & 1), nb = g < 4 ? 3 : na;
            f32x4& A = acc[ta][na]; f32x4& Bv = acc[tb][nb];
            if (k < 12) {
                const int h = k / 6, kk = k % 6;
                f32x4& X = h ? Bv : A;
                const int tx = h ? tb : ta;
                if (kk < 4) { float t = X[kk] * (g < 4 ? rr[tx] : r4[tx][kk]); asm volatile("" : "+v"(t)); X[kk] = t; }
                else { unsigned t = cvt_pk_bf16(X[2 * (kk - 4)], X[2 * (kk - 4) + 1]); asm volatile("" : "+v"(t)); pk[g][2 * h + kk - 4] = t; }
            } else if (k == 12) __builtin_amdgcn_raw_buffer_store_b128(pk[g], rso, vg + hp<HK>(g & 3), (g >> 2) * (8192 << HK), 16);
            else *(LAS u32x4*)(lds(wa) + (g >> 2) * 8192 + (g & 3) * 1024) = pk[g];
            SCHED();
        };
        auto pm = [&](int i) {
            if (i < 16) mf(1, TL.h3[i]);
            else if (i < 32) mf(((i - 16) >> 1) & 1, ((i - 16) >> 2) + 4 * (i & 1));
        };
        pm(3);
#pragma unroll
        for (int g = 0; g < 8; ++g) {
#pragma unroll
            for (int k = 0; k < 6; ++k) op(g, k);
            pm(2 * g + 4);
            if (g > 0) op(g - 1, 12);
#pragma unroll
            for (int k = 6; k < 12; ++k) op(g, k);
            pm(2 * g + 5);
            if (g > 0 && g <= LWA) op(g - 1, 13);
        }
        op(7, 12);
        QSTAMP(7);
        u32x4 qk;
        auto qop = [&](int mt, int k) {
            if (k == 0) { rr[mt] *= QSCALE; asm volatile("" : "+v"(rr[mt])); }
            else if (k < 13) {
                const int h = (k - 1) / 6, kk = (k - 1) % 6;
                f32x4& X = acc[mt][h];
                if (kk < 4) { float t = X[kk] * rr[mt]; asm volatile("" : "+v"(t)); X[kk] = t; }
                else { unsigned t = cvt_pk_bf16(X[2 * (kk - 4)], X[2 * (kk - 4) + 1]); asm volatile("" : "+v"(t)); qk[2 * h + kk - 4] = t; }
            } else *(LAS u32x4*)(lds(wq) + mt * 1024) = qk;
            SCHED();
        };
#pragma unroll
        for (int mt = 0; mt < 3; ++mt) {
            pm(20 + 4 * mt); if (3 * mt + LWA < 8) op(3 * mt + LWA, 13); qop(mt, 0); qop(mt, 1); qop(mt, 2);
            pm(21 + 4 * mt); if (3 * mt + 1 + LWA < 8) op(3 * mt + 1 + LWA, 13); qop(mt, 3); qop(mt, 4); qop(mt, 5);
            pm(22 + 4 * mt); if (3 * mt + 2 + LWA < 8) op(3 * mt + 2 + LWA, 13); qop(mt, 6); qop(mt, 7); qop(mt, 8);
            pm(23 + 4 * mt); qop(mt, 9); qop(mt, 10); qop(mt, 11); qop(mt, 12); qop(mt, 13);
        }
        if (c.wid == 0) STAMP_AT(l * 8 + 1);
        QSTAMP(8);
        {
            const u32x2 q0 = pack4(acc[3][0] * (rr[3] * QSCALE)), q1 = pack4(acc[3][1] * (rr[3] * QSCALE));
            *(LAS u32x4*)(lds(wq) + 3 * 1024) = (u32x4){q0[0], q0[1], q1[0], q1[1]};
        }
        QSTAMP(9);
        QSTORE();
        if (K0E) {
            SCHED();
            WAIT_L0();
            BAR();
            FENCE();
            SCHED();
            if (KSP == 4) {
                publish<4, 0>(smem, lane, c.ws, (unsigned)WS_FLAGQ + (c.pq * 16 + c.jq) * FQS, lane == 0, (unsigned)(4 * l + 2));
                if (c.wid == 0) STAMP_AT(l * 8 + 2);
                SCHED();
            }
        }
    } else {
        typedef unsigned u32x10 __attribute__((ext_vector_type(10)));
        u32x4 kp[4], qp[4];
        u32x10 vw[2];
        float ks[2][4], cs[2][4];
        auto pk8 = [&](const f32x4& x, const f32x4& y) { const u32x2 a = pack4(x), b = pack4(y); u32x4 r = {a[0], a[1], b[0], b[1]}; asm volatile("" : "+v"(r)); SCHED(); return r; };
        auto pkv = [&](int mt, int cc) { const u32x2 a = pack4(acc[mt][4 + cc]); vw[cc][2 * mt + 2] = a[0]; vw[cc][2 * mt + 3] = a[1]; };
        auto fr8 = [](const u32x4& x) { return __builtin_bit_cast(bf16x8, x); };
        mf(1, 24);
        unsigned ka0 = ko[0] + wr * 256, ka1 = ko[1] + wr * 256;
        asm volatile("" : "+v"(ka0), "+v"(ka1));
        SCHED();
        mf(1, 25);
#pragma unroll
        for (int mt = 0; mt < 4; ++mt) ks[0][mt] = r1(ka0, mt * 64);
        rr[0] *= QSCALE; asm volatile("" : "+v"(rr[0]));
        SCHED();
        mf(1, 11);
#pragma unroll
        for (int mt = 0; mt < 4; ++mt) ks[1][mt] = r1(ka1, mt * 64);
        rr[1] *= QSCALE; asm volatile("" : "+v"(rr[1]));
        SCHED();
        mf(1, 15);
        rr[2] *= QSCALE; asm volatile("" : "+v"(rr[2]));
        rr[3] *= QSCALE; asm volatile("" : "+v"(rr[3]));
        SCHED();
        mf(1, 19);
        const u32x4 hh = pk8(ah[0], ah[1]);
        mf(1, 23);
        *(LAS u32x2*)lds(xh_) = (u32x2){hh[0], hh[1]}; *(LAS u32x2*)(lds(xh_) + 1024) = (u32x2){hh[2], hh[3]};
        SCHED();
        mf(1, 10);
        kp[3] = pk8(acc[3][2], acc[3][3]);
        mf(1, 14);
        *(LAS u32x4*)(lds(xr) + 2048) = kp[3];
        pkv(3, 0); asm volatile("" : "+v"(vw[0]));
        *(LAS u32x2*)(lds(xr) + 3072) = (u32x2){vw[0][8], vw[0][9]};
        SCHED();
        mf(1, 3);
        pkv(3, 1); asm volatile("" : "+v"(vw[1]));
        *(LAS u32x2*)(lds(xr) + 3072 + 8) = (u32x2){vw[1][8], vw[1][9]};
        SCHED();
        mf(1, 7);
        mf(1, 9);
        kp[2] = pk8(acc[2][2], acc[2][3]);
        mf(1, 13);
        qp[3] = pk8(acc[3][0], acc[3][1]);
        mf(1, 2);
        f32x4 s0[4], s1[4];
        u32x4 pk;
        auto sc = [&](int mt, int h) {
            if (h) { s1[mt] = MFMA(fr8(kp[mt]), fr8(qp[mt]), bcur); asm volatile("" : "+v"(s1[mt])); }
            else { s0[mt] = MFMA(fr8(mt ? kp[mt - (mt > 0)] : pk), fr8(qp[mt]), mt == 0 ? bprev0 : bprev); asm volatile("" : "+v"(s0[mt])); }
            SCHED();
        };
        auto csm = [&](int mt) { cs[0][mt] = ks[0][mt] * rr[mt]; asm volatile("" : "+v"(cs[0][mt])); cs[1][mt] = ks[1][mt] * rr[mt]; asm volatile("" : "+v"(cs[1][mt])); };
        float u[2][4], mx[4], iv[4];
        auto sv = [](float x) { asm volatile("" : "+v"(x)); return x; };
        auto mx2 = [&](float a, float b) { return max3(ninf, a, b); };
        auto pick = [&](int mt, int h) { u[h][mt] = sv(max3(mx2(s0[mt][2 * h], s0[mt][2 * h + 1]), s1[mt][2 * h], s1[mt][2 * h + 1]) * cs[h][mt]); };
        auto vf = [&](int i) { const int mt = 2 - (i >> 2), nt = 4 + (i & 1), set = (i >> 1) & 1; SCHED(); acc[mt][nt] = MFMA(xa[set][mt], wb[set][nt], acc[mt][nt]); asm volatile("" : "+v"(acc[mt][nt])); SCHED(); };
        mf(1, 6);
        kp[1] = pk8(acc[1][2], acc[1][3]);
        mf(1, 8);
        WAIT_L0();
        QSTAMP(7);
        BAR();
        FENCE();
        SCHED();
        QSTAMP(8);
        mf(1, 12);
        {
            const u32x2 k0 = *(const LAS u32x2*)(lds(xr) + 8);
            FENCE(); SCHED();
            const u32x2 k1 = *(const LAS u32x2*)lds(xr);
            FENCE(); SCHED();
            const u32x2 a = *(const LAS u32x2*)(lds(xr) + 1024 + 8);
            FENCE(); SCHED();
            const u32x2 b = *(const LAS u32x2*)(lds(xr) + 1024);
            pk = (u32x4){k0[0], k0[1], k1[0], k1[1]};
            vw[0][0] = a[0]; vw[0][1] = a[1]; vw[1][0] = b[0]; vw[1][1] = b[1];
        }
        SCHED();
        auto sw16 = [](float& a, float& b) { auto r = __builtin_amdgcn_permlane16_swap(__float_as_uint(a), __float_as_uint(b), false, false); a = __uint_as_float(r[0]); b = __uint_as_float(r[1]); };
        auto sw32 = [](float& a, float& b) { auto r = __builtin_amdgcn_permlane32_swap(__float_as_uint(a), __float_as_uint(b), false, false); a = __uint_as_float(r[0]); b = __uint_as_float(r[1]); };
        auto ldr = [&](int i) { };
        auto top = [&](int mt) { mx[mt] = sv(mx2(u[0][mt], u[1][mt])); };
        auto pv1 = [&](int mt, int k) {
            SCHED();
            vw[k >> 1][2 * mt + 2 + (k & 1)] = cvt_pk_bf16(acc[mt][4 + (k >> 1)][2 * (k & 1)], acc[mt][4 + (k >> 1)][2 * (k & 1) + 1]);
            asm volatile("" : "+v"(vw[k >> 1]));
            SCHED();
        };
        auto uk = [&](int mt) { SCHED(); u[0][mt] = sv(u[0][mt] * ks[0][mt]); u[1][mt] = sv(u[1][mt] * ks[1][mt]); SCHED(); };
        mf(1, 1);
        qp[2] = pk8(acc[2][0], acc[2][1]);
        mf(1, 5);
        csm(3); SCHED();
        mf(1, 0);
        kp[0] = pk8(acc[0][2], acc[0][3]);
        mf(1, 4);
        csm(2); SCHED();
        sc(3, 1);
        qp[1] = pk8(acc[1][0], acc[1][1]);
        sc(3, 0);
        csm(1); ldr(0); SCHED();
        sc(2, 1);
        qp[0] = pk8(acc[0][0], acc[0][1]);
        sc(2, 0);
        csm(0); pick(3, 0); SCHED();
        sc(1, 1);
        pick(3, 1); ldr(1); SCHED();
        sc(1, 0);
        pick(2, 0); top(3); SCHED();
        sc(0, 1);
        pick(2, 1); ldr(2); SCHED();
        sc(0, 0);
        if (c.wid == 0) STAMP_AT(l * 8 + 1);
        pick(1, 0); top(2);
        vf(0);
        pick(1, 1); ldr(3);
        vf(1);
        sw16(mx[2], mx[3]);
        pick(0, 0);
        vf(2);
        pick(0, 1); top(1);
        vf(3);
        top(0); mx[2] = sv(mx2(mx[2], mx[3]));
        vf(4);
        sw16(mx[0], mx[1]); mx[0] = sv(mx2(mx[0], mx[1]));
        vf(5); pv1(2, 0);
        sw32(mx[0], mx[2]);
        { const float a = mx[0], b = mx[2]; mx[0] = sv(mx2(a, b)); mx[2] = sv(mx2(b, a)); }
        vf(6); pv1(2, 1);
        sw32(mx[0], mx[2]);
        mx[3] = sv(mx[2]); mx[1] = sv(mx[0]);
        vf(7);
        sw16(mx[2], mx[3]); sw16(mx[0], mx[1]);
        SCHED();
#pragma unroll
        for (int mm = 0; mm < 4; ++mm) { const int mt = 3 - mm; u[0][mt] = sv(u[0][mt] - mx[mt]); u[1][mt] = sv(u[1][mt] - mx[mt]); if (mm == 1) vf(8); }
        vf(9);
#pragma unroll
        for (int mm = 0; mm < 4; ++mm) {
            const int mt = 3 - mm;
            u[0][mt] = sv(__builtin_amdgcn_exp2f(u[0][mt])); u[1][mt] = sv(__builtin_amdgcn_exp2f(u[1][mt]));
            if (mm > 0) iv[mt + 1] = sv(u[0][mt + 1] + u[1][mt + 1]);
            if (mm < 2) vf(10 + mm);
        }
        SCHED();
        sw16(iv[2], iv[3]);
        iv[0] = sv(u[0][0] + u[1][0]);
        iv[2] = sv(iv[2] + iv[3]);
        pv1(2, 2);
        sw16(iv[0], iv[1]); iv[0] = sv(iv[0] + iv[1]);
        uk(3);
        sw32(iv[0], iv[2]);
        iv[0] = sv(iv[0] + iv[2]);
        pv1(2, 3);
        iv[0] = sv(__builtin_amdgcn_rcpf(iv[0]));
        pv1(1, 0);
        iv[2] = sv(iv[0]);
        uk(2);
        sw32(iv[0], iv[2]);
        iv[3] = sv(iv[2]); iv[1] = sv(iv[0]);
        pv1(1, 1);
        sw16(iv[2], iv[3]); sw16(iv[0], iv[1]);
        SCHED();
        f32x4 o[4][2];
        u32x4 pf[4];
        auto nrm = [&](int mt) { u[0][mt] = sv(u[0][mt] * iv[mt]); u[1][mt] = sv(u[1][mt] * iv[mt]); SCHED(); };
        auto prb = [&](int mt) {
            const unsigned p = cvt_pk_bf16(u[0][mt], u[1][mt]);
            pf[mt] = (u32x4){__builtin_amdgcn_perm(0u, p, ps[0]), __builtin_amdgcn_perm(0u, p, ps[1]), __builtin_amdgcn_perm(0u, p, ps[2]), __builtin_amdgcn_perm(0u, p, ps[3])};
            asm volatile("" : "+v"(pf[mt]));
            SCHED();
        };
        auto pvm = [&](int mt, int cc) {
            o[mt][cc] = MFMA(fr8((u32x4){vw[cc][2 * mt], vw[cc][2 * mt + 1], vw[cc][2 * mt + 2], vw[cc][2 * mt + 3]}), fr8(pf[mt]), ((f32x4){0.f, 0.f, 0.f, 0.f}));
            asm volatile("" : "+v"(o[mt][cc]));
            SCHED();
        };
        auto out = [&](int mt) {
            const u32x2 lo = pack4(o[mt][0]), hi = pack4(o[mt][1]);
            __builtin_amdgcn_raw_buffer_store_b128((u32x4){lo[0], lo[1], hi[0], hi[1]}, rso, vg + hp<HO>(mt), 0, 16);
            SCHED();
        };
        vg = (unsigned)hv<HO>(lane);
        asm volatile("" : "+v"(vg));
        nrm(3); nrm(2); prb(3);
        pv1(1, 2); pv1(1, 3);
        pvm(3, 0); prb(2); pvm(3, 1);
        uk(1); uk(0);
        pvm(2, 0); nrm(1); nrm(0); pvm(2, 1);
        prb(1); pv1(0, 0); pv1(0, 1);
        pvm(1, 0); pv1(0, 2); pv1(0, 3); prb(0);
        pvm(1, 1); out(3); pvm(0, 0); out(2); pvm(0, 1); out(1); out(0);
        SCHED();
        QSTAMP(9);
        if (c.wid == 0) STAMP_AT(l * 8 + 2);
        QSTORE();
    }
}

#define MFMA32(a, b, c) __builtin_amdgcn_mfma_f32_32x32x16_bf16(__builtin_bit_cast(bf16x8, a), __builtin_bit_cast(bf16x8, b), (c), 0, 0, 0)
DI float max16(const f32x16& s) {
    float m = max3(s[0], s[1], s[2]), n = max3(s[3], s[4], s[5]);
    m = max3(m, s[6], s[7]); n = max3(n, s[8], s[9]);
    m = max3(m, s[10], s[11]); n = max3(n, s[12], s[13]);
    m = max3(max3(m, s[14], s[15]), n, n);
    auto b = __builtin_amdgcn_permlane32_swap(__float_as_uint(m), __float_as_uint(m), false, false);
    return __builtin_amdgcn_fmed3f(__uint_as_float(b[0]), __uint_as_float(b[1]), INFINITY);
}
template <bool HI> DI void attn_a(const Ctx& c0, int l, const char* Wo, KP kp) {
    const Ctx c = fresh(c0);
    asm volatile("" : "+s"(Wo));
    LAS char* smem = c.smem;
    const int lane = c.lane, w = c.wid;
    constexpr int H = HI ? 0 : 1;
    int qt = HI ? w - 4 : 3 - w;
    asm volatile("" : "+s"(qt));
    const float NEG = -INFINITY;
    int zq = (lane & 31) - 4 * (lane >> 5);
    unsigned lb = (unsigned)(((lane >> 4) & 1) * 1024 + ((lane >> 5) * 16 + (lane & 15)) * 16);
    unsigned qa = (unsigned)(AT_Q + H * 8192 + qt * 2048) + lb, ta = (unsigned)(AT_OWN + H * 16384 - 3 * 2048 + qt * 2048) + lb;
    asm volatile("" : "+v"(lb), "+v"(qa), "+v"(ta));
    auto rd = [&](unsigned base, int off) { return *(const LAS u32x4*)(lds(base) + off); };
    f32x16 ce;
    u32x4 sel;
    unsigned tm = (unsigned)(AT_N + lane * 16);
    auto masks = [&]() {
        if (HI) {
#pragma unroll
            for (int r = 0; r < 16; ++r) ce[r] = 8 * (r >> 2) + (r & 3) <= zq ? 0.f : NEG;
            asm volatile("" : "+v"(ce));
        }
        const unsigned s1 = ((lane >> 4) ^ (lane >> 2)) & 1 ? 0u : 0x3f803f80u;
        sel = (u32x4){s1, s1, s1, s1};
        asm volatile("" : "+v"(sel));
    };
    u64 ep, pa_, kvv, nkv, kact;
    unsigned po, pk_, cnta = M_CNT, one = 1u;
    char* ob;
    const char *kvb, *s1_, *s2_;
    unsigned vk, dst, fo;
    auto setup = [&]() {
        if (HI) {
            const u32x2 e = *(const LAS u32x2*)(smem + M_EP);
            ep = ((u64)(unsigned)__builtin_amdgcn_readfirstlane(e[1]) << 32) | (unsigned)__builtin_amdgcn_readfirstlane(e[0]);
            po = o_post(c);
            pk_ = pick(lane < 2, (unsigned)(WS_KBOX + ((c.pq + 1 + lane) * 16 + c.jq) * 256 + lane * 8), (unsigned)(WS_FLAGQ + (c.pq * 16 + c.jq) * FQS));
            pa_ = c.pq < 14 ? 7ull : (c.pq < 15 ? 5ull : 4ull);
            fo = (unsigned)(WS_KBOX + (c.pq * 16 + c.jq) * 256 + lane * 8);
            asm volatile("" : "+v"(po), "+v"(pk_), "+v"(fo));
            asm volatile("" : "+s"(ep), "+s"(pa_));
        }
        ob = c.ws + WS_O + (size_t)l * (8u << 20) + (size_t)(unsigned)(((c.jq * 2 + H) * 16 + c.pq) * 8192 + qt * 2048);
        kvb = c.ws + WS_KV + (size_t)(unsigned)((l >> 1) * (16u << 20) + c.jq * 32768);
        asm volatile("" : "+s"(ob), "+s"(kvb));
    };
    auto setup2 = [&]() {
        const int p1 = c.pq >= 1 ? c.pq - 1 : c.pq, p2 = c.pq >= 2 ? c.pq - 2 : c.pq;
        s1_ = kvb + (size_t)(unsigned)(p1 * 524288);
        s2_ = kvb + (size_t)(unsigned)(p2 * 524288);
        vk = (unsigned)((w & 3) * 1024 + lane * 16); dst = (unsigned)(AT_N + (w & 3) * 1024);
        asm volatile("" : "+s"(s1_), "+s"(dst));
        if (!HI) asm volatile("" : "+s"(s2_));
        asm volatile("" : "+v"(vk));
    };
    auto arrive = [&](auto cc, unsigned msk, unsigned val) {
        unsigned old = 0;
        if (lane == 0) asm volatile("ds_add_rtn_u32 %0, %1, %2 offset:%3\n\ts_waitcnt lgkmcnt(0)" : "+v"(old) : "v"(cnta), "v"(one), "i"(decltype(cc)::value * 4) : "memory");
        if ((__builtin_amdgcn_readfirstlane(old) & msk) == msk) { if (lane < 16) post(c.ws, po, ep + (4 * l + val)); }
    };
    static_assert((MBOX & 2) && !HO && !HK, "layout");
    int tv = 0;
    auto kvs = [&]() {
        if (!HI) {
            SCHED();
            LSTAMP(2);
            asm volatile("s_mov_b64 exec, 1\n\ts_waitcnt vmcnt(0)\n\tds_add_u32 %0, %1\n\ts_mov_b64 exec, -1" :: "v"(cnta), "v"(one) : "memory");
            LSTAMP(3);
            SCHED();
        }
    };
    asm volatile("" : "+v"(cnta), "+v"(one), "+v"(tm));
    if (HI) {
        masks(); setup(); setup2();
        if (w == 5) {
#pragma unroll
            for (int i = 0; i < 4; ++i) *(LAS f32x4*)(lds(tm) + i * 1024) = (f32x4){ce[4 * i], ce[4 * i + 1], ce[4 * i + 2], ce[4 * i + 3]};
        }
        kvv = ep + (4 * l + 2); nkv = 0 - kvv;
        kact = __builtin_amdgcn_ballot_w64(w == KPW && lane < 2 && lane < c.pq);
        asm volatile("" : "+v"(kvv), "+v"(nkv));
        asm volatile("" : "+s"(kact));
        WAIT_L0();
        WAIT_V0();
        LSTAMP(0);
        if (w == 4) __builtin_amdgcn_s_setprio(HP4);
        BAR();
        FENCE();
    }
    auto kvp = [&]() {
        if (HI && w == 4) {
            __builtin_amdgcn_s_setprio(3);
#pragma unroll 1
            for (unsigned spins = 0; spins < (1u << 24); ++spins) {
                if (__builtin_amdgcn_readfirstlane(*(volatile LAS int*)lds(cnta)) == 8 * l + 4) break;
            }
            if (__builtin_amdgcn_inverse_ballot_w64(pa_)) post(c.ws, pk_, kvv);
            STAMP_AT(l * 8 + 2);
            __builtin_amdgcn_s_setprio(HP4);
        }
        SCHED();
    };
    if (KPP == -1) kvp();
    FENCE();
    SCHED();
    LSTAMP(1);
    u32x4 q0, q1, k0, k1, va, vp, pa, pb;
    f32x16 cm, s0, s1, o;
    f32x4 ls;
    auto stage = [&](f32x16& sc, f32x16& sn, const f32x16& cn, bool pend, bool zero, unsigned kb, int ko, unsigned vb, int vo1, unsigned nb, int vo0) {
        auto ex = [&](int i) { float x = __builtin_amdgcn_exp2f(sc[i]); asm volatile("" : "+v"(x)); sc[i] = x; SCHED(); };
        auto cv = [&](u32x4& p, int j, int i) { unsigned x = cvt_pk_bf16(sc[i], sc[i + 1]); asm volatile("" : "+v"(x)); p[j] = x; SCHED(); };
        sn = MFMA32(k0, q0, cn); SCHED();
        k0 = rd(kb, ko); SCHED();
        ex(0); ex(1); ex(2);
        if (pend) { o = MFMA32(vp, pb, o); SCHED(); }
        ex(3); ex(8); ex(9);
        sn = MFMA32(k1, q1, sn); SCHED();
        k1 = rd(kb, ko + 512); SCHED();
        ex(10); ex(11); cv(pa, 0, 0); cv(pa, 1, 2);
        if (pend) { ls = MFMA(__builtin_bit_cast(bf16x8, sel), __builtin_bit_cast(bf16x8, pb), ls); SCHED(); }
        vp = rd(vb, 8192 + vo1 + 512); SCHED();
        cv(pa, 2, 8); cv(pa, 3, 10); ex(4); ex(5);
        o = MFMA32(va, pa, zero ? (f32x16)(0.f) : o); SCHED();
        va = rd(nb, 8192 + vo0); SCHED();
        ex(6); ex(7); ex(12);
        ls = MFMA(__builtin_bit_cast(bf16x8, sel), __builtin_bit_cast(bf16x8, pa), zero ? (f32x4)(0.f) : ls); SCHED();
        ex(13); ex(14); ex(15); cv(pb, 0, 4); cv(pb, 1, 6); cv(pb, 2, 12); cv(pb, 3, 14);
    };
    auto flush = [&]() {
        ls = MFMA(__builtin_bit_cast(bf16x8, sel), __builtin_bit_cast(bf16x8, pb), ls); SCHED();
        o = MFMA32(vp, pb, o); SCHED();
    };
    float nm;
    {
        if (!HI) {
#pragma unroll
            for (int i = 0; i < 4; ++i) {
                const f32x4 t = *(const LAS f32x4*)(lds(tm) + i * 1024);
                ce[4 * i] = t[0]; ce[4 * i + 1] = t[1]; ce[4 * i + 2] = t[2]; ce[4 * i + 3] = t[3];
            }
        }
        q0 = rd(qa, 0);
        const u32x4 d0 = rd(ta, 6144);
        q1 = rd(qa, 512);
        const u32x4 d1 = rd(ta, 6144 + 512);
        k0 = rd(ta, 4096); k1 = rd(ta, 4096 + 512); va = rd(ta, 8192 + 6144);
        SCHED();
        if (KSP == -1) kvs();
        if (!HI) { masks(); setup(); SCHED(); }
        s0 = MFMA32(d0, q0, ce);
        s0 = MFMA32(d1, q1, s0);
        SCHED();
        if (!HI) { setup2(); SCHED(); }
        if (KSP == 0) kvs();
        if (KPP == 0) kvp();
        nm = -max16(s0);
        asm volatile("" : "+v"(nm));
#pragma unroll
        for (int r = 0; r < 16; ++r) { float t; asm("v_mov_b32 %0, %1" : "=v"(t) : "v"(nm), "i"(r)); cm[r] = t; }
        asm volatile("" : "+v"(cm));
        SCHED();
#pragma unroll
        for (int r = 0; r < 16; ++r) s0[r] = addf(s0[r], nm);
        asm volatile("" : "+v"(s0));
        SCHED();
        if (KSP == 1) kvs();
        if (KPP == 1) kvp();
        stage(s0, s1, cm, false, true, ta, 2048, ta, 6144, ta, 4096);
        if (KSP == 2) kvs();
        if (KPP == 2) kvp();
        if (qt > 0) {
            stage(s1, s0, cm, true, false, ta, 0, ta, 4096, ta, 2048);
            if (KSP == 3) kvs();
            if (qt > 1) {
                stage(s0, s1, cm, true, false, ta, 0, ta, 2048, ta, 0);
                if (qt > 2) stage(s1, s0, cm, true, false, ta, 0, ta, 0, ta, 0);
            }
        } else if (KSP == 3) kvs();
        flush();
    }
    LSTAMP(4);
    f32x16 c2;
    unsigned na, nb;
    auto rso = __builtin_amdgcn_make_buffer_rsrc(ob, 0, 2048, 0x00020000);
    auto starts = [&]() {
        const float n1 = c.pq >= 1 ? nm : NEG, n2 = c.pq >= 2 ? nm : NEG;
        asm volatile("" : "+v"(zq));
#pragma unroll
        for (int r = 0; r < 16; ++r) { cm[r] = n1; c2[r] = n2; ce[r] = 8 * (r >> 2) + (r & 3) <= zq ? NEG : n2; }
        na = (unsigned)(AT_N + H * 32768) + lb; nb = na + (unsigned)(16384 + qt * 2048);
        asm volatile("" : "+v"(cm), "+v"(c2), "+v"(ce), "+v"(na), "+v"(nb));
        asm volatile("" : "+s"(rso));
        SCHED();
    };
    {
        auto piece = [&](int b, int i) { dma16<true>(dst + b * 16384 + i * 4096, vk, ((b & 1) ? s2_ : s1_) + (b >> 1) * 16384 + i * 4096); };
        auto block = [&](int b) {
#pragma unroll
            for (int i = 0; i < 4; ++i) piece(b, i);
        };
        Ask t;
        if (HI) {
            poll_rmw<true>(t, c.ws, fo, __builtin_amdgcn_inverse_ballot_w64(kact), nkv, 62 - 4 * l);
            if (w == KPW) STAMP_AT(32 + l * 8 + 1);
        }
        WAIT_L0();
        LSTAMP(5);
        BAR();
        FENCE();
        LSTAMP(6);
        piece(0, H); piece(0, 2 + H);
        if (HI) {
            starts();
            if (APRIO) __builtin_amdgcn_s_setprio(APRIO);
            WAIT_V0();
            keep(t);
            LSTAMP(7);
            BAR();
            FENCE();
            if (w == 4) STAMP_AT(l * 8 + 3);
        } else {
            if (AB2 >= 0) block(1);
            if (AB2 > 0) block(2);
            starts();
            Ctx c2 = c;
            if (ELO) c2.wid = 0;
            unsigned vw = wo_w_off(c2);
            int qs = c.cw == 3 ? 1024 : 8 * D_ * 2;
            asm volatile("" : "+v"(vw));
            asm volatile("" : "+s"(qs));
            SCHED();
            wait_v<4 * AB2 + 4>();
            LSTAMP(7);
            BAR();
            if (AB2 < 0) block(1);
            if (AB2 <= 0) block(2);
            block(3);
            wait_v<8>();
            BAR();
            if (!ELO) {
                wo_w_issue<WA0, WA1>(c, Wo, vw);
                wait_v<WA1 - WA0>();
            } else WAIT_V0();
            LSTAMP(9);
            BAR();
            FENCE();
            if (!ELO) wo_w_issue<WA1, 26>(c, Wo, vw);
            else {
                const int s0 = w == 0 ? WA0 : (w == 1 ? WA0 + EN0 : WA0 + EN0 + EN1), s1 = w == 0 ? WA0 + EN0 : (w == 1 ? WA0 + EN0 + EN1 : (w == 2 ? 22 : 0));
#pragma unroll 1
                for (int sl = s0; sl < s1; ++sl) {
                    const char* b = Wo + (size_t)(unsigned)((2 * (sl & 15) + (sl >> 4)) * 65536);
#pragma unroll
                    for (int q = 0; q < 4; ++q) dma16<true>(sl * 4096 + q * 1024, vw, b + q * qs);
                }
            }
            if (LSL) { for (int i = 0; i < qt; ++i) __builtin_amdgcn_s_sleep(LSL); }
        }
    }
    SCHED();
    LSTAMP(8);
    {
        const u32x4 d0 = rd(na, 0), d1 = rd(na, 512);
        k0 = rd(na, 2048); k1 = rd(na, 2048 + 512); va = rd(na, 8192);
        SCHED();
        s0 = MFMA32(d0, q0, cm);
        s0 = MFMA32(d1, q1, s0);
        SCHED();
        stage(s0, s1, cm, false, false, na, 4096, na, 0, na, 2048);
        stage(s1, s0, cm, true, false, na, 6144, na, 2048, na, 4096);
        if (HI) { BAR(); FENCE(); SCHED(); }
        stage(s0, s1, cm, true, false, nb, 0, na, 4096, na, 6144);
        if (HI && CPOS == 3) { BAR(); SCHED(); }
        stage(s1, s0, ce, true, false, nb, 2048, na, 6144, nb, 0);
        if (HI && CPOS == 4) { WAIT_L0(); BAR(); SCHED(); }
        stage(s0, s1, c2, true, false, nb, 4096, nb, 0, nb, 2048);
        if (HI && CPOS == 5) { BAR(); SCHED(); }
        if (qt < 3) {
            stage(s1, s0, c2, true, false, nb, 6144, nb, 2048, nb, 4096);
            if (qt < 2) {
                stage(s0, s1, c2, true, false, nb, 0, nb, 4096, nb, 6144);
                if (qt < 1) stage(s1, s0, c2, true, false, nb, 0, nb, 6144, nb, 0);
            }
        }
        flush();
    }
    LSTAMP(11);
    auto out = [&]() {
        const float iv = __builtin_amdgcn_rcpf(ls[0]);
#pragma unroll
        for (int u = 0; u < 2; ++u) {
            u32x4 t;
#pragma unroll
            for (int i = 0; i < 4; ++i) { const int r = 4 * u + (i >> 1) * 8 + (i & 1) * 2; t[i] = cvt_pk_bf16(o[r] * iv, o[r + 1] * iv); }
            __builtin_amdgcn_raw_buffer_store_b128(t, rso, lb + u * 512, 0, 16);
        }
        SCHED();
    };
    out();
    if (__builtin_expect(__any(!(ls[0] < SAFE_SUM)), 0)) {
        auto rk = __builtin_amdgcn_make_buffer_rsrc((void*)(kvb + H * 16384), 0, 16u << 20, 0x00020000);
        const int g0 = c.pq * 4 + qt - 8;
        float m = NEG;
#pragma unroll 1
        for (int ps = 0; ps < 2; ++ps) {
            o = (f32x16)(0.f); ls = (f32x4)(0.f);
#pragma unroll 1
            for (int j = g0 < 0 ? -g0 : 0; j < 9; ++j) {
                const int so = ((g0 + j) >> 2) * 524288 + ((g0 + j) & 3) * 2048;
                u32x4 f[4];
#pragma unroll
                for (int i = 0; i < 4; ++i) f[i] = __builtin_amdgcn_raw_buffer_load_b128(rk, lb + (i & 1) * 512, so + (i >> 1) * 8192, 16);
                f32x16 s;
                const float sb = ps ? -m : 0.f;
                float sa = j == 0 ? NEG : sb, sz = j == 8 ? NEG : sb;
                asm volatile("" : "+v"(zq), "+v"(sa), "+v"(sz));
#pragma unroll
                for (int r = 0; r < 16; ++r) s[r] = 8 * (r >> 2) + (r & 3) <= zq ? sa : sz;
                s = MFMA32(f[0], q0, s);
                s = MFMA32(f[1], q1, s);
                if (ps == 0) m = fmaxf(m, max16(s));
                else {
                    u32x4 p[2];
#pragma unroll
                    for (int r = 0; r < 16; r += 2) p[(r >> 2) & 1][(r >> 3) * 2 + ((r >> 1) & 1)] = cvt_pk_bf16(__builtin_amdgcn_exp2f(s[r]), __builtin_amdgcn_exp2f(s[r + 1]));
#pragma unroll
                    for (int u = 0; u < 2; ++u) {
                        o = MFMA32(f[2 + u], p[u], o);
                        ls = MFMA(__builtin_bit_cast(bf16x8, sel), __builtin_bit_cast(bf16x8, p[u]), ls);
                    }
                }
            }
        }
        __builtin_amdgcn_s_waitcnt(0x0F70);
        WAIT_V0();
        out();
    }
    SCHED();
    if (w == 3 || w == 4) STAMP_AT(32 + l * 8 + 3 + w);
    LSTAMP(12);
    if (HI) {
        WAIT_V0();
        arrive(std::integral_constant<int, 1>{}, 3u, 3u);
        if (w == 4) STAMP_AT(32 + l * 8 + 4);
        LSTAMP(13);
        if (APRIO) __builtin_amdgcn_s_setprio(0);
        if (lane == 0) asm volatile("ds_add_u32 %0, %1 offset:12" :: "v"(cnta), "v"(one) : "memory");
        WAIT_V0();
        arrive(std::integral_constant<int, 2>{}, 7u, 4u);
        if (w == 5 || w == 6) {
            Ctx c2 = c;
            c2.wid = 2 * w - 10;
            unsigned vw = wo_w_off(c2), vw2 = vw + (c.cw == 3 ? 1024u : 8u * D_ * 2);
            asm volatile("" : "+v"(vw), "+v"(vw2));
#pragma unroll 1
            for (unsigned spins = 0; spins < (1u << 22); ++spins) {
                if (__hip_atomic_load((LAS unsigned*)(smem + M_CNT + 12), __ATOMIC_RELAXED, __HIP_MEMORY_SCOPE_WORKGROUP) == (unsigned)(2 * l + 4)) break;
                __builtin_amdgcn_s_sleep(1);
            }
            FENCE();
            wo_w_issue<ELO ? 22 : 26, 30>(c2, Wo, vw);
            c2.wid += 1;
            wo_w_issue<ELO ? 22 : 26, 30>(c2, Wo, vw2);
        }
    }
    LSTAMP(23);
    ASTORE();
}

DI void wo_load(const Ctx& c0, int l, const char* Wn, const char* Wo) {
    const Ctx c = fresh(c0);
    asm volatile("" : "+s"(Wn), "+s"(Wo));
    const u64 ep = lds_epoch(c.smem);
    const unsigned fo = o_find(c);
    if ((CP2 & 1) && l == 0 && c.wid == 4) code_again(c);
    if ((CP2 & 2) && l == 1 && c.wid == 4) code_again(c);
        if (l == 0 && c.wid == 6) {
            const int fr = c.lane & 15, od = fr & 1, j0 = 4 * (c.lane >> 4) + od;
            const bool p0 = j0 > fr, p1 = j0 + 2 > fr;
            const unsigned N = 0xff800000u, Z = 0x0c0c0c0cu, lo = od ? 0x01000c0cu : 0x0c0c0100u, hi = od ? 0x03020c0cu : 0x0c0c0302u;
            LAS u32x4* t = (LAS u32x4*)(c.smem + M_TAB + c.lane * 16);
            t[0] = (u32x4){p0 && !od ? 0u : N, p0 && od ? 0u : N, p1 && !od ? 0u : N, p1 && od ? 0u : N};
            t[64] = (u32x4){!p0 && !od ? 0u : N, !p0 && od ? 0u : N, !p1 && !od ? 0u : N, !p1 && od ? 0u : N};
            t[128] = (u32x4){p0 ? lo : Z, p1 ? hi : Z, p0 ? Z : lo, p1 ? Z : hi};
            t[192] = (u32x4){(unsigned)(M_RSTD + j0 * 4 + (p0 ? 0 : 64)), (unsigned)(M_RSTD + j0 * 4 + 8 + (p1 ? 0 : 64)), 0u, 0u};
        }
    if (CPY && l == 0 && c.wid == CKW) {
#pragma unroll 1
        for (int i = 0; i < 2; ++i) {
            const int j = i ? c.jw : c.jq, g = i ? 2 - 2 * (j >> 3) : 1 + 2 * (j >> 3);
            if ((j & 7) == 3) poll_flags(c.ws, ep, (unsigned)WS_FLAGQ + ((c.lane >> 2) * 16 + 4 * g + (c.lane & 3)) * FQS, true, 2u);
        }
    }
    Ask t, t2;
    if (!(l & 1)) {
        Ctx c2 = c;
        c2.wid = 2 * c.wid - 10;
        unsigned vw = wo_w_off(c2), vw2 = vw + (c.cw == 3 ? 1024u : 8u * D_ * 2);
        asm volatile("" : "+v"(vw), "+v"(vw2));
        poll_rmw(t, c.ws, fo, c.wid == 7 && c.lane < 16, ep + (4 * l + 3), 61 - 4 * l);
        if (c.wid == 7) STAMP_AT(32 + l * 8 + 2);
        BAR();
        if (c.wid == 5 || c.wid == 6) {
            __builtin_amdgcn_s_sleep(W30S);
            wo_w_issue<30, 32>(c2, Wo, vw);
            c2.wid += 1;
            wo_w_issue<30, 32>(c2, Wo, vw2);
        }
        if (c.wid != 7) WAIT_V0();
    } else {
        asm volatile("" : "=v"(t.v[0]), "=v"(t.v[1]), "=v"(t.v[2]), "=v"(t.v[3]));
        WAIT_V0();
    }
    poll_rmw(t2, c.ws, fo, c.wid == 7 && c.lane < 16, ep + (4 * l + 4), 60 - 4 * l);
    if (c.wid == 7) STAMP_AT(32 + l * 8 + 2 + !(l & 1));
    BAR();
    WAIT_V0();
    keep(t); keep(t2);
    if (l == 0) {
        if (c.wid == 4) WARM(WARM_C4);
        if (c.wid == 5) WARM(WARM_C5);
        if (c.wid == 6) WARM(WARM_C6);
        if (c.wid == 7) WARM(WARM_C7);
    }
    if (l == 3 && c.bid == 0 && c.wid == 7) {
#pragma unroll 1
        for (int i = 0; i < 4; ++i) poll_flags(c.ws, ep, (unsigned)WS_FLAGQ + (i * 64 + c.lane) * FQS, true, 1u);
        if (c.lane == 0) __hip_atomic_store((GAS u64*)(c.ws + WS_EPOCH), ep + 64, __ATOMIC_RELAXED, __HIP_MEMORY_SCOPE_AGENT);
    }
    if (RS0 && l == 3) BAR();
}

DI void wo_consume(const bool B, const Ctx& c0, const bf16_t* xin, bf16_t* xout, int l) {
    u32x4 res[2][2];
    const Ctx c = fresh(c0);
    LAS char* smem = c.smem;
    const int lane = c.lane, w = c.wid, fr = lane & 15, fq = lane >> 4;
    const char* obase = c.ws + WS_O + (size_t)l * (8u << 20);
    int ovo = (c.pw * 8 + w * 2) * 1024 + lane * 16;
    asm volatile("" : "+v"(ovo));
    constexpr int NP = WNQ, R = 3;
    static_assert(!HO && !HX && !HS && !RLD && BPR == 1, "layout");
    auto ro = __builtin_amdgcn_make_buffer_rsrc((void*)obase, 0, 4u << 20, 0x00020000);
    asm volatile("" : "+s"(ro));
    u32x4 xa[NP][2];
    s16x4 wf[R][8];
    f32x4 acc[2][4];
    unsigned bw[8];
    int coff, rmo, spr;
    u32x4 on = {0x3f803f80u, 0x3f803f80u, 0x3f803f80u, 0x3f803f80u};
    unsigned sto, xpo, coff1, cnta = M_CNT, one = 1;
    u64 xv, b0, b1, am;
    auto rs = __builtin_amdgcn_make_buffer_rsrc(xout, 0, S_ * D_ * 2, 0x00020000);
    auto rst = __builtin_amdgcn_make_buffer_rsrc(c.ws + WS_STATS + (size_t)((l + 1) & 3) * 524288, 0, 524288, 0x00020000);
    auto prep2 = [&]() {
        sto = fq == (fr >> 2) && l < 3 ? (unsigned)((c.pw * 16 + c.jw) * 1024 + (w * 32 + fr) * 8) : 0x80000000u;
        xpo = x_post(c);
        coff = (int)pick(l == 3, (unsigned)rmo, (unsigned)coff);
        coff1 = coff + (l == 3 ? xrow<0>(0, 16) : 2048);
        spr = l == 3 ? 64 : 1024;
        asm volatile("" : "+s"(spr));
        b0 = __builtin_amdgcn_ballot_w64(fr & 1); b1 = __builtin_amdgcn_ballot_w64(fr & 2); am = __builtin_amdgcn_ballot_w64(x_act(c, !B));
        asm volatile("" : "+v"(on), "+v"(sto), "+v"(xpo), "+v"(coff), "+v"(coff1), "+v"(xv), "+v"(cnta), "+v"(one));
        asm volatile("" : "+s"(rs), "+s"(rst), "+s"(b0), "+s"(b1), "+s"(am));
    };
    auto prep = [&]() {
        int ln = lane;
        asm volatile("" : "+v"(ln));
        const int fr = ln & 15, fq = ln >> 4, q4 = fr >> 2, p4 = ln & 3;
        coff = (c.pw * 16 + c.jw) * 16384 + w * 4096 + (((fq & 1) * 2 + (fq >> 1)) * 16 + fr) * 16;
        rmo = xrow<0>(c.pw, w * 32 + fr) + xcol<0>(c.jw) + (fq & 1) * 32 + (fq >> 1) * 16;
        asm volatile("" : "+v"(coff), "+v"(rmo));
        const int fb = (q4 & 2) | ((fq & 1) << 2);
        const unsigned swp = (B && w >= 2) ? 2048u : 0u;
#pragma unroll
        for (int cc = 0; cc < 4; ++cc) {
            const unsigned b = (unsigned)((4 * fq + q4) * 128 + (((2 * cc + (p4 >> 1)) ^ fb) << 4) + (p4 & 1) * 8);
            bw[2 * cc] = b + swp; bw[2 * cc + 1] = b + (swp ^ 2048u);
            asm volatile("" : "+v"(bw[2 * cc]), "+v"(bw[2 * cc + 1]));
        }
    };
    auto start = [&](int mt, int pr) {
            {
                auto r0 = __builtin_amdgcn_permlane16_swap(res[mt][pr][0], res[mt][pr][2], false, false);
                auto r1 = __builtin_amdgcn_permlane16_swap(res[mt][pr][1], res[mt][pr][3], false, false);
#pragma unroll
                for (int h = 0; h < 2; ++h) {
                    acc[mt][2 * pr + h] = (f32x4){bf_lo(r0[h]), bf_hi(r0[h]), bf_lo(r1[h]), bf_hi(r1[h])};
                    asm volatile("" : "+v"(acc[mt][2 * pr + h]));
                }
            }
    };
    auto rdw = [&](auto ic) {
        constexpr int i = decltype(ic)::value, h = i >> 3, k = i & 7;
        wf[h % R][k] = tr_read(lds(bw[k]) + (h & 15) * 4096);
        if constexpr (h == 15) { bw[k] += 65536; asm volatile("" : "+v"(bw[k])); }
    };
    {
        auto resid = [&]() {
            const unsigned roff = pick(l != 0, (unsigned)coff, (unsigned)rmo);
            const char* xp = (const char*)xin;
#pragma unroll
            for (int i = 0; i < 4; ++i) aload4c(res[i >> 1][i & 1], roff, xp + (i >> 1) * (l ? 2048 : xrow<0>(0, 16)) + (i & 1) * (l ? 1024 : 64));
            SCHED();
        };
        unsigned opo = o_post(c), oca = B ? M_CNT : M_CNT + 8, old = 0, msk = B ? 3u : 7u;
        xv = lds_epoch(smem) + (unsigned)(l + 2);
        u64 ov = xv + (unsigned)(3 * l + 2);
        prep();
        asm volatile("" : "+v"(opo), "+v"(oca), "+v"(ov), "+v"(old), "+v"(one));
        asm volatile("" : "+s"(msk));
        SCHED();
        if (RBP) resid();
        static_for<0, 16>([&](auto ic) { rdw(ic); SCHED(); });
        if (!P2A) prep2();
        SCHED();
        wait_v<RBP ? 4 : 0>();
        if (lane == 0) asm volatile("ds_add_rtn_u32 %0, %1, %2\n\ts_waitcnt lgkmcnt(0)" : "+v"(old) : "v"(oca), "v"(one) : "memory");
        if ((__builtin_amdgcn_readfirstlane(old) & msk) == msk) { if (lane < 16) post(c.ws, opo, ov); }
        if (w == 0) STAMP_AT(l * 8 + 4);
        SCHED();
        if (!RBP) resid();
        if (P2A == 1) prep2();
        SCHED();
        if (w == 3) STAMP_AT(32 + l * 8 + 5);
        BAR();
    }
    FENCE();
    SCHED();
    if (w == 0) STAMP_AT(l * 8 + 5);
    int so = 0;
    auto next = [&](int j) { so += j == 15 ? 131072 - 15 * 262144 : 262144; asm volatile("" : "+s"(so)); };
    auto ldo = [&](int j, int mt) { xa[j % NP][mt] = __builtin_amdgcn_raw_buffer_load_b128(ro, ovo + mt * 1024, so, 16); };
#pragma unroll
    for (int h = 0; h < NP; ++h) {
        ldo(h, 0);
        SCHED();
        if (h > SH0 && h <= SH0 + 2) { start(h - SH0 - 1, 0); SCHED(); }
        ldo(h, 1);
        if (!WAL || h + 1 < NP) next(h);
        SCHED();
        if (P2A >= 2 && h == P2A - 2) { prep2(); SCHED(); }
        if (h == SH0) {
            wait_v<2 * SH0 + 2>();
            asm volatile("" : "+v"(res[0][0]), "+v"(res[0][1]), "+v"(res[1][0]), "+v"(res[1][1]));
        }
        if (h > SH0 && h <= SH0 + 2) start(h - SH0 - 1, 1);
        SCHED();
    }
    int d0 = 262144, d1 = 131072 - 15 * 262144;
    asm volatile("" : "+s"(d0), "+s"(d1));
    static_assert(WEP || !WAL, "last step");
    static_for<0, WEP ? 31 : 32>([&](auto jc) {
        constexpr int j = decltype(jc)::value;
        constexpr int c0 = j < 30 ? 16 + 8 * j : 256, c1 = j < 30 ? c0 + 8 : 256;
        if constexpr (j + NP == 16) {
            static_assert(c0 <= 8 * 20, "weights that landed before the attention");
            if (!B) BAR();
            FENCE();
            asm volatile("" : "+v"(ovo));
            SCHED();
        }
        if constexpr (j == 0 || j + NP == 16) ALIGN8();
        constexpr int nv = WAL == 1 || j == 0 || j + NP > 32 ? 2 * (NP - 1 < 31 - j ? NP - 1 : 31 - j) : 2 * NP - 3;
        __builtin_amdgcn_s_waitcnt(0x0870 | nv);
        const int sp = so;
        static_assert(!RS0 || (RS0 + NP >= 32 && RS0 < 31 && WPRE * QW_SLOT <= (RS0 - 1) * 4096), "R0");
        if constexpr (RS0 && j == RS0) BAR();
        else asm volatile("s_add_i32 %0, %1, %2" : "=&s"(so) : "s"(sp), "s"(j + NP == 16 ? d1 : d0) : "scc");
        SCHED();
        static_for<0, 8>([&](auto mc) {
            constexpr int m = decltype(mc)::value, mt = m >> 2, nt = mt ? 3 - (m & 3) : m & 3;
            const bf16x8 wv = __builtin_shufflevector(wf[j % R][2 * nt], wf[j % R][2 * nt + 1], 0, 1, 2, 3, 4, 5, 6, 7);
            if constexpr (j < 31) asm volatile("v_mfma_f32_16x16x32_bf16 %0, %1, %2, %0" : "+v"(acc[mt][nt]) : "v"(wv), "v"(xa[j % NP][mt]));
            else acc[mt][nt] = MFMA(wv, __builtin_bit_cast(bf16x8, xa[j % NP][mt]), acc[mt][nt]);
            SCHED();
            if constexpr (c0 < c1 && m < 7) { rdw(std::integral_constant<int, c0 + m>{}); SCHED(); }
            if constexpr (c0 < c1 && m == 6) rdw(std::integral_constant<int, c0 + 7>{});
            if constexpr (m == WL1 && j > 0 && j - 1 + NP < 32) xa[(j + NP - 1) % NP][1] = __builtin_amdgcn_raw_buffer_load_b128(ro, ovo + 1024, sp, 16);
            if constexpr (m == WL1 + 4 && j + NP < 32) ldo(j, 0);
            SCHED();
        });
    });
    const bf16x8 ones = __builtin_bit_cast(bf16x8, on);
    u32x4 o[2][2];
    f32x4 sm[2], g[2];
    const f32x4 zero = {0.f, 0.f, 0.f, 0.f};
    auto sel = [&](float a, float b, u64 m) { return __builtin_amdgcn_inverse_ballot_w64(m) ? b : a; };
    auto stats = [&](int mt) {
        float qa = sel(g[mt][0], g[mt][1], b0), qb = sel(g[mt][2], g[mt][3], b0);
        asm volatile("" : "+v"(qa), "+v"(qb));
        SCHED();
        if (WEP) asm volatile("" :: "v"(sm[1][1]), "v"(sm[1][2]), "v"(sm[1][3]), "v"(o[1][0]), "v"(o[1][1]), "v"(on));
        __builtin_amdgcn_raw_buffer_store_b64((u32x2){__float_as_uint(sm[mt][0]), __float_as_uint(sel(qa, qb, b1))}, rst, sto + mt * 128, 0, 16);
        SCHED();
    };
    u32x2 pk[2];
    auto last = [&](int mt, int nt) {
        acc[mt][nt] = MFMA(__builtin_shufflevector(wf[31 % R][2 * nt], wf[31 % R][2 * nt + 1], 0, 1, 2, 3, 4, 5, 6, 7), __builtin_bit_cast(bf16x8, xa[31 % NP][mt]), acc[mt][nt]);
        SCHED();
    };
    auto cv = [&](int mt, int pr) {
        pk[0] = pack4(acc[mt][2 * pr]); pk[1] = pack4(acc[mt][2 * pr + 1]);
        asm volatile("" : "+v"(pk[0]), "+v"(pk[1]));
        SCHED();
    };
    auto st = [&](int mt, int pr) {
        o[mt][pr] = widen(pk[0], pk[1]);
        __builtin_amdgcn_raw_buffer_store_b128(o[mt][pr], rs, mt ? coff1 : coff, pr ? spr : 0, 16);
        SCHED();
    };
    auto gq = [&](int mt, int pr, bool f) { const bf16x8 of = __builtin_bit_cast(bf16x8, o[mt][pr]); g[mt] = MFMA(of, of, f ? zero : g[mt]); SCHED(); };
    auto gs = [&](int mt, int pr, bool f) { sm[mt] = MFMA(ones, __builtin_bit_cast(bf16x8, o[mt][pr]), f ? zero : sm[mt]); SCHED(); };
    WAIT_L0();
    last(0, 0); last(0, 1); last(0, 2); last(0, 3);
    last(1, 3); cv(0, 0);
    last(1, 2); st(0, 0);
    last(1, 1); cv(0, 1);
    last(1, 0); st(0, 1);
    if (w == 0) STAMP_AT(l * 8 + 6);
    gq(0, 0, true); cv(1, 1);
    gs(0, 0, true); st(1, 1);
    gq(0, 1, false); cv(1, 0);
    gs(0, 1, false); st(1, 0);
    gq(1, 1, true);
    gs(1, 1, true);
    gq(1, 0, false);
    gs(1, 0, false);
    stats(0);
    stats(1);
    if (l < 3) {
        unsigned old = 0;
        if (!RS0 || RN1 > RN0) BAR();
        wait_v<XE2>();
        if (lane == 0) asm volatile("ds_add_rtn_u32 %0, %1, %2\n\ts_waitcnt lgkmcnt(0)" : "+v"(old) : "v"(cnta), "v"(one) : "memory");
        if ((__builtin_amdgcn_readfirstlane(old) & 3) == 3) {
            if (__builtin_amdgcn_inverse_ballot_w64(am)) post(c.ws, xpo, xv);
        }
    }
    if (w == 0) STAMP_AT(l * 8 + 7);
}

__global__ __launch_bounds__(512, 2) __attribute__((aligned(4096))) void k_main(Params P) {
    extern __shared__ __attribute__((aligned(16))) char smem_[];
    KP kp = (KP)__builtin_amdgcn_kernarg_segment_ptr();
    const char *x0, *w0, *wo;
    Ctx c;
    asm volatile("s_load_dwordx2 %0, %4, 0x0\n\ts_load_dwordx2 %1, %4, 0x8\n\ts_load_dwordx2 %2, %4, 0x10\n\ts_load_dwordx2 %3, %4, 0x20" : "=&s"(x0), "=&s"(w0), "=&s"(c.ws), "=&s"(wo) : "s"(kp));
    asm volatile(".macro PADC id\n" PADPFX "\\id:\n.if " PADS "\ns_cbranch_execz .Lpad_ret\ns_nop 0\n.endif\n.endm\n"
                 "s_branch .Lpad_on\n.Lpad_ret:\n\ts_setpc_b64 vcc\n.Lpad_on:");
    c.smem = (LAS char*)smem_;
    c.lane = threadIdx.x & 63;
    c.wid = __builtin_amdgcn_readfirstlane(threadIdx.x >> 6);
    c.bid = blockIdx.x;
    const int xcd = c.bid & 7, idx = c.bid >> 3;
    c.pq = ((xcd % (16 / QXM)) * QXM + (idx % QXM)) ^ PQX; c.jq = ((xcd / (16 / QXM)) * (32 / QXM) + (idx / QXM)) ^ JQX;
    const bool loader = c.wid >= 4;
    if (CPF == 1 ? !loader : c.wid == 4) {
        const char* pc; int len;
        asm volatile("s_getpc_b64 %0\n.Lcpf_a:\n\ts_mov_b32 %1, .Lcpf_end-.Lcpf_a-4" : "=s"(pc), "=s"(len));
#pragma unroll
        for (int i = 0; i < (CPF == 1 ? 2 : 1); ++i) {
            const int o = min((CPF == 1 ? (i * 4 + c.wid) * 64 + c.lane : (CPF == 2 ? (idx & 7) * 64 + c.lane : idx * 16 + (c.lane & 15))) * 128, len);
            asm volatile("s_mov_b32 m0, %0\n\ts_nop 3\n\tglobal_load_lds_dword %1, %2" ::"s"(M_TCH), "v"(o), "s"(pc) : "memory");
        }
    }
    asm volatile("s_cmp_lt_u32 %0, 5\n\ts_cbranch_scc1 11\n\ts_mov_b64 exec, 0\n\ts_cmp_lg_u32 %0, 5\n\ts_cbranch_scc1 1\n\t" WARM_A5 "s_cmp_lg_u32 %0, 6\n\ts_cbranch_scc1 1\n\t" WARM_A6
                 "s_cmp_lg_u32 %0, 7\n\ts_cbranch_scc1 1\n\t" WARM_A7 "s_mov_b64 exec, -1" :: "s"(c.wid) : "vcc", "scc");
    unsigned va, vw;
    {
        const int w = c.wid, lr = c.lane >> 3, lc = c.lane & 7;
        int t0 = first_tile(first_order(c.pq, c.jq), 0);
        asm volatile("" : "+s"(t0));
        va = (unsigned)((c.pq * 128 + 8 * (w & 3) + lr) * (D_ * 2) + ((lc ^ lr) << 4));
        vw = (unsigned)((8 * (w & 3) + lr) * 6144 + c.jq * 128 + ((lc ^ ((lr & 2) | ((w & 1) << 2))) << 4));
        asm volatile("" : "+v"(va), "+v"(vw));
        asm volatile("s_waitcnt lgkmcnt(0)" : "+s"(x0), "+s"(w0), "+s"(c.ws), "+s"(wo));
        if (ST0 && c.wid == 3) STAMP_AT(41);
        auto stage = [&](int s, int h0, int h1, bool a) {
            const char* xb = x0 + (unsigned)(t0 * 128);
            const char* wb = w0 + (unsigned)(t0 * (64 * 6144));
            for (int h = h0; h <= h1; ++h) {
                if (a) {
#pragma unroll
                    for (int j = 0; j < 2; ++j) dma16<false>(QA_OFF + s * QA_SLOT + (j * 8 + h * 4 + (w & 3)) * 1024, va, xb + (j * 64 + h * 32) * (D_ * 2));
                }
#pragma unroll
                for (int j = 0; j < 3; ++j) dma16<false>(qw_slot(s) + (j * 8 + h * 4 + (w & 3)) * 1024, vw, wb + h * (32 * 6144) + j * 2048);
            }
        };
        if (ENT == 0) stage(0, w >> 2, w >> 2, true);
        else if (!loader) stage(0, 0, 1, true);
        if (ST0 && c.wid == 1) STAMP_AT(43);
        if (w == 0 && c.lane < 2) touch(M_EP, (unsigned)(WS_EPOCH + c.lane * 4), c.ws);
        if (w == 1 && c.lane < 5) *(LAS unsigned*)(c.smem + (c.lane < 4 ? M_CNT + c.lane * 4 : M_GO)) = 0u;
    }
    asm volatile("" : "+s"(kp), "+s"(c.bid), "+s"(c.ws));
    static_assert(WXM == 4 && ALT == 1, "map");
    c.pw = ((c.pq & 8) ^ 8) + ((c.bid >> 1) & 1) * 4 + ((c.bid >> 3) & 3); c.jw = (c.bid & 4) * 2 + (c.bid >> 5); c.cw = 0;
    if (PADN >= 0) asm volatile("s_branch .Lpad_end\n\ts_nop 0\n\t.fill %c0, 4, 0xbf800000\n.Lpad_end:" :: "i"(PADN * 64));
    const char* xi = x0;
    auto step = [&](int l) {
        asm volatile("" : "+s"(kp), "+s"(c.ws), "+s"(c.jw), "+s"(c.bid));
        c.pq ^= 8; c.pw ^= 8; c.cw = CPY ? c.jw & 7 : 0;
        xi = c.ws + WS_X + (unsigned)(l * (8u << 20));
        wo = (const char*)((l & 1) ? kp->wo0 : kp->wo1);
        if (c.cw == 3) wo = c.ws + WS_CO + (((l & 1) ? 2 : 0) + (c.jw >> 3)) * 4096;
    };
    if (loader) {
        MARK("lf");
        PADC();
        qkv_load<false, true>(c, xi, w0, wo, 0, kp, va, vw);
#pragma unroll 1
        for (int l = 0;;) {
            asm volatile("" : "+s"(l));
            MARK("ah");
            if (!(l & 1)) attn_a<true>(c, l, wo, kp);
            MARK("wl");
            wo_load(c, l, nullptr, wo);
            if (l == 3) break;
            step(l);
            ++l;
            if (l == 2) { MARK("la"); qkv_load<false, false>(c, xi, (const char*)kp->wqkv0, wo, l); }
            else { MARK("lb"); qkv_load<true, false>(c, xi, (const char*)kp->wqkv1, wo, l); }
        }
    } else {
        bf16_t* xo = (bf16_t*)(c.ws + WS_X);
#pragma unroll 1
        for (int l = 0; l < 4; ++l) {
            if (l & 1) { MARK("cb"); qkv_consume<true>(c, l, kp); }
            else {
                MARK("ca");
                qkv_consume<false>(c, l, kp, va, vw);
                MARK("al");
                attn_a<false>(c, l, wo, kp);
            }
            MARK("wc");
            wo_consume(l & 1, c, (const bf16_t*)xi, xo, l);
            MARK("we");
            step(l);
            xo = l == 2 ? kp->out : (bf16_t*)(c.ws + WS_X + (unsigned)((l + 1) * (8u << 20)));
            asm volatile("" : "=v"(va), "=v"(vw));
        }
    }
    asm volatile(".Lcpf_end:");
}

extern "C" void kernel_launch(void* const* d_in, const int* in_sizes, int n_in, void* d_out, int out_size, void* d_ws, size_t ws_size,
                              hipStream_t stream) {
    Params P;
    P.x0 = (const bf16_t*)d_in[0];
    P.wqkv0 = (const bf16_t*)d_in[1];
    P.wo0 = (const bf16_t*)d_in[2];
    P.wqkv1 = (const bf16_t*)d_in[4];
    P.wo1 = (const bf16_t*)d_in[5];
    P.out = (bf16_t*)d_out;
    P.ws = (char*)d_ws;
    (void)hipFuncSetAttribute((const void*)k_main, hipFuncAttributeMaxDynamicSharedMemorySize, LDS_TOTAL);
    k_main<<<256, 512, LDS_TOTAL, stream>>>(P);
}
```

```cpp
#include <hip/hip_runtime.h>
#include <stdint.h>
#include <type_traits>

typedef unsigned short bf16_t;
typedef short bf16x8 __attribute__((ext_vector_type(8)));
typedef short s16x4 __attribute__((ext_vector_type(4)));
typedef float f32x4 __attribute__((ext_vector_type(4)));
typedef float f32x2 __attribute__((ext_vector_type(2)));
typedef float f32x16 __attribute__((ext_vector_type(16)));
typedef unsigned u32x4 __attribute__((ext_vector_type(4)));
typedef unsigned u32x2 __attribute__((ext_vector_type(2)));
#define DI __device__ __forceinline__
#define LAS __attribute__((address_space(3)))
#define GAS __attribute__((address_space(1)))
#define STAMP 0

constexpr int S_ = 2048, D_ = 1024, KT = 16;
constexpr float LN_EPS = 1e-5f;
constexpr float QSCALE = 0.17677669529663687f * 1.4426950408889634f;

#define FQO 0
#define XBO 524288
#define HX 0
#define HO 0
#define HK 0
#define HS 0
#define HM 1
constexpr size_t WS_FLAGQ = FQO;
constexpr size_t WS_FLAGW = 98304;
constexpr size_t WS_EPOCH = 65536;
constexpr size_t WS_CSUM = 131072;
constexpr size_t WS_XBOX = XBO;
constexpr size_t WS_OBOX = 655360;
constexpr size_t WS_KBOX = 720896;
constexpr size_t WS_STATS = 1u << 20;
constexpr size_t WS_X = 4u << 20;
constexpr size_t WS_O = 96u << 20;
constexpr size_t WS_KV = 28u << 20;
constexpr size_t WS_CQ = 36u << 20;
constexpr size_t WS_CO = 42u << 20;
constexpr size_t WS_STAMP = 64u << 20;

template <int H> DI constexpr int hp(int c) { return !H ? c * 1024 : (c >> 2) * 8192 + (HM == 1 ? (c & 3) * 256 : (c & 1) * 128 + (c & 2) * 256); }
template <int H> DI constexpr int hl(int u) { return !H ? u : (HM == 1 ? u + (u & ~127) : u + (u & ~255)); }
template <int H> DI constexpr int hv(int lane) { return !H ? lane * 16 : (lane >> 3) * 1024 + (lane & 7) * 16; }
template <int H> DI constexpr int xrow(int p, int r) { return H == 0 ? (p * 128 + r) * 2048 : (H == 1 ? (p * 128 + r) * 4096 : (H == 2 ? p * 524288 + r * 256 : p * 262144 + r * 128)); }
template <int H> DI constexpr int xcol(int t) { return H == 0 ? t * 128 : (H == 1 ? hl<1>(t * 128) : (H == 2 ? t * 32768 : t * 16384)); }
#define WTC 2
#define WT0 1
#define SPR 1
#define SL 4
#define R4X 0
#define R5X 0
#define TOPJ 0x2000
#define WV0 1
#define C2X 13
#define FQS 256
#define SNK 1
#define WBE 1
#define WAE 1
#define EXF 0
#define RW0 6
#define KSP 0
#define K0E 1
#define TOPS 4
#define BPR 1
#define RLD 0
#define RCK 1
#define CPF 2
#define WN1 20
#define WLO 3
#define FPJ 2
#define WPJ 2
#define WB14 0
#define WB15 5
#define RESP 1
#define WT13 2
#define RPOS 2
#define CPOLL 1
#define ENT 1
#define MBOX 3
#define OBS 256
#define WTCH 0
#define WTDA 0
#define WTDB 4
#define WTG 0
#define PQX 8
#define ALT 1
#define RESA 1
#define CKP 1
#define ALN 1
#define EPH 2
#define NQA_ 3
#define NQW_ 4
#define WNP 8
#define LPRIO 3
#define TOUCH 1
#define TSTEP 13
#define XS_ 9
#define WROT 0
#define WROTD 1
#define JQX 0
#define ROTM 0
#define ROT0 0
#define WPRE 3
#define PSLEEP 1
#define XPRE 2
#define WE0 1
#define EPJ 3
#define TW0 5
#define TW1 99
#define KPJ 1
#define PKSUB 0
#define APRIO 1
#define QIL 0
#define QXM 8
#define WNQ 5
#define WRG 2
#define WMG 1
#define WB2 1
#define WBS 1
#define WEP 1
#define WAL 2
#define WL1 0
#define RS0 27
#define RN0 6
#define RN1 6
#define AH0 0
#define RSL 1
#define WXM 4
#define CPOS 5
#define LSL 0
#define SAFE_SUM 0x1p60f
#define AB2 0
#define W30S 8
#define STC 0
#define NOR 1
#define NRH 0
#define CPY 1
#define AGG 1
#define HT0 0
#define DEF 0
#define XST 2
#define CKW 4
#define XSP 1
#define EPOLL 1
#define ORD 2
#define RSLP 1
#define APOLL 1
#define APOST 1
#define ASLP 2
#define APIPE 4
#define RBP 0
#define P2A 1
#define SH0 2
#define SL1 0
#define KPW 4
#define LWA 7
#define WA0 12
#define WA1 22
#define KPP 1
#define HP4 1
#define TWQ 1
#define CP2 0
#define PADN -1
#define ERL 6
#define ESL 0
#define DM 0
#define DS 0
#define ELO 0
#define EN0 3
#define EN1 2
#define ST0 0
#define NC 2
#define NCM 1
#define NCB 4
#define AD1 3
#define AD2 1
#define AD3 4
#define WB0 3
#define WBN 6
#define TWO 1
#define XSB 10
#define TL0 0
#define TL1 12
#define FDM 1
#define TSP 0
#define XE1 0
#define XE2 0
#define OTB 0
#define OTJ 8
#define OTA 0
constexpr int QA_SLOT = 18432, NQA = NQA_, QW_SLOT = 24576, NQW = NQW_, QA_OFF = 3 * QW_SLOT, QW_TOP = 131072;
DI constexpr int qw_slot(int s) { return s == 3 ? QW_TOP : s * QW_SLOT; }
constexpr int AT_N = 90112, AT_OWN = AT_N + 32768, AT_Q = 73728;
constexpr int MISC = 155648, M_MEAN = MISC, M_RSTD = MISC + 640, M_CS = MISC + 1280, M_CNT = MISC + 2816, M_EP = MISC + 2832, M_GO = MISC + 2848, M_TCH = MISC + 3072, M_TAB = MISC + 4096, M_XCH = 131072;
constexpr int LDS_TOTAL = 163840;
static_assert(QA_OFF + NQA * QA_SLOT <= QW_TOP && QW_TOP + QW_SLOT <= MISC && AT_N + 65536 <= MISC && AT_Q + 16384 <= AT_N, "lds");

DI unsigned cvt_pk_bf16(float lo, float hi) {
    typedef __bf16 bf2 __attribute__((ext_vector_type(2)));
    bf2 r; r[0] = (__bf16)lo; r[1] = (__bf16)hi;
    return __builtin_bit_cast(unsigned, r);
}
DI u32x2 pack4(const f32x4& v) { return (u32x2){cvt_pk_bf16(v[0], v[1]), cvt_pk_bf16(v[2], v[3])}; }
DI bf16x8 join(const u32x2& a, const u32x2& b) { return __builtin_bit_cast(bf16x8, (u32x4){a[0], a[1], b[0], b[1]}); }
DI float bf_lo(unsigned u) { return __uint_as_float(u << 16); }
DI float bf_hi(unsigned u) { return __uint_as_float(u & 0xffff0000u); }
DI float red_max4(float x) {
    auto a = __builtin_amdgcn_permlane16_swap(__float_as_uint(x), __float_as_uint(x), false, false);
    x = fmaxf(__uint_as_float(a[0]), __uint_as_float(a[1]));
    auto b = __builtin_amdgcn_permlane32_swap(__float_as_uint(x), __float_as_uint(x), false, false);
    return fmaxf(__uint_as_float(b[0]), __uint_as_float(b[1]));
}
DI float red_sum4(float x) {
    auto a = __builtin_amdgcn_permlane16_swap(__float_as_uint(x), __float_as_uint(x), false, false);
    x = __uint_as_float(a[0]) + __uint_as_float(a[1]);
    auto b = __builtin_amdgcn_permlane32_swap(__float_as_uint(x), __float_as_uint(x), false, false);
    return __uint_as_float(b[0]) + __uint_as_float(b[1]);
}
DI u32x4 widen(const u32x2& a, const u32x2& b) {
    auto r0 = __builtin_amdgcn_permlane16_swap(a[0], b[0], false, false);
    auto r1 = __builtin_amdgcn_permlane16_swap(a[1], b[1], false, false);
    return (u32x4){r0[0], r1[0], r0[1], r1[1]};
}
#define MFMA(a, b, c) __builtin_amdgcn_mfma_f32_16x16x32_bf16((a), (b), (c), 0, 0, 0)
#define WAIT_L0() __builtin_amdgcn_s_waitcnt(0xC07F)
#define WAIT_V0() asm volatile("s_waitcnt vmcnt(0)" ::: "memory")
#define BAR() __builtin_amdgcn_s_barrier()
#define PADS "0"
#define PADPFX ".Lc"
#define PADC() asm volatile("PADC %=" ::)
#define MARK(n) asm volatile(PADPFX "m" n ":")
#define CALL(n) "s_call_b64 vcc, " PADPFX #n "\n\t"
#define NOCALL "s_nop 0\n\t"
#define WARM(list) asm volatile("s_mov_b64 exec, 0\n\t" list "s_mov_b64 exec, -1" ::: "vcc")
#define WARM_A5 NOCALL
#define WARM_A6 NOCALL
#define WARM_A7 NOCALL
#define WARM_B0(X) X(NOCALL, NOCALL, NOCALL, NOCALL)
#define WARM_B1(X) X(NOCALL, NOCALL, NOCALL, NOCALL)
#define WARM_B2(X) X(NOCALL, NOCALL, NOCALL, NOCALL)
#define WARM_B3(X) X(NOCALL, NOCALL, NOCALL, NOCALL)
#define WARM_B4(X) X(NOCALL, NOCALL, NOCALL, NOCALL)
#define WARM_B5(X) X(NOCALL, NOCALL, NOCALL, NOCALL)
#define WARM_B6(X) X(NOCALL, NOCALL, NOCALL, NOCALL)
#define WARM_B7(X) X(NOCALL, NOCALL, NOCALL, NOCALL)
#define WARM_C4 NOCALL NOCALL
#define WARM_C5 WARM_C4
#define WARM_C6 WARM_C4
#define WARM_C7 WARM_C4
template <int I> DI void warm_b(int lw) {
#define WB_(a0, a1, a2, a3) \
    asm volatile("s_mov_b64 exec, 0\n\ts_cmp_lg_u32 %0, 0\n\ts_cbranch_scc1 1\n\t" a0 "s_cmp_lg_u32 %0, 1\n\ts_cbranch_scc1 1\n\t" a1 \
                 "s_cmp_lg_u32 %0, 2\n\ts_cbranch_scc1 1\n\t" a2 "s_cmp_lg_u32 %0, 3\n\ts_cbranch_scc1 1\n\t" a3 "s_mov_b64 exec, -1" :: "s"(lw) : "vcc", "scc")
    if constexpr (I == 0) WARM_B0(WB_);
    if constexpr (I == 1) WARM_B1(WB_);
    if constexpr (I == 2) WARM_B2(WB_);
    if constexpr (I == 3) WARM_B3(WB_);
    if constexpr (I == 4) WARM_B4(WB_);
    if constexpr (I == 5) WARM_B5(WB_);
    if constexpr (I == 6) WARM_B6(WB_);
    if constexpr (I == 7) WARM_B7(WB_);
}
#define SCHED0() __builtin_amdgcn_sched_barrier(0)
#define SCHED() do { SCHED0(); PADC(); SCHED0(); } while (0)
template <int N> DI void wait_v() { asm volatile("PADC %=\n\ts_waitcnt vmcnt(%0)" ::"i"(N) : "memory"); }
template <int I, int N, class F> DI void static_for(F&& f) { if constexpr (I < N) { f(std::integral_constant<int, I>{}); static_for<I + 1, N>(f); } }

template <bool SC1> DI void dma16(unsigned lds, unsigned voff, const char* sbase) {
    if (SC1) asm volatile("PADC %=\n\ts_mov_b32 m0, %0\n\ts_nop 3\n\tglobal_load_lds_dwordx4 %1, %2 sc1" ::"s"(lds), "v"(voff), "s"(sbase) : "memory");
    else asm volatile("PADC %=\n\ts_mov_b32 m0, %0\n\ts_nop 3\n\tglobal_load_lds_dwordx4 %1, %2" ::"s"(lds), "v"(voff), "s"(sbase) : "memory");
}
DI void touch(unsigned lds, unsigned voff, const char* sbase) {
    asm volatile("s_mov_b32 m0, %0\n\ts_nop 3\n\tglobal_load_lds_dword %1, %2 sc1" ::"s"(lds), "v"(voff), "s"(sbase) : "memory");
}
DI s16x4 tr_read(const LAS char* p) {
    typedef short v4i16_t __attribute__((ext_vector_type(4)));
    return __builtin_bit_cast(s16x4, __builtin_amdgcn_ds_read_tr16_b64_v4i16((LAS v4i16_t*)p));
}
DI LAS char* lds(unsigned off) { return (LAS char*)(uintptr_t)off; }
DI unsigned long long rstamp() { unsigned long long t; asm volatile("s_memrealtime %0\n\ts_waitcnt lgkmcnt(0)" : "=s"(t)::"memory"); return t; }

template <int NAI, int NWI, bool FIRST> struct Sched {
    int w[KT];
    constexpr Sched() : w{} {
        int endA[KT] = {}, endW[KT] = {};
        int n = 0;
        if (FIRST) {
            if (ENT == 0) { n += 2; endA[0] = n; n += 3; endW[0] = n; }
            n += NAI; endA[1] = n; n += NWI; endW[1] = n;
            if (ERL >= 5) { n += NAI; endA[2] = n; }
            if (ERL == 2 || ERL == 3 || ERL == 5) { n += NWI; endW[2] = n; }
        } else {
            for (int s = 0; s < WPRE; ++s) { n += NWI; endW[s] = n; }
            for (int s = 0; s < 2; ++s) { n += NAI; endA[s] = n; }
        }
        for (int kt = 0; kt < KT; ++kt) {
            const int need = endA[kt] > endW[kt] ? endA[kt] : endW[kt];
            w[kt] = (n - need) > 63 ? 63 : (n - need);
            if (kt + 2 < KT && !(FIRST && ERL >= 5 && kt == 0)) { n += NAI; endA[kt + 2] = n; }
            if (FIRST && (ERL == 4 || ERL == 6) && kt == 0) { n += NWI; endW[2] = n; }
            if (FIRST && ERL < 2 && kt < 5) {
                if (kt < 4) { n += NWI; endW[kt + 2] = n; }
                if (kt >= 3) n += NWI / 2;
                if (kt == 4) { endW[6] = n; n += NWI; endW[7] = n; }
                continue;
            }
            if (!FIRST && WPRE == 2 && kt < 2) { n += NWI; endW[kt + 2] = n; }
            if ((FIRST || WPRE == 3) && kt == 0) { n += NWI; endW[3] = n; }
            if (kt >= 1 && kt + 3 < KT) { n += NWI; endW[kt + 3] = n; }
            if (kt == 13) n += WT13;
            if (kt == 14) n += (NAI == 5 || WA0 > 12 ? 12 : WA0) - WT13 + (NAI == 5 ? WB14 : 0);
        }
    }
};

struct Params {
    const bf16_t* x0;
    const bf16_t* wqkv0;
    char* ws;
    const bf16_t* wqkv1;
    const bf16_t* wo0;
    const bf16_t* wo1;
    bf16_t* out;
};
typedef const __attribute__((address_space(4))) Params* KP;
DI int rot_first(int pq, int jq) {
    const int pr = 2 * (pq & 7) + (pq >> 3);
    if (ROT0 == 6) return 0;
    if (ROT0 == 7) return (pq & 1) * 8;
    if (ROT0 == 8) return (pq & 3) * 4;
    if (ROT0 == 9) return pq;
    if (ROT0 == 4) return (pq + (jq ^ 3)) & 15;
    if (ROT0 == 5) return (pr + (jq ^ 3)) & 15;
    return (ROT0 == 0 ? pq + jq : (ROT0 == 1 ? pr + jq : (ROT0 == 2 ? pr + 4 * (jq & 3) + (jq >> 2) : pq + 4 * (jq & 3) + (jq >> 2)))) & 15;
}

struct Order { int base, r, re; };
DI Order first_order(int pq, int jq) { Order o; o.base = (pq & NCM) * (16 / (NCM + 1)) + NCB; o.r = (rot_first(pq, jq) - o.base) & 15; o.re = o.r > NC ? o.r : NC; return o; }
DI int first_tile(const Order& o, int s) {
    const int v = o.re + s - NC;
    if (s < NC) { int a = NC + o.base, b = s + o.base; if (NC - s == 1) asm volatile("" : "+s"(a)); return (o.r == s ? a : b) & 15; }
    return ((s == NC ? o.r : (v > 15 ? v - (16 - NC) : v)) + o.base) & 15;
}

typedef unsigned long long u64;
DI u64 lds_epoch(LAS char* smem) { return *(const LAS u64*)(smem + M_EP); }
DI bool reached(u64 v, u64 ep, unsigned target) { return (v - ep - target) < (u64)(64u - target); }
DI void poll_flags(const char* ws, u64 ep, unsigned off, bool active, unsigned target) {
    const GAS u64* f = (const GAS u64*)(ws + off);
#pragma unroll 1
    for (unsigned spins = 0; spins < (1u << 22); ++spins) {
        const u64 v = __hip_atomic_load(f, __ATOMIC_RELAXED, __HIP_MEMORY_SCOPE_AGENT);
        if (__all(!active || reached(v, ep, target))) break;
        __builtin_amdgcn_s_sleep(PSLEEP);
    }
    asm volatile("" ::: "memory");
}
struct Ask { u64 v[4]; };
DI void keep(Ask& t) { asm volatile("" : "+v"(t.v[0]), "+v"(t.v[1]), "+v"(t.v[2]), "+v"(t.v[3])); }
#define ASK(r) "global_atomic_or_x2 " r ", %[off], %[z], %[ws] sc0 sc1\n\t"
#define CHK(r) "s_waitcnt vmcnt(%[n])\n\tv_lshl_add_u64 %[d], " r ", 0, %[nb]\n\tv_cmp_gt_u64 vcc, %[span], %[d]\n\ts_cmp_eq_u64 vcc, exec\n\ts_cbranch_scc1 .Lpe%=\n\ts_sleep %[sl]\n\t"
template <bool NEG = false> DI void poll_rmw(Ask& t, const char* ws, unsigned off, bool act, u64 base, unsigned span) {
    u64 z, d, nb = NEG ? base : 0 - base, sp = span;
    unsigned cnt;
    asm volatile("" : "=v"(t.v[0]), "=v"(t.v[1]), "=v"(t.v[2]), "=v"(t.v[3]));
    if (act) {
        if (APIPE == 4)
            asm volatile("v_mov_b64 %[z], 0\n\ts_mov_b32 %[cnt], 0x80000\n\ts_nop 3\n\t" ASK("%[a]") "s_sleep %[sl]\n\t" ASK("%[b]") "s_sleep %[sl]\n\t" ASK("%[c]") "s_sleep %[sl]\n"
                         ".Lpl%=:\n\t" ASK("%[e]") CHK("%[a]") ASK("%[a]") CHK("%[b]") ASK("%[b]") CHK("%[c]") ASK("%[c]") CHK("%[e]")
                         "s_sub_u32 %[cnt], %[cnt], 1\n\ts_cbranch_scc0 .Lpl%=\n.Lpe%=:"
                         : [a] "+v"(t.v[0]), [b] "+v"(t.v[1]), [c] "+v"(t.v[2]), [e] "+v"(t.v[3]), [d] "=&v"(d), [z] "=&v"(z), [cnt] "=&s"(cnt)
                         : [off] "v"(off), [ws] "s"(ws), [nb] "v"(nb), [span] "s"(sp), [sl] "i"(ASLP), [n] "i"(3) : "vcc", "scc", "memory");
        else if (APIPE == 2)
            asm volatile("v_mov_b64 %[z], 0\n\ts_mov_b32 %[cnt], 0x100000\n\ts_nop 3\n\t" ASK("%[a]") "s_sleep %[sl]\n"
                         ".Lpl%=:\n\t" ASK("%[b]") CHK("%[a]") ASK("%[a]") CHK("%[b]")
                         "s_sub_u32 %[cnt], %[cnt], 1\n\ts_cbranch_scc0 .Lpl%=\n.Lpe%=:"
                         : [a] "+v"(t.v[0]), [b] "+v"(t.v[1]), [d] "=&v"(d), [z] "=&v"(z), [cnt] "=&s"(cnt)
                         : [off] "v"(off), [ws] "s"(ws), [nb] "v"(nb), [span] "s"(sp), [sl] "i"(ASLP), [n] "i"(1) : "vcc", "scc", "memory");
        else
            asm volatile("v_mov_b64 %[z], 0\n\ts_mov_b32 %[cnt], 0x200000\n\ts_nop 3\n"
                         ".Lpl%=:\n\t" ASK("%[a]") CHK("%[a]")
                         "s_sub_u32 %[cnt], %[cnt], 1\n\ts_cbranch_scc0 .Lpl%=\n.Lpe%=:"
                         : [a] "+v"(t.v[0]), [d] "=&v"(d), [z] "=&v"(z), [cnt] "=&s"(cnt)
                         : [off] "v"(off), [ws] "s"(ws), [nb] "v"(nb), [span] "s"(sp), [sl] "i"(ASLP), [n] "i"(0) : "vcc", "scc", "memory");
    }
}
DI void post(char* ws, unsigned off, u64 v) {
    if (APOST) asm volatile("s_nop 4\n\tglobal_atomic_swap_x2 %0, %1, %2 sc1" :: "v"(off), "v"(v), "s"(ws) : "memory");
    else __hip_atomic_store((GAS u64*)(ws + off), v, __ATOMIC_RELAXED, __HIP_MEMORY_SCOPE_AGENT);
}
template <int NW, int CNT, int NV = 0> DI void publish(LAS char* smem, int lane, char* ws, unsigned off, bool act, unsigned val) {
    const u64 ep = lds_epoch(smem) + val;
    unsigned old = 0;
    asm volatile("" :: "v"(ep), "v"(off));
    wait_v<NV>();
    if (lane == 0) asm volatile("ds_add_rtn_u32 %0, %1, %2\n\ts_waitcnt lgkmcnt(0)" : "+v"(old) : "v"(M_CNT + CNT * 4), "v"(1u) : "memory");
    if ((__builtin_amdgcn_readfirstlane(old) & (NW - 1)) == NW - 1) {
        if (act) post(ws, off, ep);
    }
}
typedef __bf16 bf16x2_t __attribute__((ext_vector_type(2)));
DI float dot2(unsigned a, unsigned b, float c) { return __builtin_amdgcn_fdot2_f32_bf16(__builtin_bit_cast(bf16x2_t, a), __builtin_bit_cast(bf16x2_t, b), c, false); }
DI u32x2 tile_stats(const u32x4& a, const u32x4& b) {
    float s = 0.f, q = 0.f;
#pragma unroll
    for (int j = 0; j < 4; ++j) { s = dot2(a[j], 0x3f803f80u, s); q = dot2(a[j], a[j], q); }
#pragma unroll
    for (int j = 0; j < 4; ++j) { s = dot2(b[j], 0x3f803f80u, s); q = dot2(b[j], b[j], q); }
    return (u32x2){__float_as_uint(red_sum4(s)), __float_as_uint(red_sum4(q))};
}

struct Ctx {
    LAS char* smem;
    char* ws;
    int lane, wid;
    int pq, jq;
    int pw, jw;
    int bid;
    int cw;
};
DI Ctx fresh(const Ctx& c0) {
    Ctx c = c0;
    PADC();
    asm volatile("" : "+v"(c.lane));
    asm volatile("" : "+s"(c.wid), "+s"(c.pq), "+s"(c.jq), "+s"(c.pw), "+s"(c.jw), "+s"(c.ws), "+s"(c.cw));
    return c;
}
DI unsigned o_post(const Ctx& c) { return (MBOX & 2) ? (unsigned)WS_OBOX + (c.pq * 16 + (c.lane & 15)) * OBS + c.jq * 8 : (unsigned)WS_FLAGQ + (c.pq * 16 + c.jq) * FQS; }
DI bool o_act(const Ctx& c) { return c.lane < ((MBOX & 2) ? 16 : 1); }
DI unsigned o_find(const Ctx& c) { return (MBOX & 2) ? (unsigned)WS_OBOX + (c.pw * 16 + c.jw) * OBS + (c.lane & 15) * 8 : (unsigned)WS_FLAGQ + (c.pw * 16 + (c.lane & 15)) * FQS; }
DI unsigned x_post(const Ctx& c) { return (MBOX & 1) ? (unsigned)WS_XBOX + (c.pw * 16 + (c.lane & 31)) * (2 * FQS) + (c.lane & 16) * (FQS / 16) + c.jw * 8 : (unsigned)WS_FLAGW + (c.pw * 16 + c.jw) * 128; }
DI bool x_act(const Ctx& c, bool halo) { return c.lane < ((MBOX & 1) ? (halo && c.pw < 15 ? 32 : 16) : 1); }
DI unsigned x_find(const Ctx& c) {
    const int pp = c.pq - (c.lane >> 4);
    return (MBOX & 1) ? (unsigned)WS_XBOX + (c.pq * 16 + c.jq) * (2 * FQS) + (c.lane & 16) * (FQS / 16) + (c.lane & 15) * 8 : (unsigned)WS_FLAGW + ((pp < 0 ? 0 : pp) * 16 + (c.lane & 15)) * 128;
}
#define STAMP_AT(slot) do { } while (0)

#define LSTAMP(i) do { } while (0)
#define LSTORE(ph) do { } while (0)
#define LMARK() do { } while (0)
#define LPUT(i) do { } while (0)
#define ASTORE() do { } while (0)
#define QSTAMP(i) do { } while (0)
#define QSTORE() do { } while (0)
#define FENCE() asm volatile("PADC %=" ::: "memory")
#define ALIGN8() do { if (ALN) { asm volatile(".p2align 3"); SCHED(); } } while (0)
DI float addf(float a, float b) { float r; asm("v_add_f32 %0, %1, %2" : "=v"(r) : "v"(a), "v"(b)); return r; }
DI f32x4 add4x4(const u32x4& a, const u32x4& b, const u32x4& c, const u32x4& d) {
    f32x4 r;
#pragma unroll
    for (int i = 0; i < 4; ++i) r[i] = addf(addf(__uint_as_float(a[i]), __uint_as_float(b[i])), addf(__uint_as_float(c[i]), __uint_as_float(d[i])));
    asm volatile("s_nop 1" : "+v"(r));
    return r;
}
DI float fold16(float a, float b) {
    auto r = __builtin_amdgcn_permlane16_swap(__float_as_uint(a), __float_as_uint(b), false, false);
    float x = __uint_as_float(r[0]) + __uint_as_float(r[1]);
    asm volatile("PADC %=" : "+v"(x));
    return x;
}
DI float fold32(float a, float b) {
    auto r = __builtin_amdgcn_permlane32_swap(__float_as_uint(a), __float_as_uint(b), false, false);
    float x = __uint_as_float(r[0]) + __uint_as_float(r[1]);
    asm volatile("PADC %=" : "+v"(x));
    return x;
}
DI void aload4(u32x4& d, unsigned voff, const char* sb) { asm volatile("s_nop 4\n\tglobal_load_dwordx4 %0, %1, %2" : "=v"(d) : "v"(voff), "s"(sb) : "memory"); }
DI void aload4p(u32x4& d, unsigned voff, const char* sb) { asm volatile("s_nop 4\n\tglobal_load_dwordx4 %0, %1, %2" : "+v"(d) : "v"(voff), "s"(sb) : "memory"); }
DI void aload4c(u32x4& d, unsigned voff, const char* sb) { asm volatile("s_nop 4\n\tglobal_load_dwordx4 %0, %1, %2 sc1" : "=v"(d) : "v"(voff), "s"(sb) : "memory"); }
DI void aload2c(u32x2& d, unsigned voff, const char* sb) { asm volatile("s_nop 4\n\tglobal_load_dwordx2 %0, %1, %2 sc1" : "=v"(d) : "v"(voff), "s"(sb) : "memory"); }
DI unsigned wo_src(int q, int lane, int jw) { return (unsigned)((8 * q + (lane >> 3)) * (D_ * 2) + jw * 128 + (((lane & 7) ^ ((lane >> 3) & 6)) << 4)); }
DI unsigned pick(bool f, unsigned a, unsigned b) { asm volatile("PADC %=" : "+v"(a), "+v"(b)); return f ? a : b; }
DI unsigned wo_w_off(const Ctx& c) { return pick(c.cw == 3, (unsigned)((c.wid & 3) * 1024 + c.lane * 16), wo_src(c.wid & 3, c.lane, c.jw)); }
DI unsigned wq_src(int ib, int lane, int jq) {
    const int seg = ib >> 3, ii = ib & 7, lr = lane >> 3;
    return (unsigned)((8 * ii + lr) * 6144 + seg * 2048 + jq * 128 + (((lane & 7) ^ ((lr & 2) | ((ii & 1) << 2))) << 4));
}
template <int S0, int S1> DI void wo_w_issue(const Ctx& c, const char* W, unsigned vw) {
    const int lw = c.wid & 3;
#pragma unroll
    for (int s = S0; s < S1; ++s) dma16<CPY != 0>(s * 4096 + lw * 1024, vw, W + (size_t)((2 * (s & 15) + (s >> 4)) * 32) * (D_ * 2));
}

DI void code_again(const Ctx& c) {
    const char* pc; int len, back;
    asm volatile("s_getpc_b64 %0\n.Lcpb%=:\n\ts_mov_b32 %2, .Lcpb%=-.Lcpf_a\n\ts_mov_b32 %1, .Lcpf_end-.Lcpf_a-4" : "=s"(pc), "=s"(len), "=s"(back));
    pc -= back;
    const int o = min((((c.bid >> 3) & 7) * 64 + c.lane) * 128, len);
    asm volatile("s_mov_b32 m0, %0\n\ts_nop 3\n\tglobal_load_lds_dword %1, %2" ::"s"(M_TCH), "v"(o), "s"(pc) : "memory");
}
template <bool HALO, bool FIRST> DI void qkv_load(const Ctx& c0, const char* xin, const char* W, const char* Wo, int l, KP kp = nullptr, unsigned va0 = 0, unsigned vw0 = 0) {
    const Ctx c = fresh(c0);
    asm volatile("" : "+s"(xin), "+s"(W), "+s"(Wo));
    LAS char* smem = c.smem;
    constexpr int NAI = HALO ? 5 : 4, NWI = 6, XS = FIRST ? XS_ : XSB, NR = HALO ? 144 : 128;
    constexpr Sched<NAI, NWI, FIRST> SC{};
    static_assert(NQA == 3 && NQW == 4, "schedule");
    const int lw = c.wid - 4, lane = c.lane, lr = lane >> 3, lc = lane & 7, fr = lane & 15, fq = lane >> 4;
    const int row0 = c.pq * 128;
    const int pr = (ROTM == 1 || (ROTM == 2 && (c.jq & 7) == 3)) ? 2 * (c.pq & 7) + (c.pq >> 3) : (ROTM == 3 ? (c.pq & 7) + ((c.pq & 7) >> 1) + (c.pq & 8) : c.pq);
    const bool cq_ = CPY && !FIRST && (c.jq & 7) == 3;
    if (cq_) W = c.ws + WS_CQ + ((HALO ? 0 : 2) + (c.jq >> 3)) * 24576;
    asm volatile("" : "+s"(W));
    const bool nor = !FIRST && l > 1 && (NOR == 1 || (NOR == 2 && (c.jq & 7) == 3) || (NOR == 3 && (c.jq & 7) != 3));
    const int rot = FIRST ? rot_first(c.pq, c.jq) : (nor ? (c.pq & 8) * NRH : pr & 15);
    Order od = first_order(c.pq, c.jq);
    if (FIRST) asm volatile("" : "+s"(od.base), "+s"(od.r), "+s"(od.re));
    auto tile = [&](int s) { return FIRST ? first_tile(od, s) : (s + rot) & 15; };
    constexpr int SP = FIRST ? NC : 0;
    unsigned va[5], vw[NWI];
#pragma unroll
    for (int j = 0; j < 4; ++j) va[j] = (unsigned)(FIRST ? va0 + j * 32 * (D_ * 2) : c.pq * 262144 + (j * 4 + lw) * 1024 + lane * 16);
    if (HALO) va[4] = (unsigned)((c.pq > 0 ? (c.pq - 1) * 262144 + 14336 : 0) + lw * 512 + lane * 16);
#pragma unroll
    for (int j = 0; j < NWI; ++j) vw[j] = FIRST ? vw0 + (j & 1) * (32 * 6144) + (j >> 1) * 2048 : pick(cq_, (unsigned)((j * 4 + lw) * 1024 + lane * 16), wq_src(j * 4 + lw, lane, c.jq));
    auto issueA = [&](int s, int slot) {
        const char* b = xin + (unsigned)(tile(s) * (FIRST ? 128 : 16384));
#pragma unroll
        for (int j = 0; j < 4; ++j) dma16<true>(QA_OFF + slot * QA_SLOT + (j * 4 + lw) * 1024, va[j], b);
        if (HALO) { if (lane < 32) dma16<true>(QA_OFF + slot * QA_SLOT + 16384 + lw * 512, va[4], b); }
    };
    auto issueW = [&](int s, int slot, int j0 = 0, int j1 = NWI) {
        const char* b = W + (unsigned)(tile(s) * (64 * 6144));
#pragma unroll
        for (int j = j0; j < j1; ++j) dma16<CPY && !FIRST>(qw_slot(slot) + (j * 4 + lw) * 1024, vw[j], b);
    };
    auto ahead = [&]() {
        const int r3 = (lane * 43) >> 7;
        const unsigned o = (unsigned)((((rot - (c.pq & 7) + 10) * 64 + (c.pq & 7) * 48 + lw * 12 + r3) & 1023) * 6144 + (lane - 3 * r3) * 2048 + c.jq * 128);
        if (lane < 36 && !nor && !cq_ && (WTC != 2 || (c.jq & 7) == 3)) asm volatile("s_mov_b32 m0, %0\n\ts_nop 3\n\tglobal_load_lds_dword %1, %2" ::"s"(M_TCH), "v"(o), "s"(W) : "memory");
    };
    if (ST0 && FIRST && lw == 1) STAMP_AT(47);
    if (FIRST && ERL) {
        SCHED();
        if (ESL) __builtin_amdgcn_s_sleep(ESL);
        issueA(1, 1); issueW(1, 1);
        SCHED();
    }
    const bool docs = FIRST || (HALO && l == 1);
    const int t = lw * 64 + lane;
    static_assert(MBOX & 1, "residual flag");
    unsigned foff, ftg, so, ho, fo_, co, l3, l4, vwo, wt[3], cso, xt, xt1, xso, tp, tl;
    u32x4 on;
    auto addr1 = [&]() {
        int lane = c.lane;
        asm volatile("" : "+v"(lane));
        const int fr = lane & 15, fq = lane >> 4;
        const int ph = c.pq > 0 ? c.pq - 1 : 0;
        foff = pick(lane != 32, (unsigned)WS_FLAGQ + pick(lane < 16, c.pq * 16 + lane, pick(lane < 32, fr * 16 + c.jq, ph * 16 + 14 + (lane & 1))) * FQS, (unsigned)WS_XBOX + (c.pw * 16 + c.jw) * (2 * FQS) + c.jw * 8);
        ftg = lane != 32 ? (unsigned)(4 * l + 1) : (unsigned)(l + 1);
        so = FIRST ? (unsigned)((c.pq * 16 + fq * 4) * 1024 + (lw * 32 + fr * 2) * 8) : (unsigned)((c.pq * 16 + (lane >> 2)) * 1024 + (c.jq * 8 + lw * 2 + (lane & 1)) * 8);
        ho = FDM ? (unsigned)(262144 + ((lw * 36 + lane < 16 ? ph * 128 + 128 : c.pq * 128) + lw * 36 + lane - 16) * 8)
                 : (unsigned)(262144 + pick(lane < 32, c.pq * 128 + lw * 32 + lane, ph * 128 + 112 + lw * 4 + (lane & 3)) * 8);
        fo_ = pick(lane < 2, (unsigned)(262144 + (c.pq * 128 + c.jq * 8 + lw * 2 + lane) * 8), 0x80000000u);
        const int cq = lw * 12 + (fr < 12 ? fr : 11);
        co = (unsigned)((fq * 4 * 3072 + (cq >> 4) * 1024 + c.jq * 64 + (cq & 15) * 4) * 4);
        l3 = (unsigned)(M_CS + (HALO ? 768 : 0) + (cq * 4 + fq) * 4);
        l4 = (unsigned)(M_MEAN + pick(lane < 32, 16 + lw * 32 + (FIRST ? fr * 2 + fq : lane), lw * 4 + (lane & 3)) * 4);
        asm volatile("" : "+v"(foff), "+v"(ftg), "+v"(so), "+v"(co), "+v"(l3), "+v"(l4));
        if (!FIRST) asm volatile("" : "+v"(ho), "+v"(fo_));
        SCHED();
    };
    auto addr2 = [&]() {
        int lane = c.lane;
        asm volatile("" : "+v"(lane));
        const int fr = lane & 15, fq = lane >> 4;
        const int q4 = fr >> 2, p4 = lane & 3, fb = (q4 & 2) | ((fq & 1) << 2);
#pragma unroll
        for (int i = 0; i < 3; ++i) {
            const int T = 3 * lw + i;
            wt[i] = (unsigned)(qw_slot(SP % NQW) + (T >> 2) * 8192 + (8 * fq + q4) * 128 + (((2 * (T & 3) + (p4 >> 1)) ^ fb) << 4) + (p4 & 1) * 8);
        }
        const int T = 3 * lw + fq;
        cso = (unsigned)((((HALO ? 16 : 0) + rot) * 3072 + (T >> 2) * 1024 + c.jq * 64 + (T & 3) * 16 + fr) * 4);
        xt = (unsigned)(QA_OFF + (SP % NQA) * QA_SLOT + (lw * 32 + fr) * 128 + ((fq ^ (fr & 7)) << 4));
        xt1 = xt ^ 64;
        xso = (unsigned)hl<HS>((c.pq * 16 + rot) * 1024 + (lw * 32 + fr) * 8);
        on = (u32x4){0x3f803f80u, 0x3f803f80u, 0x3f803f80u, 0x3f803f80u};
        if (FIRST || HALO) asm volatile("" : "+v"(wt[0]), "+v"(wt[1]), "+v"(wt[2]), "+v"(cso), "+v"(on));
        if (FIRST) asm volatile("" : "+v"(xt), "+v"(xt1), "+v"(xso));
        SCHED();
    };
    auto addr3 = [&]() {
        Ctx c2 = c;
        asm volatile("" : "+v"(c2.lane));
        asm volatile("" : "+s"(c2.bid));
        vwo = wo_w_off(c2);
        tp = (unsigned)(pick(c2.lane < 30, (c2.bid >> 3) * 30 + c2.lane, (c2.bid >> 3) * 16 + c2.lane - 30 + (int)(WS_O >> 16)) * 65536 + 65536 - 128);
        tl = (unsigned)(lw == 1 ? ((c2.bid >> 3) * 2 + c2.lane) * 65536 : c2.bid * 8192 + (TWO ? c2.lane * 128 : 0));
        asm volatile("" : "+v"(vwo));
        if (FIRST) asm volatile("" : "+v"(tp), "+v"(tl));
        SCHED();
    };
    if (!FIRST || AD1 < 0) addr1();
    if (FIRST && ERL >= 5) { issueA(2, 2); SCHED(); }
    if (FIRST && (ERL == 2 || ERL == 5)) { issueW(2, 2); SCHED(); }
    if (!FIRST || AD2 < 0) addr2();
    if (!FIRST || AD3 < 0) addr3();
#pragma unroll
    for (int j = 0; j < NAI; ++j) asm volatile("" : "+v"(va[j]));
#pragma unroll
    for (int j = 0; j < NWI; ++j) asm volatile("" : "+v"(vw[j]));
    SCHED();
    int tv = 0; unsigned long long lt = 0;
    if (FIRST) {
        LSTAMP(55);
    } else {
        static_assert(CPY == 1 && RS0 > 0 && RN0 == NWI && WPRE >= 3 && (FIRST || SC.w[0] == NAI), "order");
        unsigned vx[2], xfo = (unsigned)WS_XBOX + (c.pq * 16 + c.jq) * (2 * FQS) + (HALO && c.pq > 0 ? (lane & 16) * (FQS / 16) : 0) + fr * 8, go = M_GO;
        u64 xv = lds_epoch(smem) + (unsigned)(l + 1);
#pragma unroll
        for (int i = 0; i < 2; ++i) vx[i] = pick(cq_, (unsigned)(((2 * lw + i) * 4 + 3) * 1024 + lane * 16), wq_src((2 * lw + i) * 4 + 3, lane, c.jq));
        asm volatile("" : "+v"(vx[0]), "+v"(vx[1]), "+v"(xfo), "+v"(xv), "+v"(go));
        BAR();
        issueW(0, 0);
        auto word = [&](int off) { return __builtin_amdgcn_readfirstlane(*(volatile LAS int*)(lds(go) + off)); };
        auto until = [&](int off, int v) {
#pragma unroll 1
            for (unsigned spins = 0; spins < (1u << 24); ++spins) { if (word(off) == v) break; }
            FENCE();
        };
        auto stage = [&](int s) {
            const char* b = W + (unsigned)(((s + rot) & 15) * (64 * 6144));
            unsigned d = (unsigned)((s == 3 ? QW_TOP : s * QW_SLOT) + lw * 1024), dx = d + (unsigned)(lw * 7168 + 3072);
            asm volatile("" : "+s"(d), "+s"(dx), "+s"(b));
#pragma unroll
            for (int j = 0; j < NWI; ++j) dma16<true>(d + j * 4096, vw[j], b);
#pragma unroll
            for (int i = 0; i < 2; ++i) dma16<true>(dx + i * 4096, vx[i], b);
        };
        auto first2 = [&]() { __builtin_amdgcn_s_setprio(LPRIO); issueA(0, 0); issueA(1, 1); };
        if (lw == 3) {
            if (!EPOLL) until(M_CNT - M_GO, 8 * l);
            Ask t;
            poll_rmw(t, c.ws, xfo, lane < (HALO && c.pq > 0 ? 32 : 16), xv, 63 - l);
            *(volatile LAS int*)lds(go) = l;
            until(M_CNT - M_GO, 8 * l);
            first2();
            STAMP_AT(32 + l * 8 + 0);
            wait_v<NAI>();
            keep(t);
        } else {
            until(M_CNT - M_GO, 8 * l);
            if (RSLP) __builtin_amdgcn_s_sleep(RSLP);
            int ns = 1;
#pragma unroll 1
            for (; ns < WPRE; ++ns) {
                if (word(0) == l) break;
                stage(ns);
            }
            until(0, l);
            first2();
            const int left = WPRE - ns;
#pragma unroll 1
            for (; ns < WPRE; ++ns) stage(ns);
            if (left == 0) wait_v<NAI>();
            else if (left == 1) wait_v<NAI + 8>();
            else if (left == 2) wait_v<NAI + 16>();
            else wait_v<NAI + 24>();
        }
        FENCE();
    }
    if (FIRST && ERL == 3) issueW(2, 2);
    if (ST0 && FIRST && lw == 2) STAMP_AT(57);
    if (FIRST && !ERL) BAR();
    __builtin_amdgcn_s_setprio(LPRIO);
    if (FIRST && !ERL) { LSTAMP(57); issueA(1, 1); issueW(1, 1); LSTAMP(58); }
    u32x4 st[4], cs[4]; u32x2 sh, fl; u64 fv = 0; float rs_, rq_, hs;
    f32x4 d[3], sm[2], g[2];
    bf16x8 wf[3][2], xf[2][2];
    const bool fact = FIRST ? lane < 32 : ((lane < 16 || lane >= (docs ? 16 : 32)) && lane <= (HALO ? 34 : 32));
    static_assert(XS + 3 <= (FIRST ? 12 : 13) && SPR == 1, "aux schedule");
    auto side = [&](auto ktc) {
        constexpr int kt = decltype(ktc)::value;
        {
            constexpr int C0 = FIRST ? 12 : XS + 2, C1 = FIRST ? 13 : XS + 3, C2 = 13;
            auto acc4 = [](u32x4& a, const u32x4& b) {
#pragma unroll
                for (int i = 0; i < 4; ++i) a[i] = __float_as_uint(addf(__uint_as_float(a[i]), __uint_as_float(b[i])));
                asm volatile("PADC %=" : "+v"(a));
            };
            auto fin = [&]() {
                rs_ *= -1.f / 1024.f;
                asm volatile("" : "+v"(rs_));
                rq_ = __builtin_amdgcn_rsqf(fmaxf(rq_ * (1.f / 1024.f) - rs_ * rs_, 0.f) + LN_EPS);
                asm volatile("" : "+v"(rq_));
            };
            if constexpr (FIRST && kt == XS + 2) {
                asm volatile("" : "+v"(st[0]), "+v"(st[1]), "+v"(st[2]));
                acc4(st[0], st[1]); acc4(st[0], st[2]);
            }
            if constexpr (FIRST && kt == XS + 3) { asm volatile("" : "+v"(st[3])); acc4(st[0], st[3]); }
            if constexpr (kt == C0) {
                if (docs) { asm volatile("" : "+v"(cs[0]), "+v"(cs[1])); acc4(cs[0], cs[1]); }
            }
            if constexpr (FIRST && kt == 12) {
                asm volatile("s_nop 1" : "+v"(st[0]));
                rs_ = fold16(__uint_as_float(st[0][0]), __uint_as_float(st[0][2])); rq_ = fold16(__uint_as_float(st[0][1]), __uint_as_float(st[0][3]));
                rs_ = fold32(rs_, rs_); rq_ = fold32(rq_, rq_);
            }
            if constexpr (FIRST && kt == 13) fin();
            if constexpr (!FIRST && kt == TL0 + 2) {
                asm volatile("" : "+v"(sh));
                hs = fold16(__uint_as_float(sh[0]), __uint_as_float(sh[1]));
                hs = fold32(hs, hs);
                hs += __uint_as_float(__builtin_amdgcn_update_dpp(0u, __float_as_uint(hs), 0x124, 0xf, 0xf, false));
                hs += __uint_as_float(__builtin_amdgcn_update_dpp(0u, __float_as_uint(hs), 0x128, 0xf, 0xf, false));
                asm volatile("" : "+v"(hs));
            }
            if constexpr (!FIRST && kt == TL0 + 2 + TSP) {
                auto hh = __builtin_amdgcn_permlane16_swap(__float_as_uint(hs), __float_as_uint(hs), false, false);
                rs_ = __uint_as_float(hh[0]); rq_ = __uint_as_float(hh[1]);
                asm volatile("" : "+v"(rs_), "+v"(rq_));
                fin();
                auto rf = __builtin_amdgcn_make_buffer_rsrc(c.ws + WS_STATS + (size_t)l * 524288, 0, 524288, 0x00020000);
                __builtin_amdgcn_raw_buffer_store_b64((u32x2){__float_as_uint(rs_), __float_as_uint(rq_)}, rf, fo_, 0, 16);
            }
            if constexpr (kt == C1) {
                if (docs) { asm volatile("" : "+v"(cs[2]), "+v"(cs[3])); acc4(cs[2], cs[3]); acc4(cs[0], cs[2]); }
            }
            if constexpr (kt == C2) {
                if (docs) {
                    asm volatile("s_nop 1" : "+v"(cs[0]));
                    hs = fold32(fold16(__uint_as_float(cs[0][0]), __uint_as_float(cs[0][1])), fold16(__uint_as_float(cs[0][2]), __uint_as_float(cs[0][3])));
                }
            }
            if constexpr (kt == 13) {
                static_assert(WV0 == 1, "wait");
                if (FIRST || !FDM || docs) WAIT_V0();
                if (!FIRST && !FDM) { asm volatile("" : "+v"(sh)); rs_ = __uint_as_float(sh[0]); rq_ = __uint_as_float(sh[1]); }
                if ((FIRST || !FDM) && lane < (HALO ? 36 : 32)) {
                    *(LAS float*)lds(l4) = rs_;
                    *(LAS float*)(lds(l4) + (M_RSTD - M_MEAN)) = rq_;
                }
                if (docs) *(LAS float*)lds(l3) = hs;
                FENCE();
            }
        }
    };
    static_for<0, KT>([&](auto ktc) {
        constexpr int kt = decltype(ktc)::value;
        LSTAMP(3 * kt);
        if constexpr (FIRST || kt > 0) wait_v<SC.w[kt]>();
        LSTAMP(3 * kt + 1);
        if constexpr (kt == XS) {
            if (lw == 0) {
                asm volatile("" : "+v"(fl));
                const u64 ep = lds_epoch(smem);
                if (!__all(!fact || reached(__builtin_bit_cast(u64, fl), ep, ftg))) poll_flags(c.ws, ep, foff, fact, ftg);
            }
        }
        if constexpr (kt == 14) asm volatile("s_waitcnt lgkmcnt(0)" ::: "memory");
        BAR();
        FENCE();
        LMARK();
        LSTAMP(3 * kt + 2);
        if constexpr (TOPJ & (1 << kt)) side(ktc);
        if constexpr (kt == (FIRST ? SP + DS : TL0 + 2 + TSP) + 3) {
            if (lw == 0 && lane == 0) __hip_atomic_store((GAS u64*)(c.ws + WS_FLAGQ + (c.pq * 16 + c.jq) * FQS), fv, __ATOMIC_RELAXED, __HIP_MEMORY_SCOPE_AGENT);
        }
        if constexpr (kt == XS + 1 - NQA) {
            if (lw == 0) { if (fact) aload2c(fl, foff, c.ws); }
        }
        {
            const char* sb = c.ws + WS_STATS + (size_t)l * 524288;
            const char* cb = c.ws + WS_CSUM + (HALO ? 16 * 3072 * 4 : 0);
            constexpr int sa = 3 * (kt - XS), ca = FIRST ? 2 * (kt - XS - 1) : 2 * (kt - XS), cn = 2;
            if constexpr (FIRST) {
#pragma unroll
                for (int i = sa; i < sa + 3; ++i) if (i >= 0 && i < 4) aload4c(st[i], so, sb + i * 1024);
            }
            if constexpr (!FIRST && kt == TL0) aload2c(sh, so, sb);
            if constexpr (!FIRST && kt == TL1) {
                static_assert(TL1 <= 12, "covered by the wait of step 14");
                if (FDM) { if (lane < 36) { touch(M_MEAN + lw * 144, ho, sb); touch(M_RSTD + lw * 144, ho, sb + 4); } }
                else aload2c(sh, ho, sb);
            }
            if constexpr (ca >= 0 && ca < 4) {
                if (docs) {
#pragma unroll
                    for (int i = ca; i < ca + cn; ++i) aload4c(cs[i], co, cb + i * 12288);
                }
            }
        }
        if constexpr (kt == SP) {
            if (docs) {
#pragma unroll
                for (int i = 0; i < 3; ++i)
#pragma unroll
                    for (int ks = 0; ks < 2; ++ks) {
                        const s16x4 lo = tr_read(lds(wt[i]) + ks * 4096), hi = tr_read(lds(wt[i]) + ks * 4096 + 512);
                        wf[i][ks] = __builtin_shufflevector(lo, hi, 0, 1, 2, 3, 4, 5, 6, 7);
                    }
                if (FIRST) {
#pragma unroll
                    for (int mt = 0; mt < 2; ++mt)
#pragma unroll
                        for (int ks = 0; ks < 2; ++ks) xf[mt][ks] = *(const LAS bf16x8*)(lds(ks ? xt1 : xt) + mt * 2048);
                }
            }
            SCHED();
        }
        if constexpr (kt + 2 < KT && !(FIRST && ERL >= 5 && kt == 0)) issueA(kt + 2, (kt + 2) % NQA);
        LPUT(3 * kt + 2);
        if constexpr (kt == SP + DM) {
            SCHED();
            if (docs) {
                const bf16x8 ones = __builtin_bit_cast(bf16x8, on);
                const f32x4 zero = {0.f, 0.f, 0.f, 0.f};
#pragma unroll
                for (int ks = 0; ks < 2; ++ks) {
#pragma unroll
                    for (int i = 0; i < 3; ++i) d[i] = MFMA(ones, wf[i][ks], ks ? d[i] : zero);
                    if (FIRST) {
#pragma unroll
                        for (int mt = 0; mt < 2; ++mt) { sm[mt] = MFMA(ones, xf[mt][ks], ks ? sm[mt] : zero); g[mt] = MFMA(xf[mt][ks], xf[mt][ks], ks ? g[mt] : zero); }
                    }
                }
            }
            SCHED();
        }
        if constexpr (FIRST && ERL < 2 && kt < 5) {
            if constexpr (kt < 4) issueW(kt + 2, (kt + 2) % NQW);
            if constexpr (kt == 3) issueW(6, 2, 0, NWI / 2);
            if constexpr (kt == 4) { issueW(6, 2, NWI / 2, NWI); issueW(7, 3); }
        } else {
            if constexpr (WPRE == 2 && kt < 2) issueW(kt + 2, kt + 2);
            if constexpr (FIRST && (ERL == 4 || ERL == 6) && kt == 0) issueW(2, 2);
            if constexpr ((FIRST || WPRE == 3) && kt == 0) issueW(3, 3);
            if constexpr (kt >= 1 && kt + 3 < KT) issueW(kt + 3, (kt + 3) % NQW);
        }
        if constexpr (FIRST && kt == WT0) ahead();
        if constexpr ((CP2 & 4) && FIRST && kt == 8) { if (lw == 2) code_again(c); }
        if constexpr ((CP2 & 8) && HALO && kt == 8) { if (lw == 2 && l == 1) code_again(c); }
        if constexpr (FIRST && kt == TSTEP) {
            if (lw == 1 && lane < 46) touch(M_TCH, tp, c.ws);
        }
        if constexpr (FIRST && kt == TW0) {
            if (lw == 0) touch(M_TCH, tl, Wo);
            else if (lw == 1) { asm volatile("" : "+s"(kp)); const char* outp = (const char*)kp->out; if (lane < 2) touch(M_TCH, tl, outp); }
        }
        if constexpr (kt == SP && DM > 0) {
            SCHED();
            if (docs) {
                asm volatile("" : "+v"(wf[0][0]), "+v"(wf[0][1]), "+v"(wf[1][0]), "+v"(wf[1][1]), "+v"(wf[2][0]), "+v"(wf[2][1]));
                if (FIRST) asm volatile("" : "+v"(xf[0][0]), "+v"(xf[0][1]), "+v"(xf[1][0]), "+v"(xf[1][1]));
            }
        }
        if constexpr (kt == SP + DS) {
            SCHED();
            if (docs) {
                if (FIRST) {
                    auto rst = __builtin_amdgcn_make_buffer_rsrc(c.ws + WS_STATS, 0, 524288, 0x00020000);
                    const bool b0 = fr & 1, b1 = fr & 2;
                    u32x2 sv[2];
#pragma unroll
                    for (int mt = 0; mt < 2; ++mt) {
                        float qa = b0 ? g[mt][1] : g[mt][0], qb = b0 ? g[mt][3] : g[mt][2];
                        asm volatile("PADC %=" : "+v"(qa), "+v"(qb));
                        sv[mt] = (u32x2){__float_as_uint(sm[mt][0]), __float_as_uint(b1 ? qb : qa)};
                    }
                    if (fq == (fr >> 2)) {
                        __builtin_amdgcn_raw_buffer_store_b64(sv[0], rst, xso, 0, 16);
                        __builtin_amdgcn_raw_buffer_store_b64(sv[1], rst, xso + hl<HS>(128), 0, 16);
                    }
                }
                float d0 = d[0][0], d1 = d[1][0], d2 = d[2][0];
                asm volatile("PADC %=" : "+v"(d0), "+v"(d1), "+v"(d2));
                const float v = fq == 0 ? d0 : (fq == 1 ? d1 : d2);
                auto rc = __builtin_amdgcn_make_buffer_rsrc(c.ws + WS_CSUM, 0, 2 * 16 * 3072 * 4, 0x00020000);
                if (lane < 48) __builtin_amdgcn_raw_buffer_store_b32(__float_as_uint(v), rc, cso, 0, 16);
                asm volatile("s_waitcnt lgkmcnt(0)" ::: "memory");
            }
        }
        if constexpr (kt == 1) {
            if (lw == 0) {
                const u32x2 e = *(const LAS u32x2*)(smem + M_EP);
                const u64 es = ((u64)(unsigned)__builtin_amdgcn_readfirstlane(e[1]) << 32) | (unsigned)__builtin_amdgcn_readfirstlane(e[0]);
                fv = es + (unsigned)(4 * l + 1);
                asm volatile("" : "+s"(fv));
            }
        }
        if constexpr (FIRST && kt >= WB0 && kt < WB0 + WBN) warm_b<kt - WB0>(lw);
        if constexpr (FIRST && kt == AD1) addr1();
        if constexpr (FIRST && kt == AD2) addr2();
        if constexpr (FIRST && kt == AD3) addr3();
        if constexpr (!(TOPJ & (1 << kt))) side(ktc);
        if constexpr (kt == 13) wo_w_issue<0, WT13>(c, Wo, vwo);
        if constexpr (kt == 14) { wo_w_issue<WT13, (HALO || WA0 > 12) ? 12 : WA0>(c, Wo, vwo); if (HALO) wo_w_issue<23, 23 + WB14>(c, Wo, vwo); }
        if constexpr (kt == 15) {
            if (HALO ? WBE < 2 : WA0 > 12) wo_w_issue<12, HALO ? 18 : WA0>(c, Wo, vwo);
            if (HALO) wo_w_issue<23 + WB14, 23 + WB14 + WB15>(c, Wo, vwo);
        }
    });
    __builtin_amdgcn_s_setprio(0);
    BAR();
    LMARK(); LPUT(48);
    LSTAMP(48);
    LSTORE(0);
    if (HALO ? WBE < 1 : WAE < 1) wo_w_issue<18, HALO ? 23 : 20>(c, Wo, vwo);
    if (HALO) {
        static_assert(!WBS || (WBE == 1 && WB14 == 0 && WRG <= 12), "Wo weights of the first twelve steps are older than the last 6 + WB15");
        if (WBS) wait_v<6 + WB15>();
        BAR();
        if (WBE) wo_w_issue<WBE < 2 ? 18 : 12, 23>(c, Wo, vwo);
        wo_w_issue<23 + WB14 + WB15, 32>(c, Wo, vwo);
    }
}

DI float max3(float a, float b, float c) { return fmaxf(fmaxf(a, b), c); }
#define SGB(m, n) __builtin_amdgcn_sched_group_barrier(m, n, 0)
template <bool HALO> struct Tail {
    int f[26], n[4], t[4][26], d[4][27], v[4][27], h3[32], s3[32], o3[33], q3[112];
    constexpr Tail() : f{}, n{}, t{}, d{}, v{}, h3{}, s3{}, o3{}, q3{} {
        const int FB[26] = {0, 1, 2, 3, 4, 5, 6, 7, 8, 9, 10, 11, 12, 13, 14, 15, 24, 25, 19, 23, 18, 22, 17, 21, 16, 20};
        const int FA[24] = {0, 1, 2, 3, 4, 5, 6, 7, 8, 9, 10, 11, 12, 13, 14, 15, 16, 17, 20, 21, 18, 19, 22, 23};
        const int TA[4][26] = {{0, 1, 2, 3, 7, 6, 5, 4, 8, 9, 10, 11, 15, 14, 13, 12, 16, 17, 18, 19, 23, 22, 21, 20, 0, 0}, {4, 8, 9, 10, 11, 15, 14, 13, 12, 16, 17, 18, 19, 23, 22, 21, 20, 0, 1, 2, 3, 7, 6, 5, 0, 0}, {16, 17, 18, 19, 23, 22, 21, 20, 0, 1, 2, 3, 7, 6, 5, 4, 8, 9, 10, 11, 15, 14, 13, 12, 0, 0}, {17, 18, 19, 23, 22, 21, 20, 8, 9, 10, 11, 15, 14, 13, 12, 16, 0, 0, 0, 0, 0, 0, 0, 0, 0, 0}};
        const int DA[4][26] = {{1, 1, 1, 1, 1, 1, 1, 1, 1, 1, 1, 1, 1, 1, 1, 1, 1, 1, 1, 1, 1, 1, 0, 0, 0, 0}, {1, 1, 1, 1, 1, 1, 1, 1, 1, 1, 1, 1, 1, 1, 1, 1, 1, 1, 1, 1, 1, 1, 0, 0, 0, 0}, {1, 1, 1, 1, 1, 1, 1, 1, 1, 1, 1, 1, 1, 1, 1, 1, 0, 0, 0, 0, 0, 0, 0, 0, 0, 0}, {2, 2, 2, 2, 1, 1, 1, 1, 1, 1, 1, 1, 0, 0, 0, 0, 0, 0, 0, 0, 0, 0, 0, 0, 0, 0}};
        const int VA[4][26] = {{1, 1, 0, 0, 0, 0, 1, 1, 1, 1, 1, 1, 1, 1, 1, 1, 1, 1, 1, 1, 1, 1, 2, 2, 0, 0}, {1, 1, 1, 1, 1, 1, 1, 1, 1, 1, 1, 1, 1, 1, 1, 1, 1, 1, 1, 1, 1, 1, 2, 2, 0, 0}, {1, 1, 1, 1, 1, 1, 1, 1, 1, 1, 1, 1, 1, 1, 1, 1, 2, 2, 2, 2, 2, 3, 3, 2, 0, 0}, {0, 0, 0, 0, 1, 1, 1, 1, 1, 1, 1, 1, 2, 2, 2, 2, 0, 0, 0, 0, 0, 0, 0, 0, 0, 0}};
        const int TB[4][26] = {{0, 1, 2, 3, 7, 6, 5, 4, 24, 8, 9, 10, 11, 15, 14, 13, 12, 25, 16, 17, 18, 19, 23, 22, 21, 20}, {24, 8, 9, 10, 11, 15, 14, 13, 12, 25, 16, 17, 18, 19, 23, 22, 21, 20, 0, 1, 2, 3, 7, 6, 5, 4}, {25, 16, 17, 18, 19, 23, 22, 21, 20, 0, 1, 2, 3, 7, 6, 5, 4, 24, 8, 9, 10, 11, 15, 14, 13, 12}, {0, 1, 2, 3, 7, 6, 5, 4, 24, 8, 9, 10, 11, 15, 14, 13, 12, 25, 19, 23, 0, 0, 0, 0, 0, 0}};
        const int DB[4][26] = {{1, 1, 1, 1, 1, 1, 1, 1, 1, 1, 1, 1, 1, 1, 1, 1, 1, 1, 1, 1, 1, 1, 1, 0, 0, 0}, {1, 1, 1, 1, 1, 1, 1, 1, 1, 1, 1, 1, 1, 1, 1, 1, 1, 1, 1, 1, 1, 1, 0, 0, 0, 0}, {1, 1, 1, 1, 1, 1, 1, 1, 1, 1, 1, 1, 1, 1, 1, 1, 1, 1, 1, 1, 1, 1, 1, 0, 0, 0}, {2, 2, 1, 1, 1, 1, 1, 1, 1, 1, 1, 1, 1, 1, 1, 1, 1, 1, 1, 0, 0, 0, 0, 0, 0, 0}};
        const int VB[4][26] = {{1, 1, 1, 1, 1, 1, 1, 1, 1, 1, 1, 1, 1, 1, 1, 1, 1, 1, 1, 1, 1, 1, 1, 2, 2, 2}, {1, 1, 1, 1, 1, 1, 1, 1, 1, 1, 1, 1, 1, 1, 1, 1, 1, 1, 1, 1, 1, 1, 3, 3, 3, 2}, {1, 1, 1, 1, 1, 1, 1, 1, 1, 1, 1, 1, 1, 1, 1, 1, 1, 1, 1, 1, 1, 1, 1, 3, 3, 2}, {0, 0, 1, 1, 1, 1, 1, 1, 1, 1, 1, 1, 1, 1, 1, 1, 1, 1, 1, 2, 0, 0, 0, 0, 0, 0}};
        const int G[32] = {8, 12, 9, 13, 10, 14, 11, 15, 16, 17, 20, 21, 18, 19, 22, 23, 0, 4, 1, 5, 2, 6, 3, 7, 0, 4, 1, 5, 2, 6, 3, 7};
        const int NM = HALO ? 26 : 24;
        n[0] = n[1] = n[2] = NM; n[3] = HALO ? 20 : 16;
        for (int i = 0; i < NM; ++i) f[i] = HALO ? FB[i] : FA[i];
        int vv = 0;
        for (int h = 0; h < 4; ++h) {
            v[h][0] = vv;
            for (int i = 0; i < n[h]; ++i) {
                t[h][i] = HALO ? TB[h][i] : TA[h][i];
                d[h][i + 1] = d[h][i] + (HALO ? DB[h][i] : DA[h][i]);
                vv += HALO ? VB[h][i] : VA[h][i];
                v[h][i + 1] = vv;
            }
        }
        for (int i = 0; i < 32; ++i) { h3[i] = G[i]; s3[i] = i >= 16 && i < 24 ? 0 : 1; }
        for (int i = 3; i < 32; ++i) o3[i + 1] = o3[i] + TOPS;
        int nv = 0, ns = 0;
        for (int k = 0; k < 8 * 14; ++k) {
            const bool top = k % TOPS < 2 || nv == 96;
            if (top && ns < 16 && nv >= 12 * (ns / 2 + 1) && (nv == 96 || k % TOPS == (ns & 1))) { q3[k] = (ns / 2) * 14 + 13 - (ns & 1); ++ns; }
            else { q3[k] = (nv / 12) * 14 + nv % 12; ++nv; }
        }
    }
};
template <bool HALO> DI void qkv_consume(const Ctx& c0, int l, KP kp, unsigned va0 = 0, unsigned vw0 = 0) {
    const Ctx c = fresh(c0);
    if (ST0 && l == 0 && c.wid == 2) STAMP_AT(59);
    LAS char* smem = c.smem;
    const int lane = c.lane, wr = c.wid >> 1, wc = c.wid & 1, fr = lane & 15, fq = lane >> 4;
    const int sw = HALO ? wr : 0;
    f32x4 acc[4][6], ah[2];
#pragma unroll
    for (int mt = 0; mt < 4; ++mt)
#pragma unroll
        for (int nt = 0; nt < 6; ++nt) { acc[mt][nt] = (f32x4){0.f, 0.f, 0.f, 0.f}; asm volatile("" : "+v"(acc[mt][nt])); }
    ah[0] = ah[1] = (f32x4){0.f, 0.f, 0.f, 0.f};
    if (HALO) asm volatile("" : "+v"(ah[0]), "+v"(ah[1]));
    const unsigned aoff0 = (unsigned)(QA_OFF + wr * 8192 + (HALO || l ? lane * 16 : fr * 128 + ((fq ^ (fr & 7)) << 4)));
    const unsigned hoff0 = (unsigned)(QA_OFF + 16384 + lane * 16);
    unsigned xm = HALO || l ? 1024u : 64u;
    asm volatile("" : "+s"(xm));
    static_assert(((QA_OFF | QA_SLOT) & 2047) == 0, "halves");
    const int q4 = fr >> 2, p4 = lane & 3;
    const int fb = (q4 & 2) | ((fq & 1) << 2);
    unsigned boff[2];
#pragma unroll
    for (int cc = 0; cc < 2; ++cc)
        boff[cc] = (unsigned)((8 * fq + q4) * 128 + (((2 * (2 * wc + (cc ^ sw)) + (p4 >> 1)) ^ fb) << 4) + (p4 & 1) * 8);
    bf16x8 xa[2][4], xh[2], wb[2][6];
    auto ldfrag = [&](int set, unsigned pa, unsigned ph, unsigned pb0, unsigned pb1, int ks) {
#pragma unroll
        for (int mt = 0; mt < 4; ++mt) xa[set][mt] = *(const LAS bf16x8*)(lds(pa) + mt * 2048);
        if (HALO) xh[set] = *(const LAS bf16x8*)lds(ph);
#pragma unroll
        for (int nt = 0; nt < 6; ++nt) {
            const LAS char* pb = lds((nt & 1) ? pb1 : pb0) + (nt >> 1) * 8192 + ks * 4096;
            const s16x4 lo = tr_read(pb), hi = tr_read(pb + 512);
            wb[set][nt] = __builtin_shufflevector(lo, hi, 0, 1, 2, 3, 4, 5, 6, 7);
        }
    };
    constexpr int NM = HALO ? 26 : 24, NH = HALO ? 17 : 16;
    auto mm = [&](int set, int i0, int i1) {
#pragma unroll
        for (int ii = i0; ii < i1; ++ii) {
            const int i = !HALO || ii < 8 ? ii : (!SNK ? (ii < 10 ? ii + 16 : ii - 2) : (ii == 8 ? 24 : (ii < 17 ? ii - 1 : (ii == 17 ? 25 : ii - 2))));
            const int nt = i >> 2, mt = SNK && (nt & 1) ? 3 - (i & 3) : i & 3;
            if (i < 16) acc[mt][nt] = MFMA(wb[set][nt], xa[set][mt], acc[mt][nt]);
            else if (i < 24) acc[mt][nt] = MFMA(xa[set][mt], wb[set][nt], acc[mt][nt]);
            else if (i == 24) ah[0] = MFMA(wb[set][2], xh[set], ah[0]);
            else ah[1] = MFMA(xh[set], wb[set][4], ah[1]);
        }
    };
    auto interleave = [&]() {
#pragma unroll
        for (int i = 0; i < NH; ++i) { SGB(0x008, 1); SGB(0x100, 1); }
        SCHED();
    };
    unsigned sa = 0, sb = 0;
    unsigned pa0 = aoff0, pa1 = aoff0 ^ xm, ph0 = hoff0, ph1 = hoff0 ^ xm, pb0 = boff[0], pb1 = boff[1];
    asm volatile("" : "+v"(pa0), "+v"(pa1), "+v"(pb0), "+v"(pb1));
    if (HALO) asm volatile("" : "+v"(ph0), "+v"(ph1));
    constexpr int M_CSH = M_CS + (HALO ? 768 : 0);
    unsigned sa0 = (unsigned)(M_MEAN + 64 + (wr * 64 + fr) * 4), sa1 = (unsigned)(M_MEAN + 64 + wr * 256 + fq * 16);
    unsigned sa2 = (unsigned)(M_CSH + wc * 128 + sw * 64 + fq * 16), sa6 = (unsigned)(M_CSH + 512 + wc * 128 + sw * 64 + fr * 4);
    unsigned sa3 = sa2 ^ 64, sa7 = sa6 ^ 64, sa4 = (unsigned)(M_MEAN + fr * 4), sa5 = (unsigned)(M_MEAN + fq * 16);
    asm volatile("" : "+v"(sa0), "+v"(sa1), "+v"(sa2), "+v"(sa6), "+v"(sa3), "+v"(sa7));
    if (HALO) asm volatile("" : "+v"(sa4), "+v"(sa5));
    const int head = c.jq * 2 + wc;
    auto rso = __builtin_amdgcn_make_buffer_rsrc(HALO ? c.ws + WS_O + (size_t)l * (8u << 20) + (size_t)((head * 16 + c.pq) * 8 + wr * 4) * (1024 << HO)
                                                      : c.ws + WS_KV + (size_t)(l >> 1) * (16u << 20) + (size_t)((c.pq * 32 + head) * 4 + wr) * (4096 << HK), 0, HALO ? 4096 << HO : 12288 << HK, 0x00020000);
    unsigned xrs = (unsigned)(M_XCH + wc * 6144 + wr * 2048), xhs = (unsigned)(M_XCH + wc * 6144 + 8 - wr * 8);
    unsigned b0m = (c.pq == 0 && wr == 0) ? 15u * 16u : 0u;
    asm volatile("" : "+s"(rso));
    if (HALO) asm volatile("" : "+s"(xrs), "+s"(xhs), "+s"(b0m));
    u32x4 cpy[3];
    unsigned cv, vt, cdst;
    const char *cs_, *tw, *cd_;
    int more = !HALO && l == 0 ? KT - 3 - XST - (c.pq & 7) : 0, three = c.jq & 4;
    if (!HALO) {
        static_assert(CPY + TOUCH == 2 && XSP == 1, "extra");
        asm volatile("" : "+s"(kp));
        const int g = c.jq >> 2, n = c.pq * 4 + (c.jq & 3), m = n >> 5, kt = (n >> 1) & 15;
        const char* src = *(const char* const __attribute__((address_space(4)))*)((const __attribute__((address_space(4))) char*)kp + (three ? (m ? 8 : 24) : (m ? 32 : 40)));
        const char* tsr = *(const char* const __attribute__((address_space(4)))*)((const __attribute__((address_space(4))) char*)kp + (c.wid < 3 ? 24 : 40));
        int o1 = kt * (64 * 6144) + (n & 1) * (32 * 6144) + (3 + 4 * (g & 2) - c.jq) * 128 + 2048, o2 = (n & 31) * 65536 + (11 - 4 * g) * 128 - c.pq * 128 * (D_ * 2) + 2048;
        int d1 = (int)WS_CQ + kt * (64 * 6144) + (m * 2 + (g >> 1)) * 24576 + (n & 1) * 4096, d2 = (int)WS_CO + (n & 31) * 65536 + (m * 2 + 1 - (g >> 1)) * 4096;
        asm volatile("" : "+s"(o1), "+s"(o2), "+s"(d1), "+s"(d2));
        cs_ = src + (three ? o1 : o2);
        tw = tsr + (unsigned)((c.wid < 3 ? c.wid : 0) * (2u << 20) + c.bid * 8192);
        cd_ = c.ws + (unsigned)(three ? d1 : d2);
        cv = pick(three, vw0, va0 ^ ((lane & 8) << 1));
        cdst = (unsigned)(c.wid * 1024 + lane * 16);
        vt = TWQ ? (unsigned)(((lane & 7) == 3 ? lane - 1 : lane) * 128) : 0u;
        asm volatile("" : "+s"(more), "+s"(three), "+s"(cs_), "+s"(tw), "+s"(cd_));
        asm volatile("" : "+v"(cv), "+v"(cdst), "+v"(vt));
        asm volatile("" : "=v"(cpy[0]), "=v"(cpy[1]), "=v"(cpy[2]));
    }
    unsigned hto = HT0 == 2 ? 0u : 0x80000000u;
    const char* W0 = (const char*)kp->wqkv0;
    if (!HALO && HT0) {
        const int n = ((c.pq & 7) * 3 + (c.jq & 3)) * 64 + c.wid * 16 + fr, r3 = (n * 43691) >> 17;
        const int t0 = rot_first(c.pq & 8, c.jq | 3) + 8;
        if (l == 0 && !(c.jq & 4) && (c.jq & 3) != 3 && lane < 16) hto = (unsigned)(((t0 * 64 + r3) & 1023) * 6144 + (n - 3 * r3) * 2048 + (c.jq | 3) * 128);
        asm volatile("" : "+v"(hto));
        asm volatile("" : "+s"(W0));
    }
    if (!HALO && HT0 == 2) { asm volatile("s_mov_b32 m0, %0\n\ts_nop 3\n\tglobal_load_lds_dword %1, %2" ::"s"(M_TCH), "v"(hto), "s"(W0) : "memory"); }
    if (!HALO && !ERL && l == 0) BAR();
    if (ST0 && l == 0 && c.wid == 1) STAMP_AT(44);
    if (!HALO) { if (HT0 == 2) wait_v<1>(); else WAIT_V0(); }
    if (ST0 && l == 0 && c.wid == 1) STAMP_AT(46);
    BAR();
    if (!HALO && HT0 == 1) { if (hto < 0x80000000u) asm volatile("s_mov_b32 m0, %0\n\ts_nop 3\n\tglobal_load_lds_dword %1, %2" ::"s"(M_TCH), "v"(hto), "s"(W0) : "memory"); }
    if (c.wid == 0) STAMP_AT(l * 8 + 0);
    ldfrag(0, pa0, ph0, pb0, pb1, 0);
    SCHED();
    if (ALN) WAIT_L0();
    SCHED();
    int cnt = KT - 3 - more, tv = 0;
    QSTAMP(0);
    auto half0 = [&]() {
        ldfrag(1, pa1, ph1, pb0, pb1, 1);
        mm(0, 0, NH);
        interleave();
        mm(0, NH, NH + 1); SCHED();
        asm volatile("s_addk_i32 %0, %2\n\ts_addk_i32 %1, %3" : "+s"(sa), "+s"(sb) : "i"(QA_SLOT), "i"(QW_SLOT) : "scc"); SCHED();
        mm(0, NH + 1, NH + 2); SCHED();
        asm volatile("s_cmpk_lg_u32 %0, %2\n\ts_cselect_b32 %0, %0, 0\n\ts_cmp_lg_u32 %1, %3\n\ts_cselect_b32 %1, %1, %4" : "+s"(sa), "+s"(sb) : "i"(NQA * QA_SLOT), "i"(3 * QW_SLOT), "i"(QW_TOP) : "scc"); SCHED();
        mm(0, NH + 2, NH + 3); SCHED();
        asm volatile("s_cmp_lg_u32 %0, %2\n\ts_cselect_b32 %0, %0, 0\n\ts_addk_i32 %1, -1" : "+s"(sb), "+s"(cnt) : "i"(QW_TOP + QW_SLOT) : "scc"); SCHED();
        mm(0, NH + 3, NH + 4); SCHED();
        pa0 = aoff0 + sa; pb0 = boff[0] + sb;
        asm volatile("" : "+v"(pa0), "+v"(pb0)); SCHED();
        mm(0, NH + 4, NH + 5); SCHED();
        pb1 = boff[1] + sb; pa1 = pa0 ^ xm;
        asm volatile("" : "+v"(pa1), "+v"(pb1)); SCHED();
        mm(0, NH + 5, NH + 6); SCHED();
        if (HALO) { ph0 = hoff0 + sa; ph1 = ph0 ^ xm; asm volatile("" : "+v"(ph0), "+v"(ph1)); SCHED(); }
        mm(0, NH + 6, NM);
        SCHED();
        WAIT_L0();
        BAR();
        SCHED();
    };
    asm volatile("" : "+s"(sa), "+s"(sb), "+s"(cnt));
    do {
    ALIGN8();
    do {
        half0();
        ldfrag(0, pa0, ph0, pb0, pb1, 0);
        mm(1, 0, NH);
        interleave();
        mm(1, NH, NM - 1);
        SCHED();
        WAIT_L0();
        asm volatile("s_nop 0");
        SCHED();
        mm(1, NM - 1, NM);
        SCHED();
    } while (cnt != 0);
    if (!HALO) {
        asm volatile("s_cmp_eq_u32 %[more], 0\n\ts_cbranch_scc1 .Lex%=\n\ts_mov_b32 m0, %[tch]\n\ts_nop 3\n\tglobal_load_lds_dword %[vt], %[tw] sc1\n\t"
                     "global_load_dwordx4 %[c0], %[v0], %[cs] offset:-2048\n\ts_cmp_eq_u32 %[three], 0\n\ts_cbranch_scc1 .Lex%=\n\t"
                     "global_load_dwordx4 %[c1], %[v0], %[cs]\n\tglobal_load_dwordx4 %[c2], %[v0], %[cs] offset:2048\n.Lex%=:"
                     : [c0] "+v"(cpy[0]), [c1] "+v"(cpy[1]), [c2] "+v"(cpy[2])
                     : [more] "s"(more), [tch] "i"(M_TCH), [vt] "v"(vt), [tw] "s"(tw), [v0] "v"(cv), [cs] "s"(cs_), [three] "s"(three) : "scc", "memory");
        cnt = more; more = 0;
        asm volatile("" : "+s"(cnt), "+s"(more));
        SCHED();
    }
    } while (cnt != 0);
    if (!HALO) {
        asm volatile("s_cmp_lg_u32 %[l], 0\n\ts_cbranch_scc1 .Lex%=\n\ts_waitcnt vmcnt(0)\n\ts_nop 3\n\tglobal_store_dwordx4 %[d], %[c0], %[ws] sc1\n\ts_cmp_eq_u32 %[three], 0\n\ts_cbranch_scc1 .Lex%=\n\t"
                     "v_add_u32 %[d], 0x2000, %[d]\n\tglobal_store_dwordx4 %[d], %[c1], %[ws] sc1\n\tv_add_u32 %[d], 0x2000, %[d]\n\tglobal_store_dwordx4 %[d], %[c2], %[ws] sc1\n.Lex%=:\n\ts_nop 1"
                     : [d] "+v"(cdst) : [c0] "v"(cpy[0]), [c1] "v"(cpy[1]), [c2] "v"(cpy[2]), [l] "s"(l), [ws] "s"(cd_), [three] "s"(three) : "scc", "memory");
    }
    QSTAMP(1);
    FENCE();
    SCHED();

    f32x4 csq[4], m4[4], mh4; float csv[2], mr[4], rr[4], mh;
    auto r1 = [&](unsigned b, int off) { return *(const LAS float*)(lds(b) + off); };
    auto r4_ = [&](unsigned b, int off) { return *(const LAS f32x4*)(lds(b) + off); };
    auto mf = [&](int set, int i) {
        const int nt = i >> 2, mt = i & 3;
        if (i < 16) acc[mt][nt] = MFMA(wb[set][nt], xa[set][mt], acc[mt][nt]);
        else if (i < 24) acc[mt][nt] = MFMA(xa[set][mt], wb[set][nt], acc[mt][nt]);
        else if (i == 24) ah[0] = MFMA(wb[set][2], xh[set], ah[0]);
        else ah[1] = MFMA(xh[set], wb[set][4], ah[1]);
        SCHED();
    };
    auto fold = [&](int i, int e) {
        const int nt = i >> 2, mt = i & 3;
        float t;
        if (i < 16) t = __builtin_fmaf(mr[mt], csq[nt][e], acc[mt][nt][e]);
        else if (i < 24) t = __builtin_fmaf(m4[mt][e], csv[nt - 4], acc[mt][nt][e]);
        else if (i == 24) t = __builtin_fmaf(mh, csq[2][e], ah[0][e]);
        else t = __builtin_fmaf(mh4[e], csv[0], ah[1][e]);
        asm volatile("" : "+v"(t));
        if (i < 24) acc[mt][nt][e] = t; else ah[i - 24][e] = t;
    };
    constexpr Tail<HALO> TL{};
    constexpr int NP = HALO ? 6 : 2, FN = 4 * NM;
    static_assert(TL.v[3][TL.n[3]] == NP + FN + (HALO ? 2 : 0) && KT % NQA == 1 && KT % NQW == 0, "tail");
    s16x4 wl[6], wh[6];
    auto frd = [&](int set, int k, int ao) {
        if (k < 4) xa[set][k] = *(const LAS bf16x8*)(lds(set ? pa1 : pa0) + ao + k * 2048);
        else if (k == 16) xh[set] = *(const LAS bf16x8*)(lds(set ? ph1 : ph0) + ao);
        else {
            const int nt = (k - 4) >> 1;
            const LAS char* pb = lds((nt & 1) ? pb1 : pb0) + (nt >> 1) * 8192 + set * 4096;
            if (k & 1) { wh[nt] = tr_read(pb + 512); wb[set][nt] = __builtin_shufflevector(wl[nt], wh[nt], 0, 1, 2, 3, 4, 5, 6, 7); }
            else wl[nt] = tr_read(pb);
        }
    };
    constexpr int RS = M_RSTD - M_MEAN;
    unsigned vl, vg, tb, xr, xh_, b0a, qb0, qb1;
    f32x4 bprev, bcur, bprev0; u32x4 ps; u32x2 ko; float ninf;
    auto rdop = [&](int h, int k) {
        const int j = k - NH;
        if (h == 0) {
            if (k < 2) { mr[2 * k] = r1(sa0, k * 128); mr[2 * k + 1] = r1(sa0, k * 128 + 64); }
            else if (k < 4) csq[k - 2] = r4_((k & 1) ? sa3 : sa2, 0);
            else if (j < 4) frd(0, k - 4, 2 * QA_SLOT);
            else csq[j - 2] = r4_((j & 1) ? sa3 : sa2, 256);
        } else if (j < 0) frd(h & 1, k, h == 1 ? 2 * QA_SLOT : 0);
        else if (h == 1) {
            if (j < 2) csv[j] = r1(j ? sa7 : sa6, 0);
            else if (HALO && j == 2) mh = r1(sa4, 0);
            else if (HALO && j == 3) mh4 = r4_(sa5, 0);
            else { const int mt = HALO ? 7 - j : j - 2; m4[mt] = r4_(sa1, mt * 64); }
        } else if (h == 2) {
            if (j == 0) m4[2] = r4_(sa1, 128);
            if (j == 1) ko = *(const LAS u32x2*)(lds(tb) + 3072);
            if (j == 2) { rr[0] = r1(sa0, RS); rr[1] = r1(sa0, 64 + RS); }
            if (j == 3) { rr[2] = r1(sa0, 128 + RS); rr[3] = r1(sa0, 192 + RS); }
            if (j >= 4) m4[5 - j] = r4_(sa1, (5 - j) * 64);
        } else {
            if (j == 0) bprev = r4_(tb, 0);
            if (j == 1) bcur = r4_(tb, 1024);
            if (j == 2) ps = *(const LAS u32x4*)(lds(tb) + 2048);
            if (j == 3) bprev0 = r4_(b0a, 0);
        }
    };
    auto vop = [&](int k) {
        if (k == 0) { asm volatile("" : "+v"(boff[0])); qb0 = boff[0] + QW_TOP; asm volatile("" : "+v"(qb0)); }
        else if (k == 1) { asm volatile("" : "+v"(boff[1])); qb1 = boff[1] + QW_TOP; asm volatile("" : "+v"(qb1)); }
        else if (k < NP) {
            if (k == 2) { vl = (unsigned)(lane * 16); asm volatile("" : "+v"(vl)); }
            if (k == 3) { tb = vl + M_TAB; asm volatile("" : "+v"(tb)); }
            if (k == 4) { b0a = tb | b0m; asm volatile("" : "+v"(b0a)); }
            if (k == 5) { xr = xrs + vl; asm volatile("" : "+v"(xr)); }
        } else if (k < NP + FN) fold(TL.f[(k - NP) >> 2], (k - NP) & 3);
        else {
            if (k == NP + FN) { xh_ = xhs + vl; asm volatile("" : "+v"(xh_)); }
            if (k == NP + FN + 1) { ninf = -INFINITY; asm volatile("" : "+v"(ninf)); }
        }
    };
    unsigned wa, wq;
    int wr2 = wr, wc2 = wc;
#pragma unroll
    for (int i = 0; i < NM; ++i) {
        mf(0, TL.t[0][i]);
        if (TL.t[0][i] < 24) asm volatile("" : "+v"(acc[TL.t[0][i] & 3][TL.t[0][i] >> 2])); else asm volatile("" : "+v"(ah[TL.t[0][i] & 1]));
        FENCE();
        if (i < NH) frd(1, i, 0);
        if (!HALO && i == NH + 3) { asm volatile("" : "+s"(wr2), "+s"(wc2)); vl = (unsigned)(lane * 16); wq = (unsigned)(AT_Q + (wc2 * 8 + wr2 * 4) * 1024) + vl; asm volatile("" : "+v"(vl), "+v"(wq)); }
        if (!HALO && i == NH + 4) { asm volatile("" : "+s"(wr2), "+s"(wc2)); wa = (unsigned)(AT_OWN + wc2 * 16384 + wr2 * 4096) + vl; asm volatile("" : "+v"(wa)); }
        if (i == NH + 1) { asm volatile("" : "+v"(boff[0]), "+v"(boff[1])); pb0 = boff[0] + 2 * QW_SLOT; pb1 = boff[1] + 2 * QW_SLOT; asm volatile("" : "+v"(pb0), "+v"(pb1)); }
        if (i == NH + 2) { pa0 = aoff0; pa1 = aoff0 ^ xm; asm volatile("" : "+v"(pa1)); if (HALO) { ph0 = hoff0; ph1 = hoff0 ^ xm; asm volatile("" : "+v"(ph1)); } }
        SCHED();
    }
    WAIT_L0();
    BAR();
    FENCE();
    SCHED();
    auto frags = [&](int h) {
        static_assert(TL.d[0][NM] == NH + 6 && TL.d[1][NM] == NH + (HALO ? 5 : 6) && TL.d[2][NM] == NH + (HALO ? 6 : 0) && TL.d[3][TL.n[3]] == NH + (HALO ? 4 : 0), "reads");
        if (h == 0) __builtin_amdgcn_s_waitcnt(0xC27F);
        if (h == 1) __builtin_amdgcn_s_waitcnt(HALO ? 0xC57F : 0xC67F);
        if (h == 2) __builtin_amdgcn_s_waitcnt(HALO ? 0xC67F : 0xC07F);
        if (h == 3) __builtin_amdgcn_s_waitcnt(HALO ? 0xC47F : 0xC07F);
    };
#pragma unroll
    for (int h = 0; h < 4; ++h) {
        if (h == 2) { pb0 = qb0; pb1 = qb1; }
#pragma unroll
        for (int i = 0; i < TL.n[h]; ++i) {
            if (!(h & 1) && i == TL.n[h] - 1) { frags(h); SCHED(); if (h) QSTAMP(4); }
            mf(~h & 1, TL.t[h][i]);
#pragma unroll
            for (int k = TL.d[h][i]; k < TL.d[h][i + 1]; ++k) { FENCE(); rdop(h, k); }
            SCHED();
#pragma unroll
            for (int k = TL.v[h][i]; k < TL.v[h][i + 1]; ++k) vop(k);
            SCHED();
        }
        if (h & 1) {
            frags(h);
            if (h == 1) QSTAMP(2); else QSTAMP(5);
            BAR();
            FENCE();
            SCHED();
            if (h == 1) QSTAMP(3); else QSTAMP(6);
        }
    }
    if (!HALO) {
        f32x4 r4[4];
        mf(1, 8);
#pragma unroll
        for (int mt = 0; mt < 4; ++mt) rr[mt] = r1(sa0, mt * 64 + RS);
        SCHED();
        mf(1, 12);
#pragma unroll
        for (int mt = 0; mt < 4; ++mt) r4[mt] = r4_(sa1, mt * 64 + RS);
        SCHED();
        mf(1, 9);
        vg = HK ? (unsigned)hv<HK>(lane) : vl;
        if (HK) asm volatile("" : "+v"(vg));
        SCHED();
        u32x4 pk[8];
        auto op = [&](int g, int k) {
            const int ta = g < 4 ? g : (g - 4) & 2, tb = g < 4 ? g : ta + 1, na = g < 4 ? 2 : 4 + (g & 1), nb = g < 4 ? 3 : na;
            f32x4& A = acc[ta][na]; f32x4& Bv = acc[tb][nb];
            if (k < 12) {
                const int h = k / 6, kk = k % 6;
                f32x4& X = h ? Bv : A;
                const int tx = h ? tb : ta;
                if (kk < 4) { float t = X[kk] * (g < 4 ? rr[tx] : r4[tx][kk]); asm volatile("" : "+v"(t)); X[kk] = t; }
                else { unsigned t = cvt_pk_bf16(X[2 * (kk - 4)], X[2 * (kk - 4) + 1]); asm volatile("" : "+v"(t)); pk[g][2 * h + kk - 4] = t; }
            } else if (k == 12) __builtin_amdgcn_raw_buffer_store_b128(pk[g], rso, vg + hp<HK>(g & 3), (g >> 2) * (8192 << HK), 16);
            else *(LAS u32x4*)(lds(wa) + (g >> 2) * 8192 + (g & 3) * 1024) = pk[g];
            SCHED();
        };
        auto pm = [&](int i) {
            if (i < 16) mf(1, TL.h3[i]);
            else if (i < 32) mf(((i - 16) >> 1) & 1, ((i - 16) >> 2) + 4 * (i & 1));
        };
        pm(3);
#pragma unroll
        for (int g = 0; g < 8; ++g) {
#pragma unroll
            for (int k = 0; k < 6; ++k) op(g, k);
            pm(2 * g + 4);
            if (g > 0) op(g - 1, 12);
#pragma unroll
            for (int k = 6; k < 12; ++k) op(g, k);
            pm(2 * g + 5);
            if (g > 0 && g <= LWA) op(g - 1, 13);
        }
        op(7, 12);
        QSTAMP(7);
        u32x4 qk;
        auto qop = [&](int mt, int k) {
            if (k == 0) { rr[mt] *= QSCALE; asm volatile("" : "+v"(rr[mt])); }
            else if (k < 13) {
                const int h = (k - 1) / 6, kk = (k - 1) % 6;
                f32x4& X = acc[mt][h];
                if (kk < 4) { float t = X[kk] * rr[mt]; asm volatile("" : "+v"(t)); X[kk] = t; }
                else { unsigned t = cvt_pk_bf16(X[2 * (kk - 4)], X[2 * (kk - 4) + 1]); asm volatile("" : "+v"(t)); qk[2 * h + kk - 4] = t; }
            } else *(LAS u32x4*)(lds(wq) + mt * 1024) = qk;
            SCHED();
        };
#pragma unroll
        for (int mt = 0; mt < 3; ++mt) {
            pm(20 + 4 * mt); if (3 * mt + LWA < 8) op(3 * mt + LWA, 13); qop(mt, 0); qop(mt, 1); qop(mt, 2);
            pm(21 + 4 * mt); if (3 * mt + 1 + LWA < 8) op(3 * mt + 1 + LWA, 13); qop(mt, 3); qop(mt, 4); qop(mt, 5);
            pm(22 + 4 * mt); if (3 * mt + 2 + LWA < 8) op(3 * mt + 2 + LWA, 13); qop(mt, 6); qop(mt, 7); qop(mt, 8);
            pm(23 + 4 * mt); qop(mt, 9); qop(mt, 10); qop(mt, 11); qop(mt, 12); qop(mt, 13);
        }
        if (c.wid == 0) STAMP_AT(l * 8 + 1);
        QSTAMP(8);
        {
            const u32x2 q0 = pack4(acc[3][0] * (rr[3] * QSCALE)), q1 = pack4(acc[3][1] * (rr[3] * QSCALE));
            *(LAS u32x4*)(lds(wq) + 3 * 1024) = (u32x4){q0[0], q0[1], q1[0], q1[1]};
        }
        QSTAMP(9);
        QSTORE();
        if (K0E) {
            SCHED();
            WAIT_L0();
            BAR();
            FENCE();
            SCHED();
            if (KSP == 4) {
                publish<4, 0>(smem, lane, c.ws, (unsigned)WS_FLAGQ + (c.pq * 16 + c.jq) * FQS, lane == 0, (unsigned)(4 * l + 2));
                if (c.wid == 0) STAMP_AT(l * 8 + 2);
                SCHED();
            }
        }
    } else {
        typedef unsigned u32x10 __attribute__((ext_vector_type(10)));
        u32x4 kp[4], qp[4];
        u32x10 vw[2];
        float ks[2][4], cs[2][4];
        auto pk8 = [&](const f32x4& x, const f32x4& y) { const u32x2 a = pack4(x), b = pack4(y); u32x4 r = {a[0], a[1], b[0], b[1]}; asm volatile("" : "+v"(r)); SCHED(); return r; };
        auto pkv = [&](int mt, int cc) { const u32x2 a = pack4(acc[mt][4 + cc]); vw[cc][2 * mt + 2] = a[0]; vw[cc][2 * mt + 3] = a[1]; };
        auto fr8 = [](const u32x4& x) { return __builtin_bit_cast(bf16x8, x); };
        mf(1, 24);
        unsigned ka0 = ko[0] + wr * 256, ka1 = ko[1] + wr * 256;
        asm volatile("" : "+v"(ka0), "+v"(ka1));
        SCHED();
        mf(1, 25);
#pragma unroll
        for (int mt = 0; mt < 4; ++mt) ks[0][mt] = r1(ka0, mt * 64);
        rr[0] *= QSCALE; asm volatile("" : "+v"(rr[0]));
        SCHED();
        mf(1, 11);
#pragma unroll
        for (int mt = 0; mt < 4; ++mt) ks[1][mt] = r1(ka1, mt * 64);
        rr[1] *= QSCALE; asm volatile("" : "+v"(rr[1]));
        SCHED();
        mf(1, 15);
        rr[2] *= QSCALE; asm volatile("" : "+v"(rr[2]));
        rr[3] *= QSCALE; asm volatile("" : "+v"(rr[3]));
        SCHED();
        mf(1, 19);
        const u32x4 hh = pk8(ah[0], ah[1]);
        mf(1, 23);
        *(LAS u32x2*)lds(xh_) = (u32x2){hh[0], hh[1]}; *(LAS u32x2*)(lds(xh_) + 1024) = (u32x2){hh[2], hh[3]};
        SCHED();
        mf(1, 10);
        kp[3] = pk8(acc[3][2], acc[3][3]);
        mf(1, 14);
        *(LAS u32x4*)(lds(xr) + 2048) = kp[3];
        pkv(3, 0); asm volatile("" : "+v"(vw[0]));
        *(LAS u32x2*)(lds(xr) + 3072) = (u32x2){vw[0][8], vw[0][9]};
        SCHED();
        mf(1, 3);
        pkv(3, 1); asm volatile("" : "+v"(vw[1]));
        *(LAS u32x2*)(lds(xr) + 3072 + 8) = (u32x2){vw[1][8], vw[1][9]};
        SCHED();
        mf(1, 7);
        mf(1, 9);
        kp[2] = pk8(acc[2][2], acc[2][3]);
        mf(1, 13);
        qp[3] = pk8(acc[3][0], acc[3][1]);
        mf(1, 2);
        f32x4 s0[4], s1[4];
        u32x4 pk;
        auto sc = [&](int mt, int h) {
            if (h) { s1[mt] = MFMA(fr8(kp[mt]), fr8(qp[mt]), bcur); asm volatile("" : "+v"(s1[mt])); }
            else { s0[mt] = MFMA(fr8(mt ? kp[mt - (mt > 0)] : pk), fr8(qp[mt]), mt == 0 ? bprev0 : bprev); asm volatile("" : "+v"(s0[mt])); }
            SCHED();
        };
        auto csm = [&](int mt) { cs[0][mt] = ks[0][mt] * rr[mt]; asm volatile("" : "+v"(cs[0][mt])); cs[1][mt] = ks[1][mt] * rr[mt]; asm volatile("" : "+v"(cs[1][mt])); };
        float u[2][4], mx[4], iv[4];
        auto sv = [](float x) { asm volatile("" : "+v"(x)); return x; };
        auto mx2 = [&](float a, float b) { return max3(ninf, a, b); };
        auto pick = [&](int mt, int h) { u[h][mt] = sv(max3(mx2(s0[mt][2 * h], s0[mt][2 * h + 1]), s1[mt][2 * h], s1[mt][2 * h + 1]) * cs[h][mt]); };
        auto vf = [&](int i) { const int mt = 2 - (i >> 2), nt = 4 + (i & 1), set = (i >> 1) & 1; SCHED(); acc[mt][nt] = MFMA(xa[set][mt], wb[set][nt], acc[mt][nt]); asm volatile("" : "+v"(acc[mt][nt])); SCHED(); };
        mf(1, 6);
        kp[1] = pk8(acc[1][2], acc[1][3]);
        mf(1, 8);
        WAIT_L0();
        QSTAMP(7);
        BAR();
        FENCE();
        SCHED();
        QSTAMP(8);
        mf(1, 12);
        {
            const u32x2 k0 = *(const LAS u32x2*)(lds(xr) + 8);
            FENCE(); SCHED();
            const u32x2 k1 = *(const LAS u32x2*)lds(xr);
            FENCE(); SCHED();
            const u32x2 a = *(const LAS u32x2*)(lds(xr) + 1024 + 8);
            FENCE(); SCHED();
            const u32x2 b = *(const LAS u32x2*)(lds(xr) + 1024);
            pk = (u32x4){k0[0], k0[1], k1[0], k1[1]};
            vw[0][0] = a[0]; vw[0][1] = a[1]; vw[1][0] = b[0]; vw[1][1] = b[1];
        }
        SCHED();
        auto sw16 = [](float& a, float& b) { auto r = __builtin_amdgcn_permlane16_swap(__float_as_uint(a), __float_as_uint(b), false, false); a = __uint_as_float(r[0]); b = __uint_as_float(r[1]); };
        auto sw32 = [](float& a, float& b) { auto r = __builtin_amdgcn_permlane32_swap(__float_as_uint(a), __float_as_uint(b), false, false); a = __uint_as_float(r[0]); b = __uint_as_float(r[1]); };
        auto ldr = [&](int i) { };
        auto top = [&](int mt) { mx[mt] = sv(mx2(u[0][mt], u[1][mt])); };
        auto pv1 = [&](int mt, int k) {
            SCHED();
            vw[k >> 1][2 * mt + 2 + (k & 1)] = cvt_pk_bf16(acc[mt][4 + (k >> 1)][2 * (k & 1)], acc[mt][4 + (k >> 1)][2 * (k & 1) + 1]);
            asm volatile("" : "+v"(vw[k >> 1]));
            SCHED();
        };
        auto uk = [&](int mt) { SCHED(); u[0][mt] = sv(u[0][mt] * ks[0][mt]); u[1][mt] = sv(u[1][mt] * ks[1][mt]); SCHED(); };
        mf(1, 1);
        qp[2] = pk8(acc[2][0], acc[2][1]);
        mf(1, 5);
        csm(3); SCHED();
        mf(1, 0);
        kp[0] = pk8(acc[0][2], acc[0][3]);
        mf(1, 4);
        csm(2); SCHED();
        sc(3, 1);
        qp[1] = pk8(acc[1][0], acc[1][1]);
        sc(3, 0);
        csm(1); ldr(0); SCHED();
        sc(2, 1);
        qp[0] = pk8(acc[0][0], acc[0][1]);
        sc(2, 0);
        csm(0); pick(3, 0); SCHED();
        sc(1, 1);
        pick(3, 1); ldr(1); SCHED();
        sc(1, 0);
        pick(2, 0); top(3); SCHED();
        sc(0, 1);
        pick(2, 1); ldr(2); SCHED();
        sc(0, 0);
        if (c.wid == 0) STAMP_AT(l * 8 + 1);
        pick(1, 0); top(2);
        vf(0);
        pick(1, 1); ldr(3);
        vf(1);
        sw16(mx[2], mx[3]);
        pick(0, 0);
        vf(2);
        pick(0, 1); top(1);
        vf(3);
        top(0); mx[2] = sv(mx2(mx[2], mx[3]));
        vf(4);
        sw16(mx[0], mx[1]); mx[0] = sv(mx2(mx[0], mx[1]));
        vf(5); pv1(2, 0);
        sw32(mx[0], mx[2]);
        { const float a = mx[0], b = mx[2]; mx[0] = sv(mx2(a, b)); mx[2] = sv(mx2(b, a)); }
        vf(6); pv1(2, 1);
        sw32(mx[0], mx[2]);
        mx[3] = sv(mx[2]); mx[1] = sv(mx[0]);
        vf(7);
        sw16(mx[2], mx[3]); sw16(mx[0], mx[1]);
        SCHED();
#pragma unroll
        for (int mm = 0; mm < 4; ++mm) { const int mt = 3 - mm; u[0][mt] = sv(u[0][mt] - mx[mt]); u[1][mt] = sv(u[1][mt] - mx[mt]); if (mm == 1) vf(8); }
        vf(9);
#pragma unroll
        for (int mm = 0; mm < 4; ++mm) {
            const int mt = 3 - mm;
            u[0][mt] = sv(__builtin_amdgcn_exp2f(u[0][mt])); u[1][mt] = sv(__builtin_amdgcn_exp2f(u[1][mt]));
            if (mm > 0) iv[mt + 1] = sv(u[0][mt + 1] + u[1][mt + 1]);
            if (mm < 2) vf(10 + mm);
        }
        SCHED();
        sw16(iv[2], iv[3]);
        iv[0] = sv(u[0][0] + u[1][0]);
        iv[2] = sv(iv[2] + iv[3]);
        pv1(2, 2);
        sw16(iv[0], iv[1]); iv[0] = sv(iv[0] + iv[1]);
        uk(3);
        sw32(iv[0], iv[2]);
        iv[0] = sv(iv[0] + iv[2]);
        pv1(2, 3);
        iv[0] = sv(__builtin_amdgcn_rcpf(iv[0]));
        pv1(1, 0);
        iv[2] = sv(iv[0]);
        uk(2);
        sw32(iv[0], iv[2]);
        iv[3] = sv(iv[2]); iv[1] = sv(iv[0]);
        pv1(1, 1);
        sw16(iv[2], iv[3]); sw16(iv[0], iv[1]);
        SCHED();
        f32x4 o[4][2];
        u32x4 pf[4];
        auto nrm = [&](int mt) { u[0][mt] = sv(u[0][mt] * iv[mt]); u[1][mt] = sv(u[1][mt] * iv[mt]); SCHED(); };
        auto prb = [&](int mt) {
            const unsigned p = cvt_pk_bf16(u[0][mt], u[1][mt]);
            pf[mt] = (u32x4){__builtin_amdgcn_perm(0u, p, ps[0]), __builtin_amdgcn_perm(0u, p, ps[1]), __builtin_amdgcn_perm(0u, p, ps[2]), __builtin_amdgcn_perm(0u, p, ps[3])};
            asm volatile("" : "+v"(pf[mt]));
            SCHED();
        };
        auto pvm = [&](int mt, int cc) {
            o[mt][cc] = MFMA(fr8((u32x4){vw[cc][2 * mt], vw[cc][2 * mt + 1], vw[cc][2 * mt + 2], vw[cc][2 * mt + 3]}), fr8(pf[mt]), ((f32x4){0.f, 0.f, 0.f, 0.f}));
            asm volatile("" : "+v"(o[mt][cc]));
            SCHED();
        };
        auto out = [&](int mt) {
            const u32x2 lo = pack4(o[mt][0]), hi = pack4(o[mt][1]);
            __builtin_amdgcn_raw_buffer_store_b128((u32x4){lo[0], lo[1], hi[0], hi[1]}, rso, vg + hp<HO>(mt), 0, 16);
            SCHED();
        };
        vg = (unsigned)hv<HO>(lane);
        asm volatile("" : "+v"(vg));
        nrm(3); nrm(2); prb(3);
        pv1(1, 2); pv1(1, 3);
        pvm(3, 0); prb(2); pvm(3, 1);
        uk(1); uk(0);
        pvm(2, 0); nrm(1); nrm(0); pvm(2, 1);
        prb(1); pv1(0, 0); pv1(0, 1);
        pvm(1, 0); pv1(0, 2); pv1(0, 3); prb(0);
        pvm(1, 1); out(3); pvm(0, 0); out(2); pvm(0, 1); out(1); out(0);
        SCHED();
        QSTAMP(9);
        if (c.wid == 0) STAMP_AT(l * 8 + 2);
        QSTORE();
    }
}

#define MFMA32(a, b, c) __builtin_amdgcn_mfma_f32_32x32x16_bf16(__builtin_bit_cast(bf16x8, a), __builtin_bit_cast(bf16x8, b), (c), 0, 0, 0)
DI float max16(const f32x16& s) {
    float m = max3(s[0], s[1], s[2]), n = max3(s[3], s[4], s[5]);
    m = max3(m, s[6], s[7]); n = max3(n, s[8], s[9]);
    m = max3(m, s[10], s[11]); n = max3(n, s[12], s[13]);
    m = max3(max3(m, s[14], s[15]), n, n);
    auto b = __builtin_amdgcn_permlane32_swap(__float_as_uint(m), __float_as_uint(m), false, false);
    return __builtin_amdgcn_fmed3f(__uint_as_float(b[0]), __uint_as_float(b[1]), INFINITY);
}
template <bool HI> DI void attn_a(const Ctx& c0, int l, const char* Wo, KP kp) {
    const Ctx c = fresh(c0);
    asm volatile("" : "+s"(Wo));
    LAS char* smem = c.smem;
    const int lane = c.lane, w = c.wid;
    constexpr int H = HI ? 0 : 1;
    int qt = HI ? w - 4 : 3 - w;
    asm volatile("" : "+s"(qt));
    const float NEG = -INFINITY;
    int zq = (lane & 31) - 4 * (lane >> 5);
    unsigned lb = (unsigned)(((lane >> 4) & 1) * 1024 + ((lane >> 5) * 16 + (lane & 15)) * 16);
    unsigned qa = (unsigned)(AT_Q + H * 8192 + qt * 2048) + lb, ta = (unsigned)(AT_OWN + H * 16384 - 3 * 2048 + qt * 2048) + lb;
    asm volatile("" : "+v"(lb), "+v"(qa), "+v"(ta));
    auto rd = [&](unsigned base, int off) { return *(const LAS u32x4*)(lds(base) + off); };
    f32x16 ce;
    u32x4 sel;
    unsigned tm = (unsigned)(AT_N + lane * 16);
    auto masks = [&]() {
        if (HI) {
#pragma unroll
            for (int r = 0; r < 16; ++r) ce[r] = 8 * (r >> 2) + (r & 3) <= zq ? 0.f : NEG;
            asm volatile("" : "+v"(ce));
        }
        const unsigned s1 = ((lane >> 4) ^ (lane >> 2)) & 1 ? 0u : 0x3f803f80u;
        sel = (u32x4){s1, s1, s1, s1};
        asm volatile("" : "+v"(sel));
    };
    u64 ep, pa_, kvv, nkv, kact;
    unsigned po, pk_, cnta = M_CNT, one = 1u;
    char* ob;
    const char *kvb, *s1_, *s2_;
    unsigned vk, dst, fo;
    auto setup = [&]() {
        if (HI) {
            const u32x2 e = *(const LAS u32x2*)(smem + M_EP);
            ep = ((u64)(unsigned)__builtin_amdgcn_readfirstlane(e[1]) << 32) | (unsigned)__builtin_amdgcn_readfirstlane(e[0]);
            po = o_post(c);
            pk_ = pick(lane < 2, (unsigned)(WS_KBOX + ((c.pq + 1 + lane) * 16 + c.jq) * 256 + lane * 8), (unsigned)(WS_FLAGQ + (c.pq * 16 + c.jq) * FQS));
            pa_ = c.pq < 14 ? 7ull : (c.pq < 15 ? 5ull : 4ull);
            fo = (unsigned)(WS_KBOX + (c.pq * 16 + c.jq) * 256 + lane * 8);
            asm volatile("" : "+v"(po), "+v"(pk_), "+v"(fo));
            asm volatile("" : "+s"(ep), "+s"(pa_));
        }
        ob = c.ws + WS_O + (size_t)l * (8u << 20) + (size_t)(unsigned)(((c.jq * 2 + H) * 16 + c.pq) * 8192 + qt * 2048);
        kvb = c.ws + WS_KV + (size_t)(unsigned)((l >> 1) * (16u << 20) + c.jq * 32768);
        asm volatile("" : "+s"(ob), "+s"(kvb));
    };
    auto setup2 = [&]() {
        const int p1 = c.pq >= 1 ? c.pq - 1 : c.pq, p2 = c.pq >= 2 ? c.pq - 2 : c.pq;
        s1_ = kvb + (size_t)(unsigned)(p1 * 524288);
        s2_ = kvb + (size_t)(unsigned)(p2 * 524288);
        vk = (unsigned)((w & 3) * 1024 + lane * 16); dst = (unsigned)(AT_N + (w & 3) * 1024);
        asm volatile("" : "+s"(s1_), "+s"(dst));
        if (!HI) asm volatile("" : "+s"(s2_));
        asm volatile("" : "+v"(vk));
    };
    auto arrive = [&](auto cc, unsigned msk, unsigned val) {
        unsigned old = 0;
        if (lane == 0) asm volatile("ds_add_rtn_u32 %0, %1, %2 offset:%3\n\ts_waitcnt lgkmcnt(0)" : "+v"(old) : "v"(cnta), "v"(one), "i"(decltype(cc)::value * 4) : "memory");
        if ((__builtin_amdgcn_readfirstlane(old) & msk) == msk) { if (lane < 16) post(c.ws, po, ep + (4 * l + val)); }
    };
    static_assert((MBOX & 2) && !HO && !HK, "layout");
    int tv = 0;
    auto kvs = [&]() {
        if (!HI) {
            SCHED();
            LSTAMP(2);
            asm volatile("s_mov_b64 exec, 1\n\ts_waitcnt vmcnt(0)\n\tds_add_u32 %0, %1\n\ts_mov_b64 exec, -1" :: "v"(cnta), "v"(one) : "memory");
            LSTAMP(3);
            SCHED();
        }
    };
    asm volatile("" : "+v"(cnta), "+v"(one), "+v"(tm));
    if (HI) {
        masks(); setup(); setup2();
        if (w == 5) {
#pragma unroll
            for (int i = 0; i < 4; ++i) *(LAS f32x4*)(lds(tm) + i * 1024) = (f32x4){ce[4 * i], ce[4 * i + 1], ce[4 * i + 2], ce[4 * i + 3]};
        }
        kvv = ep + (4 * l + 2); nkv = 0 - kvv;
        kact = __builtin_amdgcn_ballot_w64(w == KPW && lane < 2 && lane < c.pq);
        asm volatile("" : "+v"(kvv), "+v"(nkv));
        asm volatile("" : "+s"(kact));
        WAIT_L0();
        WAIT_V0();
        LSTAMP(0);
        if (w == 4) __builtin_amdgcn_s_setprio(HP4);
        BAR();
        FENCE();
    }
    auto kvp = [&]() {
        if (HI && w == 4) {
            __builtin_amdgcn_s_setprio(3);
#pragma unroll 1
            for (unsigned spins = 0; spins < (1u << 24); ++spins) {
                if (__builtin_amdgcn_readfirstlane(*(volatile LAS int*)lds(cnta)) == 8 * l + 4) break;
            }
            if (__builtin_amdgcn_inverse_ballot_w64(pa_)) post(c.ws, pk_, kvv);
            STAMP_AT(l * 8 + 2);
            __builtin_amdgcn_s_setprio(HP4);
        }
        SCHED();
    };
    if (KPP == -1) kvp();
    FENCE();
    SCHED();
    LSTAMP(1);
    u32x4 q0, q1, k0, k1, va, vp, pa, pb;
    f32x16 cm, s0, s1, o;
    f32x4 ls;
    auto stage = [&](f32x16& sc, f32x16& sn, const f32x16& cn, bool pend, bool zero, unsigned kb, int ko, unsigned vb, int vo1, unsigned nb, int vo0) {
        auto ex = [&](int i) { float x = __builtin_amdgcn_exp2f(sc[i]); asm volatile("" : "+v"(x)); sc[i] = x; SCHED(); };
        auto cv = [&](u32x4& p, int j, int i) { unsigned x = cvt_pk_bf16(sc[i], sc[i + 1]); asm volatile("" : "+v"(x)); p[j] = x; SCHED(); };
        sn = MFMA32(k0, q0, cn); SCHED();
        k0 = rd(kb, ko); SCHED();
        ex(0); ex(1); ex(2);
        if (pend) { o = MFMA32(vp, pb, o); SCHED(); }
        ex(3); ex(8); ex(9);
        sn = MFMA32(k1, q1, sn); SCHED();
        k1 = rd(kb, ko + 512); SCHED();
        ex(10); ex(11); cv(pa, 0, 0); cv(pa, 1, 2);
        if (pend) { ls = MFMA(__builtin_bit_cast(bf16x8, sel), __builtin_bit_cast(bf16x8, pb), ls); SCHED(); }
        vp = rd(vb, 8192 + vo1 + 512); SCHED();
        cv(pa, 2, 8); cv(pa, 3, 10); ex(4); ex(5);
        o = MFMA32(va, pa, zero ? (f32x16)(0.f) : o); SCHED();
        va = rd(nb, 8192 + vo0); SCHED();
        ex(6); ex(7); ex(12);
        ls = MFMA(__builtin_bit_cast(bf16x8, sel), __builtin_bit_cast(bf16x8, pa), zero ? (f32x4)(0.f) : ls); SCHED();
        ex(13); ex(14); ex(15); cv(pb, 0, 4); cv(pb, 1, 6); cv(pb, 2, 12); cv(pb, 3, 14);
    };
    auto flush = [&]() {
        ls = MFMA(__builtin_bit_cast(bf16x8, sel), __builtin_bit_cast(bf16x8, pb), ls); SCHED();
        o = MFMA32(vp, pb, o); SCHED();
    };
    float nm;
    {
        if (!HI) {
#pragma unroll
            for (int i = 0; i < 4; ++i) {
                const f32x4 t = *(const LAS f32x4*)(lds(tm) + i * 1024);
                ce[4 * i] = t[0]; ce[4 * i + 1] = t[1]; ce[4 * i + 2] = t[2]; ce[4 * i + 3] = t[3];
            }
        }
        q0 = rd(qa, 0);
        const u32x4 d0 = rd(ta, 6144);
        q1 = rd(qa, 512);
        const u32x4 d1 = rd(ta, 6144 + 512);
        k0 = rd(ta, 4096); k1 = rd(ta, 4096 + 512); va = rd(ta, 8192 + 6144);
        SCHED();
        if (KSP == -1) kvs();
        if (!HI) { masks(); setup(); SCHED(); }
        s0 = MFMA32(d0, q0, ce);
        s0 = MFMA32(d1, q1, s0);
        SCHED();
        if (!HI) { setup2(); SCHED(); }
        if (KSP == 0) kvs();
        if (KPP == 0) kvp();
        nm = -max16(s0);
        asm volatile("" : "+v"(nm));
#pragma unroll
        for (int r = 0; r < 16; ++r) { float t; asm("v_mov_b32 %0, %1" : "=v"(t) : "v"(nm), "i"(r)); cm[r] = t; }
        asm volatile("" : "+v"(cm));
        SCHED();
#pragma unroll
        for (int r = 0; r < 16; ++r) s0[r] = addf(s0[r], nm);
        asm volatile("" : "+v"(s0));
        SCHED();
        if (KSP == 1) kvs();
        if (KPP == 1) kvp();
        stage(s0, s1, cm, false, true, ta, 2048, ta, 6144, ta, 4096);
        if (KSP == 2) kvs();
        if (KPP == 2) kvp();
        if (qt > 0) {
            stage(s1, s0, cm, true, false, ta, 0, ta, 4096, ta, 2048);
            if (KSP == 3) kvs();
            if (qt > 1) {
                stage(s0, s1, cm, true, false, ta, 0, ta, 2048, ta, 0);
                if (qt > 2) stage(s1, s0, cm, true, false, ta, 0, ta, 0, ta, 0);
            }
        } else if (KSP == 3) kvs();
        flush();
    }
    LSTAMP(4);
    f32x16 c2;
    unsigned na, nb;
    auto rso = __builtin_amdgcn_make_buffer_rsrc(ob, 0, 2048, 0x00020000);
    auto starts = [&]() {
        const float n1 = c.pq >= 1 ? nm : NEG, n2 = c.pq >= 2 ? nm : NEG;
        asm volatile("" : "+v"(zq));
#pragma unroll
        for (int r = 0; r < 16; ++r) { cm[r] = n1; c2[r] = n2; ce[r] = 8 * (r >> 2) + (r & 3) <= zq ? NEG : n2; }
        na = (unsigned)(AT_N + H * 32768) + lb; nb = na + (unsigned)(16384 + qt * 2048);
        asm volatile("" : "+v"(cm), "+v"(c2), "+v"(ce), "+v"(na), "+v"(nb));
        asm volatile("" : "+s"(rso));
        SCHED();
    };
    {
        auto piece = [&](int b, int i) { dma16<true>(dst + b * 16384 + i * 4096, vk, ((b & 1) ? s2_ : s1_) + (b >> 1) * 16384 + i * 4096); };
        auto block = [&](int b) {
#pragma unroll
            for (int i = 0; i < 4; ++i) piece(b, i);
        };
        Ask t;
        if (HI) {
            poll_rmw<true>(t, c.ws, fo, __builtin_amdgcn_inverse_ballot_w64(kact), nkv, 62 - 4 * l);
            if (w == KPW) STAMP_AT(32 + l * 8 + 1);
        }
        WAIT_L0();
        LSTAMP(5);
        BAR();
        FENCE();
        LSTAMP(6);
        piece(0, H); piece(0, 2 + H);
        if (HI) {
            starts();
            if (APRIO) __builtin_amdgcn_s_setprio(APRIO);
            WAIT_V0();
            keep(t);
            LSTAMP(7);
            BAR();
            FENCE();
            if (w == 4) STAMP_AT(l * 8 + 3);
        } else {
            if (AB2 >= 0) block(1);
            if (AB2 > 0) block(2);
            starts();
            Ctx c2 = c;
            if (ELO) c2.wid = 0;
            unsigned vw = wo_w_off(c2);
            int qs = c.cw == 3 ? 1024 : 8 * D_ * 2;
            asm volatile("" : "+v"(vw));
            asm volatile("" : "+s"(qs));
            SCHED();
            wait_v<4 * AB2 + 4>();
            LSTAMP(7);
            BAR();
            if (AB2 < 0) block(1);
            if (AB2 <= 0) block(2);
            block(3);
            wait_v<8>();
            BAR();
            if (!ELO) {
                wo_w_issue<WA0, WA1>(c, Wo, vw);
                wait_v<WA1 - WA0>();
            } else WAIT_V0();
            LSTAMP(9);
            BAR();
            FENCE();
            if (!ELO) wo_w_issue<WA1, 26>(c, Wo, vw);
            else {
                const int s0 = w == 0 ? WA0 : (w == 1 ? WA0 + EN0 : WA0 + EN0 + EN1), s1 = w == 0 ? WA0 + EN0 : (w == 1 ? WA0 + EN0 + EN1 : (w == 2 ? 22 : 0));
#pragma unroll 1
                for (int sl = s0; sl < s1; ++sl) {
                    const char* b = Wo + (size_t)(unsigned)((2 * (sl & 15) + (sl >> 4)) * 65536);
#pragma unroll
                    for (int q = 0; q < 4; ++q) dma16<true>(sl * 4096 + q * 1024, vw, b + q * qs);
                }
            }
            if (LSL) { for (int i = 0; i < qt; ++i) __builtin_amdgcn_s_sleep(LSL); }
        }
    }
    SCHED();
    LSTAMP(8);
    {
        const u32x4 d0 = rd(na, 0), d1 = rd(na, 512);
        k0 = rd(na, 2048); k1 = rd(na, 2048 + 512); va = rd(na, 8192);
        SCHED();
        s0 = MFMA32(d0, q0, cm);
        s0 = MFMA32(d1, q1, s0);
        SCHED();
        stage(s0, s1, cm, false, false, na, 4096, na, 0, na, 2048);
        stage(s1, s0, cm, true, false, na, 6144, na, 2048, na, 4096);
        if (HI) { BAR(); FENCE(); SCHED(); }
        stage(s0, s1, cm, true, false, nb, 0, na, 4096, na, 6144);
        if (HI && CPOS == 3) { BAR(); SCHED(); }
        stage(s1, s0, ce, true, false, nb, 2048, na, 6144, nb, 0);
        if (HI && CPOS == 4) { WAIT_L0(); BAR(); SCHED(); }
        stage(s0, s1, c2, true, false, nb, 4096, nb, 0, nb, 2048);
        if (HI && CPOS == 5) { BAR(); SCHED(); }
        if (qt < 3) {
            stage(s1, s0, c2, true, false, nb, 6144, nb, 2048, nb, 4096);
            if (qt < 2) {
                stage(s0, s1, c2, true, false, nb, 0, nb, 4096, nb, 6144);
                if (qt < 1) stage(s1, s0, c2, true, false, nb, 0, nb, 6144, nb, 0);
            }
        }
        flush();
    }
    LSTAMP(11);
    auto out = [&]() {
        const float iv = __builtin_amdgcn_rcpf(ls[0]);
#pragma unroll
        for (int u = 0; u < 2; ++u) {
            u32x4 t;
#pragma unroll
            for (int i = 0; i < 4; ++i) { const int r = 4 * u + (i >> 1) * 8 + (i & 1) * 2; t[i] = cvt_pk_bf16(o[r] * iv, o[r + 1] * iv); }
            __builtin_amdgcn_raw_buffer_store_b128(t, rso, lb + u * 512, 0, 16);
        }
        SCHED();
    };
    out();
    if (__builtin_expect(__any(!(ls[0] < SAFE_SUM)), 0)) {
        auto rk = __builtin_amdgcn_make_buffer_rsrc((void*)(kvb + H * 16384), 0, 16u << 20, 0x00020000);
        const int g0 = c.pq * 4 + qt - 8;
        float m = NEG;
#pragma unroll 1
        for (int ps = 0; ps < 2; ++ps) {
            o = (f32x16)(0.f); ls = (f32x4)(0.f);
#pragma unroll 1
            for (int j = g0 < 0 ? -g0 : 0; j < 9; ++j) {
                const int so = ((g0 + j) >> 2) * 524288 + ((g0 + j) & 3) * 2048;
                u32x4 f[4];
#pragma unroll
                for (int i = 0; i < 4; ++i) f[i] = __builtin_amdgcn_raw_buffer_load_b128(rk, lb + (i & 1) * 512, so + (i >> 1) * 8192, 16);
                f32x16 s;
                const float sb = ps ? -m : 0.f;
                float sa = j == 0 ? NEG : sb, sz = j == 8 ? NEG : sb;
                asm volatile("" : "+v"(zq), "+v"(sa), "+v"(sz));
#pragma unroll
                for (int r = 0; r < 16; ++r) s[r] = 8 * (r >> 2) + (r & 3) <= zq ? sa : sz;
                s = MFMA32(f[0], q0, s);
                s = MFMA32(f[1], q1, s);
                if (ps == 0) m = fmaxf(m, max16(s));
                else {
                    u32x4 p[2];
#pragma unroll
                    for (int r = 0; r < 16; r += 2) p[(r >> 2) & 1][(r >> 3) * 2 + ((r >> 1) & 1)] = cvt_pk_bf16(__builtin_amdgcn_exp2f(s[r]), __builtin_amdgcn_exp2f(s[r + 1]));
#pragma unroll
                    for (int u = 0; u < 2; ++u) {
                        o = MFMA32(f[2 + u], p[u], o);
                        ls = MFMA(__builtin_bit_cast(bf16x8, sel), __builtin_bit_cast(bf16x8, p[u]), ls);
                    }
                }
            }
        }
        __builtin_amdgcn_s_waitcnt(0x0F70);
        WAIT_V0();
        out();
    }
    SCHED();
    if (w == 3 || w == 4) STAMP_AT(32 + l * 8 + 3 + w);
    LSTAMP(12);
    if (HI) {
        WAIT_V0();
        arrive(std::integral_constant<int, 1>{}, 3u, 3u);
        if (w == 4) STAMP_AT(32 + l * 8 + 4);
        LSTAMP(13);
        if (APRIO) __builtin_amdgcn_s_setprio(0);
        if (lane == 0) asm volatile("ds_add_u32 %0, %1 offset:12" :: "v"(cnta), "v"(one) : "memory");
        WAIT_V0();
        arrive(std::integral_constant<int, 2>{}, 7u, 4u);
        if (w == 5 || w == 6) {
            Ctx c2 = c;
            c2.wid = 2 * w - 10;
            unsigned vw = wo_w_off(c2), vw2 = vw + (c.cw == 3 ? 1024u : 8u * D_ * 2);
            asm volatile("" : "+v"(vw), "+v"(vw2));
#pragma unroll 1
            for (unsigned spins = 0; spins < (1u << 22); ++spins) {
                if (__hip_atomic_load((LAS unsigned*)(smem + M_CNT + 12), __ATOMIC_RELAXED, __HIP_MEMORY_SCOPE_WORKGROUP) == (unsigned)(2 * l + 4)) break;
                __builtin_amdgcn_s_sleep(1);
            }
            FENCE();
            wo_w_issue<ELO ? 22 : 26, 30>(c2, Wo, vw);
            c2.wid += 1;
            wo_w_issue<ELO ? 22 : 26, 30>(c2, Wo, vw2);
        }
    }
    LSTAMP(23);
    ASTORE();
}

DI void wo_load(const Ctx& c0, int l, const char* Wn, const char* Wo) {
    const Ctx c = fresh(c0);
    asm volatile("" : "+s"(Wn), "+s"(Wo));
    const u64 ep = lds_epoch(c.smem);
    const unsigned fo = o_find(c);
    if ((CP2 & 1) && l == 0 && c.wid == 4) code_again(c);
    if ((CP2 & 2) && l == 1 && c.wid == 4) code_again(c);
        if (l == 0 && c.wid == 6) {
            const int fr = c.lane & 15, od = fr & 1, j0 = 4 * (c.lane >> 4) + od;
            const bool p0 = j0 > fr, p1 = j0 + 2 > fr;
            const unsigned N = 0xff800000u, Z = 0x0c0c0c0cu, lo = od ? 0x01000c0cu : 0x0c0c0100u, hi = od ? 0x03020c0cu : 0x0c0c0302u;
            LAS u32x4* t = (LAS u32x4*)(c.smem + M_TAB + c.lane * 16);
            t[0] = (u32x4){p0 && !od ? 0u : N, p0 && od ? 0u : N, p1 && !od ? 0u : N, p1 && od ? 0u : N};
            t[64] = (u32x4){!p0 && !od ? 0u : N, !p0 && od ? 0u : N, !p1 && !od ? 0u : N, !p1 && od ? 0u : N};
            t[128] = (u32x4){p0 ? lo : Z, p1 ? hi : Z, p0 ? Z : lo, p1 ? Z : hi};
            t[192] = (u32x4){(unsigned)(M_RSTD + j0 * 4 + (p0 ? 0 : 64)), (unsigned)(M_RSTD + j0 * 4 + 8 + (p1 ? 0 : 64)), 0u, 0u};
        }
    if (CPY && l == 0 && c.wid == CKW) {
#pragma unroll 1
        for (int i = 0; i < 2; ++i) {
            const int j = i ? c.jw : c.jq, g = i ? 2 - 2 * (j >> 3) : 1 + 2 * (j >> 3);
            if ((j & 7) == 3) poll_flags(c.ws, ep, (unsigned)WS_FLAGQ + ((c.lane >> 2) * 16 + 4 * g + (c.lane & 3)) * FQS, true, 2u);
        }
    }
    Ask t, t2;
    if (!(l & 1)) {
        Ctx c2 = c;
        c2.wid = 2 * c.wid - 10;
        unsigned vw = wo_w_off(c2), vw2 = vw + (c.cw == 3 ? 1024u : 8u * D_ * 2);
        asm volatile("" : "+v"(vw), "+v"(vw2));
        poll_rmw(t, c.ws, fo, c.wid == 7 && c.lane < 16, ep + (4 * l + 3), 61 - 4 * l);
        if (c.wid == 7) STAMP_AT(32 + l * 8 + 2);
        BAR();
        if (c.wid == 5 || c.wid == 6) {
            __builtin_amdgcn_s_sleep(W30S);
            wo_w_issue<30, 32>(c2, Wo, vw);
            c2.wid += 1;
            wo_w_issue<30, 32>(c2, Wo, vw2);
        }
        if (c.wid != 7) WAIT_V0();
    } else {
        asm volatile("" : "=v"(t.v[0]), "=v"(t.v[1]), "=v"(t.v[2]), "=v"(t.v[3]));
        WAIT_V0();
    }
    poll_rmw(t2, c.ws, fo, c.wid == 7 && c.lane < 16, ep + (4 * l + 4), 60 - 4 * l);
    if (c.wid == 7) STAMP_AT(32 + l * 8 + 2 + !(l & 1));
    BAR();
    WAIT_V0();
    keep(t); keep(t2);
    if (l == 0) {
        if (c.wid == 4) WARM(WARM_C4);
        if (c.wid == 5) WARM(WARM_C5);
        if (c.wid == 6) WARM(WARM_C6);
        if (c.wid == 7) WARM(WARM_C7);
    }
    if (l == 3 && c.bid == 0 && c.wid == 7) {
#pragma unroll 1
        for (int i = 0; i < 4; ++i) poll_flags(c.ws, ep, (unsigned)WS_FLAGQ + (i * 64 + c.lane) * FQS, true, 1u);
        if (c.lane == 0) __hip_atomic_store((GAS u64*)(c.ws + WS_EPOCH), ep + 64, __ATOMIC_RELAXED, __HIP_MEMORY_SCOPE_AGENT);
    }
    if (RS0 && l == 3) BAR();
}

DI void wo_consume(const bool B, const Ctx& c0, const bf16_t* xin, bf16_t* xout, int l) {
    u32x4 res[2][2];
    const Ctx c = fresh(c0);
    LAS char* smem = c.smem;
    const int lane = c.lane, w = c.wid, fr = lane & 15, fq = lane >> 4;
    const char* obase = c.ws + WS_O + (size_t)l * (8u << 20);
    int ovo = (c.pw * 8 + w * 2) * 1024 + lane * 16;
    asm volatile("" : "+v"(ovo));
    constexpr int NP = WNQ, R = 3;
    static_assert(!HO && !HX && !HS && !RLD && BPR == 1, "layout");
    auto ro = __builtin_amdgcn_make_buffer_rsrc((void*)obase, 0, 4u << 20, 0x00020000);
    asm volatile("" : "+s"(ro));
    u32x4 xa[NP][2];
    s16x4 wf[R][8];
    f32x4 acc[2][4];
    unsigned bw[8];
    int coff, rmo, spr;
    u32x4 on = {0x3f803f80u, 0x3f803f80u, 0x3f803f80u, 0x3f803f80u};
    unsigned sto, xpo, coff1, cnta = M_CNT, one = 1;
    u64 xv, b0, b1, am;
    auto rs = __builtin_amdgcn_make_buffer_rsrc(xout, 0, S_ * D_ * 2, 0x00020000);
    auto rst = __builtin_amdgcn_make_buffer_rsrc(c.ws + WS_STATS + (size_t)((l + 1) & 3) * 524288, 0, 524288, 0x00020000);
    auto prep2 = [&]() {
        sto = fq == (fr >> 2) && l < 3 ? (unsigned)((c.pw * 16 + c.jw) * 1024 + (w * 32 + fr) * 8) : 0x80000000u;
        xpo = x_post(c);
        coff = (int)pick(l == 3, (unsigned)rmo, (unsigned)coff);
        coff1 = coff + (l == 3 ? xrow<0>(0, 16) : 2048);
        spr = l == 3 ? 64 : 1024;
        asm volatile("" : "+s"(spr));
        b0 = __builtin_amdgcn_ballot_w64(fr & 1); b1 = __builtin_amdgcn_ballot_w64(fr & 2); am = __builtin_amdgcn_ballot_w64(x_act(c, !B));
        asm volatile("" : "+v"(on), "+v"(sto), "+v"(xpo), "+v"(coff), "+v"(coff1), "+v"(xv), "+v"(cnta), "+v"(one));
        asm volatile("" : "+s"(rs), "+s"(rst), "+s"(b0), "+s"(b1), "+s"(am));
    };
    auto prep = [&]() {
        int ln = lane;
        asm volatile("" : "+v"(ln));
        const int fr = ln & 15, fq = ln >> 4, q4 = fr >> 2, p4 = ln & 3;
        coff = (c.pw * 16 + c.jw) * 16384 + w * 4096 + (((fq & 1) * 2 + (fq >> 1)) * 16 + fr) * 16;
        rmo = xrow<0>(c.pw, w * 32 + fr) + xcol<0>(c.jw) + (fq & 1) * 32 + (fq >> 1) * 16;
        asm volatile("" : "+v"(coff), "+v"(rmo));
        const int fb = (q4 & 2) | ((fq & 1) << 2);
        const unsigned swp = (B && w >= 2) ? 2048u : 0u;
#pragma unroll
        for (int cc = 0; cc < 4; ++cc) {
            const unsigned b = (unsigned)((4 * fq + q4) * 128 + (((2 * cc + (p4 >> 1)) ^ fb) << 4) + (p4 & 1) * 8);
            bw[2 * cc] = b + swp; bw[2 * cc + 1] = b + (swp ^ 2048u);
            asm volatile("" : "+v"(bw[2 * cc]), "+v"(bw[2 * cc + 1]));
        }
    };
    auto start = [&](int mt, int pr) {
            {
                auto r0 = __builtin_amdgcn_permlane16_swap(res[mt][pr][0], res[mt][pr][2], false, false);
                auto r1 = __builtin_amdgcn_permlane16_swap(res[mt][pr][1], res[mt][pr][3], false, false);
#pragma unroll
                for (int h = 0; h < 2; ++h) {
                    acc[mt][2 * pr + h] = (f32x4){bf_lo(r0[h]), bf_hi(r0[h]), bf_lo(r1[h]), bf_hi(r1[h])};
                    asm volatile("" : "+v"(acc[mt][2 * pr + h]));
                }
            }
    };
    auto rdw = [&](auto ic) {
        constexpr int i = decltype(ic)::value, h = i >> 3, k = i & 7;
        wf[h % R][k] = tr_read(lds(bw[k]) + (h & 15) * 4096);
        if constexpr (h == 15) { bw[k] += 65536; asm volatile("" : "+v"(bw[k])); }
    };
    {
        auto resid = [&]() {
            const unsigned roff = pick(l != 0, (unsigned)coff, (unsigned)rmo);
            const char* xp = (const char*)xin;
#pragma unroll
            for (int i = 0; i < 4; ++i) aload4c(res[i >> 1][i & 1], roff, xp + (i >> 1) * (l ? 2048 : xrow<0>(0, 16)) + (i & 1) * (l ? 1024 : 64));
            SCHED();
        };
        unsigned opo = o_post(c), oca = B ? M_CNT : M_CNT + 8, old = 0, msk = B ? 3u : 7u;
        xv = lds_epoch(smem) + (unsigned)(l + 2);
        u64 ov = xv + (unsigned)(3 * l + 2);
        prep();
        asm volatile("" : "+v"(opo), "+v"(oca), "+v"(ov), "+v"(old), "+v"(one));
        asm volatile("" : "+s"(msk));
        SCHED();
        if (RBP) resid();
        static_for<0, 16>([&](auto ic) { rdw(ic); SCHED(); });
        if (!P2A) prep2();
        SCHED();
        wait_v<RBP ? 4 : 0>();
        if (lane == 0) asm volatile("ds_add_rtn_u32 %0, %1, %2\n\ts_waitcnt lgkmcnt(0)" : "+v"(old) : "v"(oca), "v"(one) : "memory");
        if ((__builtin_amdgcn_readfirstlane(old) & msk) == msk) { if (lane < 16) post(c.ws, opo, ov); }
        if (w == 0) STAMP_AT(l * 8 + 4);
        SCHED();
        if (!RBP) resid();
        if (P2A == 1) prep2();
        SCHED();
        if (w == 3) STAMP_AT(32 + l * 8 + 5);
        BAR();
    }
    FENCE();
    SCHED();
    if (w == 0) STAMP_AT(l * 8 + 5);
    int so = 0;
    auto next = [&](int j) { so += j == 15 ? 131072 - 15 * 262144 : 262144; asm volatile("" : "+s"(so)); };
    auto ldo = [&](int j, int mt) { xa[j % NP][mt] = __builtin_amdgcn_raw_buffer_load_b128(ro, ovo + mt * 1024, so, 16); };
#pragma unroll
    for (int h = 0; h < NP; ++h) {
        ldo(h, 0);
        SCHED();
        if (h > SH0 && h <= SH0 + 2) { start(h - SH0 - 1, 0); SCHED(); }
        ldo(h, 1);
        if (!WAL || h + 1 < NP) next(h);
        SCHED();
        if (P2A >= 2 && h == P2A - 2) { prep2(); SCHED(); }
        if (h == SH0) {
            wait_v<2 * SH0 + 2>();
            asm volatile("" : "+v"(res[0][0]), "+v"(res[0][1]), "+v"(res[1][0]), "+v"(res[1][1]));
        }
        if (h > SH0 && h <= SH0 + 2) start(h - SH0 - 1, 1);
        SCHED();
    }
    int d0 = 262144, d1 = 131072 - 15 * 262144;
    asm volatile("" : "+s"(d0), "+s"(d1));
    static_assert(WEP || !WAL, "last step");
    static_for<0, WEP ? 31 : 32>([&](auto jc) {
        constexpr int j = decltype(jc)::value;
        constexpr int c0 = j < 30 ? 16 + 8 * j : 256, c1 = j < 30 ? c0 + 8 : 256;
        if constexpr (j + NP == 16) {
            static_assert(c0 <= 8 * 20, "weights that landed before the attention");
            if (!B) BAR();
            FENCE();
            asm volatile("" : "+v"(ovo));
            SCHED();
        }
        if constexpr (j == 0 || j + NP == 16) ALIGN8();
        constexpr int nv = WAL == 1 || j == 0 || j + NP > 32 ? 2 * (NP - 1 < 31 - j ? NP - 1 : 31 - j) : 2 * NP - 3;
        __builtin_amdgcn_s_waitcnt(0x0870 | nv);
        const int sp = so;
        static_assert(!RS0 || (RS0 + NP >= 32 && RS0 < 31 && QW_SLOT <= (RS0 - 1) * 4096), "R0");
        if constexpr (RS0 && j == RS0) BAR();
        else asm volatile("s_add_i32 %0, %1, %2" : "=&s"(so) : "s"(sp), "s"(j + NP == 16 ? d1 : d0) : "scc");
        SCHED();
        static_for<0, 8>([&](auto mc) {
            constexpr int m = decltype(mc)::value, mt = m >> 2, nt = mt ? 3 - (m & 3) : m & 3;
            const bf16x8 wv = __builtin_shufflevector(wf[j % R][2 * nt], wf[j % R][2 * nt + 1], 0, 1, 2, 3, 4, 5, 6, 7);
            if constexpr (j < 31) asm volatile("v_mfma_f32_16x16x32_bf16 %0, %1, %2, %0" : "+v"(acc[mt][nt]) : "v"(wv), "v"(xa[j % NP][mt]));
            else acc[mt][nt] = MFMA(wv, __builtin_bit_cast(bf16x8, xa[j % NP][mt]), acc[mt][nt]);
            SCHED();
            if constexpr (c0 < c1 && m < 7) { rdw(std::integral_constant<int, c0 + m>{}); SCHED(); }
            if constexpr (c0 < c1 && m == 6) rdw(std::integral_constant<int, c0 + 7>{});
            if constexpr (m == WL1 && j > 0 && j - 1 + NP < 32) xa[(j + NP - 1) % NP][1] = __builtin_amdgcn_raw_buffer_load_b128(ro, ovo + 1024, sp, 16);
            if constexpr (m == WL1 + 4 && j + NP < 32) ldo(j, 0);
            SCHED();
        });
    });
    const bf16x8 ones = __builtin_bit_cast(bf16x8, on);
    u32x4 o[2][2];
    f32x4 sm[2], g[2];
    const f32x4 zero = {0.f, 0.f, 0.f, 0.f};
    auto sel = [&](float a, float b, u64 m) { return __builtin_amdgcn_inverse_ballot_w64(m) ? b : a; };
    auto stats = [&](int mt) {
        float qa = sel(g[mt][0], g[mt][1], b0), qb = sel(g[mt][2], g[mt][3], b0);
        asm volatile("" : "+v"(qa), "+v"(qb));
        SCHED();
        if (WEP) asm volatile("" :: "v"(sm[1][1]), "v"(sm[1][2]), "v"(sm[1][3]), "v"(o[1][0]), "v"(o[1][1]), "v"(on));
        __builtin_amdgcn_raw_buffer_store_b64((u32x2){__float_as_uint(sm[mt][0]), __float_as_uint(sel(qa, qb, b1))}, rst, sto + mt * 128, 0, 16);
        SCHED();
    };
    u32x2 pk[2];
    auto last = [&](int mt, int nt) {
        acc[mt][nt] = MFMA(__builtin_shufflevector(wf[31 % R][2 * nt], wf[31 % R][2 * nt + 1], 0, 1, 2, 3, 4, 5, 6, 7), __builtin_bit_cast(bf16x8, xa[31 % NP][mt]), acc[mt][nt]);
        SCHED();
    };
    auto cv = [&](int mt, int pr) {
        pk[0] = pack4(acc[mt][2 * pr]); pk[1] = pack4(acc[mt][2 * pr + 1]);
        asm volatile("" : "+v"(pk[0]), "+v"(pk[1]));
        SCHED();
    };
    auto st = [&](int mt, int pr) {
        o[mt][pr] = widen(pk[0], pk[1]);
        __builtin_amdgcn_raw_buffer_store_b128(o[mt][pr], rs, mt ? coff1 : coff, pr ? spr : 0, 16);
        SCHED();
    };
    auto gq = [&](int mt, int pr, bool f) { const bf16x8 of = __builtin_bit_cast(bf16x8, o[mt][pr]); g[mt] = MFMA(of, of, f ? zero : g[mt]); SCHED(); };
    auto gs = [&](int mt, int pr, bool f) { sm[mt] = MFMA(ones, __builtin_bit_cast(bf16x8, o[mt][pr]), f ? zero : sm[mt]); SCHED(); };
    WAIT_L0();
    last(0, 0); last(0, 1); last(0, 2); last(0, 3);
    last(1, 3); cv(0, 0);
    last(1, 2); st(0, 0);
    last(1, 1); cv(0, 1);
    last(1, 0); st(0, 1);
    if (w == 0) STAMP_AT(l * 8 + 6);
    gq(0, 0, true); cv(1, 1);
    gs(0, 0, true); st(1, 1);
    gq(0, 1, false); cv(1, 0);
    gs(0, 1, false); st(1, 0);
    gq(1, 1, true);
    gs(1, 1, true);
    gq(1, 0, false);
    gs(1, 0, false);
    stats(0);
    stats(1);
    if (l < 3) {
        unsigned old = 0;
        if (!RS0 || RN1 > RN0) BAR();
        wait_v<XE2>();
        if (lane == 0) asm volatile("ds_add_rtn_u32 %0, %1, %2\n\ts_waitcnt lgkmcnt(0)" : "+v"(old) : "v"(cnta), "v"(one) : "memory");
        if ((__builtin_amdgcn_readfirstlane(old) & 3) == 3) {
            if (__builtin_amdgcn_inverse_ballot_w64(am)) post(c.ws, xpo, xv);
        }
    }
    if (w == 0) STAMP_AT(l * 8 + 7);
}

__global__ __launch_bounds__(512, 2) __attribute__((aligned(4096))) void k_main(Params P) {
    extern __shared__ __attribute__((aligned(16))) char smem_[];
    KP kp = (KP)__builtin_amdgcn_kernarg_segment_ptr();
    const char *x0, *w0, *wo;
    Ctx c;
    asm volatile("s_load_dwordx2 %0, %4, 0x0\n\ts_load_dwordx2 %1, %4, 0x8\n\ts_load_dwordx2 %2, %4, 0x10\n\ts_load_dwordx2 %3, %4, 0x20" : "=&s"(x0), "=&s"(w0), "=&s"(c.ws), "=&s"(wo) : "s"(kp));
    asm volatile(".macro PADC id\n" PADPFX "\\id:\n.if " PADS "\ns_cbranch_execz .Lpad_ret\ns_nop 0\n.endif\n.endm\n"
                 "s_branch .Lpad_on\n.Lpad_ret:\n\ts_setpc_b64 vcc\n.Lpad_on:");
    c.smem = (LAS char*)smem_;
    c.lane = threadIdx.x & 63;
    c.wid = __builtin_amdgcn_readfirstlane(threadIdx.x >> 6);
    c.bid = blockIdx.x;
    const int xcd = c.bid & 7, idx = c.bid >> 3;
    c.pq = ((xcd % (16 / QXM)) * QXM + (idx % QXM)) ^ PQX; c.jq = ((xcd / (16 / QXM)) * (32 / QXM) + (idx / QXM)) ^ JQX;
    const bool loader = c.wid >= 4;
    if (CPF == 1 ? !loader : c.wid == 4) {
        const char* pc; int len;
        asm volatile("s_getpc_b64 %0\n.Lcpf_a:\n\ts_mov_b32 %1, .Lcpf_end-.Lcpf_a-4" : "=s"(pc), "=s"(len));
#pragma unroll
        for (int i = 0; i < (CPF == 1 ? 2 : 1); ++i) {
            const int o = min((CPF == 1 ? (i * 4 + c.wid) * 64 + c.lane : (CPF == 2 ? (idx & 7) * 64 + c.lane : idx * 16 + (c.lane & 15))) * 128, len);
            asm volatile("s_mov_b32 m0, %0\n\ts_nop 3\n\tglobal_load_lds_dword %1, %2" ::"s"(M_TCH), "v"(o), "s"(pc) : "memory");
        }
    }
    asm volatile("s_cmp_lt_u32 %0, 5\n\ts_cbranch_scc1 11\n\ts_mov_b64 exec, 0\n\ts_cmp_lg_u32 %0, 5\n\ts_cbranch_scc1 1\n\t" WARM_A5 "s_cmp_lg_u32 %0, 6\n\ts_cbranch_scc1 1\n\t" WARM_A6
                 "s_cmp_lg_u32 %0, 7\n\ts_cbranch_scc1 1\n\t" WARM_A7 "s_mov_b64 exec, -1" :: "s"(c.wid) : "vcc", "scc");
    unsigned va, vw;
    {
        const int w = c.wid, lr = c.lane >> 3, lc = c.lane & 7;
        int t0 = first_tile(first_order(c.pq, c.jq), 0);
        asm volatile("" : "+s"(t0));
        va = (unsigned)((c.pq * 128 + 8 * (w & 3) + lr) * (D_ * 2) + ((lc ^ lr) << 4));
        vw = (unsigned)((8 * (w & 3) + lr) * 6144 + c.jq * 128 + ((lc ^ ((lr & 2) | ((w & 1) << 2))) << 4));
        asm volatile("" : "+v"(va), "+v"(vw));
        asm volatile("s_waitcnt lgkmcnt(0)" : "+s"(x0), "+s"(w0), "+s"(c.ws), "+s"(wo));
        if (ST0 && c.wid == 3) STAMP_AT(41);
        auto stage = [&](int s, int h0, int h1, bool a) {
            const char* xb = x0 + (unsigned)(t0 * 128);
            const char* wb = w0 + (unsigned)(t0 * (64 * 6144));
            for (int h = h0; h <= h1; ++h) {
                if (a) {
#pragma unroll
                    for (int j = 0; j < 2; ++j) dma16<false>(QA_OFF + s * QA_SLOT + (j * 8 + h * 4 + (w & 3)) * 1024, va, xb + (j * 64 + h * 32) * (D_ * 2));
                }
#pragma unroll
                for (int j = 0; j < 3; ++j) dma16<false>(qw_slot(s) + (j * 8 + h * 4 + (w & 3)) * 1024, vw, wb + h * (32 * 6144) + j * 2048);
            }
        };
        if (ENT == 0) stage(0, w >> 2, w >> 2, true);
        else if (!loader) stage(0, 0, 1, true);
        if (ST0 && c.wid == 1) STAMP_AT(43);
        if (w == 0 && c.lane < 2) touch(M_EP, (unsigned)(WS_EPOCH + c.lane * 4), c.ws);
        if (w == 1 && c.lane < 5) *(LAS unsigned*)(c.smem + (c.lane < 4 ? M_CNT + c.lane * 4 : M_GO)) = 0u;
    }
    asm volatile("" : "+s"(kp), "+s"(c.bid), "+s"(c.ws));
    static_assert(WXM == 4 && ALT == 1, "map");
    c.pw = ((c.pq & 8) ^ 8) + ((c.bid >> 1) & 1) * 4 + ((c.bid >> 3) & 3); c.jw = (c.bid & 4) * 2 + (c.bid >> 5); c.cw = 0;
    if (PADN >= 0) asm volatile("s_branch .Lpad_end\n\ts_nop 0\n\t.fill %c0, 4, 0xbf800000\n.Lpad_end:" :: "i"(PADN * 64));
    const char* xi = x0;
    auto step = [&](int l) {
        asm volatile("" : "+s"(kp), "+s"(c.ws), "+s"(c.jw), "+s"(c.bid));
        c.pq ^= 8; c.pw ^= 8; c.cw = CPY ? c.jw & 7 : 0;
        xi = c.ws + WS_X + (unsigned)(l * (8u << 20));
        wo = (const char*)((l & 1) ? kp->wo0 : kp->wo1);
        if (c.cw == 3) wo = c.ws + WS_CO + (((l & 1) ? 2 : 0) + (c.jw >> 3)) * 4096;
    };
    if (loader) {
        MARK("lf");
        PADC();
        qkv_load<false, true>(c, xi, w0, wo, 0, kp, va, vw);
#pragma unroll 1
        for (int l = 0;;) {
            asm volatile("" : "+s"(l));
            MARK("ah");
            if (!(l & 1)) attn_a<true>(c, l, wo, kp);
            MARK("wl");
            wo_load(c, l, nullptr, wo);
            if (l == 3) break;
            step(l);
            ++l;
            if (l == 2) { MARK("la"); qkv_load<false, false>(c, xi, (const char*)kp->wqkv0, wo, l); }
            else { MARK("lb"); qkv_load<true, false>(c, xi, (const char*)kp->wqkv1, wo, l); }
        }
    } else {
        bf16_t* xo = (bf16_t*)(c.ws + WS_X);
#pragma unroll 1
        for (int l = 0; l < 4; ++l) {
            if (l & 1) { MARK("cb"); qkv_consume<true>(c, l, kp); }
            else {
                MARK("ca");
                qkv_consume<false>(c, l, kp, va, vw);
                MARK("al");
                attn_a<false>(c, l, wo, kp);
            }
            MARK("wc");
            wo_consume(l & 1, c, (const bf16_t*)xi, xo, l);
            MARK("we");
            step(l);
            xo = l == 2 ? kp->out : (bf16_t*)(c.ws + WS_X + (unsigned)((l + 1) * (8u << 20)));
            asm volatile("" : "=v"(va), "=v"(vw));
        }
    }
    asm volatile(".Lcpf_end:");
}

extern "C" void kernel_launch(void* const* d_in, const int* in_sizes, int n_in, void* d_out, int out_size, void* d_ws, size_t ws_size,
                              hipStream_t stream) {
    Params P;
    P.x0 = (const bf16_t*)d_in[0];
    P.wqkv0 = (const bf16_t*)d_in[1];
    P.wo0 = (const bf16_t*)d_in[2];
    P.wqkv1 = (const bf16_t*)d_in[4];
    P.wo1 = (const bf16_t*)d_in[5];
    P.out = (bf16_t*)d_out;
    P.ws = (char*)d_ws;
    (void)hipFuncSetAttribute((const void*)k_main, hipFuncAttributeMaxDynamicSharedMemorySize, LDS_TOTAL);
    k_main<<<256, 512, LDS_TOTAL, stream>>>(P);
}
```

```cpp
#include <hip/hip_runtime.h>
#include <stdint.h>
#include <type_traits>

typedef unsigned short bf16_t;
typedef short bf16x8 __attribute__((ext_vector_type(8)));
typedef short s16x4 __attribute__((ext_vector_type(4)));
typedef float f32x4 __attribute__((ext_vector_type(4)));
typedef float f32x2 __attribute__((ext_vector_type(2)));
typedef float f32x16 __attribute__((ext_vector_type(16)));
typedef unsigned u32x4 __attribute__((ext_vector_type(4)));
typedef unsigned u32x2 __attribute__((ext_vector_type(2)));
#define DI __device__ __forceinline__
#define LAS __attribute__((address_space(3)))
#define GAS __attribute__((address_space(1)))
#define STAMP 0

constexpr int S_ = 2048, D_ = 1024, KT = 16;
constexpr float LN_EPS = 1e-5f;
constexpr float QSCALE = 0.17677669529663687f * 1.4426950408889634f;

#define FQO 0
#define XBO 524288
#define HX 0
#define HO 0
#define HK 0
#define HS 0
#define HM 1
constexpr size_t WS_FLAGQ = FQO;
constexpr size_t WS_FLAGW = 98304;
constexpr size_t WS_EPOCH = 65536;
constexpr size_t WS_CSUM = 131072;
constexpr size_t WS_XBOX = XBO;
constexpr size_t WS_OBOX = 655360;
constexpr size_t WS_KBOX = 720896;
constexpr size_t WS_STATS = 1u << 20;
constexpr size_t WS_X = 4u << 20;
constexpr size_t WS_O = 96u << 20;
constexpr size_t WS_KV = 28u << 20;
constexpr size_t WS_CQ = 36u << 20;
constexpr size_t WS_CO = 42u << 20;
constexpr size_t WS_STAMP = 64u << 20;

template <int H> DI constexpr int hp(int c) { return !H ? c * 1024 : (c >> 2) * 8192 + (HM == 1 ? (c & 3) * 256 : (c & 1) * 128 + (c & 2) * 256); }
template <int H> DI constexpr int hl(int u) { return !H ? u : (HM == 1 ? u + (u & ~127) : u + (u & ~255)); }
template <int H> DI constexpr int hv(int lane) { return !H ? lane * 16 : (lane >> 3) * 1024 + (lane & 7) * 16; }
template <int H> DI constexpr int xrow(int p, int r) { return H == 0 ? (p * 128 + r) * 2048 : (H == 1 ? (p * 128 + r) * 4096 : (H == 2 ? p * 524288 + r * 256 : p * 262144 + r * 128)); }
template <int H> DI constexpr int xcol(int t) { return H == 0 ? t * 128 : (H == 1 ? hl<1>(t * 128) : (H == 2 ? t * 32768 : t * 16384)); }
#define WTC 2
#define WT0 1
#define SPR 1
#define SL 4
#define R4X 0
#define R5X 0
#define TOPJ 0x2000
#define WV0 1
#define C2X 13
#define FQS 256
#define SNK 1
#define WBE 1
#define WAE 1
#define EXF 0
#define RW0 6
#define KSP 0
#define K0E 1
#define TOPS 4
#define BPR 1
#define RLD 0
#define RCK 1
#define CPF 2
#define WN1 20
#define WLO 3
#define FPJ 2
#define WPJ 2
#define WB14 0
#define WB15 5
#define RESP 1
#define WT13 2
#define RPOS 2
#define CPOLL 1
#define ENT 1
#define MBOX 3
#define OBS 256
#define WTCH 0
#define WTDA 0
#define WTDB 4
#define WTG 0
#define PQX 8
#define ALT 1
#define RESA 1
#define CKP 1
#define ALN 1
#define EPH 2
#define NQA_ 3
#define NQW_ 4
#define WNP 8
#define LPRIO 3
#define TOUCH 1
#define TSTEP 13
#define XS_ 9
#define WROT 0
#define WROTD 1
#define JQX 0
#define ROTM 0
#define ROT0 0
#define WPRE 3
#define PSLEEP 1
#define XPRE 2
#define WE0 1
#define EPJ 3
#define TW0 5
#define TW1 99
#define KPJ 1
#define PKSUB 0
#define APRIO 1
#define QIL 0
#define QXM 8
#define WNQ 5
#define WRG 2
#define WMG 1
#define WB2 1
#define WBS 1
#define WEP 1
#define WAL 2
#define WL1 0
#define RS0 27
#define RN0 6
#define RN1 6
#define AH0 0
#define RSL 1
#define WXM 4
#define CPOS 5
#define LSL 0
#define SAFE_SUM 0x1p60f
#define AB2 0
#define W30S 8
#define STC 0
#define NOR 1
#define NRH 0
#define CPY 1
#define AGG 1
#define HT0 0
#define DEF 0
#define XST 2
#define CKW 4
#define XSP 1
#define EPOLL 1
#define ORD 2
#define RSLP 1
#define APOLL 1
#define APOST 1
#define ASLP 2
#define APIPE 4
#define RBP 0
#define P2A 1
#define SH0 2
#define SL1 0
#define KPW 4
#define LWA 7
#define WA0 12
#define WA1 22
#define KPP 1
#define HP4 1
#define TWQ 1
#define CP2 0
#define PADN -1
#define ERL 6
#define ESL 0
#define DM 0
#define DS 0
#define ELO 0
#define EN0 3
#define EN1 2
#define ST0 0
#define NC 2
#define NCM 1
#define NCB 4
#define AD1 3
#define AD2 1
#define AD3 4
#define WB0 3
#define WBN 6
#define TWO 1
#define XSB 10
#define TL0 0
#define TL1 12
#define FDM 1
#define TSP 0
#define XE1 0
#define XE2 0
#define OTB 0
#define OTJ 8
#define OTA 0
constexpr int QA_SLOT = 18432, NQA = NQA_, QW_SLOT = 24576, NQW = NQW_, QA_OFF = 3 * QW_SLOT, QW_TOP = 131072;
DI constexpr int qw_slot(int s) { return s == 3 ? QW_TOP : s * QW_SLOT; }
constexpr int AT_N = 90112, AT_OWN = AT_N + 32768, AT_Q = 73728;
constexpr int MISC = 155648, M_MEAN = MISC, M_RSTD = MISC + 640, M_CS = MISC + 1280, M_CNT = MISC + 2816, M_EP = MISC + 2832, M_GO = MISC + 2848, M_TCH = MISC + 3072, M_TAB = MISC + 4096, M_XCH = 131072;
constexpr int LDS_TOTAL = 163840;
static_assert(QA_OFF + NQA * QA_SLOT <= QW_TOP && QW_TOP + QW_SLOT <= MISC && AT_N + 65536 <= MISC && AT_Q + 16384 <= AT_N, "lds");

DI unsigned cvt_pk_bf16(float lo, float hi) {
    typedef __bf16 bf2 __attribute__((ext_vector_type(2)));
    bf2 r; r[0] = (__bf16)lo; r[1] = (__bf16)hi;
    return __builtin_bit_cast(unsigned, r);
}
DI u32x2 pack4(const f32x4& v) { return (u32x2){cvt_pk_bf16(v[0], v[1]), cvt_pk_bf16(v[2], v[3])}; }
DI bf16x8 join(const u32x2& a, const u32x2& b) { return __builtin_bit_cast(bf16x8, (u32x4){a[0], a[1], b[0], b[1]}); }
DI float bf_lo(unsigned u) { return __uint_as_float(u << 16); }
DI float bf_hi(unsigned u) { return __uint_as_float(u & 0xffff0000u); }
DI float red_max4(float x) {
    auto a = __builtin_amdgcn_permlane16_swap(__float_as_uint(x), __float_as_uint(x), false, false);
    x = fmaxf(__uint_as_float(a[0]), __uint_as_float(a[1]));
    auto b = __builtin_amdgcn_permlane32_swap(__float_as_uint(x), __float_as_uint(x), false, false);
    return fmaxf(__uint_as_float(b[0]), __uint_as_float(b[1]));
}
DI float red_sum4(float x) {
    auto a = __builtin_amdgcn_permlane16_swap(__float_as_uint(x), __float_as_uint(x), false, false);
    x = __uint_as_float(a[0]) + __uint_as_float(a[1]);
    auto b = __builtin_amdgcn_permlane32_swap(__float_as_uint(x), __float_as_uint(x), false, false);
    return __uint_as_float(b[0]) + __uint_as_float(b[1]);
}
DI u32x4 widen(const u32x2& a, const u32x2& b) {
    auto r0 = __builtin_amdgcn_permlane16_swap(a[0], b[0], false, false);
    auto r1 = __builtin_amdgcn_permlane16_swap(a[1], b[1], false, false);
    return (u32x4){r0[0], r1[0], r0[1], r1[1]};
}
#define MFMA(a, b, c) __builtin_amdgcn_mfma_f32_16x16x32_bf16((a), (b), (c), 0, 0, 0)
#define WAIT_L0() __builtin_amdgcn_s_waitcnt(0xC07F)
#define WAIT_V0() asm volatile("s_waitcnt vmcnt(0)" ::: "memory")
#define BAR() __builtin_amdgcn_s_barrier()
#define SCHED() __builtin_amdgcn_sched_barrier(0)
template <int N> DI void wait_v() { asm volatile("s_waitcnt vmcnt(%0)" ::"i"(N) : "memory"); }
template <int I, int N, class F> DI void static_for(F&& f) { if constexpr (I < N) { f(std::integral_constant<int, I>{}); static_for<I + 1, N>(f); } }

template <bool SC1> DI void dma16(unsigned lds, unsigned voff, const char* sbase) {
    if (SC1) asm volatile("s_mov_b32 m0, %0\n\ts_nop 3\n\tglobal_load_lds_dwordx4 %1, %2 sc1" ::"s"(lds), "v"(voff), "s"(sbase) : "memory");
    else asm volatile("s_mov_b32 m0, %0\n\ts_nop 3\n\tglobal_load_lds_dwordx4 %1, %2" ::"s"(lds), "v"(voff), "s"(sbase) : "memory");
}
DI void touch(unsigned lds, unsigned voff, const char* sbase) {
    asm volatile("s_mov_b32 m0, %0\n\ts_nop 3\n\tglobal_load_lds_dword %1, %2 sc1" ::"s"(lds), "v"(voff), "s"(sbase) : "memory");
}
DI s16x4 tr_read(const LAS char* p) {
    typedef short v4i16_t __attribute__((ext_vector_type(4)));
    return __builtin_bit_cast(s16x4, __builtin_amdgcn_ds_read_tr16_b64_v4i16((LAS v4i16_t*)p));
}
DI LAS char* lds(unsigned off) { return (LAS char*)(uintptr_t)off; }
DI unsigned long long rstamp() { unsigned long long t; asm volatile("s_memrealtime %0\n\ts_waitcnt lgkmcnt(0)" : "=s"(t)::"memory"); return t; }

template <int NAI, int NWI, bool FIRST> struct Sched {
    int w[KT];
    constexpr Sched() : w{} {
        int endA[KT] = {}, endW[KT] = {};
        int n = 0;
        if (FIRST) {
            if (ENT == 0) { n += 2; endA[0] = n; n += 3; endW[0] = n; }
            n += NAI; endA[1] = n; n += NWI; endW[1] = n;
            if (ERL >= 5) { n += NAI; endA[2] = n; }
            if (ERL == 2 || ERL == 3 || ERL == 5) { n += NWI; endW[2] = n; }
        } else {
            for (int s = 0; s < WPRE; ++s) { n += NWI; endW[s] = n; }
            for (int s = 0; s < 2; ++s) { n += NAI; endA[s] = n; }
        }
        for (int kt = 0; kt < KT; ++kt) {
            const int need = endA[kt] > endW[kt] ? endA[kt] : endW[kt];
            w[kt] = (n - need) > 63 ? 63 : (n - need);
            if (kt + 2 < KT && !(FIRST && ERL >= 5 && kt == 0)) { n += NAI; endA[kt + 2] = n; }
            if (FIRST && (ERL == 4 || ERL == 6) && kt == 0) { n += NWI; endW[2] = n; }
            if (FIRST && ERL < 2 && kt < 5) {
                if (kt < 4) { n += NWI; endW[kt + 2] = n; }
                if (kt >= 3) n += NWI / 2;
                if (kt == 4) { endW[6] = n; n += NWI; endW[7] = n; }
                continue;
            }
            if (!FIRST && WPRE == 2 && kt < 2) { n += NWI; endW[kt + 2] = n; }
            if ((FIRST || WPRE == 3) && kt == 0) { n += NWI; endW[3] = n; }
            if (kt >= 1 && kt + 3 < KT) { n += NWI; endW[kt + 3] = n; }
            if (kt == 13) n += WT13;
            if (kt == 14) n += (NAI == 5 || WA0 > 12 ? 12 : WA0) - WT13 + (NAI == 5 ? WB14 : 0);
        }
    }
};

struct Params {
    const bf16_t* x0;
    const bf16_t* wqkv0;
    char* ws;
    const bf16_t* wqkv1;
    const bf16_t* wo0;
    const bf16_t* wo1;
    bf16_t* out;
};
typedef const __attribute__((address_space(4))) Params* KP;
DI int rot_first(int pq, int jq) {
    const int pr = 2 * (pq & 7) + (pq >> 3);
    if (ROT0 == 6) return 0;
    if (ROT0 == 7) return (pq & 1) * 8;
    if (ROT0 == 8) return (pq & 3) * 4;
    if (ROT0 == 9) return pq;
    if (ROT0 == 4) return (pq + (jq ^ 3)) & 15;
    if (ROT0 == 5) return (pr + (jq ^ 3)) & 15;
    return (ROT0 == 0 ? pq + jq : (ROT0 == 1 ? pr + jq : (ROT0 == 2 ? pr + 4 * (jq & 3) + (jq >> 2) : pq + 4 * (jq & 3) + (jq >> 2)))) & 15;
}

struct Order { int base, r, re; };
DI Order first_order(int pq, int jq) { Order o; o.base = (pq & NCM) * (16 / (NCM + 1)) + NCB; o.r = (rot_first(pq, jq) - o.base) & 15; o.re = o.r > NC ? o.r : NC; return o; }
DI int first_tile(const Order& o, int s) {
    const int v = o.re + s - NC;
    if (s < NC) { int a = NC + o.base, b = s + o.base; if (NC - s == 1) asm volatile("" : "+s"(a)); return (o.r == s ? a : b) & 15; }
    return ((s == NC ? o.r : (v > 15 ? v - (16 - NC) : v)) + o.base) & 15;
}

typedef unsigned long long u64;
DI u64 lds_epoch(LAS char* smem) { return *(const LAS u64*)(smem + M_EP); }
DI bool reached(u64 v, u64 ep, unsigned target) { return (v - ep - target) < (u64)(64u - target); }
DI void poll_flags(const char* ws, u64 ep, unsigned off, bool active, unsigned target) {
    const GAS u64* f = (const GAS u64*)(ws + off);
#pragma unroll 1
    for (unsigned spins = 0; spins < (1u << 22); ++spins) {
        const u64 v = __hip_atomic_load(f, __ATOMIC_RELAXED, __HIP_MEMORY_SCOPE_AGENT);
        if (__all(!active || reached(v, ep, target))) break;
        __builtin_amdgcn_s_sleep(PSLEEP);
    }
    asm volatile("" ::: "memory");
}
struct Ask { u64 v[4]; };
DI void keep(Ask& t) { asm volatile("" : "+v"(t.v[0]), "+v"(t.v[1]), "+v"(t.v[2]), "+v"(t.v[3])); }
#define ASK(r) "global_atomic_or_x2 " r ", %[off], %[z], %[ws] sc0 sc1\n\t"
#define CHK(r) "s_waitcnt vmcnt(%[n])\n\tv_lshl_add_u64 %[d], " r ", 0, %[nb]\n\tv_cmp_gt_u64 vcc, %[span], %[d]\n\ts_cmp_eq_u64 vcc, exec\n\ts_cbranch_scc1 .Lpe%=\n\ts_sleep %[sl]\n\t"
template <bool NEG = false> DI void poll_rmw(Ask& t, const char* ws, unsigned off, bool act, u64 base, unsigned span) {
    u64 z, d, nb = NEG ? base : 0 - base, sp = span;
    unsigned cnt;
    asm volatile("" : "=v"(t.v[0]), "=v"(t.v[1]), "=v"(t.v[2]), "=v"(t.v[3]));
    if (act) {
        if (APIPE == 4)
            asm volatile("v_mov_b64 %[z], 0\n\ts_mov_b32 %[cnt], 0x80000\n\ts_nop 3\n\t" ASK("%[a]") "s_sleep %[sl]\n\t" ASK("%[b]") "s_sleep %[sl]\n\t" ASK("%[c]") "s_sleep %[sl]\n"
                         ".Lpl%=:\n\t" ASK("%[e]") CHK("%[a]") ASK("%[a]") CHK("%[b]") ASK("%[b]") CHK("%[c]") ASK("%[c]") CHK("%[e]")
                         "s_sub_u32 %[cnt], %[cnt], 1\n\ts_cbranch_scc0 .Lpl%=\n.Lpe%=:"
                         : [a] "+v"(t.v[0]), [b] "+v"(t.v[1]), [c] "+v"(t.v[2]), [e] "+v"(t.v[3]), [d] "=&v"(d), [z] "=&v"(z), [cnt] "=&s"(cnt)
                         : [off] "v"(off), [ws] "s"(ws), [nb] "v"(nb), [span] "s"(sp), [sl] "i"(ASLP), [n] "i"(3) : "vcc", "scc", "memory");
        else if (APIPE == 2)
            asm volatile("v_mov_b64 %[z], 0\n\ts_mov_b32 %[cnt], 0x100000\n\ts_nop 3\n\t" ASK("%[a]") "s_sleep %[sl]\n"
                         ".Lpl%=:\n\t" ASK("%[b]") CHK("%[a]") ASK("%[a]") CHK("%[b]")
                         "s_sub_u32 %[cnt], %[cnt], 1\n\ts_cbranch_scc0 .Lpl%=\n.Lpe%=:"
                         : [a] "+v"(t.v[0]), [b] "+v"(t.v[1]), [d] "=&v"(d), [z] "=&v"(z), [cnt] "=&s"(cnt)
                         : [off] "v"(off), [ws] "s"(ws), [nb] "v"(nb), [span] "s"(sp), [sl] "i"(ASLP), [n] "i"(1) : "vcc", "scc", "memory");
        else
            asm volatile("v_mov_b64 %[z], 0\n\ts_mov_b32 %[cnt], 0x200000\n\ts_nop 3\n"
                         ".Lpl%=:\n\t" ASK("%[a]") CHK("%[a]")
                         "s_sub_u32 %[cnt], %[cnt], 1\n\ts_cbranch_scc0 .Lpl%=\n.Lpe%=:"
                         : [a] "+v"(t.v[0]), [d] "=&v"(d), [z] "=&v"(z), [cnt] "=&s"(cnt)
                         : [off] "v"(off), [ws] "s"(ws), [nb] "v"(nb), [span] "s"(sp), [sl] "i"(ASLP), [n] "i"(0) : "vcc", "scc", "memory");
    }
}
DI void post(char* ws, unsigned off, u64 v) {
    if (APOST) asm volatile("s_nop 4\n\tglobal_atomic_swap_x2 %0, %1, %2 sc1" :: "v"(off), "v"(v), "s"(ws) : "memory");
    else __hip_atomic_store((GAS u64*)(ws + off), v, __ATOMIC_RELAXED, __HIP_MEMORY_SCOPE_AGENT);
}
template <int NW, int CNT, int NV = 0> DI void publish(LAS char* smem, int lane, char* ws, unsigned off, bool act, unsigned val) {
    const u64 ep = lds_epoch(smem) + val;
    unsigned old = 0;
    asm volatile("" :: "v"(ep), "v"(off));
    wait_v<NV>();
    if (lane == 0) asm volatile("ds_add_rtn_u32 %0, %1, %2\n\ts_waitcnt lgkmcnt(0)" : "+v"(old) : "v"(M_CNT + CNT * 4), "v"(1u) : "memory");
    if ((__builtin_amdgcn_readfirstlane(old) & (NW - 1)) == NW - 1) {
        if (act) post(ws, off, ep);
    }
}
typedef __bf16 bf16x2_t __attribute__((ext_vector_type(2)));
DI float dot2(unsigned a, unsigned b, float c) { return __builtin_amdgcn_fdot2_f32_bf16(__builtin_bit_cast(bf16x2_t, a), __builtin_bit_cast(bf16x2_t, b), c, false); }
DI u32x2 tile_stats(const u32x4& a, const u32x4& b) {
    float s = 0.f, q = 0.f;
#pragma unroll
    for (int j = 0; j < 4; ++j) { s = dot2(a[j], 0x3f803f80u, s); q = dot2(a[j], a[j], q); }
#pragma unroll
    for (int j = 0; j < 4; ++j) { s = dot2(b[j], 0x3f803f80u, s); q = dot2(b[j], b[j], q); }
    return (u32x2){__float_as_uint(red_sum4(s)), __float_as_uint(red_sum4(q))};
}

struct Ctx {
    LAS char* smem;
    char* ws;
    int lane, wid;
    int pq, jq;
    int pw, jw;
    int bid;
    int cw;
};
DI Ctx fresh(const Ctx& c0) {
    Ctx c = c0;
    asm volatile("" : "+v"(c.lane));
    asm volatile("" : "+s"(c.wid), "+s"(c.pq), "+s"(c.jq), "+s"(c.pw), "+s"(c.jw), "+s"(c.ws), "+s"(c.cw));
    return c;
}
DI unsigned o_post(const Ctx& c) { return (MBOX & 2) ? (unsigned)WS_OBOX + (c.pq * 16 + (c.lane & 15)) * OBS + c.jq * 8 : (unsigned)WS_FLAGQ + (c.pq * 16 + c.jq) * FQS; }
DI bool o_act(const Ctx& c) { return c.lane < ((MBOX & 2) ? 16 : 1); }
DI unsigned o_find(const Ctx& c) { return (MBOX & 2) ? (unsigned)WS_OBOX + (c.pw * 16 + c.jw) * OBS + (c.lane & 15) * 8 : (unsigned)WS_FLAGQ + (c.pw * 16 + (c.lane & 15)) * FQS; }
DI unsigned x_post(const Ctx& c) { return (MBOX & 1) ? (unsigned)WS_XBOX + (c.pw * 16 + (c.lane & 31)) * (2 * FQS) + (c.lane & 16) * (FQS / 16) + c.jw * 8 : (unsigned)WS_FLAGW + (c.pw * 16 + c.jw) * 128; }
DI bool x_act(const Ctx& c, bool halo) { return c.lane < ((MBOX & 1) ? (halo && c.pw < 15 ? 32 : 16) : 1); }
DI unsigned x_find(const Ctx& c) {
    const int pp = c.pq - (c.lane >> 4);
    return (MBOX & 1) ? (unsigned)WS_XBOX + (c.pq * 16 + c.jq) * (2 * FQS) + (c.lane & 16) * (FQS / 16) + (c.lane & 15) * 8 : (unsigned)WS_FLAGW + ((pp < 0 ? 0 : pp) * 16 + (c.lane & 15)) * 128;
}
#define STAMP_AT(slot) do { } while (0)

#define LSTAMP(i) do { } while (0)
#define LSTORE(ph) do { } while (0)
#define LMARK() do { } while (0)
#define LPUT(i) do { } while (0)
#define ASTORE() do { } while (0)
#define QSTAMP(i) do { } while (0)
#define QSTORE() do { } while (0)
#define FENCE() asm volatile("" ::: "memory")
#define ALIGN8() do { if (ALN) { asm volatile(".p2align 3"); SCHED(); } } while (0)
DI float addf(float a, float b) { float r; asm("v_add_f32 %0, %1, %2" : "=v"(r) : "v"(a), "v"(b)); return r; }
DI f32x4 add4x4(const u32x4& a, const u32x4& b, const u32x4& c, const u32x4& d) {
    f32x4 r;
#pragma unroll
    for (int i = 0; i < 4; ++i) r[i] = addf(addf(__uint_as_float(a[i]), __uint_as_float(b[i])), addf(__uint_as_float(c[i]), __uint_as_float(d[i])));
    asm volatile("s_nop 1" : "+v"(r));
    return r;
}
DI float fold16(float a, float b) {
    auto r = __builtin_amdgcn_permlane16_swap(__float_as_uint(a), __float_as_uint(b), false, false);
    float x = __uint_as_float(r[0]) + __uint_as_float(r[1]);
    asm volatile("" : "+v"(x));
    return x;
}
DI float fold32(float a, float b) {
    auto r = __builtin_amdgcn_permlane32_swap(__float_as_uint(a), __float_as_uint(b), false, false);
    float x = __uint_as_float(r[0]) + __uint_as_float(r[1]);
    asm volatile("" : "+v"(x));
    return x;
}
DI void aload4(u32x4& d, unsigned voff, const char* sb) { asm volatile("s_nop 4\n\tglobal_load_dwordx4 %0, %1, %2" : "=v"(d) : "v"(voff), "s"(sb) : "memory"); }
DI void aload4p(u32x4& d, unsigned voff, const char* sb) { asm volatile("s_nop 4\n\tglobal_load_dwordx4 %0, %1, %2" : "+v"(d) : "v"(voff), "s"(sb) : "memory"); }
DI void aload4c(u32x4& d, unsigned voff, const char* sb) { asm volatile("s_nop 4\n\tglobal_load_dwordx4 %0, %1, %2 sc1" : "=v"(d) : "v"(voff), "s"(sb) : "memory"); }
DI void aload2c(u32x2& d, unsigned voff, const char* sb) { asm volatile("s_nop 4\n\tglobal_load_dwordx2 %0, %1, %2 sc1" : "=v"(d) : "v"(voff), "s"(sb) : "memory"); }
DI unsigned wo_src(int q, int lane, int jw) { return (unsigned)((8 * q + (lane >> 3)) * (D_ * 2) + jw * 128 + (((lane & 7) ^ ((lane >> 3) & 6)) << 4)); }
DI unsigned pick(bool f, unsigned a, unsigned b) { asm volatile("" : "+v"(a), "+v"(b)); return f ? a : b; }
DI unsigned wo_w_off(const Ctx& c) { return pick(c.cw == 3, (unsigned)((c.wid & 3) * 1024 + c.lane * 16), wo_src(c.wid & 3, c.lane, c.jw)); }
DI unsigned wq_src(int ib, int lane, int jq) {
    const int seg = ib >> 3, ii = ib & 7, lr = lane >> 3;
    return (unsigned)((8 * ii + lr) * 6144 + seg * 2048 + jq * 128 + (((lane & 7) ^ ((lr & 2) | ((ii & 1) << 2))) << 4));
}
template <int S0, int S1> DI void wo_w_issue(const Ctx& c, const char* W, unsigned vw) {
    const int lw = c.wid & 3;
#pragma unroll
    for (int s = S0; s < S1; ++s) dma16<CPY != 0>(s * 4096 + lw * 1024, vw, W + (size_t)((2 * (s & 15) + (s >> 4)) * 32) * (D_ * 2));
}

DI void code_again(const Ctx& c) {
    const char* pc; int len, back;
    asm volatile("s_getpc_b64 %0\n.Lcpb%=:\n\ts_mov_b32 %2, .Lcpb%=-.Lcpf_a\n\ts_mov_b32 %1, .Lcpf_end-.Lcpf_a-4" : "=s"(pc), "=s"(len), "=s"(back));
    pc -= back;
    const int o = min((((c.bid >> 3) & 7) * 64 + c.lane) * 128, len);
    asm volatile("s_mov_b32 m0, %0\n\ts_nop 3\n\tglobal_load_lds_dword %1, %2" ::"s"(M_TCH), "v"(o), "s"(pc) : "memory");
}
template <bool HALO, bool FIRST> DI void qkv_load(const Ctx& c0, const char* xin, const char* W, const char* Wo, int l, KP kp = nullptr, unsigned va0 = 0, unsigned vw0 = 0) {
    const Ctx c = fresh(c0);
    asm volatile("" : "+s"(xin), "+s"(W), "+s"(Wo));
    LAS char* smem = c.smem;
    constexpr int NAI = HALO ? 5 : 4, NWI = 6, XS = FIRST ? XS_ : XSB, NR = HALO ? 144 : 128;
    constexpr Sched<NAI, NWI, FIRST> SC{};
    static_assert(NQA == 3 && NQW == 4, "schedule");
    const int lw = c.wid - 4, lane = c.lane, lr = lane >> 3, lc = lane & 7, fr = lane & 15, fq = lane >> 4;
    const int row0 = c.pq * 128;
    const int pr = (ROTM == 1 || (ROTM == 2 && (c.jq & 7) == 3)) ? 2 * (c.pq & 7) + (c.pq >> 3) : (ROTM == 3 ? (c.pq & 7) + ((c.pq & 7) >> 1) + (c.pq & 8) : c.pq);
    const bool cq_ = CPY && !FIRST && (c.jq & 7) == 3;
    if (cq_) W = c.ws + WS_CQ + ((HALO ? 0 : 2) + (c.jq >> 3)) * 24576;
    asm volatile("" : "+s"(W));
    const bool nor = !FIRST && l > 1 && (NOR == 1 || (NOR == 2 && (c.jq & 7) == 3) || (NOR == 3 && (c.jq & 7) != 3));
    const int rot = FIRST ? rot_first(c.pq, c.jq) : (nor ? (c.pq & 8) * NRH : pr & 15);
    Order od = first_order(c.pq, c.jq);
    if (FIRST) asm volatile("" : "+s"(od.base), "+s"(od.r), "+s"(od.re));
    auto tile = [&](int s) { return FIRST ? first_tile(od, s) : (s + rot) & 15; };
    constexpr int SP = FIRST ? NC : 0;
    unsigned va[5], vw[NWI];
#pragma unroll
    for (int j = 0; j < 4; ++j) va[j] = (unsigned)(FIRST ? va0 + j * 32 * (D_ * 2) : c.pq * 262144 + (j * 4 + lw) * 1024 + lane * 16);
    if (HALO) va[4] = (unsigned)((c.pq > 0 ? (c.pq - 1) * 262144 + 14336 : 0) + lw * 512 + lane * 16);
#pragma unroll
    for (int j = 0; j < NWI; ++j) vw[j] = FIRST ? vw0 + (j & 1) * (32 * 6144) + (j >> 1) * 2048 : pick(cq_, (unsigned)((j * 4 + lw) * 1024 + lane * 16), wq_src(j * 4 + lw, lane, c.jq));
    auto issueA = [&](int s, int slot) {
        const char* b = xin + (unsigned)(tile(s) * (FIRST ? 128 : 16384));
#pragma unroll
        for (int j = 0; j < 4; ++j) dma16<true>(QA_OFF + slot * QA_SLOT + (j * 4 + lw) * 1024, va[j], b);
        if (HALO) { if (lane < 32) dma16<true>(QA_OFF + slot * QA_SLOT + 16384 + lw * 512, va[4], b); }
    };
    auto issueW = [&](int s, int slot, int j0 = 0, int j1 = NWI) {
        const char* b = W + (unsigned)(tile(s) * (64 * 6144));
#pragma unroll
        for (int j = j0; j < j1; ++j) dma16<CPY && !FIRST>(qw_slot(slot) + (j * 4 + lw) * 1024, vw[j], b);
    };
    auto ahead = [&]() {
        const int r3 = (lane * 43) >> 7;
        const unsigned o = (unsigned)((((rot - (c.pq & 7) + 10) * 64 + (c.pq & 7) * 48 + lw * 12 + r3) & 1023) * 6144 + (lane - 3 * r3) * 2048 + c.jq * 128);
        if (lane < 36 && !nor && !cq_ && (WTC != 2 || (c.jq & 7) == 3)) asm volatile("s_mov_b32 m0, %0\n\ts_nop 3\n\tglobal_load_lds_dword %1, %2" ::"s"(M_TCH), "v"(o), "s"(W) : "memory");
    };
    if (ST0 && FIRST && lw == 1) STAMP_AT(47);
    if (FIRST && ERL) {
        SCHED();
        if (ESL) __builtin_amdgcn_s_sleep(ESL);
        issueA(1, 1); issueW(1, 1);
        SCHED();
    }
    const bool docs = FIRST || (HALO && l == 1);
    const int t = lw * 64 + lane;
    static_assert(MBOX & 1, "residual flag");
    unsigned foff, ftg, so, ho, fo_, co, l3, l4, vwo, wt[3], cso, xt, xt1, xso, tp, tl;
    u32x4 on;
    auto addr1 = [&]() {
        int lane = c.lane;
        asm volatile("" : "+v"(lane));
        const int fr = lane & 15, fq = lane >> 4;
        const int ph = c.pq > 0 ? c.pq - 1 : 0;
        foff = pick(lane != 32, (unsigned)WS_FLAGQ + pick(lane < 16, c.pq * 16 + lane, pick(lane < 32, fr * 16 + c.jq, ph * 16 + 14 + (lane & 1))) * FQS, (unsigned)WS_XBOX + (c.pw * 16 + c.jw) * (2 * FQS) + c.jw * 8);
        ftg = lane != 32 ? (unsigned)(4 * l + 1) : (unsigned)(l + 1);
        so = FIRST ? (unsigned)((c.pq * 16 + fq * 4) * 1024 + (lw * 32 + fr * 2) * 8) : (unsigned)((c.pq * 16 + (lane >> 2)) * 1024 + (c.jq * 8 + lw * 2 + (lane & 1)) * 8);
        ho = FDM ? (unsigned)(262144 + ((lw * 36 + lane < 16 ? ph * 128 + 128 : c.pq * 128) + lw * 36 + lane - 16) * 8)
                 : (unsigned)(262144 + pick(lane < 32, c.pq * 128 + lw * 32 + lane, ph * 128 + 112 + lw * 4 + (lane & 3)) * 8);
        fo_ = pick(lane < 2, (unsigned)(262144 + (c.pq * 128 + c.jq * 8 + lw * 2 + lane) * 8), 0x80000000u);
        const int cq = lw * 12 + (fr < 12 ? fr : 11);
        co = (unsigned)((fq * 4 * 3072 + (cq >> 4) * 1024 + c.jq * 64 + (cq & 15) * 4) * 4);
        l3 = (unsigned)(M_CS + (HALO ? 768 : 0) + (cq * 4 + fq) * 4);
        l4 = (unsigned)(M_MEAN + pick(lane < 32, 16 + lw * 32 + (FIRST ? fr * 2 + fq : lane), lw * 4 + (lane & 3)) * 4);
        asm volatile("" : "+v"(foff), "+v"(ftg), "+v"(so), "+v"(co), "+v"(l3), "+v"(l4));
        if (!FIRST) asm volatile("" : "+v"(ho), "+v"(fo_));
        SCHED();
    };
    auto addr2 = [&]() {
        int lane = c.lane;
        asm volatile("" : "+v"(lane));
        const int fr = lane & 15, fq = lane >> 4;
        const int q4 = fr >> 2, p4 = lane & 3, fb = (q4 & 2) | ((fq & 1) << 2);
#pragma unroll
        for (int i = 0; i < 3; ++i) {
            const int T = 3 * lw + i;
            wt[i] = (unsigned)(qw_slot(SP % NQW) + (T >> 2) * 8192 + (8 * fq + q4) * 128 + (((2 * (T & 3) + (p4 >> 1)) ^ fb) << 4) + (p4 & 1) * 8);
        }
        const int T = 3 * lw + fq;
        cso = (unsigned)((((HALO ? 16 : 0) + rot) * 3072 + (T >> 2) * 1024 + c.jq * 64 + (T & 3) * 16 + fr) * 4);
        xt = (unsigned)(QA_OFF + (SP % NQA) * QA_SLOT + (lw * 32 + fr) * 128 + ((fq ^ (fr & 7)) << 4));
        xt1 = xt ^ 64;
        xso = (unsigned)hl<HS>((c.pq * 16 + rot) * 1024 + (lw * 32 + fr) * 8);
        on = (u32x4){0x3f803f80u, 0x3f803f80u, 0x3f803f80u, 0x3f803f80u};
        if (FIRST || HALO) asm volatile("" : "+v"(wt[0]), "+v"(wt[1]), "+v"(wt[2]), "+v"(cso), "+v"(on));
        if (FIRST) asm volatile("" : "+v"(xt), "+v"(xt1), "+v"(xso));
        SCHED();
    };
    auto addr3 = [&]() {
        Ctx c2 = c;
        asm volatile("" : "+v"(c2.lane));
        asm volatile("" : "+s"(c2.bid));
        vwo = wo_w_off(c2);
        tp = (unsigned)(pick(c2.lane < 30, (c2.bid >> 3) * 30 + c2.lane, (c2.bid >> 3) * 16 + c2.lane - 30 + (int)(WS_O >> 16)) * 65536 + 65536 - 128);
        tl = (unsigned)(lw == 1 ? ((c2.bid >> 3) * 2 + c2.lane) * 65536 : c2.bid * 8192 + (TWO ? c2.lane * 128 : 0));
        asm volatile("" : "+v"(vwo));
        if (FIRST) asm volatile("" : "+v"(tp), "+v"(tl));
        SCHED();
    };
    if (!FIRST || AD1 < 0) addr1();
    if (FIRST && ERL >= 5) { issueA(2, 2); SCHED(); }
    if (FIRST && (ERL == 2 || ERL == 5)) { issueW(2, 2); SCHED(); }
    if (!FIRST || AD2 < 0) addr2();
    if (!FIRST || AD3 < 0) addr3();
#pragma unroll
    for (int j = 0; j < NAI; ++j) asm volatile("" : "+v"(va[j]));
#pragma unroll
    for (int j = 0; j < NWI; ++j) asm volatile("" : "+v"(vw[j]));
    SCHED();
    int tv = 0; unsigned long long lt = 0;
    if (FIRST) {
        LSTAMP(55);
    } else {
        static_assert(CPY == 1 && RS0 > 0 && RN0 == NWI && WPRE >= 3 && (FIRST || SC.w[0] == NAI), "order");
        unsigned vx[2], xfo = (unsigned)WS_XBOX + (c.pq * 16 + c.jq) * (2 * FQS) + (HALO && c.pq > 0 ? (lane & 16) * (FQS / 16) : 0) + fr * 8, go = M_GO;
        u64 xv = lds_epoch(smem) + (unsigned)(l + 1);
#pragma unroll
        for (int i = 0; i < 2; ++i) vx[i] = pick(cq_, (unsigned)(((2 * lw + i) * 4 + 3) * 1024 + lane * 16), wq_src((2 * lw + i) * 4 + 3, lane, c.jq));
        asm volatile("" : "+v"(vx[0]), "+v"(vx[1]), "+v"(xfo), "+v"(xv), "+v"(go));
        BAR();
        issueW(0, 0);
        auto word = [&](int off) { return __builtin_amdgcn_readfirstlane(*(volatile LAS int*)(lds(go) + off)); };
        auto until = [&](int off, int v) {
#pragma unroll 1
            for (unsigned spins = 0; spins < (1u << 24); ++spins) { if (word(off) == v) break; }
            FENCE();
        };
        auto stage = [&](int s) {
            const char* b = W + (unsigned)(((s + rot) & 15) * (64 * 6144));
            unsigned d = (unsigned)((s == 3 ? QW_TOP : s * QW_SLOT) + lw * 1024), dx = d + (unsigned)(lw * 7168 + 3072);
            asm volatile("" : "+s"(d), "+s"(dx), "+s"(b));
#pragma unroll
            for (int j = 0; j < NWI; ++j) dma16<true>(d + j * 4096, vw[j], b);
#pragma unroll
            for (int i = 0; i < 2; ++i) dma16<true>(dx + i * 4096, vx[i], b);
        };
        auto first2 = [&]() { __builtin_amdgcn_s_setprio(LPRIO); issueA(0, 0); issueA(1, 1); };
        if (lw == 3) {
            if (!EPOLL) until(M_CNT - M_GO, 8 * l);
            Ask t;
            poll_rmw(t, c.ws, xfo, lane < (HALO && c.pq > 0 ? 32 : 16), xv, 63 - l);
            *(volatile LAS int*)lds(go) = l;
            until(M_CNT - M_GO, 8 * l);
            first2();
            STAMP_AT(32 + l * 8 + 0);
            wait_v<NAI>();
            keep(t);
        } else {
            until(M_CNT - M_GO, 8 * l);
            if (RSLP) __builtin_amdgcn_s_sleep(RSLP);
            int ns = 1;
#pragma unroll 1
            for (; ns < WPRE; ++ns) {
                if (word(0) == l) break;
                stage(ns);
            }
            until(0, l);
            first2();
            const int left = WPRE - ns;
#pragma unroll 1
            for (; ns < WPRE; ++ns) stage(ns);
            if (left == 0) wait_v<NAI>();
            else if (left == 1) wait_v<NAI + 8>();
            else if (left == 2) wait_v<NAI + 16>();
            else wait_v<NAI + 24>();
        }
        FENCE();
    }
    if (FIRST && ERL == 3) issueW(2, 2);
    if (ST0 && FIRST && lw == 2) STAMP_AT(57);
    if (FIRST && !ERL) BAR();
    __builtin_amdgcn_s_setprio(LPRIO);
    if (FIRST && !ERL) { LSTAMP(57); issueA(1, 1); issueW(1, 1); LSTAMP(58); }
    u32x4 st[4], cs[4]; u32x2 sh, fl; u64 fv = 0; float rs_, rq_, hs;
    f32x4 d[3], sm[2], g[2];
    bf16x8 wf[3][2], xf[2][2];
    const bool fact = FIRST ? lane < 32 : ((lane < 16 || lane >= (docs ? 16 : 32)) && lane <= (HALO ? 34 : 32));
    static_assert(XS + 3 <= (FIRST ? 12 : 13) && SPR == 1, "aux schedule");
    auto side = [&](auto ktc) {
        constexpr int kt = decltype(ktc)::value;
        {
            constexpr int C0 = FIRST ? 12 : XS + 2, C1 = FIRST ? 13 : XS + 3, C2 = 13;
            auto acc4 = [](u32x4& a, const u32x4& b) {
#pragma unroll
                for (int i = 0; i < 4; ++i) a[i] = __float_as_uint(addf(__uint_as_float(a[i]), __uint_as_float(b[i])));
                asm volatile("" : "+v"(a));
            };
            auto fin = [&]() {
                rs_ *= -1.f / 1024.f;
                asm volatile("" : "+v"(rs_));
                rq_ = __builtin_amdgcn_rsqf(fmaxf(rq_ * (1.f / 1024.f) - rs_ * rs_, 0.f) + LN_EPS);
                asm volatile("" : "+v"(rq_));
            };
            if constexpr (FIRST && kt == XS + 2) {
                asm volatile("" : "+v"(st[0]), "+v"(st[1]), "+v"(st[2]));
                acc4(st[0], st[1]); acc4(st[0], st[2]);
            }
            if constexpr (FIRST && kt == XS + 3) { asm volatile("" : "+v"(st[3])); acc4(st[0], st[3]); }
            if constexpr (kt == C0) {
                if (docs) { asm volatile("" : "+v"(cs[0]), "+v"(cs[1])); acc4(cs[0], cs[1]); }
            }
            if constexpr (FIRST && kt == 12) {
                asm volatile("s_nop 1" : "+v"(st[0]));
                rs_ = fold16(__uint_as_float(st[0][0]), __uint_as_float(st[0][2])); rq_ = fold16(__uint_as_float(st[0][1]), __uint_as_float(st[0][3]));
                rs_ = fold32(rs_, rs_); rq_ = fold32(rq_, rq_);
            }
            if constexpr (FIRST && kt == 13) fin();
            if constexpr (!FIRST && kt == TL0 + 2) {
                asm volatile("" : "+v"(sh));
                hs = fold16(__uint_as_float(sh[0]), __uint_as_float(sh[1]));
                hs = fold32(hs, hs);
                hs += __uint_as_float(__builtin_amdgcn_update_dpp(0u, __float_as_uint(hs), 0x124, 0xf, 0xf, false));
                hs += __uint_as_float(__builtin_amdgcn_update_dpp(0u, __float_as_uint(hs), 0x128, 0xf, 0xf, false));
                asm volatile("" : "+v"(hs));
            }
            if constexpr (!FIRST && kt == TL0 + 2 + TSP) {
                auto hh = __builtin_amdgcn_permlane16_swap(__float_as_uint(hs), __float_as_uint(hs), false, false);
                rs_ = __uint_as_float(hh[0]); rq_ = __uint_as_float(hh[1]);
                asm volatile("" : "+v"(rs_), "+v"(rq_));
                fin();
                auto rf = __builtin_amdgcn_make_buffer_rsrc(c.ws + WS_STATS + (size_t)l * 524288, 0, 524288, 0x00020000);
                __builtin_amdgcn_raw_buffer_store_b64((u32x2){__float_as_uint(rs_), __float_as_uint(rq_)}, rf, fo_, 0, 16);
            }
            if constexpr (kt == C1) {
                if (docs) { asm volatile("" : "+v"(cs[2]), "+v"(cs[3])); acc4(cs[2], cs[3]); acc4(cs[0], cs[2]); }
            }
            if constexpr (kt == C2) {
                if (docs) {
                    asm volatile("s_nop 1" : "+v"(cs[0]));
                    hs = fold32(fold16(__uint_as_float(cs[0][0]), __uint_as_float(cs[0][1])), fold16(__uint_as_float(cs[0][2]), __uint_as_float(cs[0][3])));
                }
            }
            if constexpr (kt == 13) {
                static_assert(WV0 == 1, "wait");
                if (FIRST || !FDM || docs) WAIT_V0();
                if (!FIRST && !FDM) { asm volatile("" : "+v"(sh)); rs_ = __uint_as_float(sh[0]); rq_ = __uint_as_float(sh[1]); }
                if ((FIRST || !FDM) && lane < (HALO ? 36 : 32)) {
                    *(LAS float*)lds(l4) = rs_;
                    *(LAS float*)(lds(l4) + (M_RSTD - M_MEAN)) = rq_;
                }
                if (docs) *(LAS float*)lds(l3) = hs;
                FENCE();
            }
        }
    };
    static_for<0, KT>([&](auto ktc) {
        constexpr int kt = decltype(ktc)::value;
        LSTAMP(3 * kt);
        if constexpr (FIRST || kt > 0) wait_v<SC.w[kt]>();
        LSTAMP(3 * kt + 1);
        if constexpr (kt == XS) {
            if (lw == 0) {
                asm volatile("" : "+v"(fl));
                const u64 ep = lds_epoch(smem);
                if (!__all(!fact || reached(__builtin_bit_cast(u64, fl), ep, ftg))) poll_flags(c.ws, ep, foff, fact, ftg);
            }
        }
        if constexpr (kt == 14) asm volatile("s_waitcnt lgkmcnt(0)" ::: "memory");
        BAR();
        FENCE();
        LMARK();
        LSTAMP(3 * kt + 2);
        if constexpr (TOPJ & (1 << kt)) side(ktc);
        if constexpr (kt == (FIRST ? SP + DS : TL0 + 2 + TSP) + 3) {
            if (lw == 0 && lane == 0) __hip_atomic_store((GAS u64*)(c.ws + WS_FLAGQ + (c.pq * 16 + c.jq) * FQS), fv, __ATOMIC_RELAXED, __HIP_MEMORY_SCOPE_AGENT);
        }
        if constexpr (kt == XS + 1 - NQA) {
            if (lw == 0) { if (fact) aload2c(fl, foff, c.ws); }
        }
        {
            const char* sb = c.ws + WS_STATS + (size_t)l * 524288;
            const char* cb = c.ws + WS_CSUM + (HALO ? 16 * 3072 * 4 : 0);
            constexpr int sa = 3 * (kt - XS), ca = FIRST ? 2 * (kt - XS - 1) : 2 * (kt - XS), cn = 2;
            if constexpr (FIRST) {
#pragma unroll
                for (int i = sa; i < sa + 3; ++i) if (i >= 0 && i < 4) aload4c(st[i], so, sb + i * 1024);
            }
            if constexpr (!FIRST && kt == TL0) aload2c(sh, so, sb);
            if constexpr (!FIRST && kt == TL1) {
                static_assert(TL1 <= 12, "covered by the wait of step 14");
                if (FDM) { if (lane < 36) { touch(M_MEAN + lw * 144, ho, sb); touch(M_RSTD + lw * 144, ho, sb + 4); } }
                else aload2c(sh, ho, sb);
            }
            if constexpr (ca >= 0 && ca < 4) {
                if (docs) {
#pragma unroll
                    for (int i = ca; i < ca + cn; ++i) aload4c(cs[i], co, cb + i * 12288);
                }
            }
        }
        if constexpr (kt == SP) {
            if (docs) {
#pragma unroll
                for (int i = 0; i < 3; ++i)
#pragma unroll
                    for (int ks = 0; ks < 2; ++ks) {
                        const s16x4 lo = tr_read(lds(wt[i]) + ks * 4096), hi = tr_read(lds(wt[i]) + ks * 4096 + 512);
                        wf[i][ks] = __builtin_shufflevector(lo, hi, 0, 1, 2, 3, 4, 5, 6, 7);
                    }
                if (FIRST) {
#pragma unroll
                    for (int mt = 0; mt < 2; ++mt)
#pragma unroll
                        for (int ks = 0; ks < 2; ++ks) xf[mt][ks] = *(const LAS bf16x8*)(lds(ks ? xt1 : xt) + mt * 2048);
                }
            }
            SCHED();
        }
        if constexpr (kt + 2 < KT && !(FIRST && ERL >= 5 && kt == 0)) issueA(kt + 2, (kt + 2) % NQA);
        LPUT(3 * kt + 2);
        if constexpr (kt == SP + DM) {
            SCHED();
            if (docs) {
                const bf16x8 ones = __builtin_bit_cast(bf16x8, on);
                const f32x4 zero = {0.f, 0.f, 0.f, 0.f};
#pragma unroll
                for (int ks = 0; ks < 2; ++ks) {
#pragma unroll
                    for (int i = 0; i < 3; ++i) d[i] = MFMA(ones, wf[i][ks], ks ? d[i] : zero);
                    if (FIRST) {
#pragma unroll
                        for (int mt = 0; mt < 2; ++mt) { sm[mt] = MFMA(ones, xf[mt][ks], ks ? sm[mt] : zero); g[mt] = MFMA(xf[mt][ks], xf[mt][ks], ks ? g[mt] : zero); }
                    }
                }
            }
            SCHED();
        }
        if constexpr (FIRST && ERL < 2 && kt < 5) {
            if constexpr (kt < 4) issueW(kt + 2, (kt + 2) % NQW);
            if constexpr (kt == 3) issueW(6, 2, 0, NWI / 2);
            if constexpr (kt == 4) { issueW(6, 2, NWI / 2, NWI); issueW(7, 3); }
        } else {
            if constexpr (WPRE == 2 && kt < 2) issueW(kt + 2, kt + 2);
            if constexpr (FIRST && (ERL == 4 || ERL == 6) && kt == 0) issueW(2, 2);
            if constexpr ((FIRST || WPRE == 3) && kt == 0) issueW(3, 3);
            if constexpr (kt >= 1 && kt + 3 < KT) issueW(kt + 3, (kt + 3) % NQW);
        }
        if constexpr (FIRST && kt == WT0) ahead();
        if constexpr ((CP2 & 4) && FIRST && kt == 8) { if (lw == 2) code_again(c); }
        if constexpr ((CP2 & 8) && HALO && kt == 8) { if (lw == 2 && l == 1) code_again(c); }
        if constexpr (FIRST && kt == TSTEP) {
            if (lw == 1 && lane < 46) touch(M_TCH, tp, c.ws);
        }
        if constexpr (FIRST && kt == TW0) {
            if (lw == 0) touch(M_TCH, tl, Wo);
            else if (lw == 1) { asm volatile("" : "+s"(kp)); const char* outp = (const char*)kp->out; if (lane < 2) touch(M_TCH, tl, outp); }
        }
        if constexpr (kt == SP && DM > 0) {
            SCHED();
            if (docs) {
                asm volatile("" : "+v"(wf[0][0]), "+v"(wf[0][1]), "+v"(wf[1][0]), "+v"(wf[1][1]), "+v"(wf[2][0]), "+v"(wf[2][1]));
                if (FIRST) asm volatile("" : "+v"(xf[0][0]), "+v"(xf[0][1]), "+v"(xf[1][0]), "+v"(xf[1][1]));
            }
        }
        if constexpr (kt == SP + DS) {
            SCHED();
            if (docs) {
                if (FIRST) {
                    auto rst = __builtin_amdgcn_make_buffer_rsrc(c.ws + WS_STATS, 0, 524288, 0x00020000);
                    const bool b0 = fr & 1, b1 = fr & 2;
                    u32x2 sv[2];
#pragma unroll
                    for (int mt = 0; mt < 2; ++mt) {
                        float qa = b0 ? g[mt][1] : g[mt][0], qb = b0 ? g[mt][3] : g[mt][2];
                        asm volatile("" : "+v"(qa), "+v"(qb));
                        sv[mt] = (u32x2){__float_as_uint(sm[mt][0]), __float_as_uint(b1 ? qb : qa)};
                    }
                    if (fq == (fr >> 2)) {
                        __builtin_amdgcn_raw_buffer_store_b64(sv[0], rst, xso, 0, 16);
                        __builtin_amdgcn_raw_buffer_store_b64(sv[1], rst, xso + hl<HS>(128), 0, 16);
                    }
                }
                float d0 = d[0][0], d1 = d[1][0], d2 = d[2][0];
                asm volatile("" : "+v"(d0), "+v"(d1), "+v"(d2));
                const float v = fq == 0 ? d0 : (fq == 1 ? d1 : d2);
                auto rc = __builtin_amdgcn_make_buffer_rsrc(c.ws + WS_CSUM, 0, 2 * 16 * 3072 * 4, 0x00020000);
                if (lane < 48) __builtin_amdgcn_raw_buffer_store_b32(__float_as_uint(v), rc, cso, 0, 16);
                asm volatile("s_waitcnt lgkmcnt(0)" ::: "memory");
            }
        }
        if constexpr (kt == 1) {
            if (lw == 0) {
                const u32x2 e = *(const LAS u32x2*)(smem + M_EP);
                const u64 es = ((u64)(unsigned)__builtin_amdgcn_readfirstlane(e[1]) << 32) | (unsigned)__builtin_amdgcn_readfirstlane(e[0]);
                fv = es + (unsigned)(4 * l + 1);
                asm volatile("" : "+s"(fv));
            }
        }
        if constexpr (FIRST && kt == AD1) addr1();
        if constexpr (FIRST && kt == AD2) addr2();
        if constexpr (FIRST && kt == AD3) addr3();
        if constexpr (!(TOPJ & (1 << kt))) side(ktc);
        if constexpr (kt == 13) wo_w_issue<0, WT13>(c, Wo, vwo);
        if constexpr (kt == 14) { wo_w_issue<WT13, (HALO || WA0 > 12) ? 12 : WA0>(c, Wo, vwo); if (HALO) wo_w_issue<23, 23 + WB14>(c, Wo, vwo); }
        if constexpr (kt == 15) {
            if (HALO ? WBE < 2 : WA0 > 12) wo_w_issue<12, HALO ? 18 : WA0>(c, Wo, vwo);
            if (HALO) wo_w_issue<23 + WB14, 23 + WB14 + WB15>(c, Wo, vwo);
        }
    });
    __builtin_amdgcn_s_setprio(0);
    BAR();
    LMARK(); LPUT(48);
    LSTAMP(48);
    LSTORE(0);
    if (HALO ? WBE < 1 : WAE < 1) wo_w_issue<18, HALO ? 23 : 20>(c, Wo, vwo);
    if (HALO) {
        static_assert(!WBS || (WBE == 1 && WB14 == 0 && WRG <= 12), "Wo weights of the first twelve steps are older than the last 6 + WB15");
        if (WBS) wait_v<6 + WB15>();
        BAR();
        if (WBE) wo_w_issue<WBE < 2 ? 18 : 12, 23>(c, Wo, vwo);
        wo_w_issue<23 + WB14 + WB15, 32>(c, Wo, vwo);
    }
}

DI float max3(float a, float b, float c) { return fmaxf(fmaxf(a, b), c); }
#define SGB(m, n) __builtin_amdgcn_sched_group_barrier(m, n, 0)
template <bool HALO> struct Tail {
    int f[26], n[4], t[4][26], d[4][27], v[4][27], h3[32], s3[32], o3[33], q3[112];
    constexpr Tail() : f{}, n{}, t{}, d{}, v{}, h3{}, s3{}, o3{}, q3{} {
        const int FB[26] = {0, 1, 2, 3, 4, 5, 6, 7, 8, 9, 10, 11, 12, 13, 14, 15, 24, 25, 19, 23, 18, 22, 17, 21, 16, 20};
        const int FA[24] = {0, 1, 2, 3, 4, 5, 6, 7, 8, 9, 10, 11, 12, 13, 14, 15, 16, 17, 20, 21, 18, 19, 22, 23};
        const int TA[4][26] = {{0, 1, 2, 3, 7, 6, 5, 4, 8, 9, 10, 11, 15, 14, 13, 12, 16, 17, 18, 19, 23, 22, 21, 20, 0, 0}, {4, 8, 9, 10, 11, 15, 14, 13, 12, 16, 17, 18, 19, 23, 22, 21, 20, 0, 1, 2, 3, 7, 6, 5, 0, 0}, {16, 17, 18, 19, 23, 22, 21, 20, 0, 1, 2, 3, 7, 6, 5, 4, 8, 9, 10, 11, 15, 14, 13, 12, 0, 0}, {17, 18, 19, 23, 22, 21, 20, 8, 9, 10, 11, 15, 14, 13, 12, 16, 0, 0, 0, 0, 0, 0, 0, 0, 0, 0}};
        const int DA[4][26] = {{1, 1, 1, 1, 1, 1, 1, 1, 1, 1, 1, 1, 1, 1, 1, 1, 1, 1, 1, 1, 1, 1, 0, 0, 0, 0}, {1, 1, 1, 1, 1, 1, 1, 1, 1, 1, 1, 1, 1, 1, 1, 1, 1, 1, 1, 1, 1, 1, 0, 0, 0, 0}, {1, 1, 1, 1, 1, 1, 1, 1, 1, 1, 1, 1, 1, 1, 1, 1, 0, 0, 0, 0, 0, 0, 0, 0, 0, 0}, {2, 2, 2, 2, 1, 1, 1, 1, 1, 1, 1, 1, 0, 0, 0, 0, 0, 0, 0, 0, 0, 0, 0, 0, 0, 0}};
        const int VA[4][26] = {{1, 1, 0, 0, 0, 0, 1, 1, 1, 1, 1, 1, 1, 1, 1, 1, 1, 1, 1, 1, 1, 1, 2, 2, 0, 0}, {1, 1, 1, 1, 1, 1, 1, 1, 1, 1, 1, 1, 1, 1, 1, 1, 1, 1, 1, 1, 1, 1, 2, 2, 0, 0}, {1, 1, 1, 1, 1, 1, 1, 1, 1, 1, 1, 1, 1, 1, 1, 1, 2, 2, 2, 2, 2, 3, 3, 2, 0, 0}, {0, 0, 0, 0, 1, 1, 1, 1, 1, 1, 1, 1, 2, 2, 2, 2, 0, 0, 0, 0, 0, 0, 0, 0, 0, 0}};
        const int TB[4][26] = {{0, 1, 2, 3, 7, 6, 5, 4, 24, 8, 9, 10, 11, 15, 14, 13, 12, 25, 16, 17, 18, 19, 23, 22, 21, 20}, {24, 8, 9, 10, 11, 15, 14, 13, 12, 25, 16, 17, 18, 19, 23, 22, 21, 20, 0, 1, 2, 3, 7, 6, 5, 4}, {25, 16, 17, 18, 19, 23, 22, 21, 20, 0, 1, 2, 3, 7, 6, 5, 4, 24, 8, 9, 10, 11, 15, 14, 13, 12}, {0, 1, 2, 3, 7, 6, 5, 4, 24, 8, 9, 10, 11, 15, 14, 13, 12, 25, 19, 23, 0, 0, 0, 0, 0, 0}};
        const int DB[4][26] = {{1, 1, 1, 1, 1, 1, 1, 1, 1, 1, 1, 1, 1, 1, 1, 1, 1, 1, 1, 1, 1, 1, 1, 0, 0, 0}, {1, 1, 1, 1, 1, 1, 1, 1, 1, 1, 1, 1, 1, 1, 1, 1, 1, 1, 1, 1, 1, 1, 0, 0, 0, 0}, {1, 1, 1, 1, 1, 1, 1, 1, 1, 1, 1, 1, 1, 1, 1, 1, 1, 1, 1, 1, 1, 1, 1, 0, 0, 0}, {2, 2, 1, 1, 1, 1, 1, 1, 1, 1, 1, 1, 1, 1, 1, 1, 1, 1, 1, 0, 0, 0, 0, 0, 0, 0}};
        const int VB[4][26] = {{1, 1, 1, 1, 1, 1, 1, 1, 1, 1, 1, 1, 1, 1, 1, 1, 1, 1, 1, 1, 1, 1, 1, 2, 2, 2}, {1, 1, 1, 1, 1, 1, 1, 1, 1, 1, 1, 1, 1, 1, 1, 1, 1, 1, 1, 1, 1, 1, 3, 3, 3, 2}, {1, 1, 1, 1, 1, 1, 1, 1, 1, 1, 1, 1, 1, 1, 1, 1, 1, 1, 1, 1, 1, 1, 1, 3, 3, 2}, {0, 0, 1, 1, 1, 1, 1, 1, 1, 1, 1, 1, 1, 1, 1, 1, 1, 1, 1, 2, 0, 0, 0, 0, 0, 0}};
        const int G[32] = {8, 12, 9, 13, 10, 14, 11, 15, 16, 17, 20, 21, 18, 19, 22, 23, 0, 4, 1, 5, 2, 6, 3, 7, 0, 4, 1, 5, 2, 6, 3, 7};
        const int NM = HALO ? 26 : 24;
        n[0] = n[1] = n[2] = NM; n[3] = HALO ? 20 : 16;
        for (int i = 0; i < NM; ++i) f[i] = HALO ? FB[i] : FA[i];
        int vv = 0;
        for (int h = 0; h < 4; ++h) {
            v[h][0] = vv;
            for (int i = 0; i < n[h]; ++i) {
                t[h][i] = HALO ? TB[h][i] : TA[h][i];
                d[h][i + 1] = d[h][i] + (HALO ? DB[h][i] : DA[h][i]);
                vv += HALO ? VB[h][i] : VA[h][i];
                v[h][i + 1] = vv;
            }
        }
        for (int i = 0; i < 32; ++i) { h3[i] = G[i]; s3[i] = i >= 16 && i < 24 ? 0 : 1; }
        for (int i = 3; i < 32; ++i) o3[i + 1] = o3[i] + TOPS;
        int nv = 0, ns = 0;
        for (int k = 0; k < 8 * 14; ++k) {
            const bool top = k % TOPS < 2 || nv == 96;
            if (top && ns < 16 && nv >= 12 * (ns / 2 + 1) && (nv == 96 || k % TOPS == (ns & 1))) { q3[k] = (ns / 2) * 14 + 13 - (ns & 1); ++ns; }
            else { q3[k] = (nv / 12) * 14 + nv % 12; ++nv; }
        }
    }
};
template <bool HALO> DI void qkv_consume(const Ctx& c0, int l, KP kp, unsigned va0 = 0, unsigned vw0 = 0) {
    const Ctx c = fresh(c0);
    if (ST0 && l == 0 && c.wid == 2) STAMP_AT(59);
    LAS char* smem = c.smem;
    const int lane = c.lane, wr = c.wid >> 1, wc = c.wid & 1, fr = lane & 15, fq = lane >> 4;
    const int sw = HALO ? wr : 0;
    f32x4 acc[4][6], ah[2];
#pragma unroll
    for (int mt = 0; mt < 4; ++mt)
#pragma unroll
        for (int nt = 0; nt < 6; ++nt) { acc[mt][nt] = (f32x4){0.f, 0.f, 0.f, 0.f}; asm volatile("" : "+v"(acc[mt][nt])); }
    ah[0] = ah[1] = (f32x4){0.f, 0.f, 0.f, 0.f};
    if (HALO) asm volatile("" : "+v"(ah[0]), "+v"(ah[1]));
    const unsigned aoff0 = (unsigned)(QA_OFF + wr * 8192 + (HALO || l ? lane * 16 : fr * 128 + ((fq ^ (fr & 7)) << 4)));
    const unsigned hoff0 = (unsigned)(QA_OFF + 16384 + lane * 16);
    unsigned xm = HALO || l ? 1024u : 64u;
    asm volatile("" : "+s"(xm));
    static_assert(((QA_OFF | QA_SLOT) & 2047) == 0, "halves");
    const int q4 = fr >> 2, p4 = lane & 3;
    const int fb = (q4 & 2) | ((fq & 1) << 2);
    unsigned boff[2];
#pragma unroll
    for (int cc = 0; cc < 2; ++cc)
        boff[cc] = (unsigned)((8 * fq + q4) * 128 + (((2 * (2 * wc + (cc ^ sw)) + (p4 >> 1)) ^ fb) << 4) + (p4 & 1) * 8);
    bf16x8 xa[2][4], xh[2], wb[2][6];
    auto ldfrag = [&](int set, unsigned pa, unsigned ph, unsigned pb0, unsigned pb1, int ks) {
#pragma unroll
        for (int mt = 0; mt < 4; ++mt) xa[set][mt] = *(const LAS bf16x8*)(lds(pa) + mt * 2048);
        if (HALO) xh[set] = *(const LAS bf16x8*)lds(ph);
#pragma unroll
        for (int nt = 0; nt < 6; ++nt) {
            const LAS char* pb = lds((nt & 1) ? pb1 : pb0) + (nt >> 1) * 8192 + ks * 4096;
            const s16x4 lo = tr_read(pb), hi = tr_read(pb + 512);
            wb[set][nt] = __builtin_shufflevector(lo, hi, 0, 1, 2, 3, 4, 5, 6, 7);
        }
    };
    constexpr int NM = HALO ? 26 : 24, NH = HALO ? 17 : 16;
    auto mm = [&](int set, int i0, int i1) {
#pragma unroll
        for (int ii = i0; ii < i1; ++ii) {
            const int i = !HALO || ii < 8 ? ii : (!SNK ? (ii < 10 ? ii + 16 : ii - 2) : (ii == 8 ? 24 : (ii < 17 ? ii - 1 : (ii == 17 ? 25 : ii - 2))));
            const int nt = i >> 2, mt = SNK && (nt & 1) ? 3 - (i & 3) : i & 3;
            if (i < 16) acc[mt][nt] = MFMA(wb[set][nt], xa[set][mt], acc[mt][nt]);
            else if (i < 24) acc[mt][nt] = MFMA(xa[set][mt], wb[set][nt], acc[mt][nt]);
            else if (i == 24) ah[0] = MFMA(wb[set][2], xh[set], ah[0]);
            else ah[1] = MFMA(xh[set], wb[set][4], ah[1]);
        }
    };
    auto interleave = [&]() {
#pragma unroll
        for (int i = 0; i < NH; ++i) { SGB(0x008, 1); SGB(0x100, 1); }
        SCHED();
    };
    unsigned sa = 0, sb = 0;
    unsigned pa0 = aoff0, pa1 = aoff0 ^ xm, ph0 = hoff0, ph1 = hoff0 ^ xm, pb0 = boff[0], pb1 = boff[1];
    asm volatile("" : "+v"(pa0), "+v"(pa1), "+v"(pb0), "+v"(pb1));
    if (HALO) asm volatile("" : "+v"(ph0), "+v"(ph1));
    constexpr int M_CSH = M_CS + (HALO ? 768 : 0);
    unsigned sa0 = (unsigned)(M_MEAN + 64 + (wr * 64 + fr) * 4), sa1 = (unsigned)(M_MEAN + 64 + wr * 256 + fq * 16);
    unsigned sa2 = (unsigned)(M_CSH + wc * 128 + sw * 64 + fq * 16), sa6 = (unsigned)(M_CSH + 512 + wc * 128 + sw * 64 + fr * 4);
    unsigned sa3 = sa2 ^ 64, sa7 = sa6 ^ 64, sa4 = (unsigned)(M_MEAN + fr * 4), sa5 = (unsigned)(M_MEAN + fq * 16);
    asm volatile("" : "+v"(sa0), "+v"(sa1), "+v"(sa2), "+v"(sa6), "+v"(sa3), "+v"(sa7));
    if (HALO) asm volatile("" : "+v"(sa4), "+v"(sa5));
    const int head = c.jq * 2 + wc;
    auto rso = __builtin_amdgcn_make_buffer_rsrc(HALO ? c.ws + WS_O + (size_t)l * (8u << 20) + (size_t)((head * 16 + c.pq) * 8 + wr * 4) * (1024 << HO)
                                                      : c.ws + WS_KV + (size_t)(l >> 1) * (16u << 20) + (size_t)((c.pq * 32 + head) * 4 + wr) * (4096 << HK), 0, HALO ? 4096 << HO : 12288 << HK, 0x00020000);
    unsigned xrs = (unsigned)(M_XCH + wc * 6144 + wr * 2048), xhs = (unsigned)(M_XCH + wc * 6144 + 8 - wr * 8);
    unsigned b0m = (c.pq == 0 && wr == 0) ? 15u * 16u : 0u;
    asm volatile("" : "+s"(rso));
    if (HALO) asm volatile("" : "+s"(xrs), "+s"(xhs), "+s"(b0m));
    u32x4 cpy[3];
    unsigned cv, vt, cdst;
    const char *cs_, *tw, *cd_;
    int more = !HALO && l == 0 ? KT - 3 - XST - (c.pq & 7) : 0, three = c.jq & 4;
    if (!HALO) {
        static_assert(CPY + TOUCH == 2 && XSP == 1, "extra");
        asm volatile("" : "+s"(kp));
        const int g = c.jq >> 2, n = c.pq * 4 + (c.jq & 3), m = n >> 5, kt = (n >> 1) & 15;
        const char* src = *(const char* const __attribute__((address_space(4)))*)((const __attribute__((address_space(4))) char*)kp + (three ? (m ? 8 : 24) : (m ? 32 : 40)));
        const char* tsr = *(const char* const __attribute__((address_space(4)))*)((const __attribute__((address_space(4))) char*)kp + (c.wid < 3 ? 24 : 40));
        int o1 = kt * (64 * 6144) + (n & 1) * (32 * 6144) + (3 + 4 * (g & 2) - c.jq) * 128 + 2048, o2 = (n & 31) * 65536 + (11 - 4 * g) * 128 - c.pq * 128 * (D_ * 2) + 2048;
        int d1 = (int)WS_CQ + kt * (64 * 6144) + (m * 2 + (g >> 1)) * 24576 + (n & 1) * 4096, d2 = (int)WS_CO + (n & 31) * 65536 + (m * 2 + 1 - (g >> 1)) * 4096;
        asm volatile("" : "+s"(o1), "+s"(o2), "+s"(d1), "+s"(d2));
        cs_ = src + (three ? o1 : o2);
        tw = tsr + (unsigned)((c.wid < 3 ? c.wid : 0) * (2u << 20) + c.bid * 8192);
        cd_ = c.ws + (unsigned)(three ? d1 : d2);
        cv = pick(three, vw0, va0 ^ ((lane & 8) << 1));
        cdst = (unsigned)(c.wid * 1024 + lane * 16);
        vt = TWQ ? (unsigned)(((lane & 7) == 3 ? lane - 1 : lane) * 128) : 0u;
        asm volatile("" : "+s"(more), "+s"(three), "+s"(cs_), "+s"(tw), "+s"(cd_));
        asm volatile("" : "+v"(cv), "+v"(cdst), "+v"(vt));
        asm volatile("" : "=v"(cpy[0]), "=v"(cpy[1]), "=v"(cpy[2]));
    }
    unsigned hto = HT0 == 2 ? 0u : 0x80000000u;
    const char* W0 = (const char*)kp->wqkv0;
    if (!HALO && HT0) {
        const int n = ((c.pq & 7) * 3 + (c.jq & 3)) * 64 + c.wid * 16 + fr, r3 = (n * 43691) >> 17;
        const int t0 = rot_first(c.pq & 8, c.jq | 3) + 8;
        if (l == 0 && !(c.jq & 4) && (c.jq & 3) != 3 && lane < 16) hto = (unsigned)(((t0 * 64 + r3) & 1023) * 6144 + (n - 3 * r3) * 2048 + (c.jq | 3) * 128);
        asm volatile("" : "+v"(hto));
        asm volatile("" : "+s"(W0));
    }
    if (!HALO && HT0 == 2) { asm volatile("s_mov_b32 m0, %0\n\ts_nop 3\n\tglobal_load_lds_dword %1, %2" ::"s"(M_TCH), "v"(hto), "s"(W0) : "memory"); }
    if (!HALO && !ERL && l == 0) BAR();
    if (ST0 && l == 0 && c.wid == 1) STAMP_AT(44);
    if (!HALO) { if (HT0 == 2) wait_v<1>(); else WAIT_V0(); }
    if (ST0 && l == 0 && c.wid == 1) STAMP_AT(46);
    BAR();
    if (!HALO && HT0 == 1) { if (hto < 0x80000000u) asm volatile("s_mov_b32 m0, %0\n\ts_nop 3\n\tglobal_load_lds_dword %1, %2" ::"s"(M_TCH), "v"(hto), "s"(W0) : "memory"); }
    if (c.wid == 0) STAMP_AT(l * 8 + 0);
    ldfrag(0, pa0, ph0, pb0, pb1, 0);
    SCHED();
    if (ALN) WAIT_L0();
    SCHED();
    int cnt = KT - 3 - more, tv = 0;
    QSTAMP(0);
    auto half0 = [&]() {
        ldfrag(1, pa1, ph1, pb0, pb1, 1);
        mm(0, 0, NH);
        interleave();
        mm(0, NH, NH + 1); SCHED();
        asm volatile("s_addk_i32 %0, %2\n\ts_addk_i32 %1, %3" : "+s"(sa), "+s"(sb) : "i"(QA_SLOT), "i"(QW_SLOT) : "scc"); SCHED();
        mm(0, NH + 1, NH + 2); SCHED();
        asm volatile("s_cmpk_lg_u32 %0, %2\n\ts_cselect_b32 %0, %0, 0\n\ts_cmp_lg_u32 %1, %3\n\ts_cselect_b32 %1, %1, %4" : "+s"(sa), "+s"(sb) : "i"(NQA * QA_SLOT), "i"(3 * QW_SLOT), "i"(QW_TOP) : "scc"); SCHED();
        mm(0, NH + 2, NH + 3); SCHED();
        asm volatile("s_cmp_lg_u32 %0, %2\n\ts_cselect_b32 %0, %0, 0\n\ts_addk_i32 %1, -1" : "+s"(sb), "+s"(cnt) : "i"(QW_TOP + QW_SLOT) : "scc"); SCHED();
        mm(0, NH + 3, NH + 4); SCHED();
        pa0 = aoff0 + sa; pb0 = boff[0] + sb;
        asm volatile("" : "+v"(pa0), "+v"(pb0)); SCHED();
        mm(0, NH + 4, NH + 5); SCHED();
        pb1 = boff[1] + sb; pa1 = pa0 ^ xm;
        asm volatile("" : "+v"(pa1), "+v"(pb1)); SCHED();
        mm(0, NH + 5, NH + 6); SCHED();
        if (HALO) { ph0 = hoff0 + sa; ph1 = ph0 ^ xm; asm volatile("" : "+v"(ph0), "+v"(ph1)); SCHED(); }
        mm(0, NH + 6, NM);
        SCHED();
        WAIT_L0();
        BAR();
        SCHED();
    };
    asm volatile("" : "+s"(sa), "+s"(sb), "+s"(cnt));
    do {
    ALIGN8();
    do {
        half0();
        ldfrag(0, pa0, ph0, pb0, pb1, 0);
        mm(1, 0, NH);
        interleave();
        mm(1, NH, NM - 1);
        SCHED();
        WAIT_L0();
        asm volatile("s_nop 0");
        SCHED();
        mm(1, NM - 1, NM);
        SCHED();
    } while (cnt != 0);
    if (!HALO) {
        asm volatile("s_cmp_eq_u32 %[more], 0\n\ts_cbranch_scc1 .Lex%=\n\ts_mov_b32 m0, %[tch]\n\ts_nop 3\n\tglobal_load_lds_dword %[vt], %[tw] sc1\n\t"
                     "global_load_dwordx4 %[c0], %[v0], %[cs] offset:-2048\n\ts_cmp_eq_u32 %[three], 0\n\ts_cbranch_scc1 .Lex%=\n\t"
                     "global_load_dwordx4 %[c1], %[v0], %[cs]\n\tglobal_load_dwordx4 %[c2], %[v0], %[cs] offset:2048\n.Lex%=:"
                     : [c0] "+v"(cpy[0]), [c1] "+v"(cpy[1]), [c2] "+v"(cpy[2])
                     : [more] "s"(more), [tch] "i"(M_TCH), [vt] "v"(vt), [tw] "s"(tw), [v0] "v"(cv), [cs] "s"(cs_), [three] "s"(three) : "scc", "memory");
        cnt = more; more = 0;
        asm volatile("" : "+s"(cnt), "+s"(more));
        SCHED();
    }
    } while (cnt != 0);
    if (!HALO) {
        asm volatile("s_cmp_lg_u32 %[l], 0\n\ts_cbranch_scc1 .Lex%=\n\ts_waitcnt vmcnt(0)\n\ts_nop 3\n\tglobal_store_dwordx4 %[d], %[c0], %[ws] sc1\n\ts_cmp_eq_u32 %[three], 0\n\ts_cbranch_scc1 .Lex%=\n\t"
                     "v_add_u32 %[d], 0x2000, %[d]\n\tglobal_store_dwordx4 %[d], %[c1], %[ws] sc1\n\tv_add_u32 %[d], 0x2000, %[d]\n\tglobal_store_dwordx4 %[d], %[c2], %[ws] sc1\n.Lex%=:\n\ts_nop 1"
                     : [d] "+v"(cdst) : [c0] "v"(cpy[0]), [c1] "v"(cpy[1]), [c2] "v"(cpy[2]), [l] "s"(l), [ws] "s"(cd_), [three] "s"(three) : "scc", "memory");
    }
    QSTAMP(1);
    FENCE();
    SCHED();

    f32x4 csq[4], m4[4], mh4; float csv[2], mr[4], rr[4], mh;
    auto r1 = [&](unsigned b, int off) { return *(const LAS float*)(lds(b) + off); };
    auto r4_ = [&](unsigned b, int off) { return *(const LAS f32x4*)(lds(b) + off); };
    auto mf = [&](int set, int i) {
        const int nt = i >> 2, mt = i & 3;
        if (i < 16) acc[mt][nt] = MFMA(wb[set][nt], xa[set][mt], acc[mt][nt]);
        else if (i < 24) acc[mt][nt] = MFMA(xa[set][mt], wb[set][nt], acc[mt][nt]);
        else if (i == 24) ah[0] = MFMA(wb[set][2], xh[set], ah[0]);
        else ah[1] = MFMA(xh[set], wb[set][4], ah[1]);
        SCHED();
    };
    auto fold = [&](int i, int e) {
        const int nt = i >> 2, mt = i & 3;
        float t;
        if (i < 16) t = __builtin_fmaf(mr[mt], csq[nt][e], acc[mt][nt][e]);
        else if (i < 24) t = __builtin_fmaf(m4[mt][e], csv[nt - 4], acc[mt][nt][e]);
        else if (i == 24) t = __builtin_fmaf(mh, csq[2][e], ah[0][e]);
        else t = __builtin_fmaf(mh4[e], csv[0], ah[1][e]);
        asm volatile("" : "+v"(t));
        if (i < 24) acc[mt][nt][e] = t; else ah[i - 24][e] = t;
    };
    constexpr Tail<HALO> TL{};
    constexpr int NP = HALO ? 6 : 2, FN = 4 * NM;
    static_assert(TL.v[3][TL.n[3]] == NP + FN + (HALO ? 2 : 0) && KT % NQA == 1 && KT % NQW == 0, "tail");
    s16x4 wl[6], wh[6];
    auto frd = [&](int set, int k, int ao) {
        if (k < 4) xa[set][k] = *(const LAS bf16x8*)(lds(set ? pa1 : pa0) + ao + k * 2048);
        else if (k == 16) xh[set] = *(const LAS bf16x8*)(lds(set ? ph1 : ph0) + ao);
        else {
            const int nt = (k - 4) >> 1;
            const LAS char* pb = lds((nt & 1) ? pb1 : pb0) + (nt >> 1) * 8192 + set * 4096;
            if (k & 1) { wh[nt] = tr_read(pb + 512); wb[set][nt] = __builtin_shufflevector(wl[nt], wh[nt], 0, 1, 2, 3, 4, 5, 6, 7); }
            else wl[nt] = tr_read(pb);
        }
    };
    constexpr int RS = M_RSTD - M_MEAN;
    unsigned vl, vg, tb, xr, xh_, b0a, qb0, qb1;
    f32x4 bprev, bcur, bprev0; u32x4 ps; u32x2 ko; float ninf;
    auto rdop = [&](int h, int k) {
        const int j = k - NH;
        if (h == 0) {
            if (k < 2) { mr[2 * k] = r1(sa0, k * 128); mr[2 * k + 1] = r1(sa0, k * 128 + 64); }
            else if (k < 4) csq[k - 2] = r4_((k & 1) ? sa3 : sa2, 0);
            else if (j < 4) frd(0, k - 4, 2 * QA_SLOT);
            else csq[j - 2] = r4_((j & 1) ? sa3 : sa2, 256);
        } else if (j < 0) frd(h & 1, k, h == 1 ? 2 * QA_SLOT : 0);
        else if (h == 1) {
            if (j < 2) csv[j] = r1(j ? sa7 : sa6, 0);
            else if (HALO && j == 2) mh = r1(sa4, 0);
            else if (HALO && j == 3) mh4 = r4_(sa5, 0);
            else { const int mt = HALO ? 7 - j : j - 2; m4[mt] = r4_(sa1, mt * 64); }
        } else if (h == 2) {
            if (j == 0) m4[2] = r4_(sa1, 128);
            if (j == 1) ko = *(const LAS u32x2*)(lds(tb) + 3072);
            if (j == 2) { rr[0] = r1(sa0, RS); rr[1] = r1(sa0, 64 + RS); }
            if (j == 3) { rr[2] = r1(sa0, 128 + RS); rr[3] = r1(sa0, 192 + RS); }
            if (j >= 4) m4[5 - j] = r4_(sa1, (5 - j) * 64);
        } else {
            if (j == 0) bprev = r4_(tb, 0);
            if (j == 1) bcur = r4_(tb, 1024);
            if (j == 2) ps = *(const LAS u32x4*)(lds(tb) + 2048);
            if (j == 3) bprev0 = r4_(b0a, 0);
        }
    };
    auto vop = [&](int k) {
        if (k == 0) { asm volatile("" : "+v"(boff[0])); qb0 = boff[0] + QW_TOP; asm volatile("" : "+v"(qb0)); }
        else if (k == 1) { asm volatile("" : "+v"(boff[1])); qb1 = boff[1] + QW_TOP; asm volatile("" : "+v"(qb1)); }
        else if (k < NP) {
            if (k == 2) { vl = (unsigned)(lane * 16); asm volatile("" : "+v"(vl)); }
            if (k == 3) { tb = vl + M_TAB; asm volatile("" : "+v"(tb)); }
            if (k == 4) { b0a = tb | b0m; asm volatile("" : "+v"(b0a)); }
            if (k == 5) { xr = xrs + vl; asm volatile("" : "+v"(xr)); }
        } else if (k < NP + FN) fold(TL.f[(k - NP) >> 2], (k - NP) & 3);
        else {
            if (k == NP + FN) { xh_ = xhs + vl; asm volatile("" : "+v"(xh_)); }
            if (k == NP + FN + 1) { ninf = -INFINITY; asm volatile("" : "+v"(ninf)); }
        }
    };
    unsigned wa, wq;
    int wr2 = wr, wc2 = wc;
#pragma unroll
    for (int i = 0; i < NM; ++i) {
        mf(0, TL.t[0][i]);
        if (TL.t[0][i] < 24) asm volatile("" : "+v"(acc[TL.t[0][i] & 3][TL.t[0][i] >> 2])); else asm volatile("" : "+v"(ah[TL.t[0][i] & 1]));
        FENCE();
        if (i < NH) frd(1, i, 0);
        if (!HALO && i == NH + 3) { asm volatile("" : "+s"(wr2), "+s"(wc2)); vl = (unsigned)(lane * 16); wq = (unsigned)(AT_Q + (wc2 * 8 + wr2 * 4) * 1024) + vl; asm volatile("" : "+v"(vl), "+v"(wq)); }
        if (!HALO && i == NH + 4) { asm volatile("" : "+s"(wr2), "+s"(wc2)); wa = (unsigned)(AT_OWN + wc2 * 16384 + wr2 * 4096) + vl; asm volatile("" : "+v"(wa)); }
        if (i == NH + 1) { asm volatile("" : "+v"(boff[0]), "+v"(boff[1])); pb0 = boff[0] + 2 * QW_SLOT; pb1 = boff[1] + 2 * QW_SLOT; asm volatile("" : "+v"(pb0), "+v"(pb1)); }
        if (i == NH + 2) { pa0 = aoff0; pa1 = aoff0 ^ xm; asm volatile("" : "+v"(pa1)); if (HALO) { ph0 = hoff0; ph1 = hoff0 ^ xm; asm volatile("" : "+v"(ph1)); } }
        SCHED();
    }
    WAIT_L0();
    BAR();
    FENCE();
    SCHED();
    auto frags = [&](int h) {
        static_assert(TL.d[0][NM] == NH + 6 && TL.d[1][NM] == NH + (HALO ? 5 : 6) && TL.d[2][NM] == NH + (HALO ? 6 : 0) && TL.d[3][TL.n[3]] == NH + (HALO ? 4 : 0), "reads");
        if (h == 0) __builtin_amdgcn_s_waitcnt(0xC27F);
        if (h == 1) __builtin_amdgcn_s_waitcnt(HALO ? 0xC57F : 0xC67F);
        if (h == 2) __builtin_amdgcn_s_waitcnt(HALO ? 0xC67F : 0xC07F);
        if (h == 3) __builtin_amdgcn_s_waitcnt(HALO ? 0xC47F : 0xC07F);
    };
#pragma unroll
    for (int h = 0; h < 4; ++h) {
        if (h == 2) { pb0 = qb0; pb1 = qb1; }
#pragma unroll
        for (int i = 0; i < TL.n[h]; ++i) {
            if (!(h & 1) && i == TL.n[h] - 1) { frags(h); SCHED(); if (h) QSTAMP(4); }
            mf(~h & 1, TL.t[h][i]);
#pragma unroll
            for (int k = TL.d[h][i]; k < TL.d[h][i + 1]; ++k) { FENCE(); rdop(h, k); }
            SCHED();
#pragma unroll
            for (int k = TL.v[h][i]; k < TL.v[h][i + 1]; ++k) vop(k);
            SCHED();
        }
        if (h & 1) {
            frags(h);
            if (h == 1) QSTAMP(2); else QSTAMP(5);
            BAR();
            FENCE();
            SCHED();
            if (h == 1) QSTAMP(3); else QSTAMP(6);
        }
    }
    if (!HALO) {
        f32x4 r4[4];
        mf(1, 8);
#pragma unroll
        for (int mt = 0; mt < 4; ++mt) rr[mt] = r1(sa0, mt * 64 + RS);
        SCHED();
        mf(1, 12);
#pragma unroll
        for (int mt = 0; mt < 4; ++mt) r4[mt] = r4_(sa1, mt * 64 + RS);
        SCHED();
        mf(1, 9);
        vg = HK ? (unsigned)hv<HK>(lane) : vl;
        if (HK) asm volatile("" : "+v"(vg));
        SCHED();
        u32x4 pk[8];
        auto op = [&](int g, int k) {
            const int ta = g < 4 ? g : (g - 4) & 2, tb = g < 4 ? g : ta + 1, na = g < 4 ? 2 : 4 + (g & 1), nb = g < 4 ? 3 : na;
            f32x4& A = acc[ta][na]; f32x4& Bv = acc[tb][nb];
            if (k < 12) {
                const int h = k / 6, kk = k % 6;
                f32x4& X = h ? Bv : A;
                const int tx = h ? tb : ta;
                if (kk < 4) { float t = X[kk] * (g < 4 ? rr[tx] : r4[tx][kk]); asm volatile("" : "+v"(t)); X[kk] = t; }
                else { unsigned t = cvt_pk_bf16(X[2 * (kk - 4)], X[2 * (kk - 4) + 1]); asm volatile("" : "+v"(t)); pk[g][2 * h + kk - 4] = t; }
            } else if (k == 12) __builtin_amdgcn_raw_buffer_store_b128(pk[g], rso, vg + hp<HK>(g & 3), (g >> 2) * (8192 << HK), 16);
            else *(LAS u32x4*)(lds(wa) + (g >> 2) * 8192 + (g & 3) * 1024) = pk[g];
            SCHED();
        };
        auto pm = [&](int i) {
            if (i < 16) mf(1, TL.h3[i]);
            else if (i < 32) mf(((i - 16) >> 1) & 1, ((i - 16) >> 2) + 4 * (i & 1));
        };
        pm(3);
#pragma unroll
        for (int g = 0; g < 8; ++g) {
#pragma unroll
            for (int k = 0; k < 6; ++k) op(g, k);
            pm(2 * g + 4);
            if (g > 0) op(g - 1, 12);
#pragma unroll
            for (int k = 6; k < 12; ++k) op(g, k);
            pm(2 * g + 5);
            if (g > 0 && g <= LWA) op(g - 1, 13);
        }
        op(7, 12);
        QSTAMP(7);
        u32x4 qk;
        auto qop = [&](int mt, int k) {
            if (k == 0) { rr[mt] *= QSCALE; asm volatile("" : "+v"(rr[mt])); }
            else if (k < 13) {
                const int h = (k - 1) / 6, kk = (k - 1) % 6;
                f32x4& X = acc[mt][h];
                if (kk < 4) { float t = X[kk] * rr[mt]; asm volatile("" : "+v"(t)); X[kk] = t; }
                else { unsigned t = cvt_pk_bf16(X[2 * (kk - 4)], X[2 * (kk - 4) + 1]); asm volatile("" : "+v"(t)); qk[2 * h + kk - 4] = t; }
            } else *(LAS u32x4*)(lds(wq) + mt * 1024) = qk;
            SCHED();
        };
#pragma unroll
        for (int mt = 0; mt < 3; ++mt) {
            pm(20 + 4 * mt); if (3 * mt + LWA < 8) op(3 * mt + LWA, 13); qop(mt, 0); qop(mt, 1); qop(mt, 2);
            pm(21 + 4 * mt); if (3 * mt + 1 + LWA < 8) op(3 * mt + 1 + LWA, 13); qop(mt, 3); qop(mt, 4); qop(mt, 5);
            pm(22 + 4 * mt); if (3 * mt + 2 + LWA < 8) op(3 * mt + 2 + LWA, 13); qop(mt, 6); qop(mt, 7); qop(mt, 8);
            pm(23 + 4 * mt); qop(mt, 9); qop(mt, 10); qop(mt, 11); qop(mt, 12); qop(mt, 13);
        }
        if (c.wid == 0) STAMP_AT(l * 8 + 1);
        QSTAMP(8);
        {
            const u32x2 q0 = pack4(acc[3][0] * (rr[3] * QSCALE)), q1 = pack4(acc[3][1] * (rr[3] * QSCALE));
            *(LAS u32x4*)(lds(wq) + 3 * 1024) = (u32x4){q0[0], q0[1], q1[0], q1[1]};
        }
        QSTAMP(9);
        QSTORE();
        if (K0E) {
            SCHED();
            WAIT_L0();
            BAR();
            FENCE();
            SCHED();
            if (KSP == 4) {
                publish<4, 0>(smem, lane, c.ws, (unsigned)WS_FLAGQ + (c.pq * 16 + c.jq) * FQS, lane == 0, (unsigned)(4 * l + 2));
                if (c.wid == 0) STAMP_AT(l * 8 + 2);
                SCHED();
            }
        }
    } else {
        typedef unsigned u32x10 __attribute__((ext_vector_type(10)));
        u32x4 kp[4], qp[4];
        u32x10 vw[2];
        float ks[2][4], cs[2][4];
        auto pk8 = [&](const f32x4& x, const f32x4& y) { const u32x2 a = pack4(x), b = pack4(y); u32x4 r = {a[0], a[1], b[0], b[1]}; asm volatile("" : "+v"(r)); SCHED(); return r; };
        auto pkv = [&](int mt, int cc) { const u32x2 a = pack4(acc[mt][4 + cc]); vw[cc][2 * mt + 2] = a[0]; vw[cc][2 * mt + 3] = a[1]; };
        auto fr8 = [](const u32x4& x) { return __builtin_bit_cast(bf16x8, x); };
        mf(1, 24);
        unsigned ka0 = ko[0] + wr * 256, ka1 = ko[1] + wr * 256;
        asm volatile("" : "+v"(ka0), "+v"(ka1));
        SCHED();
        mf(1, 25);
#pragma unroll
        for (int mt = 0; mt < 4; ++mt) ks[0][mt] = r1(ka0, mt * 64);
        rr[0] *= QSCALE; asm volatile("" : "+v"(rr[0]));
        SCHED();
        mf(1, 11);
#pragma unroll
        for (int mt = 0; mt < 4; ++mt) ks[1][mt] = r1(ka1, mt * 64);
        rr[1] *= QSCALE; asm volatile("" : "+v"(rr[1]));
        SCHED();
        mf(1, 15);
        rr[2] *= QSCALE; asm volatile("" : "+v"(rr[2]));
        rr[3] *= QSCALE; asm volatile("" : "+v"(rr[3]));
        SCHED();
        mf(1, 19);
        const u32x4 hh = pk8(ah[0], ah[1]);
        mf(1, 23);
        *(LAS u32x2*)lds(xh_) = (u32x2){hh[0], hh[1]}; *(LAS u32x2*)(lds(xh_) + 1024) = (u32x2){hh[2], hh[3]};
        SCHED();
        mf(1, 10);
        kp[3] = pk8(acc[3][2], acc[3][3]);
        mf(1, 14);
        *(LAS u32x4*)(lds(xr) + 2048) = kp[3];
        pkv(3, 0); asm volatile("" : "+v"(vw[0]));
        *(LAS u32x2*)(lds(xr) + 3072) = (u32x2){vw[0][8], vw[0][9]};
        SCHED();
        mf(1, 3);
        pkv(3, 1); asm volatile("" : "+v"(vw[1]));
        *(LAS u32x2*)(lds(xr) + 3072 + 8) = (u32x2){vw[1][8], vw[1][9]};
        SCHED();
        mf(1, 7);
        mf(1, 9);
        kp[2] = pk8(acc[2][2], acc[2][3]);
        mf(1, 13);
        qp[3] = pk8(acc[3][0], acc[3][1]);
        mf(1, 2);
        f32x4 s0[4], s1[4];
        u32x4 pk;
        auto sc = [&](int mt, int h) {
            if (h) { s1[mt] = MFMA(fr8(kp[mt]), fr8(qp[mt]), bcur); asm volatile("" : "+v"(s1[mt])); }
            else { s0[mt] = MFMA(fr8(mt ? kp[mt - (mt > 0)] : pk), fr8(qp[mt]), mt == 0 ? bprev0 : bprev); asm volatile("" : "+v"(s0[mt])); }
            SCHED();
        };
        auto csm = [&](int mt) { cs[0][mt] = ks[0][mt] * rr[mt]; asm volatile("" : "+v"(cs[0][mt])); cs[1][mt] = ks[1][mt] * rr[mt]; asm volatile("" : "+v"(cs[1][mt])); };
        float u[2][4], mx[4], iv[4];
        auto sv = [](float x) { asm volatile("" : "+v"(x)); return x; };
        auto mx2 = [&](float a, float b) { return max3(ninf, a, b); };
        auto pick = [&](int mt, int h) { u[h][mt] = sv(max3(mx2(s0[mt][2 * h], s0[mt][2 * h + 1]), s1[mt][2 * h], s1[mt][2 * h + 1]) * cs[h][mt]); };
        auto vf = [&](int i) { const int mt = 2 - (i >> 2), nt = 4 + (i & 1), set = (i >> 1) & 1; SCHED(); acc[mt][nt] = MFMA(xa[set][mt], wb[set][nt], acc[mt][nt]); asm volatile("" : "+v"(acc[mt][nt])); SCHED(); };
        mf(1, 6);
        kp[1] = pk8(acc[1][2], acc[1][3]);
        mf(1, 8);
        WAIT_L0();
        QSTAMP(7);
        BAR();
        FENCE();
        SCHED();
        QSTAMP(8);
        mf(1, 12);
        {
            const u32x2 k0 = *(const LAS u32x2*)(lds(xr) + 8);
            FENCE(); SCHED();
            const u32x2 k1 = *(const LAS u32x2*)lds(xr);
            FENCE(); SCHED();
            const u32x2 a = *(const LAS u32x2*)(lds(xr) + 1024 + 8);
            FENCE(); SCHED();
            const u32x2 b = *(const LAS u32x2*)(lds(xr) + 1024);
            pk = (u32x4){k0[0], k0[1], k1[0], k1[1]};
            vw[0][0] = a[0]; vw[0][1] = a[1]; vw[1][0] = b[0]; vw[1][1] = b[1];
        }
        SCHED();
        auto sw16 = [](float& a, float& b) { auto r = __builtin_amdgcn_permlane16_swap(__float_as_uint(a), __float_as_uint(b), false, false); a = __uint_as_float(r[0]); b = __uint_as_float(r[1]); };
        auto sw32 = [](float& a, float& b) { auto r = __builtin_amdgcn_permlane32_swap(__float_as_uint(a), __float_as_uint(b), false, false); a = __uint_as_float(r[0]); b = __uint_as_float(r[1]); };
        auto ldr = [&](int i) { };
        auto top = [&](int mt) { mx[mt] = sv(mx2(u[0][mt], u[1][mt])); };
        auto pv1 = [&](int mt, int k) {
            SCHED();
            vw[k >> 1][2 * mt + 2 + (k & 1)] = cvt_pk_bf16(acc[mt][4 + (k >> 1)][2 * (k & 1)], acc[mt][4 + (k >> 1)][2 * (k & 1) + 1]);
            asm volatile("" : "+v"(vw[k >> 1]));
            SCHED();
        };
        auto uk = [&](int mt) { SCHED(); u[0][mt] = sv(u[0][mt] * ks[0][mt]); u[1][mt] = sv(u[1][mt] * ks[1][mt]); SCHED(); };
        mf(1, 1);
        qp[2] = pk8(acc[2][0], acc[2][1]);
        mf(1, 5);
        csm(3); SCHED();
        mf(1, 0);
        kp[0] = pk8(acc[0][2], acc[0][3]);
        mf(1, 4);
        csm(2); SCHED();
        sc(3, 1);
        qp[1] = pk8(acc[1][0], acc[1][1]);
        sc(3, 0);
        csm(1); ldr(0); SCHED();
        sc(2, 1);
        qp[0] = pk8(acc[0][0], acc[0][1]);
        sc(2, 0);
        csm(0); pick(3, 0); SCHED();
        sc(1, 1);
        pick(3, 1); ldr(1); SCHED();
        sc(1, 0);
        pick(2, 0); top(3); SCHED();
        sc(0, 1);
        pick(2, 1); ldr(2); SCHED();
        sc(0, 0);
        if (c.wid == 0) STAMP_AT(l * 8 + 1);
        pick(1, 0); top(2);
        vf(0);
        pick(1, 1); ldr(3);
        vf(1);
        sw16(mx[2], mx[3]);
        pick(0, 0);
        vf(2);
        pick(0, 1); top(1);
        vf(3);
        top(0); mx[2] = sv(mx2(mx[2], mx[3]));
        vf(4);
        sw16(mx[0], mx[1]); mx[0] = sv(mx2(mx[0], mx[1]));
        vf(5); pv1(2, 0);
        sw32(mx[0], mx[2]);
        { const float a = mx[0], b = mx[2]; mx[0] = sv(mx2(a, b)); mx[2] = sv(mx2(b, a)); }
        vf(6); pv1(2, 1);
        sw32(mx[0], mx[2]);
        mx[3] = sv(mx[2]); mx[1] = sv(mx[0]);
        vf(7);
        sw16(mx[2], mx[3]); sw16(mx[0], mx[1]);
        SCHED();
#pragma unroll
        for (int mm = 0; mm < 4; ++mm) { const int mt = 3 - mm; u[0][mt] = sv(u[0][mt] - mx[mt]); u[1][mt] = sv(u[1][mt] - mx[mt]); if (mm == 1) vf(8); }
        vf(9);
#pragma unroll
        for (int mm = 0; mm < 4; ++mm) {
            const int mt = 3 - mm;
            u[0][mt] = sv(__builtin_amdgcn_exp2f(u[0][mt])); u[1][mt] = sv(__builtin_amdgcn_exp2f(u[1][mt]));
            if (mm > 0) iv[mt + 1] = sv(u[0][mt + 1] + u[1][mt + 1]);
            if (mm < 2) vf(10 + mm);
        }
        SCHED();
        sw16(iv[2], iv[3]);
        iv[0] = sv(u[0][0] + u[1][0]);
        iv[2] = sv(iv[2] + iv[3]);
        pv1(2, 2);
        sw16(iv[0], iv[1]); iv[0] = sv(iv[0] + iv[1]);
        uk(3);
        sw32(iv[0], iv[2]);
        iv[0] = sv(iv[0] + iv[2]);
        pv1(2, 3);
        iv[0] = sv(__builtin_amdgcn_rcpf(iv[0]));
        pv1(1, 0);
        iv[2] = sv(iv[0]);
        uk(2);
        sw32(iv[0], iv[2]);
        iv[3] = sv(iv[2]); iv[1] = sv(iv[0]);
        pv1(1, 1);
        sw16(iv[2], iv[3]); sw16(iv[0], iv[1]);
        SCHED();
        f32x4 o[4][2];
        u32x4 pf[4];
        auto nrm = [&](int mt) { u[0][mt] = sv(u[0][mt] * iv[mt]); u[1][mt] = sv(u[1][mt] * iv[mt]); SCHED(); };
        auto prb = [&](int mt) {
            const unsigned p = cvt_pk_bf16(u[0][mt], u[1][mt]);
            pf[mt] = (u32x4){__builtin_amdgcn_perm(0u, p, ps[0]), __builtin_amdgcn_perm(0u, p, ps[1]), __builtin_amdgcn_perm(0u, p, ps[2]), __builtin_amdgcn_perm(0u, p, ps[3])};
            asm volatile("" : "+v"(pf[mt]));
            SCHED();
        };
        auto pvm = [&](int mt, int cc) {
            o[mt][cc] = MFMA(fr8((u32x4){vw[cc][2 * mt], vw[cc][2 * mt + 1], vw[cc][2 * mt + 2], vw[cc][2 * mt + 3]}), fr8(pf[mt]), ((f32x4){0.f, 0.f, 0.f, 0.f}));
            asm volatile("" : "+v"(o[mt][cc]));
            SCHED();
        };
        auto out = [&](int mt) {
            const u32x2 lo = pack4(o[mt][0]), hi = pack4(o[mt][1]);
            __builtin_amdgcn_raw_buffer_store_b128((u32x4){lo[0], lo[1], hi[0], hi[1]}, rso, vg + hp<HO>(mt), 0, 16);
            SCHED();
        };
        vg = (unsigned)hv<HO>(lane);
        asm volatile("" : "+v"(vg));
        nrm(3); nrm(2); prb(3);
        pv1(1, 2); pv1(1, 3);
        pvm(3, 0); prb(2); pvm(3, 1);
        uk(1); uk(0);
        pvm(2, 0); nrm(1); nrm(0); pvm(2, 1);
        prb(1); pv1(0, 0); pv1(0, 1);
        pvm(1, 0); pv1(0, 2); pv1(0, 3); prb(0);
        pvm(1, 1); out(3); pvm(0, 0); out(2); pvm(0, 1); out(1); out(0);
        SCHED();
        QSTAMP(9);
        if (c.wid == 0) STAMP_AT(l * 8 + 2);
        QSTORE();
    }
}

#define MFMA32(a, b, c) __builtin_amdgcn_mfma_f32_32x32x16_bf16(__builtin_bit_cast(bf16x8, a), __builtin_bit_cast(bf16x8, b), (c), 0, 0, 0)
DI float max16(const f32x16& s) {
    float m = max3(s[0], s[1], s[2]), n = max3(s[3], s[4], s[5]);
    m = max3(m, s[6], s[7]); n = max3(n, s[8], s[9]);
    m = max3(m, s[10], s[11]); n = max3(n, s[12], s[13]);
    m = max3(max3(m, s[14], s[15]), n, n);
    auto b = __builtin_amdgcn_permlane32_swap(__float_as_uint(m), __float_as_uint(m), false, false);
    return __builtin_amdgcn_fmed3f(__uint_as_float(b[0]), __uint_as_float(b[1]), INFINITY);
}
template <bool HI> DI void attn_a(const Ctx& c0, int l, const char* Wo, KP kp) {
    const Ctx c = fresh(c0);
    asm volatile("" : "+s"(Wo));
    LAS char* smem = c.smem;
    const int lane = c.lane, w = c.wid;
    constexpr int H = HI ? 0 : 1;
    int qt = HI ? w - 4 : 3 - w;
    asm volatile("" : "+s"(qt));
    const float NEG = -INFINITY;
    int zq = (lane & 31) - 4 * (lane >> 5);
    unsigned lb = (unsigned)(((lane >> 4) & 1) * 1024 + ((lane >> 5) * 16 + (lane & 15)) * 16);
    unsigned qa = (unsigned)(AT_Q + H * 8192 + qt * 2048) + lb, ta = (unsigned)(AT_OWN + H * 16384 - 3 * 2048 + qt * 2048) + lb;
    asm volatile("" : "+v"(lb), "+v"(qa), "+v"(ta));
    auto rd = [&](unsigned base, int off) { return *(const LAS u32x4*)(lds(base) + off); };
    f32x16 ce;
    u32x4 sel;
    unsigned tm = (unsigned)(AT_N + lane * 16);
    auto masks = [&]() {
        if (HI) {
#pragma unroll
            for (int r = 0; r < 16; ++r) ce[r] = 8 * (r >> 2) + (r & 3) <= zq ? 0.f : NEG;
            asm volatile("" : "+v"(ce));
        }
        const unsigned s1 = ((lane >> 4) ^ (lane >> 2)) & 1 ? 0u : 0x3f803f80u;
        sel = (u32x4){s1, s1, s1, s1};
        asm volatile("" : "+v"(sel));
    };
    u64 ep, pa_, kvv, nkv, kact;
    unsigned po, pk_, cnta = M_CNT, one = 1u;
    char* ob;
    const char *kvb, *s1_, *s2_;
    unsigned vk, dst, fo;
    auto setup = [&]() {
        if (HI) {
            const u32x2 e = *(const LAS u32x2*)(smem + M_EP);
            ep = ((u64)(unsigned)__builtin_amdgcn_readfirstlane(e[1]) << 32) | (unsigned)__builtin_amdgcn_readfirstlane(e[0]);
            po = o_post(c);
            pk_ = pick(lane < 2, (unsigned)(WS_KBOX + ((c.pq + 1 + lane) * 16 + c.jq) * 256 + lane * 8), (unsigned)(WS_FLAGQ + (c.pq * 16 + c.jq) * FQS));
            pa_ = c.pq < 14 ? 7ull : (c.pq < 15 ? 5ull : 4ull);
            fo = (unsigned)(WS_KBOX + (c.pq * 16 + c.jq) * 256 + lane * 8);
            asm volatile("" : "+v"(po), "+v"(pk_), "+v"(fo));
            asm volatile("" : "+s"(ep), "+s"(pa_));
        }
        ob = c.ws + WS_O + (size_t)l * (8u << 20) + (size_t)(unsigned)(((c.jq * 2 + H) * 16 + c.pq) * 8192 + qt * 2048);
        kvb = c.ws + WS_KV + (size_t)(unsigned)((l >> 1) * (16u << 20) + c.jq * 32768);
        asm volatile("" : "+s"(ob), "+s"(kvb));
    };
    auto setup2 = [&]() {
        const int p1 = c.pq >= 1 ? c.pq - 1 : c.pq, p2 = c.pq >= 2 ? c.pq - 2 : c.pq;
        s1_ = kvb + (size_t)(unsigned)(p1 * 524288);
        s2_ = kvb + (size_t)(unsigned)(p2 * 524288);
        vk = (unsigned)((w & 3) * 1024 + lane * 16); dst = (unsigned)(AT_N + (w & 3) * 1024);
        asm volatile("" : "+s"(s1_), "+s"(dst));
        if (!HI) asm volatile("" : "+s"(s2_));
        asm volatile("" : "+v"(vk));
    };
    auto arrive = [&](auto cc, unsigned msk, unsigned val) {
        unsigned old = 0;
        if (lane == 0) asm volatile("ds_add_rtn_u32 %0, %1, %2 offset:%3\n\ts_waitcnt lgkmcnt(0)" : "+v"(old) : "v"(cnta), "v"(one), "i"(decltype(cc)::value * 4) : "memory");
        if ((__builtin_amdgcn_readfirstlane(old) & msk) == msk) { if (lane < 16) post(c.ws, po, ep + (4 * l + val)); }
    };
    static_assert((MBOX & 2) && !HO && !HK, "layout");
    int tv = 0;
    auto kvs = [&]() {
        if (!HI) {
            SCHED();
            LSTAMP(2);
            asm volatile("s_mov_b64 exec, 1\n\ts_waitcnt vmcnt(0)\n\tds_add_u32 %0, %1\n\ts_mov_b64 exec, -1" :: "v"(cnta), "v"(one) : "memory");
            LSTAMP(3);
            SCHED();
        }
    };
    asm volatile("" : "+v"(cnta), "+v"(one), "+v"(tm));
    if (HI) {
        masks(); setup(); setup2();
        if (w == 5) {
#pragma unroll
            for (int i = 0; i < 4; ++i) *(LAS f32x4*)(lds(tm) + i * 1024) = (f32x4){ce[4 * i], ce[4 * i + 1], ce[4 * i + 2], ce[4 * i + 3]};
        }
        kvv = ep + (4 * l + 2); nkv = 0 - kvv;
        kact = __builtin_amdgcn_ballot_w64(w == KPW && lane < 2 && lane < c.pq);
        asm volatile("" : "+v"(kvv), "+v"(nkv));
        asm volatile("" : "+s"(kact));
        WAIT_L0();
        WAIT_V0();
        LSTAMP(0);
        if (w == 4) __builtin_amdgcn_s_setprio(HP4);
        BAR();
        FENCE();
    }
    auto kvp = [&]() {
        if (HI && w == 4) {
            __builtin_amdgcn_s_setprio(3);
#pragma unroll 1
            for (unsigned spins = 0; spins < (1u << 24); ++spins) {
                if (__builtin_amdgcn_readfirstlane(*(volatile LAS int*)lds(cnta)) == 8 * l + 4) break;
            }
            if (__builtin_amdgcn_inverse_ballot_w64(pa_)) post(c.ws, pk_, kvv);
            STAMP_AT(l * 8 + 2);
            __builtin_amdgcn_s_setprio(HP4);
        }
        SCHED();
    };
    if (KPP == -1) kvp();
    FENCE();
    SCHED();
    LSTAMP(1);
    u32x4 q0, q1, k0, k1, va, vp, pa, pb;
    f32x16 cm, s0, s1, o;
    f32x4 ls;
    auto stage = [&](f32x16& sc, f32x16& sn, const f32x16& cn, bool pend, bool zero, unsigned kb, int ko, unsigned vb, int vo1, unsigned nb, int vo0) {
        auto ex = [&](int i) { float x = __builtin_amdgcn_exp2f(sc[i]); asm volatile("" : "+v"(x)); sc[i] = x; SCHED(); };
        auto cv = [&](u32x4& p, int j, int i) { unsigned x = cvt_pk_bf16(sc[i], sc[i + 1]); asm volatile("" : "+v"(x)); p[j] = x; SCHED(); };
        sn = MFMA32(k0, q0, cn); SCHED();
        k0 = rd(kb, ko); SCHED();
        ex(0); ex(1); ex(2);
        if (pend) { o = MFMA32(vp, pb, o); SCHED(); }
        ex(3); ex(8); ex(9);
        sn = MFMA32(k1, q1, sn); SCHED();
        k1 = rd(kb, ko + 512); SCHED();
        ex(10); ex(11); cv(pa, 0, 0); cv(pa, 1, 2);
        if (pend) { ls = MFMA(__builtin_bit_cast(bf16x8, sel), __builtin_bit_cast(bf16x8, pb), ls); SCHED(); }
        vp = rd(vb, 8192 + vo1 + 512); SCHED();
        cv(pa, 2, 8); cv(pa, 3, 10); ex(4); ex(5);
        o = MFMA32(va, pa, zero ? (f32x16)(0.f) : o); SCHED();
        va = rd(nb, 8192 + vo0); SCHED();
        ex(6); ex(7); ex(12);
        ls = MFMA(__builtin_bit_cast(bf16x8, sel), __builtin_bit_cast(bf16x8, pa), zero ? (f32x4)(0.f) : ls); SCHED();
        ex(13); ex(14); ex(15); cv(pb, 0, 4); cv(pb, 1, 6); cv(pb, 2, 12); cv(pb, 3, 14);
    };
    auto flush = [&]() {
        ls = MFMA(__builtin_bit_cast(bf16x8, sel), __builtin_bit_cast(bf16x8, pb), ls); SCHED();
        o = MFMA32(vp, pb, o); SCHED();
    };
    float nm;
    {
        if (!HI) {
#pragma unroll
            for (int i = 0; i < 4; ++i) {
                const f32x4 t = *(const LAS f32x4*)(lds(tm) + i * 1024);
                ce[4 * i] = t[0]; ce[4 * i + 1] = t[1]; ce[4 * i + 2] = t[2]; ce[4 * i + 3] = t[3];
            }
        }
        q0 = rd(qa, 0);
        const u32x4 d0 = rd(ta, 6144);
        q1 = rd(qa, 512);
        const u32x4 d1 = rd(ta, 6144 + 512);
        k0 = rd(ta, 4096); k1 = rd(ta, 4096 + 512); va = rd(ta, 8192 + 6144);
        SCHED();
        if (KSP == -1) kvs();
        if (!HI) { masks(); setup(); SCHED(); }
        s0 = MFMA32(d0, q0, ce);
        s0 = MFMA32(d1, q1, s0);
        SCHED();
        if (!HI) { setup2(); SCHED(); }
        if (KSP == 0) kvs();
        if (KPP == 0) kvp();
        nm = -max16(s0);
        asm volatile("" : "+v"(nm));
#pragma unroll
        for (int r = 0; r < 16; ++r) { float t; asm("v_mov_b32 %0, %1" : "=v"(t) : "v"(nm), "i"(r)); cm[r] = t; }
        asm volatile("" : "+v"(cm));
        SCHED();
#pragma unroll
        for (int r = 0; r < 16; ++r) s0[r] = addf(s0[r], nm);
        asm volatile("" : "+v"(s0));
        SCHED();
        if (KSP == 1) kvs();
        if (KPP == 1) kvp();
        stage(s0, s1, cm, false, true, ta, 2048, ta, 6144, ta, 4096);
        if (KSP == 2) kvs();
        if (KPP == 2) kvp();
        if (qt > 0) {
            stage(s1, s0, cm, true, false, ta, 0, ta, 4096, ta, 2048);
            if (KSP == 3) kvs();
            if (qt > 1) {
                stage(s0, s1, cm, true, false, ta, 0, ta, 2048, ta, 0);
                if (qt > 2) stage(s1, s0, cm, true, false, ta, 0, ta, 0, ta, 0);
            }
        } else if (KSP == 3) kvs();
        flush();
    }
    LSTAMP(4);
    f32x16 c2;
    unsigned na, nb;
    auto rso = __builtin_amdgcn_make_buffer_rsrc(ob, 0, 2048, 0x00020000);
    auto starts = [&]() {
        const float n1 = c.pq >= 1 ? nm : NEG, n2 = c.pq >= 2 ? nm : NEG;
        asm volatile("" : "+v"(zq));
#pragma unroll
        for (int r = 0; r < 16; ++r) { cm[r] = n1; c2[r] = n2; ce[r] = 8 * (r >> 2) + (r & 3) <= zq ? NEG : n2; }
        na = (unsigned)(AT_N + H * 32768) + lb; nb = na + (unsigned)(16384 + qt * 2048);
        asm volatile("" : "+v"(cm), "+v"(c2), "+v"(ce), "+v"(na), "+v"(nb));
        asm volatile("" : "+s"(rso));
        SCHED();
    };
    {
        auto piece = [&](int b, int i) { dma16<true>(dst + b * 16384 + i * 4096, vk, ((b & 1) ? s2_ : s1_) + (b >> 1) * 16384 + i * 4096); };
        auto block = [&](int b) {
#pragma unroll
            for (int i = 0; i < 4; ++i) piece(b, i);
        };
        Ask t;
        if (HI) {
            poll_rmw<true>(t, c.ws, fo, __builtin_amdgcn_inverse_ballot_w64(kact), nkv, 62 - 4 * l);
            if (w == KPW) STAMP_AT(32 + l * 8 + 1);
        }
        WAIT_L0();
        LSTAMP(5);
        BAR();
        FENCE();
        LSTAMP(6);
        piece(0, H); piece(0, 2 + H);
        if (HI) {
            starts();
            if (APRIO) __builtin_amdgcn_s_setprio(APRIO);
            WAIT_V0();
            keep(t);
            LSTAMP(7);
            BAR();
            FENCE();
            if (w == 4) STAMP_AT(l * 8 + 3);
        } else {
            if (AB2 >= 0) block(1);
            if (AB2 > 0) block(2);
            starts();
            Ctx c2 = c;
            if (ELO) c2.wid = 0;
            unsigned vw = wo_w_off(c2);
            int qs = c.cw == 3 ? 1024 : 8 * D_ * 2;
            asm volatile("" : "+v"(vw));
            asm volatile("" : "+s"(qs));
            SCHED();
            wait_v<4 * AB2 + 4>();
            LSTAMP(7);
            BAR();
            if (AB2 < 0) block(1);
            if (AB2 <= 0) block(2);
            block(3);
            wait_v<8>();
            BAR();
            if (!ELO) {
                wo_w_issue<WA0, WA1>(c, Wo, vw);
                wait_v<WA1 - WA0>();
            } else WAIT_V0();
            LSTAMP(9);
            BAR();
            FENCE();
            if (!ELO) wo_w_issue<WA1, 26>(c, Wo, vw);
            else {
                const int s0 = w == 0 ? WA0 : (w == 1 ? WA0 + EN0 : WA0 + EN0 + EN1), s1 = w == 0 ? WA0 + EN0 : (w == 1 ? WA0 + EN0 + EN1 : (w == 2 ? 22 : 0));
#pragma unroll 1
                for (int sl = s0; sl < s1; ++sl) {
                    const char* b = Wo + (size_t)(unsigned)((2 * (sl & 15) + (sl >> 4)) * 65536);
#pragma unroll
                    for (int q = 0; q < 4; ++q) dma16<true>(sl * 4096 + q * 1024, vw, b + q * qs);
                }
            }
            if (LSL) { for (int i = 0; i < qt; ++i) __builtin_amdgcn_s_sleep(LSL); }
        }
    }
    SCHED();
    LSTAMP(8);
    {
        const u32x4 d0 = rd(na, 0), d1 = rd(na, 512);
        k0 = rd(na, 2048); k1 = rd(na, 2048 + 512); va = rd(na, 8192);
        SCHED();
        s0 = MFMA32(d0, q0, cm);
        s0 = MFMA32(d1, q1, s0);
        SCHED();
        stage(s0, s1, cm, false, false, na, 4096, na, 0, na, 2048);
        stage(s1, s0, cm, true, false, na, 6144, na, 2048, na, 4096);
        if (HI) { BAR(); FENCE(); SCHED(); }
        stage(s0, s1, cm, true, false, nb, 0, na, 4096, na, 6144);
        if (HI && CPOS == 3) { BAR(); SCHED(); }
        stage(s1, s0, ce, true, false, nb, 2048, na, 6144, nb, 0);
        if (HI && CPOS == 4) { WAIT_L0(); BAR(); SCHED(); }
        stage(s0, s1, c2, true, false, nb, 4096, nb, 0, nb, 2048);
        if (HI && CPOS == 5) { BAR(); SCHED(); }
        if (qt < 3) {
            stage(s1, s0, c2, true, false, nb, 6144, nb, 2048, nb, 4096);
            if (qt < 2) {
                stage(s0, s1, c2, true, false, nb, 0, nb, 4096, nb, 6144);
                if (qt < 1) stage(s1, s0, c2, true, false, nb, 0, nb, 6144, nb, 0);
            }
        }
        flush();
    }
    LSTAMP(11);
    auto out = [&]() {
        const float iv = __builtin_amdgcn_rcpf(ls[0]);
#pragma unroll
        for (int u = 0; u < 2; ++u) {
            u32x4 t;
#pragma unroll
            for (int i = 0; i < 4; ++i) { const int r = 4 * u + (i >> 1) * 8 + (i & 1) * 2; t[i] = cvt_pk_bf16(o[r] * iv, o[r + 1] * iv); }
            __builtin_amdgcn_raw_buffer_store_b128(t, rso, lb + u * 512, 0, 16);
        }
        SCHED();
    };
    out();
    if (__builtin_expect(__any(!(ls[0] < SAFE_SUM)), 0)) {
        auto rk = __builtin_amdgcn_make_buffer_rsrc((void*)(kvb + H * 16384), 0, 16u << 20, 0x00020000);
        const int g0 = c.pq * 4 + qt - 8;
        float m = NEG;
#pragma unroll 1
        for (int ps = 0; ps < 2; ++ps) {
            o = (f32x16)(0.f); ls = (f32x4)(0.f);
#pragma unroll 1
            for (int j = g0 < 0 ? -g0 : 0; j < 9; ++j) {
                const int so = ((g0 + j) >> 2) * 524288 + ((g0 + j) & 3) * 2048;
                u32x4 f[4];
#pragma unroll
                for (int i = 0; i < 4; ++i) f[i] = __builtin_amdgcn_raw_buffer_load_b128(rk, lb + (i & 1) * 512, so + (i >> 1) * 8192, 16);
                f32x16 s;
                const float sb = ps ? -m : 0.f;
                float sa = j == 0 ? NEG : sb, sz = j == 8 ? NEG : sb;
                asm volatile("" : "+v"(zq), "+v"(sa), "+v"(sz));
#pragma unroll
                for (int r = 0; r < 16; ++r) s[r] = 8 * (r >> 2) + (r & 3) <= zq ? sa : sz;
                s = MFMA32(f[0], q0, s);
                s = MFMA32(f[1], q1, s);
                if (ps == 0) m = fmaxf(m, max16(s));
                else {
                    u32x4 p[2];
#pragma unroll
                    for (int r = 0; r < 16; r += 2) p[(r >> 2) & 1][(r >> 3) * 2 + ((r >> 1) & 1)] = cvt_pk_bf16(__builtin_amdgcn_exp2f(s[r]), __builtin_amdgcn_exp2f(s[r + 1]));
#pragma unroll
                    for (int u = 0; u < 2; ++u) {
                        o = MFMA32(f[2 + u], p[u], o);
                        ls = MFMA(__builtin_bit_cast(bf16x8, sel), __builtin_bit_cast(bf16x8, p[u]), ls);
                    }
                }
            }
        }
        __builtin_amdgcn_s_waitcnt(0x0F70);
        WAIT_V0();
        out();
    }
    SCHED();
    if (w == 3 || w == 4) STAMP_AT(32 + l * 8 + 3 + w);
    LSTAMP(12);
    if (HI) {
        WAIT_V0();
        arrive(std::integral_constant<int, 1>{}, 3u, 3u);
        if (w == 4) STAMP_AT(32 + l * 8 + 4);
        LSTAMP(13);
        if (APRIO) __builtin_amdgcn_s_setprio(0);
        if (lane == 0) asm volatile("ds_add_u32 %0, %1 offset:12" :: "v"(cnta), "v"(one) : "memory");
        WAIT_V0();
        arrive(std::integral_constant<int, 2>{}, 7u, 4u);
        if (w == 5 || w == 6) {
            Ctx c2 = c;
            c2.wid = 2 * w - 10;
            unsigned vw = wo_w_off(c2), vw2 = vw + (c.cw == 3 ? 1024u : 8u * D_ * 2);
            asm volatile("" : "+v"(vw), "+v"(vw2));
#pragma unroll 1
            for (unsigned spins = 0; spins < (1u << 22); ++spins) {
                if (__hip_atomic_load((LAS unsigned*)(smem + M_CNT + 12), __ATOMIC_RELAXED, __HIP_MEMORY_SCOPE_WORKGROUP) == (unsigned)(2 * l + 4)) break;
                __builtin_amdgcn_s_sleep(1);
            }
            FENCE();
            wo_w_issue<ELO ? 22 : 26, 30>(c2, Wo, vw);
            c2.wid += 1;
            wo_w_issue<ELO ? 22 : 26, 30>(c2, Wo, vw2);
        }
    }
    LSTAMP(23);
    ASTORE();
}

DI void wo_load(const Ctx& c0, int l, const char* Wn, const char* Wo) {
    const Ctx c = fresh(c0);
    asm volatile("" : "+s"(Wn), "+s"(Wo));
    const u64 ep = lds_epoch(c.smem);
    const unsigned fo = o_find(c);
    if ((CP2 & 1) && l == 0 && c.wid == 4) code_again(c);
    if ((CP2 & 2) && l == 1 && c.wid == 4) code_again(c);
        if (l == 0 && c.wid == 6) {
            const int fr = c.lane & 15, od = fr & 1, j0 = 4 * (c.lane >> 4) + od;
            const bool p0 = j0 > fr, p1 = j0 + 2 > fr;
            const unsigned N = 0xff800000u, Z = 0x0c0c0c0cu, lo = od ? 0x01000c0cu : 0x0c0c0100u, hi = od ? 0x03020c0cu : 0x0c0c0302u;
            LAS u32x4* t = (LAS u32x4*)(c.smem + M_TAB + c.lane * 16);
            t[0] = (u32x4){p0 && !od ? 0u : N, p0 && od ? 0u : N, p1 && !od ? 0u : N, p1 && od ? 0u : N};
            t[64] = (u32x4){!p0 && !od ? 0u : N, !p0 && od ? 0u : N, !p1 && !od ? 0u : N, !p1 && od ? 0u : N};
            t[128] = (u32x4){p0 ? lo : Z, p1 ? hi : Z, p0 ? Z : lo, p1 ? Z : hi};
            t[192] = (u32x4){(unsigned)(M_RSTD + j0 * 4 + (p0 ? 0 : 64)), (unsigned)(M_RSTD + j0 * 4 + 8 + (p1 ? 0 : 64)), 0u, 0u};
        }
    if (CPY && l == 0 && c.wid == CKW) {
#pragma unroll 1
        for (int i = 0; i < 2; ++i) {
            const int j = i ? c.jw : c.jq, g = i ? 2 - 2 * (j >> 3) : 1 + 2 * (j >> 3);
            if ((j & 7) == 3) poll_flags(c.ws, ep, (unsigned)WS_FLAGQ + ((c.lane >> 2) * 16 + 4 * g + (c.lane & 3)) * FQS, true, 2u);
        }
    }
    Ask t, t2;
    if (!(l & 1)) {
        Ctx c2 = c;
        c2.wid = 2 * c.wid - 10;
        unsigned vw = wo_w_off(c2), vw2 = vw + (c.cw == 3 ? 1024u : 8u * D_ * 2);
        asm volatile("" : "+v"(vw), "+v"(vw2));
        poll_rmw(t, c.ws, fo, c.wid == 7 && c.lane < 16, ep + (4 * l + 3), 61 - 4 * l);
        if (c.wid == 7) STAMP_AT(32 + l * 8 + 2);
        BAR();
        if (c.wid == 5 || c.wid == 6) {
            __builtin_amdgcn_s_sleep(W30S);
            wo_w_issue<30, 32>(c2, Wo, vw);
            c2.wid += 1;
            wo_w_issue<30, 32>(c2, Wo, vw2);
        }
        if (c.wid != 7) WAIT_V0();
    } else {
        asm volatile("" : "=v"(t.v[0]), "=v"(t.v[1]), "=v"(t.v[2]), "=v"(t.v[3]));
        WAIT_V0();
    }
    poll_rmw(t2, c.ws, fo, c.wid == 7 && c.lane < 16, ep + (4 * l + 4), 60 - 4 * l);
    if (c.wid == 7) STAMP_AT(32 + l * 8 + 2 + !(l & 1));
    BAR();
    WAIT_V0();
    keep(t); keep(t2);
    if (l == 3 && c.bid == 0 && c.wid == 7) {
#pragma unroll 1
        for (int i = 0; i < 4; ++i) poll_flags(c.ws, ep, (unsigned)WS_FLAGQ + (i * 64 + c.lane) * FQS, true, 1u);
        if (c.lane == 0) __hip_atomic_store((GAS u64*)(c.ws + WS_EPOCH), ep + 64, __ATOMIC_RELAXED, __HIP_MEMORY_SCOPE_AGENT);
    }
    if (RS0 && l == 3) BAR();
}

DI void wo_consume(const bool B, const Ctx& c0, const bf16_t* xin, bf16_t* xout, int l) {
    u32x4 res[2][2];
    const Ctx c = fresh(c0);
    LAS char* smem = c.smem;
    const int lane = c.lane, w = c.wid, fr = lane & 15, fq = lane >> 4;
    const char* obase = c.ws + WS_O + (size_t)l * (8u << 20);
    int ovo = (c.pw * 8 + w * 2) * 1024 + lane * 16;
    asm volatile("" : "+v"(ovo));
    constexpr int NP = WNQ, R = 3;
    static_assert(!HO && !HX && !HS && !RLD && BPR == 1, "layout");
    auto ro = __builtin_amdgcn_make_buffer_rsrc((void*)obase, 0, 4u << 20, 0x00020000);
    asm volatile("" : "+s"(ro));
    u32x4 xa[NP][2];
    s16x4 wf[R][8];
    f32x4 acc[2][4];
    unsigned bw[8];
    int coff, rmo, spr;
    u32x4 on = {0x3f803f80u, 0x3f803f80u, 0x3f803f80u, 0x3f803f80u};
    unsigned sto, xpo, coff1, cnta = M_CNT, one = 1;
    u64 xv, b0, b1, am;
    auto rs = __builtin_amdgcn_make_buffer_rsrc(xout, 0, S_ * D_ * 2, 0x00020000);
    auto rst = __builtin_amdgcn_make_buffer_rsrc(c.ws + WS_STATS + (size_t)((l + 1) & 3) * 524288, 0, 524288, 0x00020000);
    auto prep2 = [&]() {
        sto = fq == (fr >> 2) && l < 3 ? (unsigned)((c.pw * 16 + c.jw) * 1024 + (w * 32 + fr) * 8) : 0x80000000u;
        xpo = x_post(c);
        coff = (int)pick(l == 3, (unsigned)rmo, (unsigned)coff);
        coff1 = coff + (l == 3 ? xrow<0>(0, 16) : 2048);
        spr = l == 3 ? 64 : 1024;
        asm volatile("" : "+s"(spr));
        b0 = __builtin_amdgcn_ballot_w64(fr & 1); b1 = __builtin_amdgcn_ballot_w64(fr & 2); am = __builtin_amdgcn_ballot_w64(x_act(c, !B));
        asm volatile("" : "+v"(on), "+v"(sto), "+v"(xpo), "+v"(coff), "+v"(coff1), "+v"(xv), "+v"(cnta), "+v"(one));
        asm volatile("" : "+s"(rs), "+s"(rst), "+s"(b0), "+s"(b1), "+s"(am));
    };
    auto prep = [&]() {
        int ln = lane;
        asm volatile("" : "+v"(ln));
        const int fr = ln & 15, fq = ln >> 4, q4 = fr >> 2, p4 = ln & 3;
        coff = (c.pw * 16 + c.jw) * 16384 + w * 4096 + (((fq & 1) * 2 + (fq >> 1)) * 16 + fr) * 16;
        rmo = xrow<0>(c.pw, w * 32 + fr) + xcol<0>(c.jw) + (fq & 1) * 32 + (fq >> 1) * 16;
        asm volatile("" : "+v"(coff), "+v"(rmo));
        const int fb = (q4 & 2) | ((fq & 1) << 2);
        const unsigned swp = (B && w >= 2) ? 2048u : 0u;
#pragma unroll
        for (int cc = 0; cc < 4; ++cc) {
            const unsigned b = (unsigned)((4 * fq + q4) * 128 + (((2 * cc + (p4 >> 1)) ^ fb) << 4) + (p4 & 1) * 8);
            bw[2 * cc] = b + swp; bw[2 * cc + 1] = b + (swp ^ 2048u);
            asm volatile("" : "+v"(bw[2 * cc]), "+v"(bw[2 * cc + 1]));
        }
    };
    auto start = [&](int mt, int pr) {
            {
                auto r0 = __builtin_amdgcn_permlane16_swap(res[mt][pr][0], res[mt][pr][2], false, false);
                auto r1 = __builtin_amdgcn_permlane16_swap(res[mt][pr][1], res[mt][pr][3], false, false);
#pragma unroll
                for (int h = 0; h < 2; ++h) {
                    acc[mt][2 * pr + h] = (f32x4){bf_lo(r0[h]), bf_hi(r0[h]), bf_lo(r1[h]), bf_hi(r1[h])};
                    asm volatile("" : "+v"(acc[mt][2 * pr + h]));
                }
            }
    };
    auto rdw = [&](auto ic) {
        constexpr int i = decltype(ic)::value, h = i >> 3, k = i & 7;
        wf[h % R][k] = tr_read(lds(bw[k]) + (h & 15) * 4096);
        if constexpr (h == 15) { bw[k] += 65536; asm volatile("" : "+v"(bw[k])); }
    };
    {
        auto resid = [&]() {
            const unsigned roff = pick(l != 0, (unsigned)coff, (unsigned)rmo);
            const char* xp = (const char*)xin;
#pragma unroll
            for (int i = 0; i < 4; ++i) aload4c(res[i >> 1][i & 1], roff, xp + (i >> 1) * (l ? 2048 : xrow<0>(0, 16)) + (i & 1) * (l ? 1024 : 64));
            SCHED();
        };
        unsigned opo = o_post(c), oca = B ? M_CNT : M_CNT + 8, old = 0, msk = B ? 3u : 7u;
        xv = lds_epoch(smem) + (unsigned)(l + 2);
        u64 ov = xv + (unsigned)(3 * l + 2);
        prep();
        asm volatile("" : "+v"(opo), "+v"(oca), "+v"(ov), "+v"(old), "+v"(one));
        asm volatile("" : "+s"(msk));
        SCHED();
        if (RBP) resid();
        static_for<0, 16>([&](auto ic) { rdw(ic); SCHED(); });
        if (!P2A) prep2();
        SCHED();
        wait_v<RBP ? 4 : 0>();
        if (lane == 0) asm volatile("ds_add_rtn_u32 %0, %1, %2\n\ts_waitcnt lgkmcnt(0)" : "+v"(old) : "v"(oca), "v"(one) : "memory");
        if ((__builtin_amdgcn_readfirstlane(old) & msk) == msk) { if (lane < 16) post(c.ws, opo, ov); }
        if (w == 0) STAMP_AT(l * 8 + 4);
        SCHED();
        if (!RBP) resid();
        if (P2A == 1) prep2();
        SCHED();
        if (w == 3) STAMP_AT(32 + l * 8 + 5);
        BAR();
    }
    FENCE();
    SCHED();
    if (w == 0) STAMP_AT(l * 8 + 5);
    int so = 0;
    auto next = [&](int j) { so += j == 15 ? 131072 - 15 * 262144 : 262144; asm volatile("" : "+s"(so)); };
    auto ldo = [&](int j, int mt) { xa[j % NP][mt] = __builtin_amdgcn_raw_buffer_load_b128(ro, ovo + mt * 1024, so, 16); };
#pragma unroll
    for (int h = 0; h < NP; ++h) {
        ldo(h, 0);
        SCHED();
        if (h > SH0 && h <= SH0 + 2) { start(h - SH0 - 1, 0); SCHED(); }
        ldo(h, 1);
        if (!WAL || h + 1 < NP) next(h);
        SCHED();
        if (P2A >= 2 && h == P2A - 2) { prep2(); SCHED(); }
        if (h == SH0) {
            wait_v<2 * SH0 + 2>();
            asm volatile("" : "+v"(res[0][0]), "+v"(res[0][1]), "+v"(res[1][0]), "+v"(res[1][1]));
        }
        if (h > SH0 && h <= SH0 + 2) start(h - SH0 - 1, 1);
        SCHED();
    }
    int d0 = 262144, d1 = 131072 - 15 * 262144;
    asm volatile("" : "+s"(d0), "+s"(d1));
    static_assert(WEP || !WAL, "last step");
    static_for<0, WEP ? 31 : 32>([&](auto jc) {
        constexpr int j = decltype(jc)::value;
        constexpr int c0 = j < 30 ? 16 + 8 * j : 256, c1 = j < 30 ? c0 + 8 : 256;
        if constexpr (j + NP == 16) {
            static_assert(c0 <= 8 * 20, "weights that landed before the attention");
            if (!B) BAR();
            FENCE();
            asm volatile("" : "+v"(ovo));
            SCHED();
        }
        if constexpr (j == 0 || j + NP == 16) ALIGN8();
        constexpr int nv = WAL == 1 || j == 0 || j + NP > 32 ? 2 * (NP - 1 < 31 - j ? NP - 1 : 31 - j) : 2 * NP - 3;
        __builtin_amdgcn_s_waitcnt(0x0870 | nv);
        const int sp = so;
        static_assert(!RS0 || (RS0 + NP >= 32 && RS0 < 31 && QW_SLOT <= (RS0 - 1) * 4096), "R0");
        if constexpr (RS0 && j == RS0) BAR();
        else asm volatile("s_add_i32 %0, %1, %2" : "=&s"(so) : "s"(sp), "s"(j + NP == 16 ? d1 : d0) : "scc");
        SCHED();
        static_for<0, 8>([&](auto mc) {
            constexpr int m = decltype(mc)::value, mt = m >> 2, nt = mt ? 3 - (m & 3) : m & 3;
            const bf16x8 wv = __builtin_shufflevector(wf[j % R][2 * nt], wf[j % R][2 * nt + 1], 0, 1, 2, 3, 4, 5, 6, 7);
            if constexpr (j < 31) asm volatile("v_mfma_f32_16x16x32_bf16 %0, %1, %2, %0" : "+v"(acc[mt][nt]) : "v"(wv), "v"(xa[j % NP][mt]));
            else acc[mt][nt] = MFMA(wv, __builtin_bit_cast(bf16x8, xa[j % NP][mt]), acc[mt][nt]);
            SCHED();
            if constexpr (c0 < c1 && m < 7) { rdw(std::integral_constant<int, c0 + m>{}); SCHED(); }
            if constexpr (c0 < c1 && m == 6) rdw(std::integral_constant<int, c0 + 7>{});
            if constexpr (m == WL1 && j > 0 && j - 1 + NP < 32) xa[(j + NP - 1) % NP][1] = __builtin_amdgcn_raw_buffer_load_b128(ro, ovo + 1024, sp, 16);
            if constexpr (m == WL1 + 4 && j + NP < 32) ldo(j, 0);
            SCHED();
        });
    });
    const bf16x8 ones = __builtin_bit_cast(bf16x8, on);
    u32x4 o[2][2];
    f32x4 sm[2], g[2];
    const f32x4 zero = {0.f, 0.f, 0.f, 0.f};
    auto sel = [&](float a, float b, u64 m) { return __builtin_amdgcn_inverse_ballot_w64(m) ? b : a; };
    auto stats = [&](int mt) {
        float qa = sel(g[mt][0], g[mt][1], b0), qb = sel(g[mt][2], g[mt][3], b0);
        asm volatile("" : "+v"(qa), "+v"(qb));
        SCHED();
        if (WEP) asm volatile("" :: "v"(sm[1][1]), "v"(sm[1][2]), "v"(sm[1][3]), "v"(o[1][0]), "v"(o[1][1]), "v"(on));
        __builtin_amdgcn_raw_buffer_store_b64((u32x2){__float_as_uint(sm[mt][0]), __float_as_uint(sel(qa, qb, b1))}, rst, sto + mt * 128, 0, 16);
        SCHED();
    };
    u32x2 pk[2];
    auto last = [&](int mt, int nt) {
        acc[mt][nt] = MFMA(__builtin_shufflevector(wf[31 % R][2 * nt], wf[31 % R][2 * nt + 1], 0, 1, 2, 3, 4, 5, 6, 7), __builtin_bit_cast(bf16x8, xa[31 % NP][mt]), acc[mt][nt]);
        SCHED();
    };
    auto cv = [&](int mt, int pr) {
        pk[0] = pack4(acc[mt][2 * pr]); pk[1] = pack4(acc[mt][2 * pr + 1]);
        asm volatile("" : "+v"(pk[0]), "+v"(pk[1]));
        SCHED();
    };
    auto st = [&](int mt, int pr) {
        o[mt][pr] = widen(pk[0], pk[1]);
        __builtin_amdgcn_raw_buffer_store_b128(o[mt][pr], rs, mt ? coff1 : coff, pr ? spr : 0, 16);
        SCHED();
    };
    auto gq = [&](int mt, int pr, bool f) { const bf16x8 of = __builtin_bit_cast(bf16x8, o[mt][pr]); g[mt] = MFMA(of, of, f ? zero : g[mt]); SCHED(); };
    auto gs = [&](int mt, int pr, bool f) { sm[mt] = MFMA(ones, __builtin_bit_cast(bf16x8, o[mt][pr]), f ? zero : sm[mt]); SCHED(); };
    WAIT_L0();
    last(0, 0); last(0, 1); last(0, 2); last(0, 3);
    last(1, 3); cv(0, 0);
    last(1, 2); st(0, 0);
    last(1, 1); cv(0, 1);
    last(1, 0); st(0, 1);
    if (w == 0) STAMP_AT(l * 8 + 6);
    gq(0, 0, true); cv(1, 1);
    gs(0, 0, true); st(1, 1);
    gq(0, 1, false); cv(1, 0);
    gs(0, 1, false); st(1, 0);
    gq(1, 1, true);
    gs(1, 1, true);
    gq(1, 0, false);
    gs(1, 0, false);
    stats(0);
    stats(1);
    if (l < 3) {
        unsigned old = 0;
        if (!RS0 || RN1 > RN0) BAR();
        wait_v<XE2>();
        if (lane == 0) asm volatile("ds_add_rtn_u32 %0, %1, %2\n\ts_waitcnt lgkmcnt(0)" : "+v"(old) : "v"(cnta), "v"(one) : "memory");
        if ((__builtin_amdgcn_readfirstlane(old) & 3) == 3) {
            if (__builtin_amdgcn_inverse_ballot_w64(am)) post(c.ws, xpo, xv);
        }
    }
    if (w == 0) STAMP_AT(l * 8 + 7);
}

__global__ __launch_bounds__(512, 2) __attribute__((aligned(4096))) void k_main(Params P) {
    extern __shared__ __attribute__((aligned(16))) char smem_[];
    KP kp = (KP)__builtin_amdgcn_kernarg_segment_ptr();
    const char *x0, *w0, *wo;
    Ctx c;
    asm volatile("s_load_dwordx2 %0, %4, 0x0\n\ts_load_dwordx2 %1, %4, 0x8\n\ts_load_dwordx2 %2, %4, 0x10\n\ts_load_dwordx2 %3, %4, 0x20" : "=&s"(x0), "=&s"(w0), "=&s"(c.ws), "=&s"(wo) : "s"(kp));
    c.smem = (LAS char*)smem_;
    c.lane = threadIdx.x & 63;
    c.wid = __builtin_amdgcn_readfirstlane(threadIdx.x >> 6);
    c.bid = blockIdx.x;
    const int xcd = c.bid & 7, idx = c.bid >> 3;
    c.pq = ((xcd % (16 / QXM)) * QXM + (idx % QXM)) ^ PQX; c.jq = ((xcd / (16 / QXM)) * (32 / QXM) + (idx / QXM)) ^ JQX;
    const bool loader = c.wid >= 4;
    if (CPF == 1 ? !loader : c.wid == 4) {
        const char* pc; int len;
        asm volatile("s_getpc_b64 %0\n.Lcpf_a:\n\ts_mov_b32 %1, .Lcpf_end-.Lcpf_a-4" : "=s"(pc), "=s"(len));
#pragma unroll
        for (int i = 0; i < (CPF == 1 ? 2 : 1); ++i) {
            const int o = min((CPF == 1 ? (i * 4 + c.wid) * 64 + c.lane : (CPF == 2 ? (idx & 7) * 64 + c.lane : idx * 16 + (c.lane & 15))) * 128, len);
            asm volatile("s_mov_b32 m0, %0\n\ts_nop 3\n\tglobal_load_lds_dword %1, %2" ::"s"(M_TCH), "v"(o), "s"(pc) : "memory");
        }
    }
    unsigned va, vw;
    {
        const int w = c.wid, lr = c.lane >> 3, lc = c.lane & 7;
        int t0 = first_tile(first_order(c.pq, c.jq), 0);
        asm volatile("" : "+s"(t0));
        va = (unsigned)((c.pq * 128 + 8 * (w & 3) + lr) * (D_ * 2) + ((lc ^ lr) << 4));
        vw = (unsigned)((8 * (w & 3) + lr) * 6144 + c.jq * 128 + ((lc ^ ((lr & 2) | ((w & 1) << 2))) << 4));
        asm volatile("" : "+v"(va), "+v"(vw));
        asm volatile("s_waitcnt lgkmcnt(0)" : "+s"(x0), "+s"(w0), "+s"(c.ws), "+s"(wo));
        if (ST0 && c.wid == 3) STAMP_AT(41);
        auto stage = [&](int s, int h0, int h1, bool a) {
            const char* xb = x0 + (unsigned)(t0 * 128);
            const char* wb = w0 + (unsigned)(t0 * (64 * 6144));
            for (int h = h0; h <= h1; ++h) {
                if (a) {
#pragma unroll
                    for (int j = 0; j < 2; ++j) dma16<false>(QA_OFF + s * QA_SLOT + (j * 8 + h * 4 + (w & 3)) * 1024, va, xb + (j * 64 + h * 32) * (D_ * 2));
                }
#pragma unroll
                for (int j = 0; j < 3; ++j) dma16<false>(qw_slot(s) + (j * 8 + h * 4 + (w & 3)) * 1024, vw, wb + h * (32 * 6144) + j * 2048);
            }
        };
        if (ENT == 0) stage(0, w >> 2, w >> 2, true);
        else if (!loader) stage(0, 0, 1, true);
        if (ST0 && c.wid == 1) STAMP_AT(43);
        if (w == 0 && c.lane < 2) touch(M_EP, (unsigned)(WS_EPOCH + c.lane * 4), c.ws);
        if (w == 1 && c.lane < 5) *(LAS unsigned*)(c.smem + (c.lane < 4 ? M_CNT + c.lane * 4 : M_GO)) = 0u;
    }
    asm volatile("" : "+s"(kp), "+s"(c.bid), "+s"(c.ws));
    static_assert(WXM == 4 && ALT == 1, "map");
    c.pw = ((c.pq & 8) ^ 8) + ((c.bid >> 1) & 1) * 4 + ((c.bid >> 3) & 3); c.jw = (c.bid & 4) * 2 + (c.bid >> 5); c.cw = 0;
    if (PADN >= 0) asm volatile("s_branch .Lpad_end\n\ts_nop 0\n\t.fill %c0, 4, 0xbf800000\n.Lpad_end:" :: "i"(PADN * 64));
    const char* xi = x0;
    auto step = [&](int l) {
        asm volatile("" : "+s"(kp), "+s"(c.ws), "+s"(c.jw), "+s"(c.bid));
        c.pq ^= 8; c.pw ^= 8; c.cw = CPY ? c.jw & 7 : 0;
        xi = c.ws + WS_X + (unsigned)(l * (8u << 20));
        wo = (const char*)((l & 1) ? kp->wo0 : kp->wo1);
        if (c.cw == 3) wo = c.ws + WS_CO + (((l & 1) ? 2 : 0) + (c.jw >> 3)) * 4096;
    };
    if (loader) {
            qkv_load<false, true>(c, xi, w0, wo, 0, kp, va, vw);
#pragma unroll 1
        for (int l = 0;;) {
            asm volatile("" : "+s"(l));
            if (!(l & 1)) attn_a<true>(c, l, wo, kp);
            wo_load(c, l, nullptr, wo);
            if (l == 3) break;
            step(l);
            ++l;
            if (l == 2) { qkv_load<false, false>(c, xi, (const char*)kp->wqkv0, wo, l); }
            else { qkv_load<true, false>(c, xi, (const char*)kp->wqkv1, wo, l); }
        }
    } else {
        bf16_t* xo = (bf16_t*)(c.ws + WS_X);
#pragma unroll 1
        for (int l = 0; l < 4; ++l) {
            if (l & 1) { qkv_consume<true>(c, l, kp); }
            else {
                qkv_consume<false>(c, l, kp, va, vw);
                attn_a<false>(c, l, wo, kp);
            }
            wo_consume(l & 1, c, (const bf16_t*)xi, xo, l);
            step(l);
            xo = l == 2 ? kp->out : (bf16_t*)(c.ws + WS_X + (unsigned)((l + 1) * (8u << 20)));
            asm volatile("" : "=v"(va), "=v"(vw));
        }
    }
    asm volatile(".Lcpf_end:");
}

extern "C" void kernel_launch(void* const* d_in, const int* in_sizes, int n_in, void* d_out, int out_size, void* d_ws, size_t ws_size,
                              hipStream_t stream) {
    Params P;
    P.x0 = (const bf16_t*)d_in[0];
    P.wqkv0 = (const bf16_t*)d_in[1];
    P.wo0 = (const bf16_t*)d_in[2];
    P.wqkv1 = (const bf16_t*)d_in[4];
    P.wo1 = (const bf16_t*)d_in[5];
    P.out = (bf16_t*)d_out;
    P.ws = (char*)d_ws;
    (void)hipFuncSetAttribute((const void*)k_main, hipFuncAttributeMaxDynamicSharedMemorySize, LDS_TOTAL);
    k_main<<<256, 512, LDS_TOTAL, stream>>>(P);
}
```

```cpp
#include <hip/hip_runtime.h>
#include <stdint.h>
#include <type_traits>

typedef unsigned short bf16_t;
typedef short bf16x8 __attribute__((ext_vector_type(8)));
typedef short s16x4 __attribute__((ext_vector_type(4)));
typedef float f32x4 __attribute__((ext_vector_type(4)));
typedef float f32x2 __attribute__((ext_vector_type(2)));
typedef float f32x16 __attribute__((ext_vector_type(16)));
typedef unsigned u32x4 __attribute__((ext_vector_type(4)));
typedef unsigned u32x2 __attribute__((ext_vector_type(2)));
#define DI __device__ __forceinline__
#define LAS __attribute__((address_space(3)))
#define GAS __attribute__((address_space(1)))
#define STAMP 3
#define SA 63
#define SB 63
#define SM 1
#define SX 100

constexpr int S_ = 2048, D_ = 1024, KT = 16;
constexpr float LN_EPS = 1e-5f;
constexpr float QSCALE = 0.17677669529663687f * 1.4426950408889634f;

#define FQO 0
#define XBO 524288
#define HX 0
#define HO 0
#define HK 0
#define HS 0
#define HM 1
constexpr size_t WS_FLAGQ = FQO;
constexpr size_t WS_FLAGW = 98304;
constexpr size_t WS_EPOCH = 65536;
constexpr size_t WS_CSUM = 131072;
constexpr size_t WS_XBOX = XBO;
constexpr size_t WS_OBOX = 655360;
constexpr size_t WS_KBOX = 720896;
constexpr size_t WS_STATS = 1u << 20;
constexpr size_t WS_X = 4u << 20;
constexpr size_t WS_O = 96u << 20;
constexpr size_t WS_KV = 28u << 20;
constexpr size_t WS_CQ = 36u << 20;
constexpr size_t WS_CO = 42u << 20;
constexpr size_t WS_STAMP = 64u << 20;

template <int H> DI constexpr int hp(int c) { return !H ? c * 1024 : (c >> 2) * 8192 + (HM == 1 ? (c & 3) * 256 : (c & 1) * 128 + (c & 2) * 256); }
template <int H> DI constexpr int hl(int u) { return !H ? u : (HM == 1 ? u + (u & ~127) : u + (u & ~255)); }
template <int H> DI constexpr int hv(int lane) { return !H ? lane * 16 : (lane >> 3) * 1024 + (lane & 7) * 16; }
template <int H> DI constexpr int xrow(int p, int r) { return H == 0 ? (p * 128 + r) * 2048 : (H == 1 ? (p * 128 + r) * 4096 : (H == 2 ? p * 524288 + r * 256 : p * 262144 + r * 128)); }
template <int H> DI constexpr int xcol(int t) { return H == 0 ? t * 128 : (H == 1 ? hl<1>(t * 128) : (H == 2 ? t * 32768 : t * 16384)); }
#define WTC 2
#define WT0 1
#define SPR 1
#define SL 4
#define R4X 0
#define R5X 0
#define TOPJ 0x2000
#define WV0 1
#define C2X 13
#define FQS 256
#define SNK 1
#define WBE 1
#define WAE 1
#define EXF 0
#define RW0 6
#define KSP 0
#define K0E 1
#define TOPS 4
#define BPR 1
#define RLD 0
#define RCK 1
#define CPF 2
#define WN1 20
#define WLO 3
#define FPJ 2
#define WPJ 2
#define WB14 0
#define WB15 5
#define RESP 1
#define WT13 2
#define RPOS 2
#define CPOLL 1
#define ENT 1
#define MBOX 3
#define OBS 256
#define WTCH 0
#define WTDA 0
#define WTDB 4
#define WTG 0
#define PQX 8
#define ALT 1
#define RESA 1
#define CKP 1
#define ALN 1
#define EPH 2
#define NQA_ 3
#define NQW_ 4
#define WNP 8
#define LPRIO 3
#define TOUCH 1
#define TSTEP 13
#define XS_ 9
#define WROT 0
#define WROTD 1
#define JQX 0
#define ROTM 0
#define ROT0 0
#define WPRE 3
#define PSLEEP 1
#define XPRE 2
#define WE0 1
#define EPJ 3
#define TW0 5
#define TW1 99
#define KPJ 1
#define PKSUB 0
#define APRIO 1
#define QIL 0
#define QXM 8
#define WNQ 5
#define WRG 2
#define WMG 1
#define WB2 1
#define WBS 1
#define WEP 1
#define WAL 2
#define WL1 0
#define RS0 27
#define RN0 6
#define RN1 6
#define AH0 0
#define RSL 1
#define WXM 4
#define CPOS 5
#define LSL 0
#define SAFE_SUM 0x1p80f
#define AB2 0
#define W30S 8
#define STC 0
#define NOR 1
#define NRH 0
#define CPY 1
#define AGG 1
#define HT0 0
#define DEF 0
#define XST 2
#define CKW 4
#define XSP 1
#define EPOLL 1
#define ORD 2
#define RSLP 1
#define APOLL 1
#define APOST 1
#define ASLP 2
#define APIPE 4
#define RBP 0
#define P2A 1
#define SH0 2
#define SL1 0
#define KPW 4
#define LWA 7
#define WA0 12
#define WA1 22
#define KPP 1
#define HP4 1
#define TWQ 1
#define CP2 0
#define PADN -1
#define ERL 1
#define ESL 0
#define DM 0
#define DS 0
#define ELO 0
#define EN0 3
#define EN1 2
#define ST0 0
#define NC 1
#define NCM 1
#define NCB 4
#define AD1 3
#define AD2 0
#define AD3 4
#define WB0 3
#define WBN 6
#define TWO 1
#define XSB 10
#define TL0 0
#define TL1 12
#define FDM 1
#define TSP 0
#define CPW 4
#define XE1 0
#define XE2 0
#define OTB 0
#define OTJ 8
#define OTA 0
constexpr int QA_SLOT = 18432, NQA = NQA_, QW_SLOT = 24576, NQW = NQW_, QA_OFF = 3 * QW_SLOT, QW_TOP = 131072;
DI constexpr int qw_slot(int s) { return s == 3 ? QW_TOP : s * QW_SLOT; }
constexpr int AT_N = 90112, AT_OWN = AT_N + 32768, AT_Q = 73728;
constexpr int MISC = 155648, M_MEAN = MISC, M_RSTD = MISC + 640, M_CS = MISC + 1280, M_CNT = MISC + 2816, M_EP = MISC + 2832, M_GO = MISC + 2848, M_TCH = MISC + 3072, M_TAB = MISC + 4096, M_XCH = 131072;
constexpr int LDS_TOTAL = 163840;
static_assert(QA_OFF + NQA * QA_SLOT <= QW_TOP && QW_TOP + QW_SLOT <= MISC && AT_N + 65536 <= MISC && AT_Q + 16384 <= AT_N, "lds");

DI unsigned cvt_pk_bf16(float lo, float hi) {
    typedef __bf16 bf2 __attribute__((ext_vector_type(2)));
    bf2 r; r[0] = (__bf16)lo; r[1] = (__bf16)hi;
    return __builtin_bit_cast(unsigned, r);
}
DI u32x2 pack4(const f32x4& v) { return (u32x2){cvt_pk_bf16(v[0], v[1]), cvt_pk_bf16(v[2], v[3])}; }
DI bf16x8 join(const u32x2& a, const u32x2& b) { return __builtin_bit_cast(bf16x8, (u32x4){a[0], a[1], b[0], b[1]}); }
DI float bf_lo(unsigned u) { return __uint_as_float(u << 16); }
DI float bf_hi(unsigned u) { return __uint_as_float(u & 0xffff0000u); }
DI float red_max4(float x) {
    auto a = __builtin_amdgcn_permlane16_swap(__float_as_uint(x), __float_as_uint(x), false, false);
    x = fmaxf(__uint_as_float(a[0]), __uint_as_float(a[1]));
    auto b = __builtin_amdgcn_permlane32_swap(__float_as_uint(x), __float_as_uint(x), false, false);
    return fmaxf(__uint_as_float(b[0]), __uint_as_float(b[1]));
}
DI float red_sum4(float x) {
    auto a = __builtin_amdgcn_permlane16_swap(__float_as_uint(x), __float_as_uint(x), false, false);
    x = __uint_as_float(a[0]) + __uint_as_float(a[1]);
    auto b = __builtin_amdgcn_permlane32_swap(__float_as_uint(x), __float_as_uint(x), false, false);
    return __uint_as_float(b[0]) + __uint_as_float(b[1]);
}
DI u32x4 widen(const u32x2& a, const u32x2& b) {
    auto r0 = __builtin_amdgcn_permlane16_swap(a[0], b[0], false, false);
    auto r1 = __builtin_amdgcn_permlane16_swap(a[1], b[1], false, false);
    return (u32x4){r0[0], r1[0], r0[1], r1[1]};
}
#define MFMA(a, b, c) __builtin_amdgcn_mfma_f32_16x16x32_bf16((a), (b), (c), 0, 0, 0)
#define WAIT_L0() __builtin_amdgcn_s_waitcnt(0xC07F)
#define WAIT_V0() asm volatile("s_waitcnt vmcnt(0)" ::: "memory")
#define BAR() __builtin_amdgcn_s_barrier()
#define SCHED() __builtin_amdgcn_sched_barrier(0)
template <int N> DI void wait_v() { asm volatile("s_waitcnt vmcnt(%0)" ::"i"(N) : "memory"); }
template <int I, int N, class F> DI void static_for(F&& f) { if constexpr (I < N) { f(std::integral_constant<int, I>{}); static_for<I + 1, N>(f); } }

template <bool SC1> DI void dma16(unsigned lds, unsigned voff, const char* sbase) {
    if (SC1) asm volatile("s_mov_b32 m0, %0\n\ts_nop 3\n\tglobal_load_lds_dwordx4 %1, %2 sc1" ::"s"(lds), "v"(voff), "s"(sbase) : "memory");
    else asm volatile("s_mov_b32 m0, %0\n\ts_nop 3\n\tglobal_load_lds_dwordx4 %1, %2" ::"s"(lds), "v"(voff), "s"(sbase) : "memory");
}
DI void touch(unsigned lds, unsigned voff, const char* sbase) {
    asm volatile("s_mov_b32 m0, %0\n\ts_nop 3\n\tglobal_load_lds_dword %1, %2 sc1" ::"s"(lds), "v"(voff), "s"(sbase) : "memory");
}
DI s16x4 tr_read(const LAS char* p) {
    typedef short v4i16_t __attribute__((ext_vector_type(4)));
    return __builtin_bit_cast(s16x4, __builtin_amdgcn_ds_read_tr16_b64_v4i16((LAS v4i16_t*)p));
}
DI LAS char* lds(unsigned off) { return (LAS char*)(uintptr_t)off; }
DI unsigned long long rstamp() { unsigned long long t; asm volatile("s_memrealtime %0\n\ts_waitcnt lgkmcnt(0)" : "=s"(t)::"memory"); return t; }

template <int NAI, int NWI, bool FIRST> struct Sched {
    int w[KT];
    constexpr Sched() : w{} {
        int endA[KT] = {}, endW[KT] = {};
        int n = 0;
        if (FIRST) {
            if (ENT == 0) { n += 2; endA[0] = n; n += 3; endW[0] = n; }
            n += NAI; endA[1] = n; n += NWI; endW[1] = n;
            if (ERL >= 5) { n += NAI; endA[2] = n; }
            if (ERL == 2 || ERL == 3 || ERL == 5) { n += NWI; endW[2] = n; }
        } else {
            for (int s = 0; s < WPRE; ++s) { n += NWI; endW[s] = n; }
            for (int s = 0; s < 2; ++s) { n += NAI; endA[s] = n; }
        }
        for (int kt = 0; kt < KT; ++kt) {
            const int need = endA[kt] > endW[kt] ? endA[kt] : endW[kt];
            w[kt] = (n - need) > 63 ? 63 : (n - need);
            if (kt + 2 < KT && !(FIRST && ERL >= 5 && kt == 0)) { n += NAI; endA[kt + 2] = n; }
            if (FIRST && (ERL == 4 || ERL == 6) && kt == 0) { n += NWI; endW[2] = n; }
            if (FIRST && ERL < 2 && kt < 5) {
                if (kt < 4) { n += NWI; endW[kt + 2] = n; }
                if (kt >= 3) n += NWI / 2;
                if (kt == 4) { endW[6] = n; n += NWI; endW[7] = n; }
                continue;
            }
            if (!FIRST && WPRE == 2 && kt < 2) { n += NWI; endW[kt + 2] = n; }
            if ((FIRST || WPRE == 3) && kt == 0) { n += NWI; endW[3] = n; }
            if (kt >= 1 && kt + 3 < KT) { n += NWI; endW[kt + 3] = n; }
            if (kt == 13) n += WT13;
            if (kt == 14) n += (NAI == 5 || WA0 > 12 ? 12 : WA0) - WT13 + (NAI == 5 ? WB14 : 0);
        }
    }
};

struct Params {
    const bf16_t* x0;
    const bf16_t* wqkv0;
    char* ws;
    const bf16_t* wqkv1;
    const bf16_t* wo0;
    const bf16_t* wo1;
    bf16_t* out;
};
typedef const __attribute__((address_space(4))) Params* KP;
DI int rot_first(int pq, int jq) {
    const int pr = 2 * (pq & 7) + (pq >> 3);
    if (ROT0 == 6) return 0;
    if (ROT0 == 7) return (pq & 1) * 8;
    if (ROT0 == 8) return (pq & 3) * 4;
    if (ROT0 == 9) return pq;
    if (ROT0 == 4) return (pq + (jq ^ 3)) & 15;
    if (ROT0 == 5) return (pr + (jq ^ 3)) & 15;
    return (ROT0 == 0 ? pq + jq : (ROT0 == 1 ? pr + jq : (ROT0 == 2 ? pr + 4 * (jq & 3) + (jq >> 2) : pq + 4 * (jq & 3) + (jq >> 2)))) & 15;
}

struct Order { int base, r, re; };
DI Order first_order(int pq, int jq) { Order o; o.base = (pq & NCM) * (16 / (NCM + 1)) + NCB; o.r = (rot_first(pq, jq) - o.base) & 15; o.re = o.r > NC ? o.r : NC; return o; }
DI int first_tile(const Order& o, int s) {
    const int v = o.re + s - NC;
    if (s < NC) { int a = NC + o.base, b = s + o.base; if (NC - s == 1) asm volatile("" : "+s"(a)); return (o.r == s ? a : b) & 15; }
    return ((s == NC ? o.r : (v > 15 ? v - (16 - NC) : v)) + o.base) & 15;
}

typedef unsigned long long u64;
DI u64 lds_epoch(LAS char* smem) { return *(const LAS u64*)(smem + M_EP); }
DI bool reached(u64 v, u64 ep, unsigned target) { return (v - ep - target) < (u64)(64u - target); }
DI void poll_flags(const char* ws, u64 ep, unsigned off, bool active, unsigned target) {
    const GAS u64* f = (const GAS u64*)(ws + off);
#pragma unroll 1
    for (unsigned spins = 0; spins < (1u << 22); ++spins) {
        const u64 v = __hip_atomic_load(f, __ATOMIC_RELAXED, __HIP_MEMORY_SCOPE_AGENT);
        if (__all(!active || reached(v, ep, target))) break;
        __builtin_amdgcn_s_sleep(PSLEEP);
    }
    asm volatile("" ::: "memory");
}
struct Ask { u64 v[4]; };
DI void keep(Ask& t) { asm volatile("" : "+v"(t.v[0]), "+v"(t.v[1]), "+v"(t.v[2]), "+v"(t.v[3])); }
#define ASK(r) "global_atomic_or_x2 " r ", %[off], %[z], %[ws] sc0 sc1\n\t"
#define CHK(r) "s_waitcnt vmcnt(%[n])\n\tv_lshl_add_u64 %[d], " r ", 0, %[nb]\n\tv_cmp_gt_u64 vcc, %[span], %[d]\n\ts_cmp_eq_u64 vcc, exec\n\ts_cbranch_scc1 .Lpe%=\n\ts_sleep %[sl]\n\t"
template <bool NEG = false> DI void poll_rmw(Ask& t, const char* ws, unsigned off, bool act, u64 base, unsigned span) {
    u64 z, d, nb = NEG ? base : 0 - base, sp = span;
    unsigned cnt;
    asm volatile("" : "=v"(t.v[0]), "=v"(t.v[1]), "=v"(t.v[2]), "=v"(t.v[3]));
    if (act) {
        if (APIPE == 4)
            asm volatile("v_mov_b64 %[z], 0\n\ts_mov_b32 %[cnt], 0x80000\n\ts_nop 3\n\t" ASK("%[a]") "s_sleep %[sl]\n\t" ASK("%[b]") "s_sleep %[sl]\n\t" ASK("%[c]") "s_sleep %[sl]\n"
                         ".Lpl%=:\n\t" ASK("%[e]") CHK("%[a]") ASK("%[a]") CHK("%[b]") ASK("%[b]") CHK("%[c]") ASK("%[c]") CHK("%[e]")
                         "s_sub_u32 %[cnt], %[cnt], 1\n\ts_cbranch_scc0 .Lpl%=\n.Lpe%=:"
                         : [a] "+v"(t.v[0]), [b] "+v"(t.v[1]), [c] "+v"(t.v[2]), [e] "+v"(t.v[3]), [d] "=&v"(d), [z] "=&v"(z), [cnt] "=&s"(cnt)
                         : [off] "v"(off), [ws] "s"(ws), [nb] "v"(nb), [span] "s"(sp), [sl] "i"(ASLP), [n] "i"(3) : "vcc", "scc", "memory");
        else if (APIPE == 2)
            asm volatile("v_mov_b64 %[z], 0\n\ts_mov_b32 %[cnt], 0x100000\n\ts_nop 3\n\t" ASK("%[a]") "s_sleep %[sl]\n"
                         ".Lpl%=:\n\t" ASK("%[b]") CHK("%[a]") ASK("%[a]") CHK("%[b]")
                         "s_sub_u32 %[cnt], %[cnt], 1\n\ts_cbranch_scc0 .Lpl%=\n.Lpe%=:"
                         : [a] "+v"(t.v[0]), [b] "+v"(t.v[1]), [d] "=&v"(d), [z] "=&v"(z), [cnt] "=&s"(cnt)
                         : [off] "v"(off), [ws] "s"(ws), [nb] "v"(nb), [span] "s"(sp), [sl] "i"(ASLP), [n] "i"(1) : "vcc", "scc", "memory");
        else
            asm volatile("v_mov_b64 %[z], 0\n\ts_mov_b32 %[cnt], 0x200000\n\ts_nop 3\n"
                         ".Lpl%=:\n\t" ASK("%[a]") CHK("%[a]")
                         "s_sub_u32 %[cnt], %[cnt], 1\n\ts_cbranch_scc0 .Lpl%=\n.Lpe%=:"
                         : [a] "+v"(t.v[0]), [d] "=&v"(d), [z] "=&v"(z), [cnt] "=&s"(cnt)
                         : [off] "v"(off), [ws] "s"(ws), [nb] "v"(nb), [span] "s"(sp), [sl] "i"(ASLP), [n] "i"(0) : "vcc", "scc", "memory");
    }
}
DI void post(char* ws, unsigned off, u64 v) {
    if (APOST) asm volatile("s_nop 4\n\tglobal_atomic_swap_x2 %0, %1, %2 sc1" :: "v"(off), "v"(v), "s"(ws) : "memory");
    else __hip_atomic_store((GAS u64*)(ws + off), v, __ATOMIC_RELAXED, __HIP_MEMORY_SCOPE_AGENT);
}
template <int NW, int CNT, int NV = 0> DI void publish(LAS char* smem, int lane, char* ws, unsigned off, bool act, unsigned val) {
    const u64 ep = lds_epoch(smem) + val;
    unsigned old = 0;
    asm volatile("" :: "v"(ep), "v"(off));
    wait_v<NV>();
    if (lane == 0) asm volatile("ds_add_rtn_u32 %0, %1, %2\n\ts_waitcnt lgkmcnt(0)" : "+v"(old) : "v"(M_CNT + CNT * 4), "v"(1u) : "memory");
    if ((__builtin_amdgcn_readfirstlane(old) & (NW - 1)) == NW - 1) {
        if (act) post(ws, off, ep);
    }
}
typedef __bf16 bf16x2_t __attribute__((ext_vector_type(2)));
DI float dot2(unsigned a, unsigned b, float c) { return __builtin_amdgcn_fdot2_f32_bf16(__builtin_bit_cast(bf16x2_t, a), __builtin_bit_cast(bf16x2_t, b), c, false); }
DI u32x2 tile_stats(const u32x4& a, const u32x4& b) {
    float s = 0.f, q = 0.f;
#pragma unroll
    for (int j = 0; j < 4; ++j) { s = dot2(a[j], 0x3f803f80u, s); q = dot2(a[j], a[j], q); }
#pragma unroll
    for (int j = 0; j < 4; ++j) { s = dot2(b[j], 0x3f803f80u, s); q = dot2(b[j], b[j], q); }
    return (u32x2){__float_as_uint(red_sum4(s)), __float_as_uint(red_sum4(q))};
}

struct Ctx {
    LAS char* smem;
    char* ws;
    int lane, wid;
    int pq, jq;
    int pw, jw;
    int bid;
    int cw;
};
DI Ctx fresh(const Ctx& c0) {
    Ctx c = c0;
    asm volatile("" : "+v"(c.lane));
    asm volatile("" : "+s"(c.wid), "+s"(c.pq), "+s"(c.jq), "+s"(c.pw), "+s"(c.jw), "+s"(c.ws), "+s"(c.cw));
    return c;
}
DI unsigned o_post(const Ctx& c) { return (MBOX & 2) ? (unsigned)WS_OBOX + (c.pq * 16 + (c.lane & 15)) * OBS + c.jq * 8 : (unsigned)WS_FLAGQ + (c.pq * 16 + c.jq) * FQS; }
DI bool o_act(const Ctx& c) { return c.lane < ((MBOX & 2) ? 16 : 1); }
DI unsigned o_find(const Ctx& c) { return (MBOX & 2) ? (unsigned)WS_OBOX + (c.pw * 16 + c.jw) * OBS + (c.lane & 15) * 8 : (unsigned)WS_FLAGQ + (c.pw * 16 + (c.lane & 15)) * FQS; }
DI unsigned x_post(const Ctx& c) { return (MBOX & 1) ? (unsigned)WS_XBOX + (c.pw * 16 + (c.lane & 31)) * (2 * FQS) + (c.lane & 16) * (FQS / 16) + c.jw * 8 : (unsigned)WS_FLAGW + (c.pw * 16 + c.jw) * 128; }
DI bool x_act(const Ctx& c, bool halo) { return c.lane < ((MBOX & 1) ? (halo && c.pw < 15 ? 32 : 16) : 1); }
DI unsigned x_find(const Ctx& c) {
    const int pp = c.pq - (c.lane >> 4);
    return (MBOX & 1) ? (unsigned)WS_XBOX + (c.pq * 16 + c.jq) * (2 * FQS) + (c.lane & 16) * (FQS / 16) + (c.lane & 15) * 8 : (unsigned)WS_FLAGW + ((pp < 0 ? 0 : pp) * 16 + (c.lane & 15)) * 128;
}
#define STAMP_AT(slot) do { if ((slot) == SA || (slot) == SB) { if (c.lane == 0) *(LAS unsigned long long*)lds(MISC + 3328 + (slot) * 8) = rstamp(); } } while (0)

#define LSTAMP(i) do { } while (0)
#define LSTORE(ph) do { } while (0)
#define LMARK() do { } while (0)
#define LPUT(i) do { } while (0)
#define ASTORE() do { } while (0)
#define QSTAMP(i) do { } while (0)
#define QSTORE() do { } while (0)
#define FENCE() asm volatile("" ::: "memory")
#define ALIGN8() do { if (ALN) { asm volatile(".p2align 3"); SCHED(); } } while (0)
DI float addf(float a, float b) { float r; asm("v_add_f32 %0, %1, %2" : "=v"(r) : "v"(a), "v"(b)); return r; }
DI f32x4 add4x4(const u32x4& a, const u32x4& b, const u32x4& c, const u32x4& d) {
    f32x4 r;
#pragma unroll
    for (int i = 0; i < 4; ++i) r[i] = addf(addf(__uint_as_float(a[i]), __uint_as_float(b[i])), addf(__uint_as_float(c[i]), __uint_as_float(d[i])));
    asm volatile("s_nop 1" : "+v"(r));
    return r;
}
DI float fold16(float a, float b) {
    auto r = __builtin_amdgcn_permlane16_swap(__float_as_uint(a), __float_as_uint(b), false, false);
    float x = __uint_as_float(r[0]) + __uint_as_float(r[1]);
    asm volatile("" : "+v"(x));
    return x;
}
DI float fold32(float a, float b) {
    auto r = __builtin_amdgcn_permlane32_swap(__float_as_uint(a), __float_as_uint(b), false, false);
    float x = __uint_as_float(r[0]) + __uint_as_float(r[1]);
    asm volatile("" : "+v"(x));
    return x;
}
DI void aload4(u32x4& d, unsigned voff, const char* sb) { asm volatile("s_nop 4\n\tglobal_load_dwordx4 %0, %1, %2" : "=v"(d) : "v"(voff), "s"(sb) : "memory"); }
DI void aload4p(u32x4& d, unsigned voff, const char* sb) { asm volatile("s_nop 4\n\tglobal_load_dwordx4 %0, %1, %2" : "+v"(d) : "v"(voff), "s"(sb) : "memory"); }
DI void aload4c(u32x4& d, unsigned voff, const char* sb) { asm volatile("s_nop 4\n\tglobal_load_dwordx4 %0, %1, %2 sc1" : "=v"(d) : "v"(voff), "s"(sb) : "memory"); }
DI void aload2c(u32x2& d, unsigned voff, const char* sb) { asm volatile("s_nop 4\n\tglobal_load_dwordx2 %0, %1, %2 sc1" : "=v"(d) : "v"(voff), "s"(sb) : "memory"); }
DI unsigned wo_src(int q, int lane, int jw) { return (unsigned)((8 * q + (lane >> 3)) * (D_ * 2) + jw * 128 + (((lane & 7) ^ ((lane >> 3) & 6)) << 4)); }
DI unsigned pick(bool f, unsigned a, unsigned b) { asm volatile("" : "+v"(a), "+v"(b)); return f ? a : b; }
DI unsigned wo_w_off(const Ctx& c) { return pick(c.cw == 3, (unsigned)((c.wid & 3) * 1024 + c.lane * 16), wo_src(c.wid & 3, c.lane, c.jw)); }
DI unsigned wq_src(int ib, int lane, int jq) {
    const int seg = ib >> 3, ii = ib & 7, lr = lane >> 3;
    return (unsigned)((8 * ii + lr) * 6144 + seg * 2048 + jq * 128 + (((lane & 7) ^ ((lr & 2) | ((ii & 1) << 2))) << 4));
}
template <int S0, int S1> DI void wo_w_issue(const Ctx& c, const char* W, unsigned vw) {
    const int lw = c.wid & 3;
#pragma unroll
    for (int s = S0; s < S1; ++s) dma16<CPY != 0>(s * 4096 + lw * 1024, vw, W + (size_t)((2 * (s & 15) + (s >> 4)) * 32) * (D_ * 2));
}

DI void code_again(const Ctx& c) {
    const char* pc; int len, back;
    asm volatile("s_getpc_b64 %0\n.Lcpb%=:\n\ts_mov_b32 %2, .Lcpb%=-.Lcpf_a\n\ts_mov_b32 %1, .Lcpf_end-.Lcpf_a-4" : "=s"(pc), "=s"(len), "=s"(back));
    pc -= back;
    const int o = min((((c.bid >> 3) & 7) * 64 + c.lane) * 128, len);
    asm volatile("s_mov_b32 m0, %0\n\ts_nop 3\n\tglobal_load_lds_dword %1, %2" ::"s"(M_TCH), "v"(o), "s"(pc) : "memory");
}
template <bool HALO, bool FIRST> DI void qkv_load(const Ctx& c0, const char* xin, const char* W, const char* Wo, int l, KP kp = nullptr, unsigned va0 = 0, unsigned vw0 = 0) {
    const Ctx c = fresh(c0);
    asm volatile("" : "+s"(xin), "+s"(W), "+s"(Wo));
    LAS char* smem = c.smem;
    constexpr int NAI = HALO ? 5 : 4, NWI = 6, XS = FIRST ? XS_ : XSB, NR = HALO ? 144 : 128;
    constexpr Sched<NAI, NWI, FIRST> SC{};
    static_assert(NQA == 3 && NQW == 4, "schedule");
    const int lw = c.wid - 4, lane = c.lane, lr = lane >> 3, lc = lane & 7, fr = lane & 15, fq = lane >> 4;
    const int row0 = c.pq * 128;
    const int pr = (ROTM == 1 || (ROTM == 2 && (c.jq & 7) == 3)) ? 2 * (c.pq & 7) + (c.pq >> 3) : (ROTM == 3 ? (c.pq & 7) + ((c.pq & 7) >> 1) + (c.pq & 8) : c.pq);
    const bool cq_ = CPY && !FIRST && (c.jq & 7) == 3;
    if (cq_) W = c.ws + WS_CQ + ((HALO ? 0 : 2) + (c.jq >> 3)) * 24576;
    asm volatile("" : "+s"(W));
    const bool nor = !FIRST && l > 1 && (NOR == 1 || (NOR == 2 && (c.jq & 7) == 3) || (NOR == 3 && (c.jq & 7) != 3));
    const int rot = FIRST ? rot_first(c.pq, c.jq) : (nor ? (c.pq & 8) * NRH : pr & 15);
    Order od = first_order(c.pq, c.jq);
    if (FIRST) asm volatile("" : "+s"(od.base), "+s"(od.r), "+s"(od.re));
    auto tile = [&](int s) { return FIRST ? first_tile(od, s) : (s + rot) & 15; };
    constexpr int SP = FIRST ? NC : 0;
    unsigned va[5], vw[NWI];
#pragma unroll
    for (int j = 0; j < 4; ++j) va[j] = (unsigned)(FIRST ? va0 + j * 32 * (D_ * 2) : c.pq * 262144 + (j * 4 + lw) * 1024 + lane * 16);
    if (HALO) va[4] = (unsigned)((c.pq > 0 ? (c.pq - 1) * 262144 + 14336 : 0) + lw * 512 + lane * 16);
#pragma unroll
    for (int j = 0; j < NWI; ++j) vw[j] = FIRST ? vw0 + (j & 1) * (32 * 6144) + (j >> 1) * 2048 : pick(cq_, (unsigned)((j * 4 + lw) * 1024 + lane * 16), wq_src(j * 4 + lw, lane, c.jq));
    auto issueA = [&](int s, int slot) {
        const char* b = xin + (unsigned)(tile(s) * (FIRST ? 128 : 16384));
#pragma unroll
        for (int j = 0; j < 4; ++j) dma16<true>(QA_OFF + slot * QA_SLOT + (j * 4 + lw) * 1024, va[j], b);
        if (HALO) { if (lane < 32) dma16<true>(QA_OFF + slot * QA_SLOT + 16384 + lw * 512, va[4], b); }
    };
    auto issueW = [&](int s, int slot, int j0 = 0, int j1 = NWI) {
        const char* b = W + (unsigned)(tile(s) * (64 * 6144));
#pragma unroll
        for (int j = j0; j < j1; ++j) dma16<CPY && !FIRST>(qw_slot(slot) + (j * 4 + lw) * 1024, vw[j], b);
    };
    auto ahead = [&]() {
        const int r3 = (lane * 43) >> 7;
        const unsigned o = (unsigned)((((rot - (c.pq & 7) + 10) * 64 + (c.pq & 7) * 48 + lw * 12 + r3) & 1023) * 6144 + (lane - 3 * r3) * 2048 + c.jq * 128);
        if (lane < 36 && !nor && !cq_ && (WTC != 2 || (c.jq & 7) == 3)) asm volatile("s_mov_b32 m0, %0\n\ts_nop 3\n\tglobal_load_lds_dword %1, %2" ::"s"(M_TCH), "v"(o), "s"(W) : "memory");
    };
    if (ST0 && FIRST && lw == 1) STAMP_AT(47);
    if (FIRST && ERL) {
        SCHED();
        issueA(1, 1); issueW(1, 1);
        SCHED();
    }
    const bool docs = FIRST || (HALO && l == 1);
    const int t = lw * 64 + lane;
    static_assert(MBOX & 1, "residual flag");
    unsigned foff, ftg, so, ho, fo_, co, l3, l4, vwo, wt[3], cso, xt, xt1, xso, tp, tl;
    u32x4 on;
    auto addr1 = [&]() {
        int lane = c.lane;
        asm volatile("" : "+v"(lane));
        const int fr = lane & 15, fq = lane >> 4;
        const int ph = c.pq > 0 ? c.pq - 1 : 0;
        foff = pick(lane != 32, (unsigned)WS_FLAGQ + pick(lane < 16, c.pq * 16 + lane, pick(lane < 32, fr * 16 + c.jq, ph * 16 + 14 + (lane & 1))) * FQS, (unsigned)WS_XBOX + (c.pw * 16 + c.jw) * (2 * FQS) + c.jw * 8);
        ftg = lane != 32 ? (unsigned)(4 * l + 1) : (unsigned)(l + 1);
        so = FIRST ? (unsigned)((c.pq * 16 + fq * 4) * 1024 + (lw * 32 + fr * 2) * 8) : (unsigned)((c.pq * 16 + (lane >> 2)) * 1024 + (c.jq * 8 + lw * 2 + (lane & 1)) * 8);
        ho = FDM ? (unsigned)(262144 + ((lw * 36 + lane < 16 ? ph * 128 + 128 : c.pq * 128) + lw * 36 + lane - 16) * 8)
                 : (unsigned)(262144 + pick(lane < 32, c.pq * 128 + lw * 32 + lane, ph * 128 + 112 + lw * 4 + (lane & 3)) * 8);
        fo_ = pick(lane < 2, (unsigned)(262144 + (c.pq * 128 + c.jq * 8 + lw * 2 + lane) * 8), 0x80000000u);
        const int cq = lw * 12 + (fr < 12 ? fr : 11);
        co = (unsigned)((fq * 4 * 3072 + (cq >> 4) * 1024 + c.jq * 64 + (cq & 15) * 4) * 4);
        l3 = (unsigned)(M_CS + (HALO ? 768 : 0) + (cq * 4 + fq) * 4);
        l4 = (unsigned)(M_MEAN + pick(lane < 32, 16 + lw * 32 + (FIRST ? fr * 2 + fq : lane), lw * 4 + (lane & 3)) * 4);
        asm volatile("" : "+v"(foff), "+v"(ftg), "+v"(so), "+v"(co), "+v"(l3), "+v"(l4));
        if (!FIRST) asm volatile("" : "+v"(ho), "+v"(fo_));
        SCHED();
    };
    auto addr2 = [&]() {
        int lane = c.lane;
        asm volatile("" : "+v"(lane));
        const int fr = lane & 15, fq = lane >> 4;
        const int q4 = fr >> 2, p4 = lane & 3, fb = (q4 & 2) | ((fq & 1) << 2);
#pragma unroll
        for (int i = 0; i < 3; ++i) {
            const int T = 3 * lw + i;
            wt[i] = (unsigned)(qw_slot(SP % NQW) + (T >> 2) * 8192 + (8 * fq + q4) * 128 + (((2 * (T & 3) + (p4 >> 1)) ^ fb) << 4) + (p4 & 1) * 8);
        }
        const int T = 3 * lw + fq;
        cso = (unsigned)((((HALO ? 16 : 0) + rot) * 3072 + (T >> 2) * 1024 + c.jq * 64 + (T & 3) * 16 + fr) * 4);
        xt = (unsigned)(QA_OFF + (SP % NQA) * QA_SLOT + (lw * 32 + fr) * 128 + ((fq ^ (fr & 7)) << 4));
        xt1 = xt ^ 64;
        xso = (unsigned)hl<HS>((c.pq * 16 + rot) * 1024 + (lw * 32 + fr) * 8);
        on = (u32x4){0x3f803f80u, 0x3f803f80u, 0x3f803f80u, 0x3f803f80u};
        if (FIRST || HALO) asm volatile("" : "+v"(wt[0]), "+v"(wt[1]), "+v"(wt[2]), "+v"(cso), "+v"(on));
        if (FIRST) asm volatile("" : "+v"(xt), "+v"(xt1), "+v"(xso));
        SCHED();
    };
    auto addr3 = [&]() {
        Ctx c2 = c;
        asm volatile("" : "+v"(c2.lane));
        asm volatile("" : "+s"(c2.bid));
        vwo = wo_w_off(c2);
        tp = (unsigned)(pick(c2.lane < 30, (c2.bid >> 3) * 30 + c2.lane, (c2.bid >> 3) * 16 + c2.lane - 30 + (int)(WS_O >> 16)) * 65536 + 65536 - 128);
        tl = (unsigned)(lw == 1 ? ((c2.bid >> 3) * 2 + c2.lane) * 65536 : c2.bid * 8192 + (TWO ? c2.lane * 128 : 0));
        asm volatile("" : "+v"(vwo));
        if (FIRST) asm volatile("" : "+v"(tp), "+v"(tl));
        SCHED();
    };
    if (!FIRST || AD1 < 0) addr1();
    if (FIRST && ERL >= 5) { issueA(2, 2); SCHED(); }
    if (FIRST && (ERL == 2 || ERL == 5)) { issueW(2, 2); SCHED(); }
    if (!FIRST || AD2 < 0) addr2();
    if (!FIRST || AD3 < 0) addr3();
#pragma unroll
    for (int j = 0; j < NAI; ++j) asm volatile("" : "+v"(va[j]));
#pragma unroll
    for (int j = 0; j < NWI; ++j) asm volatile("" : "+v"(vw[j]));
    SCHED();
    int tv = 0; unsigned long long lt = 0;
    if (FIRST) {
        LSTAMP(55);
    } else {
        static_assert(CPY == 1 && RS0 > 0 && RN0 == NWI && WPRE >= 3 && (FIRST || SC.w[0] == NAI), "order");
        unsigned vx[2], xfo = (unsigned)WS_XBOX + (c.pq * 16 + c.jq) * (2 * FQS) + (HALO && c.pq > 0 ? (lane & 16) * (FQS / 16) : 0) + fr * 8, go = M_GO;
        u64 xv = lds_epoch(smem) + (unsigned)(l + 1);
#pragma unroll
        for (int i = 0; i < 2; ++i) vx[i] = pick(cq_, (unsigned)(((2 * lw + i) * 4 + 3) * 1024 + lane * 16), wq_src((2 * lw + i) * 4 + 3, lane, c.jq));
        asm volatile("" : "+v"(vx[0]), "+v"(vx[1]), "+v"(xfo), "+v"(xv), "+v"(go));
        BAR();
        issueW(0, 0);
        auto word = [&](int off) { return __builtin_amdgcn_readfirstlane(*(volatile LAS int*)(lds(go) + off)); };
        auto until = [&](int off, int v) {
#pragma unroll 1
            for (unsigned spins = 0; spins < (1u << 24); ++spins) { if (word(off) == v) break; }
            FENCE();
        };
        auto stage = [&](int s) {
            const char* b = W + (unsigned)(((s + rot) & 15) * (64 * 6144));
            unsigned d = (unsigned)((s == 3 ? QW_TOP : s * QW_SLOT) + lw * 1024), dx = d + (unsigned)(lw * 7168 + 3072);
            asm volatile("" : "+s"(d), "+s"(dx), "+s"(b));
#pragma unroll
            for (int j = 0; j < NWI; ++j) dma16<true>(d + j * 4096, vw[j], b);
#pragma unroll
            for (int i = 0; i < 2; ++i) dma16<true>(dx + i * 4096, vx[i], b);
        };
        auto first2 = [&]() { __builtin_amdgcn_s_setprio(LPRIO); issueA(0, 0); issueA(1, 1); };
        if (lw == 3) {
            if (!EPOLL) until(M_CNT - M_GO, 8 * l);
            Ask t;
            poll_rmw(t, c.ws, xfo, lane < (HALO && c.pq > 0 ? 32 : 16), xv, 63 - l);
            *(volatile LAS int*)lds(go) = l;
            until(M_CNT - M_GO, 8 * l);
            first2();
            STAMP_AT(32 + l * 8 + 0);
            wait_v<NAI>();
            keep(t);
        } else {
            until(M_CNT - M_GO, 8 * l);
            if (RSLP) __builtin_amdgcn_s_sleep(RSLP);
            int ns = 1;
#pragma unroll 1
            for (; ns < WPRE; ++ns) {
                if (word(0) == l) break;
                stage(ns);
            }
            until(0, l);
            first2();
            const int left = WPRE - ns;
#pragma unroll 1
            for (; ns < WPRE; ++ns) stage(ns);
            if (left == 0) wait_v<NAI>();
            else if (left == 1) wait_v<NAI + 8>();
            else if (left == 2) wait_v<NAI + 16>();
            else wait_v<NAI + 24>();
        }
        FENCE();
    }
    if (FIRST && ERL == 3) issueW(2, 2);
    if (ST0 && FIRST && lw == 2) STAMP_AT(57);
    if (FIRST && !ERL) BAR();
    __builtin_amdgcn_s_setprio(LPRIO);
    if (FIRST && !ERL) { LSTAMP(57); issueA(1, 1); issueW(1, 1); LSTAMP(58); }
    u32x4 st[4], cs[4]; u32x2 sh, fl; u64 fv = 0; float rs_, rq_, hs;
    f32x4 d[3], sm[2], g[2];
    bf16x8 wf[3][2], xf[2][2];
    const bool fact = FIRST ? lane < 32 : ((lane < 16 || lane >= (docs ? 16 : 32)) && lane <= (HALO ? 34 : 32));
    static_assert(XS + 3 <= (FIRST ? 12 : 13) && SPR == 1, "aux schedule");
    auto side = [&](auto ktc) {
        constexpr int kt = decltype(ktc)::value;
        {
            constexpr int C0 = FIRST ? 12 : XS + 2, C1 = FIRST ? 13 : XS + 3, C2 = 13;
            auto acc4 = [](u32x4& a, const u32x4& b) {
#pragma unroll
                for (int i = 0; i < 4; ++i) a[i] = __float_as_uint(addf(__uint_as_float(a[i]), __uint_as_float(b[i])));
                asm volatile("" : "+v"(a));
            };
            auto fin = [&]() {
                rs_ *= -1.f / 1024.f;
                asm volatile("" : "+v"(rs_));
                rq_ = __builtin_amdgcn_rsqf(fmaxf(rq_ * (1.f / 1024.f) - rs_ * rs_, 0.f) + LN_EPS);
                asm volatile("" : "+v"(rq_));
            };
            if constexpr (FIRST && kt == XS + 2) {
                asm volatile("" : "+v"(st[0]), "+v"(st[1]), "+v"(st[2]));
                acc4(st[0], st[1]); acc4(st[0], st[2]);
            }
            if constexpr (FIRST && kt == XS + 3) { asm volatile("" : "+v"(st[3])); acc4(st[0], st[3]); }
            if constexpr (kt == C0) {
                if (docs) { asm volatile("" : "+v"(cs[0]), "+v"(cs[1])); acc4(cs[0], cs[1]); }
            }
            if constexpr (FIRST && kt == 12) {
                asm volatile("s_nop 1" : "+v"(st[0]));
                rs_ = fold16(__uint_as_float(st[0][0]), __uint_as_float(st[0][2])); rq_ = fold16(__uint_as_float(st[0][1]), __uint_as_float(st[0][3]));
                rs_ = fold32(rs_, rs_); rq_ = fold32(rq_, rq_);
            }
            if constexpr (FIRST && kt == 13) fin();
            if constexpr (!FIRST && kt == TL0 + 2) {
                asm volatile("" : "+v"(sh));
                hs = fold16(__uint_as_float(sh[0]), __uint_as_float(sh[1]));
                hs = fold32(hs, hs);
                hs += __uint_as_float(__builtin_amdgcn_update_dpp(0u, __float_as_uint(hs), 0x124, 0xf, 0xf, false));
                hs += __uint_as_float(__builtin_amdgcn_update_dpp(0u, __float_as_uint(hs), 0x128, 0xf, 0xf, false));
                asm volatile("" : "+v"(hs));
            }
            if constexpr (!FIRST && kt == TL0 + 2 + TSP) {
                auto hh = __builtin_amdgcn_permlane16_swap(__float_as_uint(hs), __float_as_uint(hs), false, false);
                rs_ = __uint_as_float(hh[0]); rq_ = __uint_as_float(hh[1]);
                asm volatile("" : "+v"(rs_), "+v"(rq_));
                fin();
                auto rf = __builtin_amdgcn_make_buffer_rsrc(c.ws + WS_STATS + (size_t)l * 524288, 0, 524288, 0x00020000);
                __builtin_amdgcn_raw_buffer_store_b64((u32x2){__float_as_uint(rs_), __float_as_uint(rq_)}, rf, fo_, 0, 16);
            }
            if constexpr (kt == C1) {
                if (docs) { asm volatile("" : "+v"(cs[2]), "+v"(cs[3])); acc4(cs[2], cs[3]); acc4(cs[0], cs[2]); }
            }
            if constexpr (kt == C2) {
                if (docs) {
                    asm volatile("s_nop 1" : "+v"(cs[0]));
                    hs = fold32(fold16(__uint_as_float(cs[0][0]), __uint_as_float(cs[0][1])), fold16(__uint_as_float(cs[0][2]), __uint_as_float(cs[0][3])));
                }
            }
            if constexpr (kt == 13) {
                static_assert(WV0 == 1, "wait");
                if (FIRST || !FDM || docs) WAIT_V0();
                if (!FIRST && !FDM) { asm volatile("" : "+v"(sh)); rs_ = __uint_as_float(sh[0]); rq_ = __uint_as_float(sh[1]); }
                if ((FIRST || !FDM) && lane < (HALO ? 36 : 32)) {
                    *(LAS float*)lds(l4) = rs_;
                    *(LAS float*)(lds(l4) + (M_RSTD - M_MEAN)) = rq_;
                }
                if (docs) *(LAS float*)lds(l3) = hs;
                FENCE();
            }
        }
    };
    static_for<0, KT>([&](auto ktc) {
        constexpr int kt = decltype(ktc)::value;
        LSTAMP(3 * kt);
        if constexpr (FIRST || kt > 0) wait_v<SC.w[kt]>();
        LSTAMP(3 * kt + 1);
        if constexpr (kt == XS) {
            if (lw == 0) {
                asm volatile("" : "+v"(fl));
                const u64 ep = lds_epoch(smem);
                if (!__all(!fact || reached(__builtin_bit_cast(u64, fl), ep, ftg))) poll_flags(c.ws, ep, foff, fact, ftg);
            }
        }
        if constexpr (kt == 14) asm volatile("s_waitcnt lgkmcnt(0)" ::: "memory");
        BAR();
        FENCE();
        LMARK();
        LSTAMP(3 * kt + 2);
        if constexpr (TOPJ & (1 << kt)) side(ktc);
        if constexpr (kt == (FIRST ? SP + DS : TL0 + 2 + TSP) + 3) {
            if (lw == 0 && lane == 0) __hip_atomic_store((GAS u64*)(c.ws + WS_FLAGQ + (c.pq * 16 + c.jq) * FQS), fv, __ATOMIC_RELAXED, __HIP_MEMORY_SCOPE_AGENT);
        }
        if constexpr (kt == XS + 1 - NQA) {
            if (lw == 0) { if (fact) aload2c(fl, foff, c.ws); }
        }
        {
            const char* sb = c.ws + WS_STATS + (size_t)l * 524288;
            const char* cb = c.ws + WS_CSUM + (HALO ? 16 * 3072 * 4 : 0);
            constexpr int sa = 3 * (kt - XS), ca = FIRST ? 2 * (kt - XS - 1) : 2 * (kt - XS), cn = 2;
            if constexpr (FIRST) {
#pragma unroll
                for (int i = sa; i < sa + 3; ++i) if (i >= 0 && i < 4) aload4c(st[i], so, sb + i * 1024);
            }
            if constexpr (!FIRST && kt == TL0) aload2c(sh, so, sb);
            if constexpr (!FIRST && kt == TL1) {
                static_assert(TL1 <= 12, "covered by the wait of step 14");
                if (FDM) { if (lane < 36) { touch(M_MEAN + lw * 144, ho, sb); touch(M_RSTD + lw * 144, ho, sb + 4); } }
                else aload2c(sh, ho, sb);
            }
            if constexpr (ca >= 0 && ca < 4) {
                if (docs) {
#pragma unroll
                    for (int i = ca; i < ca + cn; ++i) aload4c(cs[i], co, cb + i * 12288);
                }
            }
        }
        if constexpr (kt == SP) {
            if (docs) {
#pragma unroll
                for (int i = 0; i < 3; ++i)
#pragma unroll
                    for (int ks = 0; ks < 2; ++ks) {
                        const s16x4 lo = tr_read(lds(wt[i]) + ks * 4096), hi = tr_read(lds(wt[i]) + ks * 4096 + 512);
                        wf[i][ks] = __builtin_shufflevector(lo, hi, 0, 1, 2, 3, 4, 5, 6, 7);
                    }
                if (FIRST) {
#pragma unroll
                    for (int mt = 0; mt < 2; ++mt)
#pragma unroll
                        for (int ks = 0; ks < 2; ++ks) xf[mt][ks] = *(const LAS bf16x8*)(lds(ks ? xt1 : xt) + mt * 2048);
                }
            }
            SCHED();
        }
        if constexpr (kt + 2 < KT && !(FIRST && ERL >= 5 && kt == 0)) issueA(kt + 2, (kt + 2) % NQA);
        LPUT(3 * kt + 2);
        if constexpr (kt == SP + DM) {
            SCHED();
            if (docs) {
                const bf16x8 ones = __builtin_bit_cast(bf16x8, on);
                const f32x4 zero = {0.f, 0.f, 0.f, 0.f};
#pragma unroll
                for (int ks = 0; ks < 2; ++ks) {
#pragma unroll
                    for (int i = 0; i < 3; ++i) d[i] = MFMA(ones, wf[i][ks], ks ? d[i] : zero);
                    if (FIRST) {
#pragma unroll
                        for (int mt = 0; mt < 2; ++mt) { sm[mt] = MFMA(ones, xf[mt][ks], ks ? sm[mt] : zero); g[mt] = MFMA(xf[mt][ks], xf[mt][ks], ks ? g[mt] : zero); }
                    }
                }
            }
            SCHED();
        }
        if constexpr (FIRST && ERL < 2 && kt < 5) {
            if constexpr (kt < 4) issueW(kt + 2, (kt + 2) % NQW);
            if constexpr (kt == 3) issueW(6, 2, 0, NWI / 2);
            if constexpr (kt == 4) { issueW(6, 2, NWI / 2, NWI); issueW(7, 3); }
        } else {
            if constexpr (WPRE == 2 && kt < 2) issueW(kt + 2, kt + 2);
            if constexpr (FIRST && (ERL == 4 || ERL == 6) && kt == 0) issueW(2, 2);
            if constexpr ((FIRST || WPRE == 3) && kt == 0) issueW(3, 3);
            if constexpr (kt >= 1 && kt + 3 < KT) issueW(kt + 3, (kt + 3) % NQW);
        }
        if constexpr (FIRST && kt == WT0) ahead();
        if constexpr ((CP2 & 4) && FIRST && kt == 8) { if (lw == 2) code_again(c); }
        if constexpr ((CP2 & 8) && HALO && kt == 8) { if (lw == 2 && l == 1) code_again(c); }
        if constexpr (FIRST && kt == TSTEP) {
            if (lw == 1 && lane < 46) touch(M_TCH, tp, c.ws);
        }
        if constexpr (FIRST && kt == TW0) {
            if (lw == 0) touch(M_TCH, tl, Wo);
            else if (lw == 1) { asm volatile("" : "+s"(kp)); const char* outp = (const char*)kp->out; if (lane < 2) touch(M_TCH, tl, outp); }
        }
        if constexpr (kt == SP && DM > 0) {
            SCHED();
            if (docs) {
                asm volatile("" : "+v"(wf[0][0]), "+v"(wf[0][1]), "+v"(wf[1][0]), "+v"(wf[1][1]), "+v"(wf[2][0]), "+v"(wf[2][1]));
                if (FIRST) asm volatile("" : "+v"(xf[0][0]), "+v"(xf[0][1]), "+v"(xf[1][0]), "+v"(xf[1][1]));
            }
        }
        if constexpr (kt == SP + DS) {
            SCHED();
            if (docs) {
                if (FIRST) {
                    auto rst = __builtin_amdgcn_make_buffer_rsrc(c.ws + WS_STATS, 0, 524288, 0x00020000);
                    const bool b0 = fr & 1, b1 = fr & 2;
                    u32x2 sv[2];
#pragma unroll
                    for (int mt = 0; mt < 2; ++mt) {
                        float qa = b0 ? g[mt][1] : g[mt][0], qb = b0 ? g[mt][3] : g[mt][2];
                        asm volatile("" : "+v"(qa), "+v"(qb));
                        sv[mt] = (u32x2){__float_as_uint(sm[mt][0]), __float_as_uint(b1 ? qb : qa)};
                    }
                    if (fq == (fr >> 2)) {
                        __builtin_amdgcn_raw_buffer_store_b64(sv[0], rst, xso, 0, 16);
                        __builtin_amdgcn_raw_buffer_store_b64(sv[1], rst, xso + hl<HS>(128), 0, 16);
                    }
                }
                float d0 = d[0][0], d1 = d[1][0], d2 = d[2][0];
                asm volatile("" : "+v"(d0), "+v"(d1), "+v"(d2));
                const float v = fq == 0 ? d0 : (fq == 1 ? d1 : d2);
                auto rc = __builtin_amdgcn_make_buffer_rsrc(c.ws + WS_CSUM, 0, 2 * 16 * 3072 * 4, 0x00020000);
                if (lane < 48) __builtin_amdgcn_raw_buffer_store_b32(__float_as_uint(v), rc, cso, 0, 16);
                asm volatile("s_waitcnt lgkmcnt(0)" ::: "memory");
            }
        }
        if constexpr (kt == 1) {
            if (lw == 0) {
                const u32x2 e = *(const LAS u32x2*)(smem + M_EP);
                const u64 es = ((u64)(unsigned)__builtin_amdgcn_readfirstlane(e[1]) << 32) | (unsigned)__builtin_amdgcn_readfirstlane(e[0]);
                fv = es + (unsigned)(4 * l + 1);
                asm volatile("" : "+s"(fv));
            }
        }
        if constexpr (FIRST && kt == AD1) addr1();
        if constexpr (FIRST && kt == AD2) addr2();
        if constexpr (FIRST && kt == AD3) addr3();
        if constexpr (!(TOPJ & (1 << kt))) side(ktc);
        if constexpr (kt == 13) wo_w_issue<0, WT13>(c, Wo, vwo);
        if constexpr (kt == 14) { wo_w_issue<WT13, (HALO || WA0 > 12) ? 12 : WA0>(c, Wo, vwo); if (HALO) wo_w_issue<23, 23 + WB14>(c, Wo, vwo); }
        if constexpr (kt == 15) {
            if (HALO ? WBE < 2 : WA0 > 12) wo_w_issue<12, HALO ? 18 : WA0>(c, Wo, vwo);
            if (HALO) wo_w_issue<23 + WB14, 23 + WB14 + WB15>(c, Wo, vwo);
        }
    });
    __builtin_amdgcn_s_setprio(0);
    BAR();
    LMARK(); LPUT(48);
    LSTAMP(48);
    LSTORE(0);
    if (HALO ? WBE < 1 : WAE < 1) wo_w_issue<18, HALO ? 23 : 20>(c, Wo, vwo);
    if (HALO) {
        static_assert(!WBS || (WBE == 1 && WB14 == 0 && WRG <= 12), "Wo weights of the first twelve steps are older than the last 6 + WB15");
        if (WBS) wait_v<6 + WB15>();
        BAR();
        if (WBE) wo_w_issue<WBE < 2 ? 18 : 12, 23>(c, Wo, vwo);
        wo_w_issue<23 + WB14 + WB15, 32>(c, Wo, vwo);
    }
}

DI float max3(float a, float b, float c) { return fmaxf(fmaxf(a, b), c); }
#define SGB(m, n) __builtin_amdgcn_sched_group_barrier(m, n, 0)
template <bool HALO> struct Tail {
    int f[26], n[4], t[4][26], d[4][27], v[4][27], h3[32], s3[32], o3[33], q3[112];
    constexpr Tail() : f{}, n{}, t{}, d{}, v{}, h3{}, s3{}, o3{}, q3{} {
        const int FB[26] = {0, 1, 2, 3, 4, 5, 6, 7, 8, 9, 10, 11, 12, 13, 14, 15, 24, 25, 19, 23, 18, 22, 17, 21, 16, 20};
        const int FA[24] = {0, 1, 2, 3, 4, 5, 6, 7, 8, 9, 10, 11, 12, 13, 14, 15, 16, 17, 20, 21, 18, 19, 22, 23};
        const int TA[4][26] = {{0, 1, 2, 3, 7, 6, 5, 4, 8, 9, 10, 11, 15, 14, 13, 12, 16, 17, 18, 19, 23, 22, 21, 20, 0, 0}, {4, 8, 9, 10, 11, 15, 14, 13, 12, 16, 17, 18, 19, 23, 22, 21, 20, 0, 1, 2, 3, 7, 6, 5, 0, 0}, {16, 17, 18, 19, 23, 22, 21, 20, 0, 1, 2, 3, 7, 6, 5, 4, 8, 9, 10, 11, 15, 14, 13, 12, 0, 0}, {17, 18, 19, 23, 22, 21, 20, 8, 9, 10, 11, 15, 14, 13, 12, 16, 0, 0, 0, 0, 0, 0, 0, 0, 0, 0}};
        const int DA[4][26] = {{1, 1, 1, 1, 1, 1, 1, 1, 1, 1, 1, 1, 1, 1, 1, 1, 1, 1, 1, 1, 1, 1, 0, 0, 0, 0}, {1, 1, 1, 1, 1, 1, 1, 1, 1, 1, 1, 1, 1, 1, 1, 1, 1, 1, 1, 1, 1, 1, 0, 0, 0, 0}, {1, 1, 1, 1, 1, 1, 1, 1, 1, 1, 1, 1, 1, 1, 1, 1, 0, 0, 0, 0, 0, 0, 0, 0, 0, 0}, {2, 2, 2, 2, 1, 1, 1, 1, 1, 1, 1, 1, 0, 0, 0, 0, 0, 0, 0, 0, 0, 0, 0, 0, 0, 0}};
        const int VA[4][26] = {{1, 1, 0, 0, 0, 0, 1, 1, 1, 1, 1, 1, 1, 1, 1, 1, 1, 1, 1, 1, 1, 1, 2, 2, 0, 0}, {1, 1, 1, 1, 1, 1, 1, 1, 1, 1, 1, 1, 1, 1, 1, 1, 1, 1, 1, 1, 1, 1, 2, 2, 0, 0}, {1, 1, 1, 1, 1, 1, 1, 1, 1, 1, 1, 1, 1, 1, 1, 1, 2, 2, 2, 2, 2, 3, 3, 2, 0, 0}, {0, 0, 0, 0, 1, 1, 1, 1, 1, 1, 1, 1, 2, 2, 2, 2, 0, 0, 0, 0, 0, 0, 0, 0, 0, 0}};
        const int TB[4][26] = {{0, 1, 2, 3, 7, 6, 5, 4, 24, 8, 9, 10, 11, 15, 14, 13, 12, 25, 16, 17, 18, 19, 23, 22, 21, 20}, {24, 8, 9, 10, 11, 15, 14, 13, 12, 25, 16, 17, 18, 19, 23, 22, 21, 20, 0, 1, 2, 3, 7, 6, 5, 4}, {25, 16, 17, 18, 19, 23, 22, 21, 20, 0, 1, 2, 3, 7, 6, 5, 4, 24, 8, 9, 10, 11, 15, 14, 13, 12}, {0, 1, 2, 3, 7, 6, 5, 4, 24, 8, 9, 10, 11, 15, 14, 13, 12, 25, 19, 23, 0, 0, 0, 0, 0, 0}};
        const int DB[4][26] = {{1, 1, 1, 1, 1, 1, 1, 1, 1, 1, 1, 1, 1, 1, 1, 1, 1, 1, 1, 1, 1, 1, 1, 0, 0, 0}, {1, 1, 1, 1, 1, 1, 1, 1, 1, 1, 1, 1, 1, 1, 1, 1, 1, 1, 1, 1, 1, 1, 0, 0, 0, 0}, {1, 1, 1, 1, 1, 1, 1, 1, 1, 1, 1, 1, 1, 1, 1, 1, 1, 1, 1, 1, 1, 1, 1, 0, 0, 0}, {2, 2, 1, 1, 1, 1, 1, 1, 1, 1, 1, 1, 1, 1, 1, 1, 1, 1, 1, 0, 0, 0, 0, 0, 0, 0}};
        const int VB[4][26] = {{1, 1, 1, 1, 1, 1, 1, 1, 1, 1, 1, 1, 1, 1, 1, 1, 1, 1, 1, 1, 1, 1, 1, 2, 2, 2}, {1, 1, 1, 1, 1, 1, 1, 1, 1, 1, 1, 1, 1, 1, 1, 1, 1, 1, 1, 1, 1, 1, 3, 3, 3, 2}, {1, 1, 1, 1, 1, 1, 1, 1, 1, 1, 1, 1, 1, 1, 1, 1, 1, 1, 1, 1, 1, 1, 1, 3, 3, 2}, {0, 0, 1, 1, 1, 1, 1, 1, 1, 1, 1, 1, 1, 1, 1, 1, 1, 1, 1, 2, 0, 0, 0, 0, 0, 0}};
        const int G[32] = {8, 12, 9, 13, 10, 14, 11, 15, 16, 17, 20, 21, 18, 19, 22, 23, 0, 4, 1, 5, 2, 6, 3, 7, 0, 4, 1, 5, 2, 6, 3, 7};
        const int NM = HALO ? 26 : 24;
        n[0] = n[1] = n[2] = NM; n[3] = HALO ? 20 : 16;
        for (int i = 0; i < NM; ++i) f[i] = HALO ? FB[i] : FA[i];
        int vv = 0;
        for (int h = 0; h < 4; ++h) {
            v[h][0] = vv;
            for (int i = 0; i < n[h]; ++i) {
                t[h][i] = HALO ? TB[h][i] : TA[h][i];
                d[h][i + 1] = d[h][i] + (HALO ? DB[h][i] : DA[h][i]);
                vv += HALO ? VB[h][i] : VA[h][i];
                v[h][i + 1] = vv;
            }
        }
        for (int i = 0; i < 32; ++i) { h3[i] = G[i]; s3[i] = i >= 16 && i < 24 ? 0 : 1; }
        for (int i = 3; i < 32; ++i) o3[i + 1] = o3[i] + TOPS;
        int nv = 0, ns = 0;
        for (int k = 0; k < 8 * 14; ++k) {
            const bool top = k % TOPS < 2 || nv == 96;
            if (top && ns < 16 && nv >= 12 * (ns / 2 + 1) && (nv == 96 || k % TOPS == (ns & 1))) { q3[k] = (ns / 2) * 14 + 13 - (ns & 1); ++ns; }
            else { q3[k] = (nv / 12) * 14 + nv % 12; ++nv; }
        }
    }
};
template <bool HALO> DI void qkv_consume(const Ctx& c0, int l, KP kp, unsigned va0 = 0, unsigned vw0 = 0) {
    const Ctx c = fresh(c0);
    if (ST0 && l == 0 && c.wid == 2) STAMP_AT(59);
    LAS char* smem = c.smem;
    const int lane = c.lane, wr = c.wid >> 1, wc = c.wid & 1, fr = lane & 15, fq = lane >> 4;
    const int sw = HALO ? wr : 0;
    f32x4 acc[4][6], ah[2];
#pragma unroll
    for (int mt = 0; mt < 4; ++mt)
#pragma unroll
        for (int nt = 0; nt < 6; ++nt) { acc[mt][nt] = (f32x4){0.f, 0.f, 0.f, 0.f}; asm volatile("" : "+v"(acc[mt][nt])); }
    ah[0] = ah[1] = (f32x4){0.f, 0.f, 0.f, 0.f};
    if (HALO) asm volatile("" : "+v"(ah[0]), "+v"(ah[1]));
    const unsigned aoff0 = (unsigned)(QA_OFF + wr * 8192 + (HALO || l ? lane * 16 : fr * 128 + ((fq ^ (fr & 7)) << 4)));
    const unsigned hoff0 = (unsigned)(QA_OFF + 16384 + lane * 16);
    unsigned xm = HALO || l ? 1024u : 64u;
    asm volatile("" : "+s"(xm));
    static_assert(((QA_OFF | QA_SLOT) & 2047) == 0, "halves");
    const int q4 = fr >> 2, p4 = lane & 3;
    const int fb = (q4 & 2) | ((fq & 1) << 2);
    unsigned boff[2];
#pragma unroll
    for (int cc = 0; cc < 2; ++cc)
        boff[cc] = (unsigned)((8 * fq + q4) * 128 + (((2 * (2 * wc + (cc ^ sw)) + (p4 >> 1)) ^ fb) << 4) + (p4 & 1) * 8);
    bf16x8 xa[2][4], xh[2], wb[2][6];
    auto ldfrag = [&](int set, unsigned pa, unsigned ph, unsigned pb0, unsigned pb1, int ks) {
#pragma unroll
        for (int mt = 0; mt < 4; ++mt) xa[set][mt] = *(const LAS bf16x8*)(lds(pa) + mt * 2048);
        if (HALO) xh[set] = *(const LAS bf16x8*)lds(ph);
#pragma unroll
        for (int nt = 0; nt < 6; ++nt) {
            const LAS char* pb = lds((nt & 1) ? pb1 : pb0) + (nt >> 1) * 8192 + ks * 4096;
            const s16x4 lo = tr_read(pb), hi = tr_read(pb + 512);
            wb[set][nt] = __builtin_shufflevector(lo, hi, 0, 1, 2, 3, 4, 5, 6, 7);
        }
    };
    constexpr int NM = HALO ? 26 : 24, NH = HALO ? 17 : 16;
    auto mm = [&](int set, int i0, int i1) {
#pragma unroll
        for (int ii = i0; ii < i1; ++ii) {
            const int i = !HALO || ii < 8 ? ii : (!SNK ? (ii < 10 ? ii + 16 : ii - 2) : (ii == 8 ? 24 : (ii < 17 ? ii - 1 : (ii == 17 ? 25 : ii - 2))));
            const int nt = i >> 2, mt = SNK && (nt & 1) ? 3 - (i & 3) : i & 3;
            if (i < 16) acc[mt][nt] = MFMA(wb[set][nt], xa[set][mt], acc[mt][nt]);
            else if (i < 24) acc[mt][nt] = MFMA(xa[set][mt], wb[set][nt], acc[mt][nt]);
            else if (i == 24) ah[0] = MFMA(wb[set][2], xh[set], ah[0]);
            else ah[1] = MFMA(xh[set], wb[set][4], ah[1]);
        }
    };
    auto interleave = [&]() {
#pragma unroll
        for (int i = 0; i < NH; ++i) { SGB(0x008, 1); SGB(0x100, 1); }
        SCHED();
    };
    unsigned sa = 0, sb = 0;
    unsigned pa0 = aoff0, pa1 = aoff0 ^ xm, ph0 = hoff0, ph1 = hoff0 ^ xm, pb0 = boff[0], pb1 = boff[1];
    asm volatile("" : "+v"(pa0), "+v"(pa1), "+v"(pb0), "+v"(pb1));
    if (HALO) asm volatile("" : "+v"(ph0), "+v"(ph1));
    constexpr int M_CSH = M_CS + (HALO ? 768 : 0);
    unsigned sa0 = (unsigned)(M_MEAN + 64 + (wr * 64 + fr) * 4), sa1 = (unsigned)(M_MEAN + 64 + wr * 256 + fq * 16);
    unsigned sa2 = (unsigned)(M_CSH + wc * 128 + sw * 64 + fq * 16), sa6 = (unsigned)(M_CSH + 512 + wc * 128 + sw * 64 + fr * 4);
    unsigned sa3 = sa2 ^ 64, sa7 = sa6 ^ 64, sa4 = (unsigned)(M_MEAN + fr * 4), sa5 = (unsigned)(M_MEAN + fq * 16);
    asm volatile("" : "+v"(sa0), "+v"(sa1), "+v"(sa2), "+v"(sa6), "+v"(sa3), "+v"(sa7));
    if (HALO) asm volatile("" : "+v"(sa4), "+v"(sa5));
    const int head = c.jq * 2 + wc;
    auto rso = __builtin_amdgcn_make_buffer_rsrc(HALO ? c.ws + WS_O + (size_t)l * (8u << 20) + (size_t)((head * 16 + c.pq) * 8 + wr * 4) * (1024 << HO)
                                                      : c.ws + WS_KV + (size_t)(l >> 1) * (16u << 20) + (size_t)((c.pq * 32 + head) * 4 + wr) * (4096 << HK), 0, HALO ? 4096 << HO : 12288 << HK, 0x00020000);
    unsigned xrs = (unsigned)(M_XCH + wc * 6144 + wr * 2048), xhs = (unsigned)(M_XCH + wc * 6144 + 8 - wr * 8);
    unsigned b0m = (c.pq == 0 && wr == 0) ? 15u * 16u : 0u;
    asm volatile("" : "+s"(rso));
    if (HALO) asm volatile("" : "+s"(xrs), "+s"(xhs), "+s"(b0m));
    u32x4 cpy[3];
    unsigned cv, vt, cdst;
    const char *cs_, *tw, *cd_;
    int more = !HALO && l == 0 ? KT - 3 - XST - (c.pq & 7) : 0, three = c.jq & 4;
    if (!HALO) {
        static_assert(CPY + TOUCH == 2 && XSP == 1, "extra");
        asm volatile("" : "+s"(kp));
        const int g = c.jq >> 2, n = c.pq * 4 + (c.jq & 3), m = n >> 5, kt = (n >> 1) & 15;
        const char* src = *(const char* const __attribute__((address_space(4)))*)((const __attribute__((address_space(4))) char*)kp + (three ? (m ? 8 : 24) : (m ? 32 : 40)));
        const char* tsr = *(const char* const __attribute__((address_space(4)))*)((const __attribute__((address_space(4))) char*)kp + (c.wid < 3 ? 24 : 40));
        int o1 = kt * (64 * 6144) + (n & 1) * (32 * 6144) + (3 + 4 * (g & 2) - c.jq) * 128 + 2048, o2 = (n & 31) * 65536 + (11 - 4 * g) * 128 - c.pq * 128 * (D_ * 2) + 2048;
        int d1 = (int)WS_CQ + kt * (64 * 6144) + (m * 2 + (g >> 1)) * 24576 + (n & 1) * 4096, d2 = (int)WS_CO + (n & 31) * 65536 + (m * 2 + 1 - (g >> 1)) * 4096;
        asm volatile("" : "+s"(o1), "+s"(o2), "+s"(d1), "+s"(d2));
        cs_ = src + (three ? o1 : o2);
        tw = tsr + (unsigned)((c.wid < 3 ? c.wid : 0) * (2u << 20) + c.bid * 8192);
        cd_ = c.ws + (unsigned)(three ? d1 : d2);
        cv = pick(three, vw0, va0 ^ ((lane & 8) << 1));
        cdst = (unsigned)(c.wid * 1024 + lane * 16);
        vt = TWQ ? (unsigned)(((lane & 7) == 3 ? lane - 1 : lane) * 128) : 0u;
        asm volatile("" : "+s"(more), "+s"(three), "+s"(cs_), "+s"(tw), "+s"(cd_));
        asm volatile("" : "+v"(cv), "+v"(cdst), "+v"(vt));
        asm volatile("" : "=v"(cpy[0]), "=v"(cpy[1]), "=v"(cpy[2]));
    }
    unsigned hto = HT0 == 2 ? 0u : 0x80000000u;
    const char* W0 = (const char*)kp->wqkv0;
    if (!HALO && HT0) {
        const int n = ((c.pq & 7) * 3 + (c.jq & 3)) * 64 + c.wid * 16 + fr, r3 = (n * 43691) >> 17;
        const int t0 = rot_first(c.pq & 8, c.jq | 3) + 8;
        if (l == 0 && !(c.jq & 4) && (c.jq & 3) != 3 && lane < 16) hto = (unsigned)(((t0 * 64 + r3) & 1023) * 6144 + (n - 3 * r3) * 2048 + (c.jq | 3) * 128);
        asm volatile("" : "+v"(hto));
        asm volatile("" : "+s"(W0));
    }
    if (!HALO && HT0 == 2) { asm volatile("s_mov_b32 m0, %0\n\ts_nop 3\n\tglobal_load_lds_dword %1, %2" ::"s"(M_TCH), "v"(hto), "s"(W0) : "memory"); }
    if (!HALO && !ERL && l == 0) BAR();
    if (ST0 && l == 0 && c.wid == 1) STAMP_AT(44);
    if (!HALO) { if (HT0 == 2) wait_v<1>(); else WAIT_V0(); }
    if (ST0 && l == 0 && c.wid == 1) STAMP_AT(46);
    BAR();
    if (!HALO && HT0 == 1) { if (hto < 0x80000000u) asm volatile("s_mov_b32 m0, %0\n\ts_nop 3\n\tglobal_load_lds_dword %1, %2" ::"s"(M_TCH), "v"(hto), "s"(W0) : "memory"); }
    if (c.wid == 0) STAMP_AT(l * 8 + 0);
    ldfrag(0, pa0, ph0, pb0, pb1, 0);
    SCHED();
    if (ALN) WAIT_L0();
    SCHED();
    int cnt = KT - 3 - more, tv = 0;
    QSTAMP(0);
    auto half0 = [&]() {
        ldfrag(1, pa1, ph1, pb0, pb1, 1);
        mm(0, 0, NH);
        interleave();
        mm(0, NH, NH + 1); SCHED();
        asm volatile("s_addk_i32 %0, %2\n\ts_addk_i32 %1, %3" : "+s"(sa), "+s"(sb) : "i"(QA_SLOT), "i"(QW_SLOT) : "scc"); SCHED();
        mm(0, NH + 1, NH + 2); SCHED();
        asm volatile("s_cmpk_lg_u32 %0, %2\n\ts_cselect_b32 %0, %0, 0\n\ts_cmp_lg_u32 %1, %3\n\ts_cselect_b32 %1, %1, %4" : "+s"(sa), "+s"(sb) : "i"(NQA * QA_SLOT), "i"(3 * QW_SLOT), "i"(QW_TOP) : "scc"); SCHED();
        mm(0, NH + 2, NH + 3); SCHED();
        asm volatile("s_cmp_lg_u32 %0, %2\n\ts_cselect_b32 %0, %0, 0\n\ts_addk_i32 %1, -1" : "+s"(sb), "+s"(cnt) : "i"(QW_TOP + QW_SLOT) : "scc"); SCHED();
        mm(0, NH + 3, NH + 4); SCHED();
        pa0 = aoff0 + sa; pb0 = boff[0] + sb;
        asm volatile("" : "+v"(pa0), "+v"(pb0)); SCHED();
        mm(0, NH + 4, NH + 5); SCHED();
        pb1 = boff[1] + sb; pa1 = pa0 ^ xm;
        asm volatile("" : "+v"(pa1), "+v"(pb1)); SCHED();
        mm(0, NH + 5, NH + 6); SCHED();
        if (HALO) { ph0 = hoff0 + sa; ph1 = ph0 ^ xm; asm volatile("" : "+v"(ph0), "+v"(ph1)); SCHED(); }
        mm(0, NH + 6, NM);
        SCHED();
        WAIT_L0();
        BAR();
        SCHED();
    };
    asm volatile("" : "+s"(sa), "+s"(sb), "+s"(cnt));
    do {
    ALIGN8();
    do {
        half0();
        ldfrag(0, pa0, ph0, pb0, pb1, 0);
        mm(1, 0, NH);
        interleave();
        mm(1, NH, NM - 1);
        SCHED();
        WAIT_L0();
        asm volatile("s_nop 0");
        SCHED();
        mm(1, NM - 1, NM);
        SCHED();
    } while (cnt != 0);
    if (!HALO) {
        asm volatile("s_cmp_eq_u32 %[more], 0\n\ts_cbranch_scc1 .Lex%=\n\ts_mov_b32 m0, %[tch]\n\ts_nop 3\n\tglobal_load_lds_dword %[vt], %[tw] sc1\n\t"
                     "global_load_dwordx4 %[c0], %[v0], %[cs] offset:-2048\n\ts_cmp_eq_u32 %[three], 0\n\ts_cbranch_scc1 .Lex%=\n\t"
                     "global_load_dwordx4 %[c1], %[v0], %[cs]\n\tglobal_load_dwordx4 %[c2], %[v0], %[cs] offset:2048\n.Lex%=:"
                     : [c0] "+v"(cpy[0]), [c1] "+v"(cpy[1]), [c2] "+v"(cpy[2])
                     : [more] "s"(more), [tch] "i"(M_TCH), [vt] "v"(vt), [tw] "s"(tw), [v0] "v"(cv), [cs] "s"(cs_), [three] "s"(three) : "scc", "memory");
        cnt = more; more = 0;
        asm volatile("" : "+s"(cnt), "+s"(more));
        SCHED();
    }
    } while (cnt != 0);
    if (!HALO) {
        asm volatile("s_cmp_lg_u32 %[l], 0\n\ts_cbranch_scc1 .Lex%=\n\ts_waitcnt vmcnt(0)\n\ts_nop 3\n\tglobal_store_dwordx4 %[d], %[c0], %[ws] sc1\n\ts_cmp_eq_u32 %[three], 0\n\ts_cbranch_scc1 .Lex%=\n\t"
                     "v_add_u32 %[d], 0x2000, %[d]\n\tglobal_store_dwordx4 %[d], %[c1], %[ws] sc1\n\tv_add_u32 %[d], 0x2000, %[d]\n\tglobal_store_dwordx4 %[d], %[c2], %[ws] sc1\n.Lex%=:\n\ts_nop 1"
                     : [d] "+v"(cdst) : [c0] "v"(cpy[0]), [c1] "v"(cpy[1]), [c2] "v"(cpy[2]), [l] "s"(l), [ws] "s"(cd_), [three] "s"(three) : "scc", "memory");
    }
    QSTAMP(1);
    FENCE();
    SCHED();

    f32x4 csq[4], m4[4], mh4; float csv[2], mr[4], rr[4], mh;
    auto r1 = [&](unsigned b, int off) { return *(const LAS float*)(lds(b) + off); };
    auto r4_ = [&](unsigned b, int off) { return *(const LAS f32x4*)(lds(b) + off); };
    auto mf = [&](int set, int i) {
        const int nt = i >> 2, mt = i & 3;
        if (i < 16) acc[mt][nt] = MFMA(wb[set][nt], xa[set][mt], acc[mt][nt]);
        else if (i < 24) acc[mt][nt] = MFMA(xa[set][mt], wb[set][nt], acc[mt][nt]);
        else if (i == 24) ah[0] = MFMA(wb[set][2], xh[set], ah[0]);
        else ah[1] = MFMA(xh[set], wb[set][4], ah[1]);
        SCHED();
    };
    auto fold = [&](int i, int e) {
        const int nt = i >> 2, mt = i & 3;
        float t;
        if (i < 16) t = __builtin_fmaf(mr[mt], csq[nt][e], acc[mt][nt][e]);
        else if (i < 24) t = __builtin_fmaf(m4[mt][e], csv[nt - 4], acc[mt][nt][e]);
        else if (i == 24) t = __builtin_fmaf(mh, csq[2][e], ah[0][e]);
        else t = __builtin_fmaf(mh4[e], csv[0], ah[1][e]);
        asm volatile("" : "+v"(t));
        if (i < 24) acc[mt][nt][e] = t; else ah[i - 24][e] = t;
    };
    constexpr Tail<HALO> TL{};
    constexpr int NP = HALO ? 6 : 2, FN = 4 * NM;
    static_assert(TL.v[3][TL.n[3]] == NP + FN + (HALO ? 2 : 0) && KT % NQA == 1 && KT % NQW == 0, "tail");
    s16x4 wl[6], wh[6];
    auto frd = [&](int set, int k, int ao) {
        if (k < 4) xa[set][k] = *(const LAS bf16x8*)(lds(set ? pa1 : pa0) + ao + k * 2048);
        else if (k == 16) xh[set] = *(const LAS bf16x8*)(lds(set ? ph1 : ph0) + ao);
        else {
            const int nt = (k - 4) >> 1;
            const LAS char* pb = lds((nt & 1) ? pb1 : pb0) + (nt >> 1) * 8192 + set * 4096;
            if (k & 1) { wh[nt] = tr_read(pb + 512); wb[set][nt] = __builtin_shufflevector(wl[nt], wh[nt], 0, 1, 2, 3, 4, 5, 6, 7); }
            else wl[nt] = tr_read(pb);
        }
    };
    constexpr int RS = M_RSTD - M_MEAN;
    unsigned vl, vg, tb, xr, xh_, b0a, qb0, qb1;
    f32x4 bprev, bcur, bprev0; u32x4 ps; u32x2 ko; float ninf;
    auto rdop = [&](int h, int k) {
        const int j = k - NH;
        if (h == 0) {
            if (k < 2) { mr[2 * k] = r1(sa0, k * 128); mr[2 * k + 1] = r1(sa0, k * 128 + 64); }
            else if (k < 4) csq[k - 2] = r4_((k & 1) ? sa3 : sa2, 0);
            else if (j < 4) frd(0, k - 4, 2 * QA_SLOT);
            else csq[j - 2] = r4_((j & 1) ? sa3 : sa2, 256);
        } else if (j < 0) frd(h & 1, k, h == 1 ? 2 * QA_SLOT : 0);
        else if (h == 1) {
            if (j < 2) csv[j] = r1(j ? sa7 : sa6, 0);
            else if (HALO && j == 2) mh = r1(sa4, 0);
            else if (HALO && j == 3) mh4 = r4_(sa5, 0);
            else { const int mt = HALO ? 7 - j : j - 2; m4[mt] = r4_(sa1, mt * 64); }
        } else if (h == 2) {
            if (j == 0) m4[2] = r4_(sa1, 128);
            if (j == 1) ko = *(const LAS u32x2*)(lds(tb) + 3072);
            if (j == 2) { rr[0] = r1(sa0, RS); rr[1] = r1(sa0, 64 + RS); }
            if (j == 3) { rr[2] = r1(sa0, 128 + RS); rr[3] = r1(sa0, 192 + RS); }
            if (j >= 4) m4[5 - j] = r4_(sa1, (5 - j) * 64);
        } else {
            if (j == 0) bprev = r4_(tb, 0);
            if (j == 1) bcur = r4_(tb, 1024);
            if (j == 2) ps = *(const LAS u32x4*)(lds(tb) + 2048);
            if (j == 3) bprev0 = r4_(b0a, 0);
        }
    };
    auto vop = [&](int k) {
        if (k == 0) { asm volatile("" : "+v"(boff[0])); qb0 = boff[0] + QW_TOP; asm volatile("" : "+v"(qb0)); }
        else if (k == 1) { asm volatile("" : "+v"(boff[1])); qb1 = boff[1] + QW_TOP; asm volatile("" : "+v"(qb1)); }
        else if (k < NP) {
            if (k == 2) { vl = (unsigned)(lane * 16); asm volatile("" : "+v"(vl)); }
            if (k == 3) { tb = vl + M_TAB; asm volatile("" : "+v"(tb)); }
            if (k == 4) { b0a = tb | b0m; asm volatile("" : "+v"(b0a)); }
            if (k == 5) { xr = xrs + vl; asm volatile("" : "+v"(xr)); }
        } else if (k < NP + FN) fold(TL.f[(k - NP) >> 2], (k - NP) & 3);
        else {
            if (k == NP + FN) { xh_ = xhs + vl; asm volatile("" : "+v"(xh_)); }
            if (k == NP + FN + 1) { ninf = -INFINITY; asm volatile("" : "+v"(ninf)); }
        }
    };
    unsigned wa, wq;
    int wr2 = wr, wc2 = wc;
#pragma unroll
    for (int i = 0; i < NM; ++i) {
        mf(0, TL.t[0][i]);
        if (TL.t[0][i] < 24) asm volatile("" : "+v"(acc[TL.t[0][i] & 3][TL.t[0][i] >> 2])); else asm volatile("" : "+v"(ah[TL.t[0][i] & 1]));
        FENCE();
        if (i < NH) frd(1, i, 0);
        if (!HALO && i == NH + 3) { asm volatile("" : "+s"(wr2), "+s"(wc2)); vl = (unsigned)(lane * 16); wq = (unsigned)(AT_Q + (wc2 * 8 + wr2 * 4) * 1024) + vl; asm volatile("" : "+v"(vl), "+v"(wq)); }
        if (!HALO && i == NH + 4) { asm volatile("" : "+s"(wr2), "+s"(wc2)); wa = (unsigned)(AT_OWN + wc2 * 16384 + wr2 * 4096) + vl; asm volatile("" : "+v"(wa)); }
        if (i == NH + 1) { asm volatile("" : "+v"(boff[0]), "+v"(boff[1])); pb0 = boff[0] + 2 * QW_SLOT; pb1 = boff[1] + 2 * QW_SLOT; asm volatile("" : "+v"(pb0), "+v"(pb1)); }
        if (i == NH + 2) { pa0 = aoff0; pa1 = aoff0 ^ xm; asm volatile("" : "+v"(pa1)); if (HALO) { ph0 = hoff0; ph1 = hoff0 ^ xm; asm volatile("" : "+v"(ph1)); } }
        SCHED();
    }
    WAIT_L0();
    BAR();
    FENCE();
    SCHED();
    auto frags = [&](int h) {
        static_assert(TL.d[0][NM] == NH + 6 && TL.d[1][NM] == NH + (HALO ? 5 : 6) && TL.d[2][NM] == NH + (HALO ? 6 : 0) && TL.d[3][TL.n[3]] == NH + (HALO ? 4 : 0), "reads");
        if (h == 0) __builtin_amdgcn_s_waitcnt(0xC27F);
        if (h == 1) __builtin_amdgcn_s_waitcnt(HALO ? 0xC57F : 0xC67F);
        if (h == 2) __builtin_amdgcn_s_waitcnt(HALO ? 0xC67F : 0xC07F);
        if (h == 3) __builtin_amdgcn_s_waitcnt(HALO ? 0xC47F : 0xC07F);
    };
#pragma unroll
    for (int h = 0; h < 4; ++h) {
        if (h == 2) { pb0 = qb0; pb1 = qb1; }
#pragma unroll
        for (int i = 0; i < TL.n[h]; ++i) {
            if (!(h & 1) && i == TL.n[h] - 1) { frags(h); SCHED(); if (h) QSTAMP(4); }
            mf(~h & 1, TL.t[h][i]);
#pragma unroll
            for (int k = TL.d[h][i]; k < TL.d[h][i + 1]; ++k) { FENCE(); rdop(h, k); }
            SCHED();
#pragma unroll
            for (int k = TL.v[h][i]; k < TL.v[h][i + 1]; ++k) vop(k);
            SCHED();
        }
        if (h & 1) {
            frags(h);
            if (h == 1) QSTAMP(2); else QSTAMP(5);
            BAR();
            FENCE();
            SCHED();
            if (h == 1) QSTAMP(3); else QSTAMP(6);
        }
    }
    if (!HALO) {
        f32x4 r4[4];
        mf(1, 8);
#pragma unroll
        for (int mt = 0; mt < 4; ++mt) rr[mt] = r1(sa0, mt * 64 + RS);
        SCHED();
        mf(1, 12);
#pragma unroll
        for (int mt = 0; mt < 4; ++mt) r4[mt] = r4_(sa1, mt * 64 + RS);
        SCHED();
        mf(1, 9);
        vg = HK ? (unsigned)hv<HK>(lane) : vl;
        if (HK) asm volatile("" : "+v"(vg));
        SCHED();
        u32x4 pk[8];
        auto op = [&](int g, int k) {
            const int ta = g < 4 ? g : (g - 4) & 2, tb = g < 4 ? g : ta + 1, na = g < 4 ? 2 : 4 + (g & 1), nb = g < 4 ? 3 : na;
            f32x4& A = acc[ta][na]; f32x4& Bv = acc[tb][nb];
            if (k < 12) {
                const int h = k / 6, kk = k % 6;
                f32x4& X = h ? Bv : A;
                const int tx = h ? tb : ta;
                if (kk < 4) { float t = X[kk] * (g < 4 ? rr[tx] : r4[tx][kk]); asm volatile("" : "+v"(t)); X[kk] = t; }
                else { unsigned t = cvt_pk_bf16(X[2 * (kk - 4)], X[2 * (kk - 4) + 1]); asm volatile("" : "+v"(t)); pk[g][2 * h + kk - 4] = t; }
            } else if (k == 12) __builtin_amdgcn_raw_buffer_store_b128(pk[g], rso, vg + hp<HK>(g & 3), (g >> 2) * (8192 << HK), 16);
            else *(LAS u32x4*)(lds(wa) + (g >> 2) * 8192 + (g & 3) * 1024) = pk[g];
            SCHED();
        };
        auto pm = [&](int i) {
            if (i < 16) mf(1, TL.h3[i]);
            else if (i < 32) mf(((i - 16) >> 1) & 1, ((i - 16) >> 2) + 4 * (i & 1));
        };
        pm(3);
#pragma unroll
        for (int g = 0; g < 8; ++g) {
#pragma unroll
            for (int k = 0; k < 6; ++k) op(g, k);
            pm(2 * g + 4);
            if (g > 0) op(g - 1, 12);
#pragma unroll
            for (int k = 6; k < 12; ++k) op(g, k);
            pm(2 * g + 5);
            if (g > 0 && g <= LWA) op(g - 1, 13);
        }
        op(7, 12);
        QSTAMP(7);
        u32x4 qk;
        auto qop = [&](int mt, int k) {
            if (k == 0) { rr[mt] *= QSCALE; asm volatile("" : "+v"(rr[mt])); }
            else if (k < 13) {
                const int h = (k - 1) / 6, kk = (k - 1) % 6;
                f32x4& X = acc[mt][h];
                if (kk < 4) { float t = X[kk] * rr[mt]; asm volatile("" : "+v"(t)); X[kk] = t; }
                else { unsigned t = cvt_pk_bf16(X[2 * (kk - 4)], X[2 * (kk - 4) + 1]); asm volatile("" : "+v"(t)); qk[2 * h + kk - 4] = t; }
            } else *(LAS u32x4*)(lds(wq) + mt * 1024) = qk;
            SCHED();
        };
#pragma unroll
        for (int mt = 0; mt < 3; ++mt) {
            pm(20 + 4 * mt); if (3 * mt + LWA < 8) op(3 * mt + LWA, 13); qop(mt, 0); qop(mt, 1); qop(mt, 2);
            pm(21 + 4 * mt); if (3 * mt + 1 + LWA < 8) op(3 * mt + 1 + LWA, 13); qop(mt, 3); qop(mt, 4); qop(mt, 5);
            pm(22 + 4 * mt); if (3 * mt + 2 + LWA < 8) op(3 * mt + 2 + LWA, 13); qop(mt, 6); qop(mt, 7); qop(mt, 8);
            pm(23 + 4 * mt); qop(mt, 9); qop(mt, 10); qop(mt, 11); qop(mt, 12); qop(mt, 13);
        }
        if (c.wid == 0) STAMP_AT(l * 8 + 1);
        QSTAMP(8);
        {
            const u32x2 q0 = pack4(acc[3][0] * (rr[3] * QSCALE)), q1 = pack4(acc[3][1] * (rr[3] * QSCALE));
            *(LAS u32x4*)(lds(wq) + 3 * 1024) = (u32x4){q0[0], q0[1], q1[0], q1[1]};
        }
        QSTAMP(9);
        QSTORE();
        if (K0E) {
            SCHED();
            WAIT_L0();
            BAR();
            FENCE();
            SCHED();
            if (KSP == 4) {
                publish<4, 0>(smem, lane, c.ws, (unsigned)WS_FLAGQ + (c.pq * 16 + c.jq) * FQS, lane == 0, (unsigned)(4 * l + 2));
                if (c.wid == 0) STAMP_AT(l * 8 + 2);
                SCHED();
            }
        }
    } else {
        typedef unsigned u32x10 __attribute__((ext_vector_type(10)));
        u32x4 kp[4], qp[4];
        u32x10 vw[2];
        float ks[2][4], cs[2][4];
        auto pk8 = [&](const f32x4& x, const f32x4& y) { const u32x2 a = pack4(x), b = pack4(y); u32x4 r = {a[0], a[1], b[0], b[1]}; asm volatile("" : "+v"(r)); SCHED(); return r; };
        auto pkv = [&](int mt, int cc) { const u32x2 a = pack4(acc[mt][4 + cc]); vw[cc][2 * mt + 2] = a[0]; vw[cc][2 * mt + 3] = a[1]; };
        auto fr8 = [](const u32x4& x) { return __builtin_bit_cast(bf16x8, x); };
        mf(1, 24);
        unsigned ka0 = ko[0] + wr * 256, ka1 = ko[1] + wr * 256;
        asm volatile("" : "+v"(ka0), "+v"(ka1));
        SCHED();
        mf(1, 25);
#pragma unroll
        for (int mt = 0; mt < 4; ++mt) ks[0][mt] = r1(ka0, mt * 64);
        rr[0] *= QSCALE; asm volatile("" : "+v"(rr[0]));
        SCHED();
        mf(1, 11);
#pragma unroll
        for (int mt = 0; mt < 4; ++mt) ks[1][mt] = r1(ka1, mt * 64);
        rr[1] *= QSCALE; asm volatile("" : "+v"(rr[1]));
        SCHED();
        mf(1, 15);
        rr[2] *= QSCALE; asm volatile("" : "+v"(rr[2]));
        rr[3] *= QSCALE; asm volatile("" : "+v"(rr[3]));
        SCHED();
        mf(1, 19);
        const u32x4 hh = pk8(ah[0], ah[1]);
        mf(1, 23);
        *(LAS u32x2*)lds(xh_) = (u32x2){hh[0], hh[1]}; *(LAS u32x2*)(lds(xh_) + 1024) = (u32x2){hh[2], hh[3]};
        SCHED();
        mf(1, 10);
        kp[3] = pk8(acc[3][2], acc[3][3]);
        mf(1, 14);
        *(LAS u32x4*)(lds(xr) + 2048) = kp[3];
        pkv(3, 0); asm volatile("" : "+v"(vw[0]));
        *(LAS u32x2*)(lds(xr) + 3072) = (u32x2){vw[0][8], vw[0][9]};
        SCHED();
        mf(1, 3);
        pkv(3, 1); asm volatile("" : "+v"(vw[1]));
        *(LAS u32x2*)(lds(xr) + 3072 + 8) = (u32x2){vw[1][8], vw[1][9]};
        SCHED();
        mf(1, 7);
        mf(1, 9);
        kp[2] = pk8(acc[2][2], acc[2][3]);
        mf(1, 13);
        qp[3] = pk8(acc[3][0], acc[3][1]);
        mf(1, 2);
        f32x4 s0[4], s1[4];
        u32x4 pk;
        auto sc = [&](int mt, int h) {
            if (h) { s1[mt] = MFMA(fr8(kp[mt]), fr8(qp[mt]), bcur); asm volatile("" : "+v"(s1[mt])); }
            else { s0[mt] = MFMA(fr8(mt ? kp[mt - (mt > 0)] : pk), fr8(qp[mt]), mt == 0 ? bprev0 : bprev); asm volatile("" : "+v"(s0[mt])); }
            SCHED();
        };
        auto csm = [&](int mt) { cs[0][mt] = ks[0][mt] * rr[mt]; asm volatile("" : "+v"(cs[0][mt])); cs[1][mt] = ks[1][mt] * rr[mt]; asm volatile("" : "+v"(cs[1][mt])); };
        float u[2][4], mx[4], iv[4];
        auto sv = [](float x) { asm volatile("" : "+v"(x)); return x; };
        auto mx2 = [&](float a, float b) { return max3(ninf, a, b); };
        auto pick = [&](int mt, int h) { u[h][mt] = sv(max3(mx2(s0[mt][2 * h], s0[mt][2 * h + 1]), s1[mt][2 * h], s1[mt][2 * h + 1]) * cs[h][mt]); };
        auto vf = [&](int i) { const int mt = 2 - (i >> 2), nt = 4 + (i & 1), set = (i >> 1) & 1; SCHED(); acc[mt][nt] = MFMA(xa[set][mt], wb[set][nt], acc[mt][nt]); asm volatile("" : "+v"(acc[mt][nt])); SCHED(); };
        mf(1, 6);
        kp[1] = pk8(acc[1][2], acc[1][3]);
        mf(1, 8);
        WAIT_L0();
        QSTAMP(7);
        BAR();
        FENCE();
        SCHED();
        QSTAMP(8);
        mf(1, 12);
        {
            const u32x2 k0 = *(const LAS u32x2*)(lds(xr) + 8);
            FENCE(); SCHED();
            const u32x2 k1 = *(const LAS u32x2*)lds(xr);
            FENCE(); SCHED();
            const u32x2 a = *(const LAS u32x2*)(lds(xr) + 1024 + 8);
            FENCE(); SCHED();
            const u32x2 b = *(const LAS u32x2*)(lds(xr) + 1024);
            pk = (u32x4){k0[0], k0[1], k1[0], k1[1]};
            vw[0][0] = a[0]; vw[0][1] = a[1]; vw[1][0] = b[0]; vw[1][1] = b[1];
        }
        SCHED();
        auto sw16 = [](float& a, float& b) { auto r = __builtin_amdgcn_permlane16_swap(__float_as_uint(a), __float_as_uint(b), false, false); a = __uint_as_float(r[0]); b = __uint_as_float(r[1]); };
        auto sw32 = [](float& a, float& b) { auto r = __builtin_amdgcn_permlane32_swap(__float_as_uint(a), __float_as_uint(b), false, false); a = __uint_as_float(r[0]); b = __uint_as_float(r[1]); };
        auto ldr = [&](int i) { };
        auto top = [&](int mt) { mx[mt] = sv(mx2(u[0][mt], u[1][mt])); };
        auto pv1 = [&](int mt, int k) {
            SCHED();
            vw[k >> 1][2 * mt + 2 + (k & 1)] = cvt_pk_bf16(acc[mt][4 + (k >> 1)][2 * (k & 1)], acc[mt][4 + (k >> 1)][2 * (k & 1) + 1]);
            asm volatile("" : "+v"(vw[k >> 1]));
            SCHED();
        };
        auto uk = [&](int mt) { SCHED(); u[0][mt] = sv(u[0][mt] * ks[0][mt]); u[1][mt] = sv(u[1][mt] * ks[1][mt]); SCHED(); };
        mf(1, 1);
        qp[2] = pk8(acc[2][0], acc[2][1]);
        mf(1, 5);
        csm(3); SCHED();
        mf(1, 0);
        kp[0] = pk8(acc[0][2], acc[0][3]);
        mf(1, 4);
        csm(2); SCHED();
        sc(3, 1);
        qp[1] = pk8(acc[1][0], acc[1][1]);
        sc(3, 0);
        csm(1); ldr(0); SCHED();
        sc(2, 1);
        qp[0] = pk8(acc[0][0], acc[0][1]);
        sc(2, 0);
        csm(0); pick(3, 0); SCHED();
        sc(1, 1);
        pick(3, 1); ldr(1); SCHED();
        sc(1, 0);
        pick(2, 0); top(3); SCHED();
        sc(0, 1);
        pick(2, 1); ldr(2); SCHED();
        sc(0, 0);
        if (c.wid == 0) STAMP_AT(l * 8 + 1);
        pick(1, 0); top(2);
        vf(0);
        pick(1, 1); ldr(3);
        vf(1);
        sw16(mx[2], mx[3]);
        pick(0, 0);
        vf(2);
        pick(0, 1); top(1);
        vf(3);
        top(0); mx[2] = sv(mx2(mx[2], mx[3]));
        vf(4);
        sw16(mx[0], mx[1]); mx[0] = sv(mx2(mx[0], mx[1]));
        vf(5); pv1(2, 0);
        sw32(mx[0], mx[2]);
        { const float a = mx[0], b = mx[2]; mx[0] = sv(mx2(a, b)); mx[2] = sv(mx2(b, a)); }
        vf(6); pv1(2, 1);
        sw32(mx[0], mx[2]);
        mx[3] = sv(mx[2]); mx[1] = sv(mx[0]);
        vf(7);
        sw16(mx[2], mx[3]); sw16(mx[0], mx[1]);
        SCHED();
#pragma unroll
        for (int mm = 0; mm < 4; ++mm) { const int mt = 3 - mm; u[0][mt] = sv(u[0][mt] - mx[mt]); u[1][mt] = sv(u[1][mt] - mx[mt]); if (mm == 1) vf(8); }
        vf(9);
#pragma unroll
        for (int mm = 0; mm < 4; ++mm) {
            const int mt = 3 - mm;
            u[0][mt] = sv(__builtin_amdgcn_exp2f(u[0][mt])); u[1][mt] = sv(__builtin_amdgcn_exp2f(u[1][mt]));
            if (mm > 0) iv[mt + 1] = sv(u[0][mt + 1] + u[1][mt + 1]);
            if (mm < 2) vf(10 + mm);
        }
        SCHED();
        sw16(iv[2], iv[3]);
        iv[0] = sv(u[0][0] + u[1][0]);
        iv[2] = sv(iv[2] + iv[3]);
        pv1(2, 2);
        sw16(iv[0], iv[1]); iv[0] = sv(iv[0] + iv[1]);
        uk(3);
        sw32(iv[0], iv[2]);
        iv[0] = sv(iv[0] + iv[2]);
        pv1(2, 3);
        iv[0] = sv(__builtin_amdgcn_rcpf(iv[0]));
        pv1(1, 0);
        iv[2] = sv(iv[0]);
        uk(2);
        sw32(iv[0], iv[2]);
        iv[3] = sv(iv[2]); iv[1] = sv(iv[0]);
        pv1(1, 1);
        sw16(iv[2], iv[3]); sw16(iv[0], iv[1]);
        SCHED();
        f32x4 o[4][2];
        u32x4 pf[4];
        auto nrm = [&](int mt) { u[0][mt] = sv(u[0][mt] * iv[mt]); u[1][mt] = sv(u[1][mt] * iv[mt]); SCHED(); };
        auto prb = [&](int mt) {
            const unsigned p = cvt_pk_bf16(u[0][mt], u[1][mt]);
            pf[mt] = (u32x4){__builtin_amdgcn_perm(0u, p, ps[0]), __builtin_amdgcn_perm(0u, p, ps[1]), __builtin_amdgcn_perm(0u, p, ps[2]), __builtin_amdgcn_perm(0u, p, ps[3])};
            asm volatile("" : "+v"(pf[mt]));
            SCHED();
        };
        auto pvm = [&](int mt, int cc) {
            o[mt][cc] = MFMA(fr8((u32x4){vw[cc][2 * mt], vw[cc][2 * mt + 1], vw[cc][2 * mt + 2], vw[cc][2 * mt + 3]}), fr8(pf[mt]), ((f32x4){0.f, 0.f, 0.f, 0.f}));
            asm volatile("" : "+v"(o[mt][cc]));
            SCHED();
        };
        auto out = [&](int mt) {
            const u32x2 lo = pack4(o[mt][0]), hi = pack4(o[mt][1]);
            __builtin_amdgcn_raw_buffer_store_b128((u32x4){lo[0], lo[1], hi[0], hi[1]}, rso, vg + hp<HO>(mt), 0, 16);
            SCHED();
        };
        vg = (unsigned)hv<HO>(lane);
        asm volatile("" : "+v"(vg));
        nrm(3); nrm(2); prb(3);
        pv1(1, 2); pv1(1, 3);
        pvm(3, 0); prb(2); pvm(3, 1);
        uk(1); uk(0);
        pvm(2, 0); nrm(1); nrm(0); pvm(2, 1);
        prb(1); pv1(0, 0); pv1(0, 1);
        pvm(1, 0); pv1(0, 2); pv1(0, 3); prb(0);
        pvm(1, 1); out(3); pvm(0, 0); out(2); pvm(0, 1); out(1); out(0);
        SCHED();
        QSTAMP(9);
        if (c.wid == 0) STAMP_AT(l * 8 + 2);
        QSTORE();
    }
}

#define MFMA32(a, b, c) __builtin_amdgcn_mfma_f32_32x32x16_bf16(__builtin_bit_cast(bf16x8, a), __builtin_bit_cast(bf16x8, b), (c), 0, 0, 0)
DI float max16(const f32x16& s) {
    float m = max3(s[0], s[1], s[2]), n = max3(s[3], s[4], s[5]);
    m = max3(m, s[6], s[7]); n = max3(n, s[8], s[9]);
    m = max3(m, s[10], s[11]); n = max3(n, s[12], s[13]);
    m = max3(max3(m, s[14], s[15]), n, n);
    auto b = __builtin_amdgcn_permlane32_swap(__float_as_uint(m), __float_as_uint(m), false, false);
    return __builtin_amdgcn_fmed3f(__uint_as_float(b[0]), __uint_as_float(b[1]), INFINITY);
}
template <bool HI> DI void attn_a(const Ctx& c0, int l, const char* Wo, KP kp) {
    const Ctx c = fresh(c0);
    asm volatile("" : "+s"(Wo));
    LAS char* smem = c.smem;
    const int lane = c.lane, w = c.wid;
    constexpr int H = HI ? 0 : 1;
    int qt = HI ? w - 4 : 3 - w;
    asm volatile("" : "+s"(qt));
    const float NEG = -INFINITY;
    int zq = (lane & 31) - 4 * (lane >> 5);
    unsigned lb = (unsigned)(((lane >> 4) & 1) * 1024 + ((lane >> 5) * 16 + (lane & 15)) * 16);
    unsigned qa = (unsigned)(AT_Q + H * 8192 + qt * 2048) + lb, ta = (unsigned)(AT_OWN + H * 16384 - 3 * 2048 + qt * 2048) + lb;
    asm volatile("" : "+v"(lb), "+v"(qa), "+v"(ta));
    auto rd = [&](unsigned base, int off) { return *(const LAS u32x4*)(lds(base) + off); };
    f32x16 ce;
    u32x4 sel;
    unsigned tm = (unsigned)(AT_N + lane * 16);
    auto masks = [&]() {
        if (HI) {
#pragma unroll
            for (int r = 0; r < 16; ++r) ce[r] = 8 * (r >> 2) + (r & 3) <= zq ? 0.f : NEG;
            asm volatile("" : "+v"(ce));
        }
        const unsigned s1 = ((lane >> 4) ^ (lane >> 2)) & 1 ? 0u : 0x3f803f80u;
        sel = (u32x4){s1, s1, s1, s1};
        asm volatile("" : "+v"(sel));
    };
    u64 ep, pa_, kvv, nkv, kact;
    unsigned po, pk_, cnta = M_CNT, one = 1u;
    char* ob;
    const char *kvb, *s1_, *s2_;
    unsigned vk, dst, fo;
    auto setup = [&]() {
        if (HI) {
            const u32x2 e = *(const LAS u32x2*)(smem + M_EP);
            ep = ((u64)(unsigned)__builtin_amdgcn_readfirstlane(e[1]) << 32) | (unsigned)__builtin_amdgcn_readfirstlane(e[0]);
            po = o_post(c);
            pk_ = pick(lane < 2, (unsigned)(WS_KBOX + ((c.pq + 1 + lane) * 16 + c.jq) * 256 + lane * 8), (unsigned)(WS_FLAGQ + (c.pq * 16 + c.jq) * FQS));
            pa_ = c.pq < 14 ? 7ull : (c.pq < 15 ? 5ull : 4ull);
            fo = (unsigned)(WS_KBOX + (c.pq * 16 + c.jq) * 256 + lane * 8);
            asm volatile("" : "+v"(po), "+v"(pk_), "+v"(fo));
            asm volatile("" : "+s"(ep), "+s"(pa_));
        }
        ob = c.ws + WS_O + (size_t)l * (8u << 20) + (size_t)(unsigned)(((c.jq * 2 + H) * 16 + c.pq) * 8192 + qt * 2048);
        kvb = c.ws + WS_KV + (size_t)(unsigned)((l >> 1) * (16u << 20) + c.jq * 32768);
        asm volatile("" : "+s"(ob), "+s"(kvb));
    };
    auto setup2 = [&]() {
        const int p1 = c.pq >= 1 ? c.pq - 1 : c.pq, p2 = c.pq >= 2 ? c.pq - 2 : c.pq;
        s1_ = kvb + (size_t)(unsigned)(p1 * 524288);
        s2_ = kvb + (size_t)(unsigned)(p2 * 524288);
        vk = (unsigned)((w & 3) * 1024 + lane * 16); dst = (unsigned)(AT_N + (w & 3) * 1024);
        asm volatile("" : "+s"(s1_), "+s"(dst));
        if (!HI) asm volatile("" : "+s"(s2_));
        asm volatile("" : "+v"(vk));
    };
    auto arrive = [&](auto cc, unsigned msk, unsigned val) {
        unsigned old = 0;
        if (lane == 0) asm volatile("ds_add_rtn_u32 %0, %1, %2 offset:%3\n\ts_waitcnt lgkmcnt(0)" : "+v"(old) : "v"(cnta), "v"(one), "i"(decltype(cc)::value * 4) : "memory");
        if ((__builtin_amdgcn_readfirstlane(old) & msk) == msk) { if (lane < 16) post(c.ws, po, ep + (4 * l + val)); }
    };
    static_assert((MBOX & 2) && !HO && !HK, "layout");
    int tv = 0;
    auto kvs = [&]() {
        if (!HI) {
            SCHED();
            LSTAMP(2);
            asm volatile("s_mov_b64 exec, 1\n\ts_waitcnt vmcnt(0)\n\tds_add_u32 %0, %1\n\ts_mov_b64 exec, -1" :: "v"(cnta), "v"(one) : "memory");
            LSTAMP(3);
            SCHED();
        }
    };
    asm volatile("" : "+v"(cnta), "+v"(one), "+v"(tm));
    if (HI) {
        masks(); setup(); setup2();
        if (w == 5) {
#pragma unroll
            for (int i = 0; i < 4; ++i) *(LAS f32x4*)(lds(tm) + i * 1024) = (f32x4){ce[4 * i], ce[4 * i + 1], ce[4 * i + 2], ce[4 * i + 3]};
        }
        kvv = ep + (4 * l + 2); nkv = 0 - kvv;
        kact = __builtin_amdgcn_ballot_w64(w == KPW && lane < 2 && lane < c.pq);
        asm volatile("" : "+v"(kvv), "+v"(nkv));
        asm volatile("" : "+s"(kact));
        WAIT_L0();
        WAIT_V0();
        LSTAMP(0);
        if (w == 4) __builtin_amdgcn_s_setprio(HP4);
        BAR();
        FENCE();
    }
    auto kvp = [&]() {
        if (HI && w == 4) {
            __builtin_amdgcn_s_setprio(3);
#pragma unroll 1
            for (unsigned spins = 0; spins < (1u << 24); ++spins) {
                if (__builtin_amdgcn_readfirstlane(*(volatile LAS int*)lds(cnta)) == 8 * l + 4) break;
            }
            if (__builtin_amdgcn_inverse_ballot_w64(pa_)) post(c.ws, pk_, kvv);
            STAMP_AT(l * 8 + 2);
            __builtin_amdgcn_s_setprio(HP4);
        }
        SCHED();
    };
    if (KPP == -1) kvp();
    FENCE();
    SCHED();
    LSTAMP(1);
    u32x4 q0, q1, k0, k1, va, vp, pa, pb;
    f32x16 cm, s0, s1, o;
    f32x4 ls;
    auto stage = [&](f32x16& sc, f32x16& sn, const f32x16& cn, bool pend, bool zero, unsigned kb, int ko, unsigned vb, int vo1, unsigned nb, int vo0) {
        auto ex = [&](int i) { float x = __builtin_amdgcn_exp2f(sc[i]); asm volatile("" : "+v"(x)); sc[i] = x; SCHED(); };
        auto cv = [&](u32x4& p, int j, int i) { unsigned x = cvt_pk_bf16(sc[i], sc[i + 1]); asm volatile("" : "+v"(x)); p[j] = x; SCHED(); };
        sn = MFMA32(k0, q0, cn); SCHED();
        k0 = rd(kb, ko); SCHED();
        ex(0); ex(1); ex(2);
        if (pend) { o = MFMA32(vp, pb, o); SCHED(); }
        ex(3); ex(8); ex(9);
        sn = MFMA32(k1, q1, sn); SCHED();
        k1 = rd(kb, ko + 512); SCHED();
        ex(10); ex(11); cv(pa, 0, 0); cv(pa, 1, 2);
        if (pend) { ls = MFMA(__builtin_bit_cast(bf16x8, sel), __builtin_bit_cast(bf16x8, pb), ls); SCHED(); }
        vp = rd(vb, 8192 + vo1 + 512); SCHED();
        cv(pa, 2, 8); cv(pa, 3, 10); ex(4); ex(5);
        o = MFMA32(va, pa, zero ? (f32x16)(0.f) : o); SCHED();
        va = rd(nb, 8192 + vo0); SCHED();
        ex(6); ex(7); ex(12);
        ls = MFMA(__builtin_bit_cast(bf16x8, sel), __builtin_bit_cast(bf16x8, pa), zero ? (f32x4)(0.f) : ls); SCHED();
        ex(13); ex(14); ex(15); cv(pb, 0, 4); cv(pb, 1, 6); cv(pb, 2, 12); cv(pb, 3, 14);
    };
    auto flush = [&]() {
        ls = MFMA(__builtin_bit_cast(bf16x8, sel), __builtin_bit_cast(bf16x8, pb), ls); SCHED();
        o = MFMA32(vp, pb, o); SCHED();
    };
    float nm;
    {
        if (!HI) {
#pragma unroll
            for (int i = 0; i < 4; ++i) {
                const f32x4 t = *(const LAS f32x4*)(lds(tm) + i * 1024);
                ce[4 * i] = t[0]; ce[4 * i + 1] = t[1]; ce[4 * i + 2] = t[2]; ce[4 * i + 3] = t[3];
            }
        }
        q0 = rd(qa, 0);
        const u32x4 d0 = rd(ta, 6144);
        q1 = rd(qa, 512);
        const u32x4 d1 = rd(ta, 6144 + 512);
        k0 = rd(ta, 4096); k1 = rd(ta, 4096 + 512); va = rd(ta, 8192 + 6144);
        SCHED();
        if (KSP == -1) kvs();
        if (!HI) { masks(); setup(); SCHED(); }
        s0 = MFMA32(d0, q0, ce);
        s0 = MFMA32(d1, q1, s0);
        SCHED();
        if (!HI) { setup2(); SCHED(); }
        if (KSP == 0) kvs();
        if (KPP == 0) kvp();
        nm = -max16(s0);
        asm volatile("" : "+v"(nm));
#pragma unroll
        for (int r = 0; r < 16; ++r) { float t; asm("v_mov_b32 %0, %1" : "=v"(t) : "v"(nm), "i"(r)); cm[r] = t; }
        asm volatile("" : "+v"(cm));
        SCHED();
#pragma unroll
        for (int r = 0; r < 16; ++r) s0[r] = addf(s0[r], nm);
        asm volatile("" : "+v"(s0));
        SCHED();
        if (KSP == 1) kvs();
        if (KPP == 1) kvp();
        stage(s0, s1, cm, false, true, ta, 2048, ta, 6144, ta, 4096);
        if (KSP == 2) kvs();
        if (KPP == 2) kvp();
        if (qt > 0) {
            stage(s1, s0, cm, true, false, ta, 0, ta, 4096, ta, 2048);
            if (KSP == 3) kvs();
            if (qt > 1) {
                stage(s0, s1, cm, true, false, ta, 0, ta, 2048, ta, 0);
                if (qt > 2) stage(s1, s0, cm, true, false, ta, 0, ta, 0, ta, 0);
            }
        } else if (KSP == 3) kvs();
        flush();
    }
    LSTAMP(4);
    f32x16 c2;
    unsigned na, nb;
    auto rso = __builtin_amdgcn_make_buffer_rsrc(ob, 0, 2048, 0x00020000);
    auto starts = [&]() {
        const float n1 = c.pq >= 1 ? nm : NEG, n2 = c.pq >= 2 ? nm : NEG;
        asm volatile("" : "+v"(zq));
#pragma unroll
        for (int r = 0; r < 16; ++r) { cm[r] = n1; c2[r] = n2; ce[r] = 8 * (r >> 2) + (r & 3) <= zq ? NEG : n2; }
        na = (unsigned)(AT_N + H * 32768) + lb; nb = na + (unsigned)(16384 + qt * 2048);
        asm volatile("" : "+v"(cm), "+v"(c2), "+v"(ce), "+v"(na), "+v"(nb));
        asm volatile("" : "+s"(rso));
        SCHED();
    };
    {
        auto piece = [&](int b, int i) { dma16<true>(dst + b * 16384 + i * 4096, vk, ((b & 1) ? s2_ : s1_) + (b >> 1) * 16384 + i * 4096); };
        auto block = [&](int b) {
#pragma unroll
            for (int i = 0; i < 4; ++i) piece(b, i);
        };
        Ask t;
        if (HI) {
            poll_rmw<true>(t, c.ws, fo, __builtin_amdgcn_inverse_ballot_w64(kact), nkv, 62 - 4 * l);
            if (w == KPW) STAMP_AT(32 + l * 8 + 1);
        }
        WAIT_L0();
        LSTAMP(5);
        BAR();
        FENCE();
        LSTAMP(6);
        piece(0, H); piece(0, 2 + H);
        if (HI) {
            starts();
            if (APRIO) __builtin_amdgcn_s_setprio(APRIO);
            WAIT_V0();
            keep(t);
            LSTAMP(7);
            BAR();
            FENCE();
            if (w == 4) STAMP_AT(l * 8 + 3);
        } else {
            if (AB2 >= 0) block(1);
            if (AB2 > 0) block(2);
            starts();
            Ctx c2 = c;
            if (ELO) c2.wid = 0;
            unsigned vw = wo_w_off(c2);
            int qs = c.cw == 3 ? 1024 : 8 * D_ * 2;
            asm volatile("" : "+v"(vw));
            asm volatile("" : "+s"(qs));
            SCHED();
            wait_v<4 * AB2 + 4>();
            LSTAMP(7);
            BAR();
            if (AB2 < 0) block(1);
            if (AB2 <= 0) block(2);
            block(3);
            wait_v<8>();
            BAR();
            if (!ELO) {
                wo_w_issue<WA0, WA1>(c, Wo, vw);
                wait_v<WA1 - WA0>();
            } else WAIT_V0();
            LSTAMP(9);
            BAR();
            FENCE();
            if (!ELO) wo_w_issue<WA1, 26>(c, Wo, vw);
            else {
                const int s0 = w == 0 ? WA0 : (w == 1 ? WA0 + EN0 : WA0 + EN0 + EN1), s1 = w == 0 ? WA0 + EN0 : (w == 1 ? WA0 + EN0 + EN1 : (w == 2 ? 22 : 0));
#pragma unroll 1
                for (int sl = s0; sl < s1; ++sl) {
                    const char* b = Wo + (size_t)(unsigned)((2 * (sl & 15) + (sl >> 4)) * 65536);
#pragma unroll
                    for (int q = 0; q < 4; ++q) dma16<true>(sl * 4096 + q * 1024, vw, b + q * qs);
                }
            }
            if (LSL) { for (int i = 0; i < qt; ++i) __builtin_amdgcn_s_sleep(LSL); }
        }
    }
    SCHED();
    LSTAMP(8);
    {
        const u32x4 d0 = rd(na, 0), d1 = rd(na, 512);
        k0 = rd(na, 2048); k1 = rd(na, 2048 + 512); va = rd(na, 8192);
        SCHED();
        s0 = MFMA32(d0, q0, cm);
        s0 = MFMA32(d1, q1, s0);
        SCHED();
        stage(s0, s1, cm, false, false, na, 4096, na, 0, na, 2048);
        stage(s1, s0, cm, true, false, na, 6144, na, 2048, na, 4096);
        if (HI) { BAR(); FENCE(); SCHED(); }
        stage(s0, s1, cm, true, false, nb, 0, na, 4096, na, 6144);
        if (HI && CPOS == 3) { BAR(); SCHED(); }
        stage(s1, s0, ce, true, false, nb, 2048, na, 6144, nb, 0);
        if (HI && CPOS == 4) { WAIT_L0(); BAR(); SCHED(); }
        stage(s0, s1, c2, true, false, nb, 4096, nb, 0, nb, 2048);
        if (HI && CPOS == 5) { BAR(); SCHED(); }
        if (qt < 3) {
            stage(s1, s0, c2, true, false, nb, 6144, nb, 2048, nb, 4096);
            if (qt < 2) {
                stage(s0, s1, c2, true, false, nb, 0, nb, 4096, nb, 6144);
                if (qt < 1) stage(s1, s0, c2, true, false, nb, 0, nb, 6144, nb, 0);
            }
        }
        flush();
    }
    LSTAMP(11);
    auto out = [&]() {
        const float iv = __builtin_amdgcn_rcpf(ls[0]);
#pragma unroll
        for (int u = 0; u < 2; ++u) {
            u32x4 t;
#pragma unroll
            for (int i = 0; i < 4; ++i) { const int r = 4 * u + (i >> 1) * 8 + (i & 1) * 2; t[i] = cvt_pk_bf16(o[r] * iv, o[r + 1] * iv); }
            __builtin_amdgcn_raw_buffer_store_b128(t, rso, lb + u * 512, 0, 16);
        }
        SCHED();
    };
    out();
    if (__builtin_expect(__any(!(ls[0] < SAFE_SUM)), 0)) {
        auto rk = __builtin_amdgcn_make_buffer_rsrc((void*)(kvb + H * 16384), 0, 16u << 20, 0x00020000);
        const int g0 = c.pq * 4 + qt - 8;
        float m = NEG;
#pragma unroll 1
        for (int ps = 0; ps < 2; ++ps) {
            o = (f32x16)(0.f); ls = (f32x4)(0.f);
#pragma unroll 1
            for (int j = g0 < 0 ? -g0 : 0; j < 9; ++j) {
                const int so = ((g0 + j) >> 2) * 524288 + ((g0 + j) & 3) * 2048;
                u32x4 f[4];
#pragma unroll
                for (int i = 0; i < 4; ++i) f[i] = __builtin_amdgcn_raw_buffer_load_b128(rk, lb + (i & 1) * 512, so + (i >> 1) * 8192, 16);
                f32x16 s;
                const float sb = ps ? -m : 0.f;
                float sa = j == 0 ? NEG : sb, sz = j == 8 ? NEG : sb;
                asm volatile("" : "+v"(zq), "+v"(sa), "+v"(sz));
#pragma unroll
                for (int r = 0; r < 16; ++r) s[r] = 8 * (r >> 2) + (r & 3) <= zq ? sa : sz;
                s = MFMA32(f[0], q0, s);
                s = MFMA32(f[1], q1, s);
                if (ps == 0) m = fmaxf(m, max16(s));
                else {
                    u32x4 p[2];
#pragma unroll
                    for (int r = 0; r < 16; r += 2) p[(r >> 2) & 1][(r >> 3) * 2 + ((r >> 1) & 1)] = cvt_pk_bf16(__builtin_amdgcn_exp2f(s[r]), __builtin_amdgcn_exp2f(s[r + 1]));
#pragma unroll
                    for (int u = 0; u < 2; ++u) {
                        o = MFMA32(f[2 + u], p[u], o);
                        ls = MFMA(__builtin_bit_cast(bf16x8, sel), __builtin_bit_cast(bf16x8, p[u]), ls);
                    }
                }
            }
        }
        __builtin_amdgcn_s_waitcnt(0x0F70);
        WAIT_V0();
        out();
    }
    SCHED();
    if (w == 3 || w == 4) STAMP_AT(32 + l * 8 + 3 + w);
    LSTAMP(12);
    if (HI) {
        WAIT_V0();
        arrive(std::integral_constant<int, 1>{}, 3u, 3u);
        if (w == 4) STAMP_AT(32 + l * 8 + 4);
        LSTAMP(13);
        if (APRIO) __builtin_amdgcn_s_setprio(0);
        if (lane == 0) asm volatile("ds_add_u32 %0, %1 offset:12" :: "v"(cnta), "v"(one) : "memory");
        WAIT_V0();
        arrive(std::integral_constant<int, 2>{}, 7u, 4u);
        if (w == 5 || w == 6) {
            Ctx c2 = c;
            c2.wid = 2 * w - 10;
            unsigned vw = wo_w_off(c2), vw2 = vw + (c.cw == 3 ? 1024u : 8u * D_ * 2);
            asm volatile("" : "+v"(vw), "+v"(vw2));
#pragma unroll 1
            for (unsigned spins = 0; spins < (1u << 22); ++spins) {
                if (__hip_atomic_load((LAS unsigned*)(smem + M_CNT + 12), __ATOMIC_RELAXED, __HIP_MEMORY_SCOPE_WORKGROUP) == (unsigned)(2 * l + 4)) break;
                __builtin_amdgcn_s_sleep(1);
            }
            FENCE();
            wo_w_issue<ELO ? 22 : 26, 30>(c2, Wo, vw);
            c2.wid += 1;
            wo_w_issue<ELO ? 22 : 26, 30>(c2, Wo, vw2);
        }
    }
    LSTAMP(23);
    ASTORE();
}

DI void wo_load(const Ctx& c0, int l, const char* Wn, const char* Wo) {
    const Ctx c = fresh(c0);
    asm volatile("" : "+s"(Wn), "+s"(Wo));
    const u64 ep = lds_epoch(c.smem);
    const unsigned fo = o_find(c);
    if ((CP2 & 1) && l == 0 && c.wid == 4) code_again(c);
    if ((CP2 & 2) && l == 1 && c.wid == 4) code_again(c);
        if (l == 0 && c.wid == 6) {
            const int fr = c.lane & 15, od = fr & 1, j0 = 4 * (c.lane >> 4) + od;
            const bool p0 = j0 > fr, p1 = j0 + 2 > fr;
            const unsigned N = 0xff800000u, Z = 0x0c0c0c0cu, lo = od ? 0x01000c0cu : 0x0c0c0100u, hi = od ? 0x03020c0cu : 0x0c0c0302u;
            LAS u32x4* t = (LAS u32x4*)(c.smem + M_TAB + c.lane * 16);
            t[0] = (u32x4){p0 && !od ? 0u : N, p0 && od ? 0u : N, p1 && !od ? 0u : N, p1 && od ? 0u : N};
            t[64] = (u32x4){!p0 && !od ? 0u : N, !p0 && od ? 0u : N, !p1 && !od ? 0u : N, !p1 && od ? 0u : N};
            t[128] = (u32x4){p0 ? lo : Z, p1 ? hi : Z, p0 ? Z : lo, p1 ? Z : hi};
            t[192] = (u32x4){(unsigned)(M_RSTD + j0 * 4 + (p0 ? 0 : 64)), (unsigned)(M_RSTD + j0 * 4 + 8 + (p1 ? 0 : 64)), 0u, 0u};
        }
    if (CPY && l == 0 && c.wid == CKW) {
#pragma unroll 1
        for (int i = 0; i < 2; ++i) {
            const int j = i ? c.jw : c.jq, g = i ? 2 - 2 * (j >> 3) : 1 + 2 * (j >> 3);
            if ((j & 7) == 3) poll_flags(c.ws, ep, (unsigned)WS_FLAGQ + ((c.lane >> 2) * 16 + 4 * g + (c.lane & 3)) * FQS, true, 2u);
        }
    }
    Ask t, t2;
    if (!(l & 1)) {
        Ctx c2 = c;
        c2.wid = 2 * c.wid - 10;
        unsigned vw = wo_w_off(c2), vw2 = vw + (c.cw == 3 ? 1024u : 8u * D_ * 2);
        asm volatile("" : "+v"(vw), "+v"(vw2));
        poll_rmw(t, c.ws, fo, c.wid == 7 && c.lane < 16, ep + (4 * l + 3), 61 - 4 * l);
        if (c.wid == 7) STAMP_AT(32 + l * 8 + 2);
        BAR();
        if (c.wid == 5 || c.wid == 6) {
            __builtin_amdgcn_s_sleep(W30S);
            wo_w_issue<30, 32>(c2, Wo, vw);
            c2.wid += 1;
            wo_w_issue<30, 32>(c2, Wo, vw2);
        }
        if (c.wid != 7) WAIT_V0();
    } else {
        asm volatile("" : "=v"(t.v[0]), "=v"(t.v[1]), "=v"(t.v[2]), "=v"(t.v[3]));
        WAIT_V0();
    }
    poll_rmw(t2, c.ws, fo, c.wid == 7 && c.lane < 16, ep + (4 * l + 4), 60 - 4 * l);
    if (c.wid == 7) STAMP_AT(32 + l * 8 + 2 + !(l & 1));
    BAR();
    WAIT_V0();
    keep(t); keep(t2);
    if (l == 3 && c.bid == 0 && c.wid == 7) {
#pragma unroll 1
        for (int i = 0; i < 4; ++i) poll_flags(c.ws, ep, (unsigned)WS_FLAGQ + (i * 64 + c.lane) * FQS, true, 1u);
        if (c.lane == 0) __hip_atomic_store((GAS u64*)(c.ws + WS_EPOCH), ep + 64, __ATOMIC_RELAXED, __HIP_MEMORY_SCOPE_AGENT);
    }
    if (RS0 && l == 3) BAR();
}

DI void wo_consume(const bool B, const Ctx& c0, const bf16_t* xin, bf16_t* xout, int l) {
    u32x4 res[2][2];
    const Ctx c = fresh(c0);
    LAS char* smem = c.smem;
    const int lane = c.lane, w = c.wid, fr = lane & 15, fq = lane >> 4;
    const char* obase = c.ws + WS_O + (size_t)l * (8u << 20);
    int ovo = (c.pw * 8 + w * 2) * 1024 + lane * 16;
    asm volatile("" : "+v"(ovo));
    constexpr int NP = WNQ, R = 3;
    static_assert(!HO && !HX && !HS && !RLD && BPR == 1, "layout");
    auto ro = __builtin_amdgcn_make_buffer_rsrc((void*)obase, 0, 4u << 20, 0x00020000);
    asm volatile("" : "+s"(ro));
    u32x4 xa[NP][2];
    s16x4 wf[R][8];
    f32x4 acc[2][4];
    unsigned bw[8];
    int coff, rmo, spr;
    u32x4 on = {0x3f803f80u, 0x3f803f80u, 0x3f803f80u, 0x3f803f80u};
    unsigned sto, xpo, coff1, cnta = M_CNT, one = 1;
    u64 xv, b0, b1, am;
    auto rs = __builtin_amdgcn_make_buffer_rsrc(xout, 0, S_ * D_ * 2, 0x00020000);
    auto rst = __builtin_amdgcn_make_buffer_rsrc(c.ws + WS_STATS + (size_t)((l + 1) & 3) * 524288, 0, 524288, 0x00020000);
    auto prep2 = [&]() {
        sto = fq == (fr >> 2) && l < 3 ? (unsigned)((c.pw * 16 + c.jw) * 1024 + (w * 32 + fr) * 8) : 0x80000000u;
        xpo = x_post(c);
        coff = (int)pick(l == 3, (unsigned)rmo, (unsigned)coff);
        coff1 = coff + (l == 3 ? xrow<0>(0, 16) : 2048);
        spr = l == 3 ? 64 : 1024;
        asm volatile("" : "+s"(spr));
        b0 = __builtin_amdgcn_ballot_w64(fr & 1); b1 = __builtin_amdgcn_ballot_w64(fr & 2); am = __builtin_amdgcn_ballot_w64(x_act(c, !B));
        asm volatile("" : "+v"(on), "+v"(sto), "+v"(xpo), "+v"(coff), "+v"(coff1), "+v"(xv), "+v"(cnta), "+v"(one));
        asm volatile("" : "+s"(rs), "+s"(rst), "+s"(b0), "+s"(b1), "+s"(am));
    };
    auto prep = [&]() {
        int ln = lane;
        asm volatile("" : "+v"(ln));
        const int fr = ln & 15, fq = ln >> 4, q4 = fr >> 2, p4 = ln & 3;
        coff = (c.pw * 16 + c.jw) * 16384 + w * 4096 + (((fq & 1) * 2 + (fq >> 1)) * 16 + fr) * 16;
        rmo = xrow<0>(c.pw, w * 32 + fr) + xcol<0>(c.jw) + (fq & 1) * 32 + (fq >> 1) * 16;
        asm volatile("" : "+v"(coff), "+v"(rmo));
        const int fb = (q4 & 2) | ((fq & 1) << 2);
        const unsigned swp = (B && w >= 2) ? 2048u : 0u;
#pragma unroll
        for (int cc = 0; cc < 4; ++cc) {
            const unsigned b = (unsigned)((4 * fq + q4) * 128 + (((2 * cc + (p4 >> 1)) ^ fb) << 4) + (p4 & 1) * 8);
            bw[2 * cc] = b + swp; bw[2 * cc + 1] = b + (swp ^ 2048u);
            asm volatile("" : "+v"(bw[2 * cc]), "+v"(bw[2 * cc + 1]));
        }
    };
    auto start = [&](int mt, int pr) {
            {
                auto r0 = __builtin_amdgcn_permlane16_swap(res[mt][pr][0], res[mt][pr][2], false, false);
                auto r1 = __builtin_amdgcn_permlane16_swap(res[mt][pr][1], res[mt][pr][3], false, false);
#pragma unroll
                for (int h = 0; h < 2; ++h) {
                    acc[mt][2 * pr + h] = (f32x4){bf_lo(r0[h]), bf_hi(r0[h]), bf_lo(r1[h]), bf_hi(r1[h])};
                    asm volatile("" : "+v"(acc[mt][2 * pr + h]));
                }
            }
    };
    auto rdw = [&](auto ic) {
        constexpr int i = decltype(ic)::value, h = i >> 3, k = i & 7;
        wf[h % R][k] = tr_read(lds(bw[k]) + (h & 15) * 4096);
        if constexpr (h == 15) { bw[k] += 65536; asm volatile("" : "+v"(bw[k])); }
    };
    {
        auto resid = [&]() {
            const unsigned roff = pick(l != 0, (unsigned)coff, (unsigned)rmo);
            const char* xp = (const char*)xin;
#pragma unroll
            for (int i = 0; i < 4; ++i) aload4c(res[i >> 1][i & 1], roff, xp + (i >> 1) * (l ? 2048 : xrow<0>(0, 16)) + (i & 1) * (l ? 1024 : 64));
            SCHED();
        };
        unsigned opo = o_post(c), oca = B ? M_CNT : M_CNT + 8, old = 0, msk = B ? 3u : 7u;
        xv = lds_epoch(smem) + (unsigned)(l + 2);
        u64 ov = xv + (unsigned)(3 * l + 2);
        prep();
        asm volatile("" : "+v"(opo), "+v"(oca), "+v"(ov), "+v"(old), "+v"(one));
        asm volatile("" : "+s"(msk));
        SCHED();
        if (RBP) resid();
        static_for<0, 16>([&](auto ic) { rdw(ic); SCHED(); });
        if (!P2A) prep2();
        SCHED();
        wait_v<RBP ? 4 : 0>();
        if (lane == 0) asm volatile("ds_add_rtn_u32 %0, %1, %2\n\ts_waitcnt lgkmcnt(0)" : "+v"(old) : "v"(oca), "v"(one) : "memory");
        if ((__builtin_amdgcn_readfirstlane(old) & msk) == msk) { if (lane < 16) post(c.ws, opo, ov); }
        if (w == 0) STAMP_AT(l * 8 + 4);
        SCHED();
        if (!RBP) resid();
        if (P2A == 1) prep2();
        SCHED();
        if (w == 3) STAMP_AT(32 + l * 8 + 5);
        BAR();
    }
    FENCE();
    SCHED();
    if (w == 0) STAMP_AT(l * 8 + 5);
    int so = 0;
    auto next = [&](int j) { so += j == 15 ? 131072 - 15 * 262144 : 262144; asm volatile("" : "+s"(so)); };
    auto ldo = [&](int j, int mt) { xa[j % NP][mt] = __builtin_amdgcn_raw_buffer_load_b128(ro, ovo + mt * 1024, so, 16); };
#pragma unroll
    for (int h = 0; h < NP; ++h) {
        ldo(h, 0);
        SCHED();
        if (h > SH0 && h <= SH0 + 2) { start(h - SH0 - 1, 0); SCHED(); }
        ldo(h, 1);
        if (!WAL || h + 1 < NP) next(h);
        SCHED();
        if (P2A >= 2 && h == P2A - 2) { prep2(); SCHED(); }
        if (h == SH0) {
            wait_v<2 * SH0 + 2>();
            asm volatile("" : "+v"(res[0][0]), "+v"(res[0][1]), "+v"(res[1][0]), "+v"(res[1][1]));
        }
        if (h > SH0 && h <= SH0 + 2) start(h - SH0 - 1, 1);
        SCHED();
    }
    int d0 = 262144, d1 = 131072 - 15 * 262144;
    asm volatile("" : "+s"(d0), "+s"(d1));
    static_assert(WEP || !WAL, "last step");
    static_for<0, WEP ? 31 : 32>([&](auto jc) {
        constexpr int j = decltype(jc)::value;
        constexpr int c0 = j < 30 ? 16 + 8 * j : 256, c1 = j < 30 ? c0 + 8 : 256;
        if constexpr (j + NP == 16) {
            static_assert(c0 <= 8 * 20, "weights that landed before the attention");
            if (!B) BAR();
            FENCE();
            asm volatile("" : "+v"(ovo));
            SCHED();
        }
        if constexpr (j == 0 || j + NP == 16) ALIGN8();
        constexpr int nv = WAL == 1 || j == 0 || j + NP > 32 ? 2 * (NP - 1 < 31 - j ? NP - 1 : 31 - j) : 2 * NP - 3;
        __builtin_amdgcn_s_waitcnt(0x0870 | nv);
        const int sp = so;
        static_assert(!RS0 || (RS0 + NP >= 32 && RS0 < 31 && QW_SLOT <= (RS0 - 1) * 4096), "R0");
        if constexpr (RS0 && j == RS0) BAR();
        else asm volatile("s_add_i32 %0, %1, %2" : "=&s"(so) : "s"(sp), "s"(j + NP == 16 ? d1 : d0) : "scc");
        SCHED();
        static_for<0, 8>([&](auto mc) {
            constexpr int m = decltype(mc)::value, mt = m >> 2, nt = mt ? 3 - (m & 3) : m & 3;
            const bf16x8 wv = __builtin_shufflevector(wf[j % R][2 * nt], wf[j % R][2 * nt + 1], 0, 1, 2, 3, 4, 5, 6, 7);
            if constexpr (j < 31) asm volatile("v_mfma_f32_16x16x32_bf16 %0, %1, %2, %0" : "+v"(acc[mt][nt]) : "v"(wv), "v"(xa[j % NP][mt]));
            else acc[mt][nt] = MFMA(wv, __builtin_bit_cast(bf16x8, xa[j % NP][mt]), acc[mt][nt]);
            SCHED();
            if constexpr (c0 < c1 && m < 7) { rdw(std::integral_constant<int, c0 + m>{}); SCHED(); }
            if constexpr (c0 < c1 && m == 6) rdw(std::integral_constant<int, c0 + 7>{});
            if constexpr (m == WL1 && j > 0 && j - 1 + NP < 32) xa[(j + NP - 1) % NP][1] = __builtin_amdgcn_raw_buffer_load_b128(ro, ovo + 1024, sp, 16);
            if constexpr (m == WL1 + 4 && j + NP < 32) ldo(j, 0);
            SCHED();
        });
    });
    const bf16x8 ones = __builtin_bit_cast(bf16x8, on);
    u32x4 o[2][2];
    f32x4 sm[2], g[2];
    const f32x4 zero = {0.f, 0.f, 0.f, 0.f};
    auto sel = [&](float a, float b, u64 m) { return __builtin_amdgcn_inverse_ballot_w64(m) ? b : a; };
    auto stats = [&](int mt) {
        float qa = sel(g[mt][0], g[mt][1], b0), qb = sel(g[mt][2], g[mt][3], b0);
        asm volatile("" : "+v"(qa), "+v"(qb));
        SCHED();
        if (WEP) asm volatile("" :: "v"(sm[1][1]), "v"(sm[1][2]), "v"(sm[1][3]), "v"(o[1][0]), "v"(o[1][1]), "v"(on));
        __builtin_amdgcn_raw_buffer_store_b64((u32x2){__float_as_uint(sm[mt][0]), __float_as_uint(sel(qa, qb, b1))}, rst, sto + mt * 128, 0, 16);
        SCHED();
    };
    u32x2 pk[2];
    auto last = [&](int mt, int nt) {
        acc[mt][nt] = MFMA(__builtin_shufflevector(wf[31 % R][2 * nt], wf[31 % R][2 * nt + 1], 0, 1, 2, 3, 4, 5, 6, 7), __builtin_bit_cast(bf16x8, xa[31 % NP][mt]), acc[mt][nt]);
        SCHED();
    };
    auto cv = [&](int mt, int pr) {
        pk[0] = pack4(acc[mt][2 * pr]); pk[1] = pack4(acc[mt][2 * pr + 1]);
        asm volatile("" : "+v"(pk[0]), "+v"(pk[1]));
        SCHED();
    };
    auto st = [&](int mt, int pr) {
        o[mt][pr] = widen(pk[0], pk[1]);
        __builtin_amdgcn_raw_buffer_store_b128(o[mt][pr], rs, mt ? coff1 : coff, pr ? spr : 0, 16);
        SCHED();
    };
    auto gq = [&](int mt, int pr, bool f) { const bf16x8 of = __builtin_bit_cast(bf16x8, o[mt][pr]); g[mt] = MFMA(of, of, f ? zero : g[mt]); SCHED(); };
    auto gs = [&](int mt, int pr, bool f) { sm[mt] = MFMA(ones, __builtin_bit_cast(bf16x8, o[mt][pr]), f ? zero : sm[mt]); SCHED(); };
    WAIT_L0();
    last(0, 0); last(0, 1); last(0, 2); last(0, 3);
    last(1, 3); cv(0, 0);
    last(1, 2); st(0, 0);
    last(1, 1); cv(0, 1);
    last(1, 0); st(0, 1);
    if (w == 0) STAMP_AT(l * 8 + 6);
    gq(0, 0, true); cv(1, 1);
    gs(0, 0, true); st(1, 1);
    gq(0, 1, false); cv(1, 0);
    gs(0, 1, false); st(1, 0);
    gq(1, 1, true);
    gs(1, 1, true);
    gq(1, 0, false);
    gs(1, 0, false);
    stats(0);
    stats(1);
    if (l < 3) {
        unsigned old = 0;
        if (!RS0 || RN1 > RN0) BAR();
        wait_v<XE2>();
        if (lane == 0) asm volatile("ds_add_rtn_u32 %0, %1, %2\n\ts_waitcnt lgkmcnt(0)" : "+v"(old) : "v"(cnta), "v"(one) : "memory");
        if ((__builtin_amdgcn_readfirstlane(old) & 3) == 3) {
            if (__builtin_amdgcn_inverse_ballot_w64(am)) post(c.ws, xpo, xv);
        }
    }
    if (w == 0) STAMP_AT(l * 8 + 7);
}

__global__ __launch_bounds__(512, 2) __attribute__((aligned(4096))) void k_main(Params P) {
    extern __shared__ __attribute__((aligned(16))) char smem_[];
    KP kp = (KP)__builtin_amdgcn_kernarg_segment_ptr();
    unsigned long long tstart = rstamp();
    const char *x0, *w0, *wo;
    Ctx c;
    c.smem = (LAS char*)smem_;
    c.lane = threadIdx.x & 63;
    c.wid = __builtin_amdgcn_readfirstlane(threadIdx.x >> 6);
    c.bid = blockIdx.x;
    const bool loader = c.wid >= 4;
    unsigned va, vw;
    auto enter = [&](const bool LD) {
        if (LD) asm volatile("s_load_dwordx2 %0, %4, 0x0\n\ts_load_dwordx2 %1, %4, 0x8\n\ts_load_dwordx2 %2, %4, 0x10\n\ts_load_dwordx2 %3, %4, 0x20 ; loader" : "=&s"(x0), "=&s"(w0), "=&s"(c.ws), "=&s"(wo) : "s"(kp));
        else asm volatile("s_load_dwordx2 %0, %4, 0x0\n\ts_load_dwordx2 %1, %4, 0x8\n\ts_load_dwordx2 %2, %4, 0x10\n\ts_load_dwordx2 %3, %4, 0x20" : "=&s"(x0), "=&s"(w0), "=&s"(c.ws), "=&s"(wo) : "s"(kp));
        if (LD) asm volatile("; loader" : "+s"(c.bid), "+s"(c.wid)); else asm volatile("; consumer" : "+s"(c.bid), "+s"(c.wid));
        if (LD) asm volatile("; loader" : "+v"(c.lane)); else asm volatile("; consumer" : "+v"(c.lane));
        static_assert(QXM == 8 && WXM == 4 && ALT == 1, "map");
        c.pq = (((c.bid & 1) * 8) ^ PQX) + ((c.bid >> 3) & 7); c.jq = (c.bid & 6) * 2 + (c.bid >> 6);
        const int w = c.wid, lr = c.lane >> 3, lc = c.lane & 7;
        va = (unsigned)((c.pq * 128 + 8 * (w & 3) + lr) * (D_ * 2) + ((lc ^ lr) << 4));
        vw = (unsigned)((8 * (w & 3) + lr) * 6144 + c.jq * 128 + ((lc ^ ((lr & 2) | ((w & 1) << 2))) << 4));
        asm volatile("" : "+v"(va), "+v"(vw));
        c.pw = ((c.pq & 8) ^ 8) + ((c.bid >> 1) & 1) * 4 + ((c.bid >> 3) & 3); c.jw = (c.bid & 4) * 2 + (c.bid >> 5); c.cw = 0;
        if (LD) {
            asm volatile("" : "+s"(c.pw), "+s"(c.jw));
            asm volatile("s_waitcnt lgkmcnt(0)" : "+s"(x0), "+s"(w0), "+s"(c.ws), "+s"(wo));
            if (ESL) __builtin_amdgcn_s_sleep(ESL);
        } else {
            int t0 = first_tile(first_order(c.pq, c.jq), 0);
            asm volatile("" : "+s"(t0));
            asm volatile("s_waitcnt lgkmcnt(0) ; c" : "+s"(x0), "+s"(w0), "+s"(c.ws), "+s"(wo));
            if (ST0 && c.wid == 3) STAMP_AT(41);
            const char* xb = x0 + (unsigned)(t0 * 128);
            const char* wb = w0 + (unsigned)(t0 * (64 * 6144));
#pragma unroll
            for (int h = 0; h < 2; ++h) {
#pragma unroll
                for (int j = 0; j < 2; ++j) dma16<false>(QA_OFF + (j * 8 + h * 4 + w) * 1024, va, xb + (j * 64 + h * 32) * (D_ * 2));
#pragma unroll
                for (int j = 0; j < 3; ++j) dma16<false>((j * 8 + h * 4 + w) * 1024, vw, wb + h * (32 * 6144) + j * 2048);
            }
            if (ST0 && c.wid == 1) STAMP_AT(43);
            if (w == 0 && c.lane < 2) touch(M_EP, (unsigned)(WS_EPOCH + c.lane * 4), c.ws);
            if (w == 1 && c.lane < 5) *(LAS unsigned*)(c.smem + (c.lane < 4 ? M_CNT + c.lane * 4 : M_GO)) = 0u;
            if (c.wid == 0 && c.lane == 0) *(LAS unsigned long long*)lds(MISC + 3328 + 63 * 8) = tstart;
        }
        asm volatile("" : "+s"(kp), "+s"(c.bid), "+s"(c.ws));
    };
    if (c.wid == CPW) {
        const char* pc; int len;
        asm volatile("s_getpc_b64 %0\n.Lcpf_a:\n\ts_mov_b32 %1, .Lcpf_end-.Lcpf_a-4" : "=s"(pc), "=s"(len));
        const int o = min((int)(((c.bid >> 3) & 7) * 64 + c.lane) * 128, len);
        asm volatile("s_mov_b32 m0, %0\n\ts_nop 3\n\tglobal_load_lds_dword %1, %2" ::"s"(M_TCH), "v"(o), "s"(pc) : "memory");
    }
    const char* xi;
    auto step = [&](int l) {
        asm volatile("" : "+s"(kp), "+s"(c.ws), "+s"(c.jw), "+s"(c.bid));
        c.pq ^= 8; c.pw ^= 8; c.cw = CPY ? c.jw & 7 : 0;
        xi = c.ws + WS_X + (unsigned)(l * (8u << 20));
        wo = (const char*)((l & 1) ? kp->wo0 : kp->wo1);
        if (c.cw == 3) wo = c.ws + WS_CO + (((l & 1) ? 2 : 0) + (c.jw >> 3)) * 4096;
    };
    if (loader) {
        enter(true);
        xi = x0;
            qkv_load<false, true>(c, xi, w0, wo, 0, kp, va, vw);
#pragma unroll 1
        for (int l = 0;;) {
            asm volatile("" : "+s"(l));
            if (!(l & 1)) attn_a<true>(c, l, wo, kp);
            wo_load(c, l, nullptr, wo);
            if (l == 3) break;
            step(l);
            ++l;
            if (l == 2) { qkv_load<false, false>(c, xi, (const char*)kp->wqkv0, wo, l); }
            else { qkv_load<true, false>(c, xi, (const char*)kp->wqkv1, wo, l); }
        }
    } else {
        enter(false);
        xi = x0;
        bf16_t* xo = (bf16_t*)(c.ws + WS_X);
#pragma unroll 1
        for (int l = 0; l < 4; ++l) {
            if (l & 1) { qkv_consume<true>(c, l, kp); }
            else {
                qkv_consume<false>(c, l, kp, va, vw);
                attn_a<false>(c, l, wo, kp);
            }
            wo_consume(l & 1, c, (const bf16_t*)xi, xo, l);
            step(l);
            xo = l == 2 ? kp->out : (bf16_t*)(c.ws + WS_X + (unsigned)((l + 1) * (8u << 20)));
            asm volatile("" : "=v"(va), "=v"(vw));
        }
    }
    asm volatile(".Lcpf_end:");
    if (c.wid == 0) {
        WAIT_V0();
        const unsigned long long te = rstamp();
        if (c.lane == 0) *(LAS unsigned long long*)lds(MISC + 3328 + 62 * 8) = te;
        asm volatile("s_waitcnt lgkmcnt(0)" ::: "memory");
        const unsigned long long t0 = *(volatile LAS unsigned long long*)lds(MISC + 3328 + 63 * 8);
        unsigned long long ta = *(volatile LAS unsigned long long*)lds(MISC + 3328 + (SA < 64 ? SA : 63) * 8), tb = *(volatile LAS unsigned long long*)lds(MISC + 3328 + SB * 8);
        if (SA == 65) {
            if (c.bid == SX) {
                u64 m = 0;
                for (int i = 0; i < 4; ++i) { const u64 v = __hip_atomic_load((GAS u64*)(c.ws + WS_STAMP + 4096 + (i * 64 + c.lane) * 8), __ATOMIC_RELAXED, __HIP_MEMORY_SCOPE_AGENT); m = v > m ? v : m; }
                for (int o = 1; o < 64; o <<= 1) { const u64 v = __shfl_xor(m, o); m = v > m ? v : m; }
                ta = m;
            }
        } else if (SA == 66) {
            if (c.lane == 0) __hip_atomic_store((GAS u64*)(c.ws + WS_STAMP + c.bid * 8), te, __ATOMIC_RELAXED, __HIP_MEMORY_SCOPE_AGENT);
            return;
        } else if (SA >= 64) {
            if (c.lane == 0) __hip_atomic_store((GAS u64*)(c.ws + WS_STAMP + c.bid * 8), t0, __ATOMIC_RELAXED, __HIP_MEMORY_SCOPE_AGENT);
            if (c.bid == SX) {
                while (rstamp() - te < 500) { }
                u64 m = ~0ull;
                for (int i = 0; i < 4; ++i) { const u64 v = __hip_atomic_load((GAS u64*)(c.ws + WS_STAMP + (i * 64 + c.lane) * 8), __ATOMIC_RELAXED, __HIP_MEMORY_SCOPE_AGENT); m = v < m ? v : m; }
                for (int o = 1; o < 64; o <<= 1) { const u64 v = __shfl_xor(m, o); m = v < m ? v : m; }
                ta = m;
                if (SA == 67) {
                    u64 x = 0;
                    for (int i = 0; i < 4; ++i) { const u64 v = __hip_atomic_load((GAS u64*)(c.ws + WS_STAMP + (i * 64 + c.lane) * 8), __ATOMIC_RELAXED, __HIP_MEMORY_SCOPE_AGENT); x = v > x ? v : x; }
                    for (int o = 1; o < 64; o <<= 1) { const u64 v = __shfl_xor(x, o); x = v > x ? v : x; }
                    tb = x;
                }
            }
        }
        if (c.bid == SX) {
            const unsigned long long d = (tb - ta) * SM + 1000;
            while (rstamp() - te < d && d < 100000) { }
        }
    }
}

__global__ void k_pre(char* ws) {
    if (threadIdx.x == 0) __hip_atomic_store((GAS u64*)(ws + WS_STAMP + 4096 + blockIdx.x * 8), rstamp(), __ATOMIC_RELAXED, __HIP_MEMORY_SCOPE_AGENT);
}
__global__ void k_post(char* ws) {
    const u64 t0 = rstamp();
    if (blockIdx.x == SX) {
        u64 m = 0;
        for (int i = 0; i < 4; ++i) { const u64 v = __hip_atomic_load((GAS u64*)(ws + WS_STAMP + (i * 64 + threadIdx.x) * 8), __ATOMIC_RELAXED, __HIP_MEMORY_SCOPE_AGENT); m = v > m ? v : m; }
        for (int o = 1; o < 64; o <<= 1) { const u64 v = __shfl_xor(m, o); m = v > m ? v : m; }
        const u64 d = (t0 - m) * SM + 1000;
        while (rstamp() - t0 < d && d < 100000) { }
    }
}
extern "C" void kernel_launch(void* const* d_in, const int* in_sizes, int n_in, void* d_out, int out_size, void* d_ws, size_t ws_size,
                              hipStream_t stream) {
    Params P;
    P.x0 = (const bf16_t*)d_in[0];
    P.wqkv0 = (const bf16_t*)d_in[1];
    P.wo0 = (const bf16_t*)d_in[2];
    P.wqkv1 = (const bf16_t*)d_in[4];
    P.wo1 = (const bf16_t*)d_in[5];
    P.out = (bf16_t*)d_out;
    P.ws = (char*)d_ws;
    (void)hipFuncSetAttribute((const void*)k_main, hipFuncAttributeMaxDynamicSharedMemorySize, LDS_TOTAL);
    if (SA == 65) k_pre<<<256, 64, 0, stream>>>(P.ws);
    k_main<<<256, 512, LDS_TOTAL, stream>>>(P);
    if (SA == 66) k_post<<<256, 64, 0, stream>>>(P.ws);
}
```

```cpp
#include <hip/hip_runtime.h>
#include <stdint.h>
#include <type_traits>

typedef unsigned short bf16_t;
typedef short bf16x8 __attribute__((ext_vector_type(8)));
typedef short s16x4 __attribute__((ext_vector_type(4)));
typedef float f32x4 __attribute__((ext_vector_type(4)));
typedef float f32x2 __attribute__((ext_vector_type(2)));
typedef float f32x16 __attribute__((ext_vector_type(16)));
typedef unsigned u32x4 __attribute__((ext_vector_type(4)));
typedef unsigned u32x2 __attribute__((ext_vector_type(2)));
#define DI __device__ __forceinline__
#define LAS __attribute__((address_space(3)))
#define GAS __attribute__((address_space(1)))
#define STAMP 0

constexpr int S_ = 2048, D_ = 1024, KT = 16;
constexpr float LN_EPS = 1e-5f;
constexpr float QSCALE = 0.17677669529663687f * 1.4426950408889634f;

#define FQO 0
#define XBO 524288
#define HX 0
#define HO 0
#define HK 0
#define HS 0
#define HM 1
constexpr size_t WS_FLAGQ = FQO;
constexpr size_t WS_FLAGW = 98304;
constexpr size_t WS_EPOCH = 65536;
constexpr size_t WS_CSUM = 131072;
constexpr size_t WS_XBOX = XBO;
constexpr size_t WS_OBOX = 655360;
constexpr size_t WS_KBOX = 720896;
constexpr size_t WS_STATS = 1u << 20;
constexpr size_t WS_X = 4u << 20;
constexpr size_t WS_O = 96u << 20;
constexpr size_t WS_KV = 28u << 20;
constexpr size_t WS_CQ = 36u << 20;
constexpr size_t WS_CO = 42u << 20;
constexpr size_t WS_STAMP = 64u << 20;

template <int H> DI constexpr int hp(int c) { return !H ? c * 1024 : (c >> 2) * 8192 + (HM == 1 ? (c & 3) * 256 : (c & 1) * 128 + (c & 2) * 256); }
template <int H> DI constexpr int hl(int u) { return !H ? u : (HM == 1 ? u + (u & ~127) : u + (u & ~255)); }
template <int H> DI constexpr int hv(int lane) { return !H ? lane * 16 : (lane >> 3) * 1024 + (lane & 7) * 16; }
template <int H> DI constexpr int xrow(int p, int r) { return H == 0 ? (p * 128 + r) * 2048 : (H == 1 ? (p * 128 + r) * 4096 : (H == 2 ? p * 524288 + r * 256 : p * 262144 + r * 128)); }
template <int H> DI constexpr int xcol(int t) { return H == 0 ? t * 128 : (H == 1 ? hl<1>(t * 128) : (H == 2 ? t * 32768 : t * 16384)); }
#define WTC 2
#define WT0 1
#define SPR 1
#define SL 4
#define R4X 0
#define R5X 0
#define TOPJ 0x2000
#define WV0 1
#define C2X 13
#define FQS 256
#define SNK 1
#define WBE 1
#define WAE 1
#define EXF 0
#define RW0 6
#define KSP 0
#define K0E 1
#define TOPS 4
#define BPR 1
#define RLD 0
#define RCK 1
#define CPF 2
#define WN1 20
#define WLO 3
#define FPJ 2
#define WPJ 2
#define WB14 0
#define WB15 5
#define RESP 1
#define WT13 2
#define RPOS 2
#define CPOLL 1
#define ENT 1
#define MBOX 3
#define OBS 256
#define WTCH 0
#define WTDA 0
#define WTDB 4
#define WTG 0
#define PQX 8
#define ALT 1
#define RESA 1
#define CKP 1
#define ALN 1
#define EPH 2
#define NQA_ 3
#define NQW_ 4
#define WNP 8
#define LPRIO 3
#define TOUCH 1
#define TSTEP 13
#define XS_ 9
#define WROT 0
#define WROTD 1
#define JQX 0
#define ROTM 0
#define ROT0 0
#define WPRE 3
#define PSLEEP 1
#define XPRE 2
#define WE0 1
#define EPJ 3
#define TW0 5
#define TW1 99
#define KPJ 1
#define PKSUB 0
#define APRIO 1
#define QIL 0
#define QXM 8
#define WNQ 5
#define WRG 2
#define WMG 1
#define WB2 1
#define WBS 1
#define WEP 1
#define WAL 2
#define WL1 0
#define RS0 27
#define RN0 6
#define RN1 6
#define AH0 0
#define RSL 1
#define WXM 4
#define CPOS 5
#define LSL 0
#define SAFE_SUM 0x1p80f
#define AB2 0
#define W30S 8
#define STC 0
#define NOR 1
#define NRH 0
#define CPY 1
#define AGG 1
#define HT0 0
#define DEF 0
#define XST 2
#define CKW 4
#define XSP 1
#define EPOLL 1
#define ORD 2
#define RSLP 1
#define APOLL 1
#define APOST 1
#define ASLP 2
#define APIPE 4
#define RBP 0
#define P2A 1
#define SH0 2
#define SL1 0
#define KPW 4
#define LWA 7
#define WA0 12
#define WA1 22
#define KPP 1
#define HP4 1
#define TWQ 1
#define CP2 0
#define PADN -1
#define ERL 1
#define ESL 0
#define DM 0
#define DS 0
#define ELO 0
#define EN0 3
#define EN1 2
#define ST0 0
#define NC 1
#define NCM 1
#define NCB 4
#define AD1 3
#define AD2 0
#define AD3 4
#define WB0 3
#define WBN 6
#define TWO 1
#define XSB 10
#define TL0 0
#define TL1 12
#define FDM 1
#define TSP 0
#define CPW 4
#define EXS 1
#define NCH 2
#define XE1 0
#define XE2 0
#define OTB 0
#define OTJ 8
#define OTA 0
constexpr int QA_SLOT = 18432, NQA = NQA_, QW_SLOT = 24576, NQW = NQW_, QA_OFF = 3 * QW_SLOT, QW_TOP = 131072;
DI constexpr int qw_slot(int s) { return s == 3 ? QW_TOP : s * QW_SLOT; }
constexpr int AT_N = 90112, AT_OWN = AT_N + 32768, AT_Q = 73728;
constexpr int MISC = 155648, M_MEAN = MISC, M_RSTD = MISC + 640, M_CS = MISC + 1280, M_CNT = MISC + 2816, M_EP = MISC + 2832, M_GO = MISC + 2848, M_TCH = MISC + 3072, M_TAB = MISC + 4096, M_XCH = 131072;
constexpr int LDS_TOTAL = 163840;
static_assert(QA_OFF + NQA * QA_SLOT <= QW_TOP && QW_TOP + QW_SLOT <= MISC && AT_N + 65536 <= MISC && AT_Q + 16384 <= AT_N, "lds");

DI unsigned cvt_pk_bf16(float lo, float hi) {
    typedef __bf16 bf2 __attribute__((ext_vector_type(2)));
    bf2 r; r[0] = (__bf16)lo; r[1] = (__bf16)hi;
    return __builtin_bit_cast(unsigned, r);
}
DI u32x2 pack4(const f32x4& v) { return (u32x2){cvt_pk_bf16(v[0], v[1]), cvt_pk_bf16(v[2], v[3])}; }
DI bf16x8 join(const u32x2& a, const u32x2& b) { return __builtin_bit_cast(bf16x8, (u32x4){a[0], a[1], b[0], b[1]}); }
DI float bf_lo(unsigned u) { return __uint_as_float(u << 16); }
DI float bf_hi(unsigned u) { return __uint_as_float(u & 0xffff0000u); }
DI float red_max4(float x) {
    auto a = __builtin_amdgcn_permlane16_swap(__float_as_uint(x), __float_as_uint(x), false, false);
    x = fmaxf(__uint_as_float(a[0]), __uint_as_float(a[1]));
    auto b = __builtin_amdgcn_permlane32_swap(__float_as_uint(x), __float_as_uint(x), false, false);
    return fmaxf(__uint_as_float(b[0]), __uint_as_float(b[1]));
}
DI float red_sum4(float x) {
    auto a = __builtin_amdgcn_permlane16_swap(__float_as_uint(x), __float_as_uint(x), false, false);
    x = __uint_as_float(a[0]) + __uint_as_float(a[1]);
    auto b = __builtin_amdgcn_permlane32_swap(__float_as_uint(x), __float_as_uint(x), false, false);
    return __uint_as_float(b[0]) + __uint_as_float(b[1]);
}
DI u32x4 widen(const u32x2& a, const u32x2& b) {
    auto r0 = __builtin_amdgcn_permlane16_swap(a[0], b[0], false, false);
    auto r1 = __builtin_amdgcn_permlane16_swap(a[1], b[1], false, false);
    return (u32x4){r0[0], r1[0], r0[1], r1[1]};
}
#define MFMA(a, b, c) __builtin_amdgcn_mfma_f32_16x16x32_bf16((a), (b), (c), 0, 0, 0)
#define WAIT_L0() __builtin_amdgcn_s_waitcnt(0xC07F)
#define WAIT_V0() asm volatile("s_waitcnt vmcnt(0)" ::: "memory")
#define BAR() __builtin_amdgcn_s_barrier()
#define SCHED() __builtin_amdgcn_sched_barrier(0)
template <int N> DI void wait_v() { asm volatile("s_waitcnt vmcnt(%0)" ::"i"(N) : "memory"); }
template <int I, int N, class F> DI void static_for(F&& f) { if constexpr (I < N) { f(std::integral_constant<int, I>{}); static_for<I + 1, N>(f); } }

template <bool SC1> DI void dma16(unsigned lds, unsigned voff, const char* sbase) {
    if (SC1) asm volatile("s_mov_b32 m0, %0\n\ts_nop 3\n\tglobal_load_lds_dwordx4 %1, %2 sc1" ::"s"(lds), "v"(voff), "s"(sbase) : "memory");
    else asm volatile("s_mov_b32 m0, %0\n\ts_nop 3\n\tglobal_load_lds_dwordx4 %1, %2" ::"s"(lds), "v"(voff), "s"(sbase) : "memory");
}
DI void touch(unsigned lds, unsigned voff, const char* sbase) {
    asm volatile("s_mov_b32 m0, %0\n\ts_nop 3\n\tglobal_load_lds_dword %1, %2 sc1" ::"s"(lds), "v"(voff), "s"(sbase) : "memory");
}
DI s16x4 tr_read(const LAS char* p) {
    typedef short v4i16_t __attribute__((ext_vector_type(4)));
    return __builtin_bit_cast(s16x4, __builtin_amdgcn_ds_read_tr16_b64_v4i16((LAS v4i16_t*)p));
}
DI LAS char* lds(unsigned off) { return (LAS char*)(uintptr_t)off; }
DI unsigned long long rstamp() { unsigned long long t; asm volatile("s_memrealtime %0\n\ts_waitcnt lgkmcnt(0)" : "=s"(t)::"memory"); return t; }

template <int NAI, int NWI, bool FIRST> struct Sched {
    int w[KT];
    constexpr Sched() : w{} {
        int endA[KT] = {}, endW[KT] = {};
        int n = 0;
        if (FIRST) {
            if (ENT == 0) { n += 2; endA[0] = n; n += 3; endW[0] = n; }
            n += NAI; endA[1] = n; n += NWI; endW[1] = n;
            if (ERL >= 5) { n += NAI; endA[2] = n; }
            if (ERL == 2 || ERL == 3 || ERL == 5) { n += NWI; endW[2] = n; }
        } else {
            for (int s = 0; s < WPRE; ++s) { n += NWI; endW[s] = n; }
            for (int s = 0; s < 2; ++s) { n += NAI; endA[s] = n; }
        }
        for (int kt = 0; kt < KT; ++kt) {
            const int need = endA[kt] > endW[kt] ? endA[kt] : endW[kt];
            w[kt] = (n - need) > 63 ? 63 : (n - need);
            if (kt + 2 < KT && !(FIRST && ERL >= 5 && kt == 0)) { n += NAI; endA[kt + 2] = n; }
            if (FIRST && (ERL == 4 || ERL == 6) && kt == 0) { n += NWI; endW[2] = n; }
            if (FIRST && ERL < 2 && kt < 5) {
                if (kt < 4) { n += NWI; endW[kt + 2] = n; }
                if (kt >= 3) n += NWI / 2;
                if (kt == 4) { endW[6] = n; n += NWI; endW[7] = n; }
                continue;
            }
            if (!FIRST && WPRE == 2 && kt < 2) { n += NWI; endW[kt + 2] = n; }
            if ((FIRST || WPRE == 3) && kt == 0) { n += NWI; endW[3] = n; }
            if (kt >= 1 && kt + 3 < KT) { n += NWI; endW[kt + 3] = n; }
            if (kt == 13) n += WT13;
            if (kt == 14) n += (NAI == 5 || WA0 > 12 ? 12 : WA0) - WT13 + (NAI == 5 ? WB14 : 0);
        }
    }
};

struct Params {
    const bf16_t* x0;
    const bf16_t* wqkv0;
    char* ws;
    const bf16_t* wqkv1;
    const bf16_t* wo0;
    const bf16_t* wo1;
    bf16_t* out;
};
typedef const __attribute__((address_space(4))) Params* KP;
DI int rot_first(int pq, int jq) {
    const int pr = 2 * (pq & 7) + (pq >> 3);
    if (ROT0 == 6) return 0;
    if (ROT0 == 7) return (pq & 1) * 8;
    if (ROT0 == 8) return (pq & 3) * 4;
    if (ROT0 == 9) return pq;
    if (ROT0 == 4) return (pq + (jq ^ 3)) & 15;
    if (ROT0 == 5) return (pr + (jq ^ 3)) & 15;
    return (ROT0 == 0 ? pq + jq : (ROT0 == 1 ? pr + jq : (ROT0 == 2 ? pr + 4 * (jq & 3) + (jq >> 2) : pq + 4 * (jq & 3) + (jq >> 2)))) & 15;
}

struct Order { int base, r, re; };
template <int N> DI Order make_order(int base, int rot) { Order o; o.base = base; o.r = (rot - base) & 15; o.re = o.r > N ? o.r : N; return o; }
DI Order first_order(int pq, int jq) { return make_order<NC>((pq & NCM) * (16 / (NCM + 1)) + NCB, rot_first(pq, jq)); }
template <int N = NC> DI int first_tile(const Order& o, int s) {
    const int v = o.re + s - N;
    if (s < N) { int a = N + o.base, b = s + o.base; if (N - s == 1) asm volatile("" : "+s"(a)); return (o.r == s ? a : b) & 15; }
    return ((s == N ? o.r : (v > 15 ? v - (16 - N) : v)) + o.base) & 15;
}

typedef unsigned long long u64;
DI u64 lds_epoch(LAS char* smem) { return *(const LAS u64*)(smem + M_EP); }
DI bool reached(u64 v, u64 ep, unsigned target) { return (v - ep - target) < (u64)(64u - target); }
DI void poll_flags(const char* ws, u64 ep, unsigned off, bool active, unsigned target) {
    const GAS u64* f = (const GAS u64*)(ws + off);
#pragma unroll 1
    for (unsigned spins = 0; spins < (1u << 22); ++spins) {
        const u64 v = __hip_atomic_load(f, __ATOMIC_RELAXED, __HIP_MEMORY_SCOPE_AGENT);
        if (__all(!active || reached(v, ep, target))) break;
        __builtin_amdgcn_s_sleep(PSLEEP);
    }
    asm volatile("" ::: "memory");
}
struct Ask { u64 v[4]; };
DI void keep(Ask& t) { asm volatile("" : "+v"(t.v[0]), "+v"(t.v[1]), "+v"(t.v[2]), "+v"(t.v[3])); }
#define ASK(r) "global_atomic_or_x2 " r ", %[off], %[z], %[ws] sc0 sc1\n\t"
#define CHK(r) "s_waitcnt vmcnt(%[n])\n\tv_lshl_add_u64 %[d], " r ", 0, %[nb]\n\tv_cmp_gt_u64 vcc, %[span], %[d]\n\ts_cmp_eq_u64 vcc, exec\n\ts_cbranch_scc1 .Lpe%=\n\ts_sleep %[sl]\n\t"
template <bool NEG = false> DI void poll_rmw(Ask& t, const char* ws, unsigned off, bool act, u64 base, unsigned span) {
    u64 z, d, nb = NEG ? base : 0 - base, sp = span;
    unsigned cnt;
    asm volatile("" : "=v"(t.v[0]), "=v"(t.v[1]), "=v"(t.v[2]), "=v"(t.v[3]));
    if (act) {
        if (APIPE == 4)
            asm volatile("v_mov_b64 %[z], 0\n\ts_mov_b32 %[cnt], 0x80000\n\ts_nop 3\n\t" ASK("%[a]") "s_sleep %[sl]\n\t" ASK("%[b]") "s_sleep %[sl]\n\t" ASK("%[c]") "s_sleep %[sl]\n"
                         ".Lpl%=:\n\t" ASK("%[e]") CHK("%[a]") ASK("%[a]") CHK("%[b]") ASK("%[b]") CHK("%[c]") ASK("%[c]") CHK("%[e]")
                         "s_sub_u32 %[cnt], %[cnt], 1\n\ts_cbranch_scc0 .Lpl%=\n.Lpe%=:"
                         : [a] "+v"(t.v[0]), [b] "+v"(t.v[1]), [c] "+v"(t.v[2]), [e] "+v"(t.v[3]), [d] "=&v"(d), [z] "=&v"(z), [cnt] "=&s"(cnt)
                         : [off] "v"(off), [ws] "s"(ws), [nb] "v"(nb), [span] "s"(sp), [sl] "i"(ASLP), [n] "i"(3) : "vcc", "scc", "memory");
        else if (APIPE == 2)
            asm volatile("v_mov_b64 %[z], 0\n\ts_mov_b32 %[cnt], 0x100000\n\ts_nop 3\n\t" ASK("%[a]") "s_sleep %[sl]\n"
                         ".Lpl%=:\n\t" ASK("%[b]") CHK("%[a]") ASK("%[a]") CHK("%[b]")
                         "s_sub_u32 %[cnt], %[cnt], 1\n\ts_cbranch_scc0 .Lpl%=\n.Lpe%=:"
                         : [a] "+v"(t.v[0]), [b] "+v"(t.v[1]), [d] "=&v"(d), [z] "=&v"(z), [cnt] "=&s"(cnt)
                         : [off] "v"(off), [ws] "s"(ws), [nb] "v"(nb), [span] "s"(sp), [sl] "i"(ASLP), [n] "i"(1) : "vcc", "scc", "memory");
        else
            asm volatile("v_mov_b64 %[z], 0\n\ts_mov_b32 %[cnt], 0x200000\n\ts_nop 3\n"
                         ".Lpl%=:\n\t" ASK("%[a]") CHK("%[a]")
                         "s_sub_u32 %[cnt], %[cnt], 1\n\ts_cbranch_scc0 .Lpl%=\n.Lpe%=:"
                         : [a] "+v"(t.v[0]), [d] "=&v"(d), [z] "=&v"(z), [cnt] "=&s"(cnt)
                         : [off] "v"(off), [ws] "s"(ws), [nb] "v"(nb), [span] "s"(sp), [sl] "i"(ASLP), [n] "i"(0) : "vcc", "scc", "memory");
    }
}
DI void post(char* ws, unsigned off, u64 v) {
    if (APOST) asm volatile("s_nop 4\n\tglobal_atomic_swap_x2 %0, %1, %2 sc1" :: "v"(off), "v"(v), "s"(ws) : "memory");
    else __hip_atomic_store((GAS u64*)(ws + off), v, __ATOMIC_RELAXED, __HIP_MEMORY_SCOPE_AGENT);
}
template <int NW, int CNT, int NV = 0> DI void publish(LAS char* smem, int lane, char* ws, unsigned off, bool act, unsigned val) {
    const u64 ep = lds_epoch(smem) + val;
    unsigned old = 0;
    asm volatile("" :: "v"(ep), "v"(off));
    wait_v<NV>();
    if (lane == 0) asm volatile("ds_add_rtn_u32 %0, %1, %2\n\ts_waitcnt lgkmcnt(0)" : "+v"(old) : "v"(M_CNT + CNT * 4), "v"(1u) : "memory");
    if ((__builtin_amdgcn_readfirstlane(old) & (NW - 1)) == NW - 1) {
        if (act) post(ws, off, ep);
    }
}
typedef __bf16 bf16x2_t __attribute__((ext_vector_type(2)));
DI float dot2(unsigned a, unsigned b, float c) { return __builtin_amdgcn_fdot2_f32_bf16(__builtin_bit_cast(bf16x2_t, a), __builtin_bit_cast(bf16x2_t, b), c, false); }
DI u32x2 tile_stats(const u32x4& a, const u32x4& b) {
    float s = 0.f, q = 0.f;
#pragma unroll
    for (int j = 0; j < 4; ++j) { s = dot2(a[j], 0x3f803f80u, s); q = dot2(a[j], a[j], q); }
#pragma unroll
    for (int j = 0; j < 4; ++j) { s = dot2(b[j], 0x3f803f80u, s); q = dot2(b[j], b[j], q); }
    return (u32x2){__float_as_uint(red_sum4(s)), __float_as_uint(red_sum4(q))};
}

struct Ctx {
    LAS char* smem;
    char* ws;
    int lane, wid;
    int pq, jq;
    int pw, jw;
    int bid;
    int cw;
};
DI Ctx fresh(const Ctx& c0) {
    Ctx c = c0;
    asm volatile("" : "+v"(c.lane));
    asm volatile("" : "+s"(c.wid), "+s"(c.pq), "+s"(c.jq), "+s"(c.pw), "+s"(c.jw), "+s"(c.ws), "+s"(c.cw));
    return c;
}
DI unsigned o_post(const Ctx& c) { return (MBOX & 2) ? (unsigned)WS_OBOX + (c.pq * 16 + (c.lane & 15)) * OBS + c.jq * 8 : (unsigned)WS_FLAGQ + (c.pq * 16 + c.jq) * FQS; }
DI bool o_act(const Ctx& c) { return c.lane < ((MBOX & 2) ? 16 : 1); }
DI unsigned o_find(const Ctx& c) { return (MBOX & 2) ? (unsigned)WS_OBOX + (c.pw * 16 + c.jw) * OBS + (c.lane & 15) * 8 : (unsigned)WS_FLAGQ + (c.pw * 16 + (c.lane & 15)) * FQS; }
DI unsigned x_post(const Ctx& c) { return (MBOX & 1) ? (unsigned)WS_XBOX + (c.pw * 16 + (c.lane & 31)) * (2 * FQS) + (c.lane & 16) * (FQS / 16) + c.jw * 8 : (unsigned)WS_FLAGW + (c.pw * 16 + c.jw) * 128; }
DI bool x_act(const Ctx& c, bool halo) { return c.lane < ((MBOX & 1) ? (halo && c.pw < 15 ? 32 : 16) : 1); }
DI unsigned x_find(const Ctx& c) {
    const int pp = c.pq - (c.lane >> 4);
    return (MBOX & 1) ? (unsigned)WS_XBOX + (c.pq * 16 + c.jq) * (2 * FQS) + (c.lane & 16) * (FQS / 16) + (c.lane & 15) * 8 : (unsigned)WS_FLAGW + ((pp < 0 ? 0 : pp) * 16 + (c.lane & 15)) * 128;
}
#define STAMP_AT(slot) do { } while (0)

#define LSTAMP(i) do { } while (0)
#define LSTORE(ph) do { } while (0)
#define LMARK() do { } while (0)
#define LPUT(i) do { } while (0)
#define ASTORE() do { } while (0)
#define QSTAMP(i) do { } while (0)
#define QSTORE() do { } while (0)
#define FENCE() asm volatile("" ::: "memory")
#define ALIGN8() do { if (ALN) { asm volatile(".p2align 3"); SCHED(); } } while (0)
DI float addf(float a, float b) { float r; asm("v_add_f32 %0, %1, %2" : "=v"(r) : "v"(a), "v"(b)); return r; }
DI f32x4 add4x4(const u32x4& a, const u32x4& b, const u32x4& c, const u32x4& d) {
    f32x4 r;
#pragma unroll
    for (int i = 0; i < 4; ++i) r[i] = addf(addf(__uint_as_float(a[i]), __uint_as_float(b[i])), addf(__uint_as_float(c[i]), __uint_as_float(d[i])));
    asm volatile("s_nop 1" : "+v"(r));
    return r;
}
DI float fold16(float a, float b) {
    auto r = __builtin_amdgcn_permlane16_swap(__float_as_uint(a), __float_as_uint(b), false, false);
    float x = __uint_as_float(r[0]) + __uint_as_float(r[1]);
    asm volatile("" : "+v"(x));
    return x;
}
DI float fold32(float a, float b) {
    auto r = __builtin_amdgcn_permlane32_swap(__float_as_uint(a), __float_as_uint(b), false, false);
    float x = __uint_as_float(r[0]) + __uint_as_float(r[1]);
    asm volatile("" : "+v"(x));
    return x;
}
DI void aload4(u32x4& d, unsigned voff, const char* sb) { asm volatile("s_nop 4\n\tglobal_load_dwordx4 %0, %1, %2" : "=v"(d) : "v"(voff), "s"(sb) : "memory"); }
DI void aload4p(u32x4& d, unsigned voff, const char* sb) { asm volatile("s_nop 4\n\tglobal_load_dwordx4 %0, %1, %2" : "+v"(d) : "v"(voff), "s"(sb) : "memory"); }
DI void aload4c(u32x4& d, unsigned voff, const char* sb) { asm volatile("s_nop 4\n\tglobal_load_dwordx4 %0, %1, %2 sc1" : "=v"(d) : "v"(voff), "s"(sb) : "memory"); }
DI void aload2c(u32x2& d, unsigned voff, const char* sb) { asm volatile("s_nop 4\n\tglobal_load_dwordx2 %0, %1, %2 sc1" : "=v"(d) : "v"(voff), "s"(sb) : "memory"); }
DI unsigned wo_src(int q, int lane, int jw) { return (unsigned)((8 * q + (lane >> 3)) * (D_ * 2) + jw * 128 + (((lane & 7) ^ ((lane >> 3) & 6)) << 4)); }
DI unsigned pick(bool f, unsigned a, unsigned b) { asm volatile("" : "+v"(a), "+v"(b)); return f ? a : b; }
DI unsigned wo_w_off(const Ctx& c) { return pick(c.cw == 3, (unsigned)((c.wid & 3) * 1024 + c.lane * 16), wo_src(c.wid & 3, c.lane, c.jw)); }
DI unsigned wq_src(int ib, int lane, int jq) {
    const int seg = ib >> 3, ii = ib & 7, lr = lane >> 3;
    return (unsigned)((8 * ii + lr) * 6144 + seg * 2048 + jq * 128 + (((lane & 7) ^ ((lr & 2) | ((ii & 1) << 2))) << 4));
}
template <int S0, int S1> DI void wo_w_issue(const Ctx& c, const char* W, unsigned vw) {
    const int lw = c.wid & 3;
#pragma unroll
    for (int s = S0; s < S1; ++s) dma16<CPY != 0>(s * 4096 + lw * 1024, vw, W + (size_t)((2 * (s & 15) + (s >> 4)) * 32) * (D_ * 2));
}

DI void code_again(const Ctx& c) {
    const char* pc; int len, back;
    asm volatile("s_getpc_b64 %0\n.Lcpb%=:\n\ts_mov_b32 %2, .Lcpb%=-.Lcpf_a\n\ts_mov_b32 %1, .Lcpf_end-.Lcpf_a-4" : "=s"(pc), "=s"(len), "=s"(back));
    pc -= back;
    const int o = min((((c.bid >> 3) & 7) * 64 + c.lane) * 128, len);
    asm volatile("s_mov_b32 m0, %0\n\ts_nop 3\n\tglobal_load_lds_dword %1, %2" ::"s"(M_TCH), "v"(o), "s"(pc) : "memory");
}
template <bool HALO, bool FIRST> DI void qkv_load(const Ctx& c0, const char* xin, const char* W, const char* Wo, int l, KP kp = nullptr, unsigned va0 = 0, unsigned vw0 = 0) {
    const Ctx c = fresh(c0);
    asm volatile("" : "+s"(xin), "+s"(W), "+s"(Wo));
    LAS char* smem = c.smem;
    constexpr int NAI = HALO ? 5 : 4, NWI = 6, XS = FIRST ? XS_ : XSB, NR = HALO ? 144 : 128;
    constexpr Sched<NAI, NWI, FIRST> SC{};
    static_assert(NQA == 3 && NQW == 4, "schedule");
    const int lw = c.wid - 4, lane = c.lane, lr = lane >> 3, lc = lane & 7, fr = lane & 15, fq = lane >> 4;
    const int row0 = c.pq * 128;
    const bool cq_ = CPY && !FIRST && (c.jq & 7) == 3;
    if (cq_) W = c.ws + WS_CQ + ((HALO ? 0 : 2) + (c.jq >> 3)) * 24576;
    asm volatile("" : "+s"(W));
    constexpr int SP = FIRST ? NC : (HALO ? NCH : 0);
    static_assert(FIRST || SP + DS <= TL0 + 2 + TSP, "one flag for both kinds of sums");
    const int rot = FIRST ? rot_first(c.pq, c.jq) : (l == 1 ? c.pq : SP);
    Order od = FIRST ? first_order(c.pq, c.jq) : make_order<SP>(0, rot);
    if (FIRST) asm volatile("" : "+s"(od.base));
    if (FIRST || HALO) asm volatile("" : "+s"(od.r), "+s"(od.re));
    auto tile = [&](int s) { return FIRST || HALO ? first_tile<SP>(od, s) : s; };
    unsigned va[5], vw[NWI];
#pragma unroll
    for (int j = 0; j < 4; ++j) va[j] = (unsigned)(FIRST ? va0 + j * 32 * (D_ * 2) : c.pq * 262144 + (j * 4 + lw) * 1024 + lane * 16);
    if (HALO) va[4] = (unsigned)((c.pq > 0 ? (c.pq - 1) * 262144 + 14336 : 0) + lw * 512 + lane * 16);
#pragma unroll
    for (int j = 0; j < NWI; ++j) vw[j] = FIRST ? vw0 + (j & 1) * (32 * 6144) + (j >> 1) * 2048 : pick(cq_, (unsigned)((j * 4 + lw) * 1024 + lane * 16), wq_src(j * 4 + lw, lane, c.jq));
    auto issueA = [&](int s, int slot) {
        const char* b = xin + (unsigned)(tile(s) * (FIRST ? 128 : 16384));
#pragma unroll
        for (int j = 0; j < 4; ++j) dma16<true>(QA_OFF + slot * QA_SLOT + (j * 4 + lw) * 1024, va[j], b);
        if (HALO) { if (lane < 32) dma16<true>(QA_OFF + slot * QA_SLOT + 16384 + lw * 512, va[4], b); }
    };
    auto issueW = [&](int s, int slot, int j0 = 0, int j1 = NWI) {
        const char* b = W + (unsigned)(tile(s) * (64 * 6144));
#pragma unroll
        for (int j = j0; j < j1; ++j) dma16<CPY && !FIRST>(qw_slot(slot) + (j * 4 + lw) * 1024, vw[j], b);
    };
    auto ahead = [&]() {
        const int r3 = (lane * 43) >> 7;
        const unsigned o = (unsigned)((((rot - (c.pq & 7) + 10) * 64 + (c.pq & 7) * 48 + lw * 12 + r3) & 1023) * 6144 + (lane - 3 * r3) * 2048 + c.jq * 128);
        if (lane < 36 && (WTC != 2 || (c.jq & 7) == 3)) asm volatile("s_mov_b32 m0, %0\n\ts_nop 3\n\tglobal_load_lds_dword %1, %2" ::"s"(M_TCH), "v"(o), "s"(W) : "memory");
    };
    if (ST0 && FIRST && lw == 1) STAMP_AT(47);
    if (FIRST && ERL) {
        SCHED();
        issueA(1, 1); issueW(1, 1);
        SCHED();
    }
    const bool docs = FIRST || (HALO && l == 1);
    const int t = lw * 64 + lane;
    static_assert(MBOX & 1, "residual flag");
    unsigned foff, ftg, so, ho, fo_, co, l3, l4, vwo, wt[3], cso, xt, xt1, xso, tp, tl;
    u32x4 on;
    auto addr1 = [&]() {
        int lane = c.lane;
        asm volatile("" : "+v"(lane));
        const int fr = lane & 15, fq = lane >> 4;
        const int ph = c.pq > 0 ? c.pq - 1 : 0;
        foff = pick(lane != 32, (unsigned)WS_FLAGQ + pick(lane < 16, c.pq * 16 + lane, pick(lane < 32, fr * 16 + c.jq, ph * 16 + 14 + (lane & 1))) * FQS, (unsigned)WS_XBOX + (c.pw * 16 + c.jw) * (2 * FQS) + c.jw * 8);
        ftg = lane != 32 ? (unsigned)(4 * l + 1) : (unsigned)(l + 1);
        so = FIRST ? (unsigned)((c.pq * 16 + fq * 4) * 1024 + (lw * 32 + fr * 2) * 8) : (unsigned)((c.pq * 16 + (lane >> 2)) * 1024 + (c.jq * 8 + lw * 2 + (lane & 1)) * 8);
        ho = FDM ? (unsigned)(262144 + ((lw * 36 + lane < 16 ? ph * 128 + 128 : c.pq * 128) + lw * 36 + lane - 16) * 8)
                 : (unsigned)(262144 + pick(lane < 32, c.pq * 128 + lw * 32 + lane, ph * 128 + 112 + lw * 4 + (lane & 3)) * 8);
        fo_ = pick(lane < 2, (unsigned)(262144 + (c.pq * 128 + c.jq * 8 + lw * 2 + lane) * 8), 0x80000000u);
        const int cq = lw * 12 + (fr < 12 ? fr : 11);
        co = (unsigned)((fq * 4 * 3072 + (cq >> 4) * 1024 + c.jq * 64 + (cq & 15) * 4) * 4);
        l3 = (unsigned)(M_CS + (HALO ? 768 : 0) + (cq * 4 + fq) * 4);
        l4 = (unsigned)(M_MEAN + pick(lane < 32, 16 + lw * 32 + (FIRST ? fr * 2 + fq : lane), lw * 4 + (lane & 3)) * 4);
        asm volatile("" : "+v"(foff), "+v"(ftg), "+v"(so), "+v"(co), "+v"(l3), "+v"(l4));
        if (!FIRST) asm volatile("" : "+v"(ho), "+v"(fo_));
        SCHED();
    };
    auto addr2 = [&]() {
        int lane = c.lane;
        asm volatile("" : "+v"(lane));
        const int fr = lane & 15, fq = lane >> 4;
        const int q4 = fr >> 2, p4 = lane & 3, fb = (q4 & 2) | ((fq & 1) << 2);
#pragma unroll
        for (int i = 0; i < 3; ++i) {
            const int T = 3 * lw + i;
            wt[i] = (unsigned)(qw_slot(SP % NQW) + (T >> 2) * 8192 + (8 * fq + q4) * 128 + (((2 * (T & 3) + (p4 >> 1)) ^ fb) << 4) + (p4 & 1) * 8);
        }
        const int T = 3 * lw + fq;
        cso = (unsigned)((((HALO ? 16 : 0) + rot) * 3072 + (T >> 2) * 1024 + c.jq * 64 + (T & 3) * 16 + fr) * 4);
        xt = (unsigned)(QA_OFF + (SP % NQA) * QA_SLOT + (lw * 32 + fr) * 128 + ((fq ^ (fr & 7)) << 4));
        xt1 = xt ^ 64;
        xso = (unsigned)hl<HS>((c.pq * 16 + rot) * 1024 + (lw * 32 + fr) * 8);
        on = (u32x4){0x3f803f80u, 0x3f803f80u, 0x3f803f80u, 0x3f803f80u};
        if (FIRST || HALO) asm volatile("" : "+v"(wt[0]), "+v"(wt[1]), "+v"(wt[2]), "+v"(cso), "+v"(on));
        if (FIRST) asm volatile("" : "+v"(xt), "+v"(xt1), "+v"(xso));
        SCHED();
    };
    auto addr3 = [&]() {
        Ctx c2 = c;
        asm volatile("" : "+v"(c2.lane));
        asm volatile("" : "+s"(c2.bid));
        vwo = wo_w_off(c2);
        tp = (unsigned)(pick(c2.lane < 30, (c2.bid >> 3) * 30 + c2.lane, (c2.bid >> 3) * 16 + c2.lane - 30 + (int)(WS_O >> 16)) * 65536 + 65536 - 128);
        tl = (unsigned)(lw == 1 ? ((c2.bid >> 3) * 2 + c2.lane) * 65536 : c2.bid * 8192 + (TWO && !(EXS && (c.jq & 7) == 3) ? c2.lane * 128 : 0));
        asm volatile("" : "+v"(vwo));
        if (FIRST) asm volatile("" : "+v"(tp), "+v"(tl));
        SCHED();
    };
    if (!FIRST || AD1 < 0) addr1();
    if (FIRST && ERL >= 5) { issueA(2, 2); SCHED(); }
    if (FIRST && (ERL == 2 || ERL == 5)) { issueW(2, 2); SCHED(); }
    if (!FIRST || AD2 < 0) addr2();
    if (!FIRST || AD3 < 0) addr3();
#pragma unroll
    for (int j = 0; j < NAI; ++j) asm volatile("" : "+v"(va[j]));
#pragma unroll
    for (int j = 0; j < NWI; ++j) asm volatile("" : "+v"(vw[j]));
    SCHED();
    int tv = 0; unsigned long long lt = 0;
    if (FIRST) {
        LSTAMP(55);
    } else {
        static_assert(CPY == 1 && RS0 > 0 && RN0 == NWI && WPRE >= 3 && (FIRST || SC.w[0] == NAI), "order");
        unsigned vx[2], xfo = (unsigned)WS_XBOX + (c.pq * 16 + c.jq) * (2 * FQS) + (HALO && c.pq > 0 ? (lane & 16) * (FQS / 16) : 0) + fr * 8, go = M_GO;
        u64 xv = lds_epoch(smem) + (unsigned)(l + 1);
#pragma unroll
        for (int i = 0; i < 2; ++i) vx[i] = pick(cq_, (unsigned)(((2 * lw + i) * 4 + 3) * 1024 + lane * 16), wq_src((2 * lw + i) * 4 + 3, lane, c.jq));
        asm volatile("" : "+v"(vx[0]), "+v"(vx[1]), "+v"(xfo), "+v"(xv), "+v"(go));
        BAR();
        issueW(0, 0);
        auto word = [&](int off) { return __builtin_amdgcn_readfirstlane(*(volatile LAS int*)(lds(go) + off)); };
        auto until = [&](int off, int v) {
#pragma unroll 1
            for (unsigned spins = 0; spins < (1u << 24); ++spins) { if (word(off) == v) break; }
            FENCE();
        };
        static_assert(WPRE == 3, "stages");
        int t1 = tile(1), t2 = tile(2);
        asm volatile("" : "+s"(t1), "+s"(t2));
        auto stage = [&](int s) {
            const char* b = W + (unsigned)((s == 1 ? t1 : t2) * (64 * 6144));
            unsigned d = (unsigned)((s == 3 ? QW_TOP : s * QW_SLOT) + lw * 1024), dx = d + (unsigned)(lw * 7168 + 3072);
            asm volatile("" : "+s"(d), "+s"(dx), "+s"(b));
#pragma unroll
            for (int j = 0; j < NWI; ++j) dma16<true>(d + j * 4096, vw[j], b);
#pragma unroll
            for (int i = 0; i < 2; ++i) dma16<true>(dx + i * 4096, vx[i], b);
        };
        auto first2 = [&]() { __builtin_amdgcn_s_setprio(LPRIO); issueA(0, 0); issueA(1, 1); };
        if (lw == 3) {
            if (!EPOLL) until(M_CNT - M_GO, 8 * l);
            Ask t;
            poll_rmw(t, c.ws, xfo, lane < (HALO && c.pq > 0 ? 32 : 16), xv, 63 - l);
            *(volatile LAS int*)lds(go) = l;
            until(M_CNT - M_GO, 8 * l);
            first2();
            STAMP_AT(32 + l * 8 + 0);
            wait_v<NAI>();
            keep(t);
        } else {
            until(M_CNT - M_GO, 8 * l);
            if (RSLP) __builtin_amdgcn_s_sleep(RSLP);
            int ns = 1;
#pragma unroll 1
            for (; ns < WPRE; ++ns) {
                if (word(0) == l) break;
                stage(ns);
            }
            until(0, l);
            first2();
            const int left = WPRE - ns;
#pragma unroll 1
            for (; ns < WPRE; ++ns) stage(ns);
            if (left == 0) wait_v<NAI>();
            else if (left == 1) wait_v<NAI + 8>();
            else if (left == 2) wait_v<NAI + 16>();
            else wait_v<NAI + 24>();
        }
        FENCE();
    }
    if (FIRST && ERL == 3) issueW(2, 2);
    if (ST0 && FIRST && lw == 2) STAMP_AT(57);
    if (FIRST && !ERL) BAR();
    __builtin_amdgcn_s_setprio(LPRIO);
    if (FIRST && !ERL) { LSTAMP(57); issueA(1, 1); issueW(1, 1); LSTAMP(58); }
    u32x4 st[4], cs[4]; u32x2 sh, fl; u64 fv = 0; float rs_, rq_, hs;
    f32x4 d[3], sm[2], g[2];
    bf16x8 wf[3][2], xf[2][2];
    const bool fact = FIRST ? lane < 32 : ((lane < 16 || lane >= (docs ? 16 : 32)) && lane <= (HALO ? 34 : 32));
    static_assert(XS + 3 <= (FIRST ? 12 : 13) && SPR == 1, "aux schedule");
    auto side = [&](auto ktc) {
        constexpr int kt = decltype(ktc)::value;
        {
            constexpr int C0 = FIRST ? 12 : XS + 2, C1 = FIRST ? 13 : XS + 3, C2 = 13;
            auto acc4 = [](u32x4& a, const u32x4& b) {
#pragma unroll
                for (int i = 0; i < 4; ++i) a[i] = __float_as_uint(addf(__uint_as_float(a[i]), __uint_as_float(b[i])));
                asm volatile("" : "+v"(a));
            };
            auto fin = [&]() {
                rs_ *= -1.f / 1024.f;
                asm volatile("" : "+v"(rs_));
                rq_ = __builtin_amdgcn_rsqf(fmaxf(rq_ * (1.f / 1024.f) - rs_ * rs_, 0.f) + LN_EPS);
                asm volatile("" : "+v"(rq_));
            };
            if constexpr (FIRST && kt == XS + 2) {
                asm volatile("" : "+v"(st[0]), "+v"(st[1]), "+v"(st[2]));
                acc4(st[0], st[1]); acc4(st[0], st[2]);
            }
            if constexpr (FIRST && kt == XS + 3) { asm volatile("" : "+v"(st[3])); acc4(st[0], st[3]); }
            if constexpr (kt == C0) {
                if (docs) { asm volatile("" : "+v"(cs[0]), "+v"(cs[1])); acc4(cs[0], cs[1]); }
            }
            if constexpr (FIRST && kt == 12) {
                asm volatile("s_nop 1" : "+v"(st[0]));
                rs_ = fold16(__uint_as_float(st[0][0]), __uint_as_float(st[0][2])); rq_ = fold16(__uint_as_float(st[0][1]), __uint_as_float(st[0][3]));
                rs_ = fold32(rs_, rs_); rq_ = fold32(rq_, rq_);
            }
            if constexpr (FIRST && kt == 13) fin();
            if constexpr (!FIRST && kt == TL0 + 2) {
                asm volatile("" : "+v"(sh));
                hs = fold16(__uint_as_float(sh[0]), __uint_as_float(sh[1]));
                hs = fold32(hs, hs);
                hs += __uint_as_float(__builtin_amdgcn_update_dpp(0u, __float_as_uint(hs), 0x124, 0xf, 0xf, false));
                hs += __uint_as_float(__builtin_amdgcn_update_dpp(0u, __float_as_uint(hs), 0x128, 0xf, 0xf, false));
                asm volatile("" : "+v"(hs));
            }
            if constexpr (!FIRST && kt == TL0 + 2 + TSP) {
                auto hh = __builtin_amdgcn_permlane16_swap(__float_as_uint(hs), __float_as_uint(hs), false, false);
                rs_ = __uint_as_float(hh[0]); rq_ = __uint_as_float(hh[1]);
                asm volatile("" : "+v"(rs_), "+v"(rq_));
                fin();
                auto rf = __builtin_amdgcn_make_buffer_rsrc(c.ws + WS_STATS + (size_t)l * 524288, 0, 524288, 0x00020000);
                __builtin_amdgcn_raw_buffer_store_b64((u32x2){__float_as_uint(rs_), __float_as_uint(rq_)}, rf, fo_, 0, 16);
            }
            if constexpr (kt == C1) {
                if (docs) { asm volatile("" : "+v"(cs[2]), "+v"(cs[3])); acc4(cs[2], cs[3]); acc4(cs[0], cs[2]); }
            }
            if constexpr (kt == C2) {
                if (docs) {
                    asm volatile("s_nop 1" : "+v"(cs[0]));
                    hs = fold32(fold16(__uint_as_float(cs[0][0]), __uint_as_float(cs[0][1])), fold16(__uint_as_float(cs[0][2]), __uint_as_float(cs[0][3])));
                }
            }
            if constexpr (kt == 13) {
                static_assert(WV0 == 1, "wait");
                if (FIRST || !FDM || docs) WAIT_V0();
                if (!FIRST && !FDM) { asm volatile("" : "+v"(sh)); rs_ = __uint_as_float(sh[0]); rq_ = __uint_as_float(sh[1]); }
                if ((FIRST || !FDM) && lane < (HALO ? 36 : 32)) {
                    *(LAS float*)lds(l4) = rs_;
                    *(LAS float*)(lds(l4) + (M_RSTD - M_MEAN)) = rq_;
                }
                if (docs) *(LAS float*)lds(l3) = hs;
                FENCE();
            }
        }
    };
    static_for<0, KT>([&](auto ktc) {
        constexpr int kt = decltype(ktc)::value;
        LSTAMP(3 * kt);
        if constexpr (FIRST || kt > 0) wait_v<SC.w[kt]>();
        LSTAMP(3 * kt + 1);
        if constexpr (kt == XS) {
            if (lw == 0) {
                asm volatile("" : "+v"(fl));
                const u64 ep = lds_epoch(smem);
                if (!__all(!fact || reached(__builtin_bit_cast(u64, fl), ep, ftg))) poll_flags(c.ws, ep, foff, fact, ftg);
            }
        }
        if constexpr (kt == 14) asm volatile("s_waitcnt lgkmcnt(0)" ::: "memory");
        BAR();
        FENCE();
        LMARK();
        LSTAMP(3 * kt + 2);
        if constexpr (TOPJ & (1 << kt)) side(ktc);
        if constexpr (kt == (FIRST ? SP + DS : TL0 + 2 + TSP) + 3) {
            if (lw == 0 && lane == 0) __hip_atomic_store((GAS u64*)(c.ws + WS_FLAGQ + (c.pq * 16 + c.jq) * FQS), fv, __ATOMIC_RELAXED, __HIP_MEMORY_SCOPE_AGENT);
        }
        if constexpr (kt == XS + 1 - NQA) {
            if (lw == 0) { if (fact) aload2c(fl, foff, c.ws); }
        }
        {
            const char* sb = c.ws + WS_STATS + (size_t)l * 524288;
            const char* cb = c.ws + WS_CSUM + (HALO ? 16 * 3072 * 4 : 0);
            constexpr int sa = 3 * (kt - XS), ca = FIRST ? 2 * (kt - XS - 1) : 2 * (kt - XS), cn = 2;
            if constexpr (FIRST) {
#pragma unroll
                for (int i = sa; i < sa + 3; ++i) if (i >= 0 && i < 4) aload4c(st[i], so, sb + i * 1024);
            }
            if constexpr (!FIRST && kt == TL0) aload2c(sh, so, sb);
            if constexpr (!FIRST && kt == TL1) {
                static_assert(TL1 <= 12, "covered by the wait of step 14");
                if (FDM) { if (lane < 36) { touch(M_MEAN + lw * 144, ho, sb); touch(M_RSTD + lw * 144, ho, sb + 4); } }
                else aload2c(sh, ho, sb);
            }
            if constexpr (ca >= 0 && ca < 4) {
                if (docs) {
#pragma unroll
                    for (int i = ca; i < ca + cn; ++i) aload4c(cs[i], co, cb + i * 12288);
                }
            }
        }
        if constexpr (kt == SP) {
            if (docs) {
#pragma unroll
                for (int i = 0; i < 3; ++i)
#pragma unroll
                    for (int ks = 0; ks < 2; ++ks) {
                        const s16x4 lo = tr_read(lds(wt[i]) + ks * 4096), hi = tr_read(lds(wt[i]) + ks * 4096 + 512);
                        wf[i][ks] = __builtin_shufflevector(lo, hi, 0, 1, 2, 3, 4, 5, 6, 7);
                    }
                if (FIRST) {
#pragma unroll
                    for (int mt = 0; mt < 2; ++mt)
#pragma unroll
                        for (int ks = 0; ks < 2; ++ks) xf[mt][ks] = *(const LAS bf16x8*)(lds(ks ? xt1 : xt) + mt * 2048);
                }
            }
            SCHED();
        }
        if constexpr (kt + 2 < KT && !(FIRST && ERL >= 5 && kt == 0)) issueA(kt + 2, (kt + 2) % NQA);
        LPUT(3 * kt + 2);
        if constexpr (kt == SP + DM) {
            SCHED();
            if (docs) {
                const bf16x8 ones = __builtin_bit_cast(bf16x8, on);
                const f32x4 zero = {0.f, 0.f, 0.f, 0.f};
#pragma unroll
                for (int ks = 0; ks < 2; ++ks) {
#pragma unroll
                    for (int i = 0; i < 3; ++i) d[i] = MFMA(ones, wf[i][ks], ks ? d[i] : zero);
                    if (FIRST) {
#pragma unroll
                        for (int mt = 0; mt < 2; ++mt) { sm[mt] = MFMA(ones, xf[mt][ks], ks ? sm[mt] : zero); g[mt] = MFMA(xf[mt][ks], xf[mt][ks], ks ? g[mt] : zero); }
                    }
                }
            }
            SCHED();
        }
        if constexpr (FIRST && ERL < 2 && kt < 5) {
            if constexpr (kt < 4) issueW(kt + 2, (kt + 2) % NQW);
            if constexpr (kt == 3) issueW(6, 2, 0, NWI / 2);
            if constexpr (kt == 4) { issueW(6, 2, NWI / 2, NWI); issueW(7, 3); }
        } else {
            if constexpr (WPRE == 2 && kt < 2) issueW(kt + 2, kt + 2);
            if constexpr (FIRST && (ERL == 4 || ERL == 6) && kt == 0) issueW(2, 2);
            if constexpr ((FIRST || WPRE == 3) && kt == 0) issueW(3, 3);
            if constexpr (kt >= 1 && kt + 3 < KT) issueW(kt + 3, (kt + 3) % NQW);
        }
        if constexpr (FIRST && kt == WT0) ahead();
        if constexpr ((CP2 & 4) && FIRST && kt == 8) { if (lw == 2) code_again(c); }
        if constexpr ((CP2 & 8) && HALO && kt == 8) { if (lw == 2 && l == 1) code_again(c); }
        if constexpr (FIRST && kt == TSTEP) {
            if (lw == 1 && lane < 46) touch(M_TCH, tp, c.ws);
        }
        if constexpr (FIRST && kt == TW0) {
            if (lw == 0) touch(M_TCH, tl, Wo);
            else if (lw == 1) { asm volatile("" : "+s"(kp)); const char* outp = (const char*)kp->out; if (lane < 2) touch(M_TCH, tl, outp); }
        }
        if constexpr (kt == SP && DM > 0) {
            SCHED();
            if (docs) {
                asm volatile("" : "+v"(wf[0][0]), "+v"(wf[0][1]), "+v"(wf[1][0]), "+v"(wf[1][1]), "+v"(wf[2][0]), "+v"(wf[2][1]));
                if (FIRST) asm volatile("" : "+v"(xf[0][0]), "+v"(xf[0][1]), "+v"(xf[1][0]), "+v"(xf[1][1]));
            }
        }
        if constexpr (kt == SP + DS) {
            SCHED();
            if (docs) {
                if (FIRST) {
                    auto rst = __builtin_amdgcn_make_buffer_rsrc(c.ws + WS_STATS, 0, 524288, 0x00020000);
                    const bool b0 = fr & 1, b1 = fr & 2;
                    u32x2 sv[2];
#pragma unroll
                    for (int mt = 0; mt < 2; ++mt) {
                        float qa = b0 ? g[mt][1] : g[mt][0], qb = b0 ? g[mt][3] : g[mt][2];
                        asm volatile("" : "+v"(qa), "+v"(qb));
                        sv[mt] = (u32x2){__float_as_uint(sm[mt][0]), __float_as_uint(b1 ? qb : qa)};
                    }
                    if (fq == (fr >> 2)) {
                        __builtin_amdgcn_raw_buffer_store_b64(sv[0], rst, xso, 0, 16);
                        __builtin_amdgcn_raw_buffer_store_b64(sv[1], rst, xso + hl<HS>(128), 0, 16);
                    }
                }
                float d0 = d[0][0], d1 = d[1][0], d2 = d[2][0];
                asm volatile("" : "+v"(d0), "+v"(d1), "+v"(d2));
                const float v = fq == 0 ? d0 : (fq == 1 ? d1 : d2);
                auto rc = __builtin_amdgcn_make_buffer_rsrc(c.ws + WS_CSUM, 0, 2 * 16 * 3072 * 4, 0x00020000);
                if (lane < 48) __builtin_amdgcn_raw_buffer_store_b32(__float_as_uint(v), rc, cso, 0, 16);
                asm volatile("s_waitcnt lgkmcnt(0)" ::: "memory");
            }
        }
        if constexpr (kt == 1) {
            if (lw == 0) {
                const u32x2 e = *(const LAS u32x2*)(smem + M_EP);
                const u64 es = ((u64)(unsigned)__builtin_amdgcn_readfirstlane(e[1]) << 32) | (unsigned)__builtin_amdgcn_readfirstlane(e[0]);
                fv = es + (unsigned)(4 * l + 1);
                asm volatile("" : "+s"(fv));
            }
        }
        if constexpr (FIRST && kt == AD1) addr1();
        if constexpr (FIRST && kt == AD2) addr2();
        if constexpr (FIRST && kt == AD3) addr3();
        if constexpr (!(TOPJ & (1 << kt))) side(ktc);
        if constexpr (kt == 13) wo_w_issue<0, WT13>(c, Wo, vwo);
        if constexpr (kt == 14) { wo_w_issue<WT13, (HALO || WA0 > 12) ? 12 : WA0>(c, Wo, vwo); if (HALO) wo_w_issue<23, 23 + WB14>(c, Wo, vwo); }
        if constexpr (kt == 15) {
            if (HALO ? WBE < 2 : WA0 > 12) wo_w_issue<12, HALO ? 18 : WA0>(c, Wo, vwo);
            if (HALO) wo_w_issue<23 + WB14, 23 + WB14 + WB15>(c, Wo, vwo);
        }
    });
    __builtin_amdgcn_s_setprio(0);
    BAR();
    LMARK(); LPUT(48);
    LSTAMP(48);
    LSTORE(0);
    if (HALO ? WBE < 1 : WAE < 1) wo_w_issue<18, HALO ? 23 : 20>(c, Wo, vwo);
    if (HALO) {
        static_assert(!WBS || (WBE == 1 && WB14 == 0 && WRG <= 12), "Wo weights of the first twelve steps are older than the last 6 + WB15");
        if (WBS) wait_v<6 + WB15>();
        BAR();
        if (WBE) wo_w_issue<WBE < 2 ? 18 : 12, 23>(c, Wo, vwo);
        wo_w_issue<23 + WB14 + WB15, 32>(c, Wo, vwo);
    }
}

DI float max3(float a, float b, float c) { return fmaxf(fmaxf(a, b), c); }
#define SGB(m, n) __builtin_amdgcn_sched_group_barrier(m, n, 0)
template <bool HALO> struct Tail {
    int f[26], n[4], t[4][26], d[4][27], v[4][27], h3[32], s3[32], o3[33], q3[112];
    constexpr Tail() : f{}, n{}, t{}, d{}, v{}, h3{}, s3{}, o3{}, q3{} {
        const int FB[26] = {0, 1, 2, 3, 4, 5, 6, 7, 8, 9, 10, 11, 12, 13, 14, 15, 24, 25, 19, 23, 18, 22, 17, 21, 16, 20};
        const int FA[24] = {0, 1, 2, 3, 4, 5, 6, 7, 8, 9, 10, 11, 12, 13, 14, 15, 16, 17, 20, 21, 18, 19, 22, 23};
        const int TA[4][26] = {{0, 1, 2, 3, 7, 6, 5, 4, 8, 9, 10, 11, 15, 14, 13, 12, 16, 17, 18, 19, 23, 22, 21, 20, 0, 0}, {4, 8, 9, 10, 11, 15, 14, 13, 12, 16, 17, 18, 19, 23, 22, 21, 20, 0, 1, 2, 3, 7, 6, 5, 0, 0}, {16, 17, 18, 19, 23, 22, 21, 20, 0, 1, 2, 3, 7, 6, 5, 4, 8, 9, 10, 11, 15, 14, 13, 12, 0, 0}, {17, 18, 19, 23, 22, 21, 20, 8, 9, 10, 11, 15, 14, 13, 12, 16, 0, 0, 0, 0, 0, 0, 0, 0, 0, 0}};
        const int DA[4][26] = {{1, 1, 1, 1, 1, 1, 1, 1, 1, 1, 1, 1, 1, 1, 1, 1, 1, 1, 1, 1, 1, 1, 0, 0, 0, 0}, {1, 1, 1, 1, 1, 1, 1, 1, 1, 1, 1, 1, 1, 1, 1, 1, 1, 1, 1, 1, 1, 1, 0, 0, 0, 0}, {1, 1, 1, 1, 1, 1, 1, 1, 1, 1, 1, 1, 1, 1, 1, 1, 0, 0, 0, 0, 0, 0, 0, 0, 0, 0}, {2, 2, 2, 2, 1, 1, 1, 1, 1, 1, 1, 1, 0, 0, 0, 0, 0, 0, 0, 0, 0, 0, 0, 0, 0, 0}};
        const int VA[4][26] = {{1, 1, 0, 0, 0, 0, 1, 1, 1, 1, 1, 1, 1, 1, 1, 1, 1, 1, 1, 1, 1, 1, 2, 2, 0, 0}, {1, 1, 1, 1, 1, 1, 1, 1, 1, 1, 1, 1, 1, 1, 1, 1, 1, 1, 1, 1, 1, 1, 2, 2, 0, 0}, {1, 1, 1, 1, 1, 1, 1, 1, 1, 1, 1, 1, 1, 1, 1, 1, 2, 2, 2, 2, 2, 3, 3, 2, 0, 0}, {0, 0, 0, 0, 1, 1, 1, 1, 1, 1, 1, 1, 2, 2, 2, 2, 0, 0, 0, 0, 0, 0, 0, 0, 0, 0}};
        const int TB[4][26] = {{0, 1, 2, 3, 7, 6, 5, 4, 24, 8, 9, 10, 11, 15, 14, 13, 12, 25, 16, 17, 18, 19, 23, 22, 21, 20}, {24, 8, 9, 10, 11, 15, 14, 13, 12, 25, 16, 17, 18, 19, 23, 22, 21, 20, 0, 1, 2, 3, 7, 6, 5, 4}, {25, 16, 17, 18, 19, 23, 22, 21, 20, 0, 1, 2, 3, 7, 6, 5, 4, 24, 8, 9, 10, 11, 15, 14, 13, 12}, {0, 1, 2, 3, 7, 6, 5, 4, 24, 8, 9, 10, 11, 15, 14, 13, 12, 25, 19, 23, 0, 0, 0, 0, 0, 0}};
        const int DB[4][26] = {{1, 1, 1, 1, 1, 1, 1, 1, 1, 1, 1, 1, 1, 1, 1, 1, 1, 1, 1, 1, 1, 1, 1, 0, 0, 0}, {1, 1, 1, 1, 1, 1, 1, 1, 1, 1, 1, 1, 1, 1, 1, 1, 1, 1, 1, 1, 1, 1, 0, 0, 0, 0}, {1, 1, 1, 1, 1, 1, 1, 1, 1, 1, 1, 1, 1, 1, 1, 1, 1, 1, 1, 1, 1, 1, 1, 0, 0, 0}, {2, 2, 1, 1, 1, 1, 1, 1, 1, 1, 1, 1, 1, 1, 1, 1, 1, 1, 1, 0, 0, 0, 0, 0, 0, 0}};
        const int VB[4][26] = {{1, 1, 1, 1, 1, 1, 1, 1, 1, 1, 1, 1, 1, 1, 1, 1, 1, 1, 1, 1, 1, 1, 1, 2, 2, 2}, {1, 1, 1, 1, 1, 1, 1, 1, 1, 1, 1, 1, 1, 1, 1, 1, 1, 1, 1, 1, 1, 1, 3, 3, 3, 2}, {1, 1, 1, 1, 1, 1, 1, 1, 1, 1, 1, 1, 1, 1, 1, 1, 1, 1, 1, 1, 1, 1, 1, 3, 3, 2}, {0, 0, 1, 1, 1, 1, 1, 1, 1, 1, 1, 1, 1, 1, 1, 1, 1, 1, 1, 2, 0, 0, 0, 0, 0, 0}};
        const int G[32] = {8, 12, 9, 13, 10, 14, 11, 15, 16, 17, 20, 21, 18, 19, 22, 23, 0, 4, 1, 5, 2, 6, 3, 7, 0, 4, 1, 5, 2, 6, 3, 7};
        const int NM = HALO ? 26 : 24;
        n[0] = n[1] = n[2] = NM; n[3] = HALO ? 20 : 16;
        for (int i = 0; i < NM; ++i) f[i] = HALO ? FB[i] : FA[i];
        int vv = 0;
        for (int h = 0; h < 4; ++h) {
            v[h][0] = vv;
            for (int i = 0; i < n[h]; ++i) {
                t[h][i] = HALO ? TB[h][i] : TA[h][i];
                d[h][i + 1] = d[h][i] + (HALO ? DB[h][i] : DA[h][i]);
                vv += HALO ? VB[h][i] : VA[h][i];
                v[h][i + 1] = vv;
            }
        }
        for (int i = 0; i < 32; ++i) { h3[i] = G[i]; s3[i] = i >= 16 && i < 24 ? 0 : 1; }
        for (int i = 3; i < 32; ++i) o3[i + 1] = o3[i] + TOPS;
        int nv = 0, ns = 0;
        for (int k = 0; k < 8 * 14; ++k) {
            const bool top = k % TOPS < 2 || nv == 96;
            if (top && ns < 16 && nv >= 12 * (ns / 2 + 1) && (nv == 96 || k % TOPS == (ns & 1))) { q3[k] = (ns / 2) * 14 + 13 - (ns & 1); ++ns; }
            else { q3[k] = (nv / 12) * 14 + nv % 12; ++nv; }
        }
    }
};
template <bool HALO> DI void qkv_consume(const Ctx& c0, int l, KP kp, unsigned va0 = 0, unsigned vw0 = 0) {
    const Ctx c = fresh(c0);
    if (ST0 && l == 0 && c.wid == 2) STAMP_AT(59);
    LAS char* smem = c.smem;
    const int lane = c.lane, wr = c.wid >> 1, wc = c.wid & 1, fr = lane & 15, fq = lane >> 4;
    const int sw = HALO ? wr : 0;
    f32x4 acc[4][6], ah[2];
#pragma unroll
    for (int mt = 0; mt < 4; ++mt)
#pragma unroll
        for (int nt = 0; nt < 6; ++nt) { acc[mt][nt] = (f32x4){0.f, 0.f, 0.f, 0.f}; asm volatile("" : "+v"(acc[mt][nt])); }
    ah[0] = ah[1] = (f32x4){0.f, 0.f, 0.f, 0.f};
    if (HALO) asm volatile("" : "+v"(ah[0]), "+v"(ah[1]));
    const unsigned aoff0 = (unsigned)(QA_OFF + wr * 8192 + (HALO || l ? lane * 16 : fr * 128 + ((fq ^ (fr & 7)) << 4)));
    const unsigned hoff0 = (unsigned)(QA_OFF + 16384 + lane * 16);
    unsigned xm = HALO || l ? 1024u : 64u;
    asm volatile("" : "+s"(xm));
    static_assert(((QA_OFF | QA_SLOT) & 2047) == 0, "halves");
    const int q4 = fr >> 2, p4 = lane & 3;
    const int fb = (q4 & 2) | ((fq & 1) << 2);
    unsigned boff[2];
#pragma unroll
    for (int cc = 0; cc < 2; ++cc)
        boff[cc] = (unsigned)((8 * fq + q4) * 128 + (((2 * (2 * wc + (cc ^ sw)) + (p4 >> 1)) ^ fb) << 4) + (p4 & 1) * 8);
    bf16x8 xa[2][4], xh[2], wb[2][6];
    auto ldfrag = [&](int set, unsigned pa, unsigned ph, unsigned pb0, unsigned pb1, int ks) {
#pragma unroll
        for (int mt = 0; mt < 4; ++mt) xa[set][mt] = *(const LAS bf16x8*)(lds(pa) + mt * 2048);
        if (HALO) xh[set] = *(const LAS bf16x8*)lds(ph);
#pragma unroll
        for (int nt = 0; nt < 6; ++nt) {
            const LAS char* pb = lds((nt & 1) ? pb1 : pb0) + (nt >> 1) * 8192 + ks * 4096;
            const s16x4 lo = tr_read(pb), hi = tr_read(pb + 512);
            wb[set][nt] = __builtin_shufflevector(lo, hi, 0, 1, 2, 3, 4, 5, 6, 7);
        }
    };
    constexpr int NM = HALO ? 26 : 24, NH = HALO ? 17 : 16;
    auto mm = [&](int set, int i0, int i1) {
#pragma unroll
        for (int ii = i0; ii < i1; ++ii) {
            const int i = !HALO || ii < 8 ? ii : (!SNK ? (ii < 10 ? ii + 16 : ii - 2) : (ii == 8 ? 24 : (ii < 17 ? ii - 1 : (ii == 17 ? 25 : ii - 2))));
            const int nt = i >> 2, mt = SNK && (nt & 1) ? 3 - (i & 3) : i & 3;
            if (i < 16) acc[mt][nt] = MFMA(wb[set][nt], xa[set][mt], acc[mt][nt]);
            else if (i < 24) acc[mt][nt] = MFMA(xa[set][mt], wb[set][nt], acc[mt][nt]);
            else if (i == 24) ah[0] = MFMA(wb[set][2], xh[set], ah[0]);
            else ah[1] = MFMA(xh[set], wb[set][4], ah[1]);
        }
    };
    auto interleave = [&]() {
#pragma unroll
        for (int i = 0; i < NH; ++i) { SGB(0x008, 1); SGB(0x100, 1); }
        SCHED();
    };
    unsigned sa = 0, sb = 0;
    unsigned pa0 = aoff0, pa1 = aoff0 ^ xm, ph0 = hoff0, ph1 = hoff0 ^ xm, pb0 = boff[0], pb1 = boff[1];
    asm volatile("" : "+v"(pa0), "+v"(pa1), "+v"(pb0), "+v"(pb1));
    if (HALO) asm volatile("" : "+v"(ph0), "+v"(ph1));
    constexpr int M_CSH = M_CS + (HALO ? 768 : 0);
    unsigned sa0 = (unsigned)(M_MEAN + 64 + (wr * 64 + fr) * 4), sa1 = (unsigned)(M_MEAN + 64 + wr * 256 + fq * 16);
    unsigned sa2 = (unsigned)(M_CSH + wc * 128 + sw * 64 + fq * 16), sa6 = (unsigned)(M_CSH + 512 + wc * 128 + sw * 64 + fr * 4);
    unsigned sa3 = sa2 ^ 64, sa7 = sa6 ^ 64, sa4 = (unsigned)(M_MEAN + fr * 4), sa5 = (unsigned)(M_MEAN + fq * 16);
    asm volatile("" : "+v"(sa0), "+v"(sa1), "+v"(sa2), "+v"(sa6), "+v"(sa3), "+v"(sa7));
    if (HALO) asm volatile("" : "+v"(sa4), "+v"(sa5));
    const int head = c.jq * 2 + wc;
    auto rso = __builtin_amdgcn_make_buffer_rsrc(HALO ? c.ws + WS_O + (size_t)l * (8u << 20) + (size_t)((head * 16 + c.pq) * 8 + wr * 4) * (1024 << HO)
                                                      : c.ws + WS_KV + (size_t)(l >> 1) * (16u << 20) + (size_t)((c.pq * 32 + head) * 4 + wr) * (4096 << HK), 0, HALO ? 4096 << HO : 12288 << HK, 0x00020000);
    unsigned xrs = (unsigned)(M_XCH + wc * 6144 + wr * 2048), xhs = (unsigned)(M_XCH + wc * 6144 + 8 - wr * 8);
    unsigned b0m = (c.pq == 0 && wr == 0) ? 15u * 16u : 0u;
    asm volatile("" : "+s"(rso));
    if (HALO) asm volatile("" : "+s"(xrs), "+s"(xhs), "+s"(b0m));
    u32x4 cpy[3];
    unsigned cv, vt, cdst;
    const char *cs_, *tw, *cd_;
    int more = !HALO && l == 0 ? KT - 3 - XST - (c.pq & 7) : 0, three = c.jq & 4;
    if (!HALO) {
        static_assert(CPY + TOUCH == 2 && XSP == 1, "extra");
        asm volatile("" : "+s"(kp));
        const int g = c.jq >> 2, n = c.pq * 4 + (c.jq & 3), m = n >> 5, kt = (n >> 1) & 15;
        const char* src = *(const char* const __attribute__((address_space(4)))*)((const __attribute__((address_space(4))) char*)kp + (three ? (m ? 8 : 24) : (m ? 32 : 40)));
        const char* tsr = *(const char* const __attribute__((address_space(4)))*)((const __attribute__((address_space(4))) char*)kp + (c.wid < 3 ? 24 : 40));
        int o1 = kt * (64 * 6144) + (n & 1) * (32 * 6144) + (3 + 4 * (g & 2) - c.jq) * 128 + 2048, o2 = (n & 31) * 65536 + (11 - 4 * g) * 128 - c.pq * 128 * (D_ * 2) + 2048;
        int d1 = (int)WS_CQ + kt * (64 * 6144) + (m * 2 + (g >> 1)) * 24576 + (n & 1) * 4096, d2 = (int)WS_CO + (n & 31) * 65536 + (m * 2 + 1 - (g >> 1)) * 4096;
        asm volatile("" : "+s"(o1), "+s"(o2), "+s"(d1), "+s"(d2));
        cs_ = src + (three ? o1 : o2);
        tw = tsr + (unsigned)((c.wid < 3 ? c.wid : 0) * (2u << 20) + c.bid * 8192);
        cd_ = c.ws + (unsigned)(three ? d1 : d2);
        cv = pick(three, vw0, va0 ^ ((lane & 8) << 1));
        cdst = (unsigned)(c.wid * 1024 + lane * 16);
        vt = TWQ ? (unsigned)(((lane & 7) == 3 ? lane - 1 : lane) * 128) : 0u;
        if (EXS) vt &= (c.jq & 7) == 3 ? 0u : ~0u;
        asm volatile("" : "+s"(more), "+s"(three), "+s"(cs_), "+s"(tw), "+s"(cd_));
        asm volatile("" : "+v"(cv), "+v"(cdst), "+v"(vt));
        asm volatile("" : "=v"(cpy[0]), "=v"(cpy[1]), "=v"(cpy[2]));
    }
    unsigned hto = HT0 == 2 ? 0u : 0x80000000u;
    const char* W0 = (const char*)kp->wqkv0;
    if (!HALO && HT0) {
        const int n = ((c.pq & 7) * 3 + (c.jq & 3)) * 64 + c.wid * 16 + fr, r3 = (n * 43691) >> 17;
        const int t0 = rot_first(c.pq & 8, c.jq | 3) + 8;
        if (l == 0 && !(c.jq & 4) && (c.jq & 3) != 3 && lane < 16) hto = (unsigned)(((t0 * 64 + r3) & 1023) * 6144 + (n - 3 * r3) * 2048 + (c.jq | 3) * 128);
        asm volatile("" : "+v"(hto));
        asm volatile("" : "+s"(W0));
    }
    if (!HALO && HT0 == 2) { asm volatile("s_mov_b32 m0, %0\n\ts_nop 3\n\tglobal_load_lds_dword %1, %2" ::"s"(M_TCH), "v"(hto), "s"(W0) : "memory"); }
    if (!HALO && !ERL && l == 0) BAR();
    if (ST0 && l == 0 && c.wid == 1) STAMP_AT(44);
    if (!HALO) { if (HT0 == 2) wait_v<1>(); else WAIT_V0(); }
    if (ST0 && l == 0 && c.wid == 1) STAMP_AT(46);
    BAR();
    if (!HALO && HT0 == 1) { if (hto < 0x80000000u) asm volatile("s_mov_b32 m0, %0\n\ts_nop 3\n\tglobal_load_lds_dword %1, %2" ::"s"(M_TCH), "v"(hto), "s"(W0) : "memory"); }
    if (c.wid == 0) STAMP_AT(l * 8 + 0);
    ldfrag(0, pa0, ph0, pb0, pb1, 0);
    SCHED();
    if (ALN) WAIT_L0();
    SCHED();
    int cnt = KT - 3 - more, tv = 0;
    QSTAMP(0);
    auto half0 = [&]() {
        ldfrag(1, pa1, ph1, pb0, pb1, 1);
        mm(0, 0, NH);
        interleave();
        mm(0, NH, NH + 1); SCHED();
        asm volatile("s_addk_i32 %0, %2\n\ts_addk_i32 %1, %3" : "+s"(sa), "+s"(sb) : "i"(QA_SLOT), "i"(QW_SLOT) : "scc"); SCHED();
        mm(0, NH + 1, NH + 2); SCHED();
        asm volatile("s_cmpk_lg_u32 %0, %2\n\ts_cselect_b32 %0, %0, 0\n\ts_cmp_lg_u32 %1, %3\n\ts_cselect_b32 %1, %1, %4" : "+s"(sa), "+s"(sb) : "i"(NQA * QA_SLOT), "i"(3 * QW_SLOT), "i"(QW_TOP) : "scc"); SCHED();
        mm(0, NH + 2, NH + 3); SCHED();
        asm volatile("s_cmp_lg_u32 %0, %2\n\ts_cselect_b32 %0, %0, 0\n\ts_addk_i32 %1, -1" : "+s"(sb), "+s"(cnt) : "i"(QW_TOP + QW_SLOT) : "scc"); SCHED();
        mm(0, NH + 3, NH + 4); SCHED();
        pa0 = aoff0 + sa; pb0 = boff[0] + sb;
        asm volatile("" : "+v"(pa0), "+v"(pb0)); SCHED();
        mm(0, NH + 4, NH + 5); SCHED();
        pb1 = boff[1] + sb; pa1 = pa0 ^ xm;
        asm volatile("" : "+v"(pa1), "+v"(pb1)); SCHED();
        mm(0, NH + 5, NH + 6); SCHED();
        if (HALO) { ph0 = hoff0 + sa; ph1 = ph0 ^ xm; asm volatile("" : "+v"(ph0), "+v"(ph1)); SCHED(); }
        mm(0, NH + 6, NM);
        SCHED();
        WAIT_L0();
        BAR();
        SCHED();
    };
    asm volatile("" : "+s"(sa), "+s"(sb), "+s"(cnt));
    do {
    ALIGN8();
    do {
        half0();
        ldfrag(0, pa0, ph0, pb0, pb1, 0);
        mm(1, 0, NH);
        interleave();
        mm(1, NH, NM - 1);
        SCHED();
        WAIT_L0();
        asm volatile("s_nop 0");
        SCHED();
        mm(1, NM - 1, NM);
        SCHED();
    } while (cnt != 0);
    if (!HALO) {
        asm volatile("s_cmp_eq_u32 %[more], 0\n\ts_cbranch_scc1 .Lex%=\n\ts_mov_b32 m0, %[tch]\n\ts_nop 3\n\tglobal_load_lds_dword %[vt], %[tw] sc1\n\t"
                     "global_load_dwordx4 %[c0], %[v0], %[cs] offset:-2048\n\ts_cmp_eq_u32 %[three], 0\n\ts_cbranch_scc1 .Lex%=\n\t"
                     "global_load_dwordx4 %[c1], %[v0], %[cs]\n\tglobal_load_dwordx4 %[c2], %[v0], %[cs] offset:2048\n.Lex%=:"
                     : [c0] "+v"(cpy[0]), [c1] "+v"(cpy[1]), [c2] "+v"(cpy[2])
                     : [more] "s"(more), [tch] "i"(M_TCH), [vt] "v"(vt), [tw] "s"(tw), [v0] "v"(cv), [cs] "s"(cs_), [three] "s"(three) : "scc", "memory");
        cnt = more; more = 0;
        asm volatile("" : "+s"(cnt), "+s"(more));
        SCHED();
    }
    } while (cnt != 0);
    if (!HALO) {
        asm volatile("s_cmp_lg_u32 %[l], 0\n\ts_cbranch_scc1 .Lex%=\n\ts_waitcnt vmcnt(0)\n\ts_nop 3\n\tglobal_store_dwordx4 %[d], %[c0], %[ws] sc1\n\ts_cmp_eq_u32 %[three], 0\n\ts_cbranch_scc1 .Lex%=\n\t"
                     "v_add_u32 %[d], 0x2000, %[d]\n\tglobal_store_dwordx4 %[d], %[c1], %[ws] sc1\n\tv_add_u32 %[d], 0x2000, %[d]\n\tglobal_store_dwordx4 %[d], %[c2], %[ws] sc1\n.Lex%=:\n\ts_nop 1"
                     : [d] "+v"(cdst) : [c0] "v"(cpy[0]), [c1] "v"(cpy[1]), [c2] "v"(cpy[2]), [l] "s"(l), [ws] "s"(cd_), [three] "s"(three) : "scc", "memory");
    }
    QSTAMP(1);
    FENCE();
    SCHED();

    f32x4 csq[4], m4[4], mh4; float csv[2], mr[4], rr[4], mh;
    auto r1 = [&](unsigned b, int off) { return *(const LAS float*)(lds(b) + off); };
    auto r4_ = [&](unsigned b, int off) { return *(const LAS f32x4*)(lds(b) + off); };
    auto mf = [&](int set, int i) {
        const int nt = i >> 2, mt = i & 3;
        if (i < 16) acc[mt][nt] = MFMA(wb[set][nt], xa[set][mt], acc[mt][nt]);
        else if (i < 24) acc[mt][nt] = MFMA(xa[set][mt], wb[set][nt], acc[mt][nt]);
        else if (i == 24) ah[0] = MFMA(wb[set][2], xh[set], ah[0]);
        else ah[1] = MFMA(xh[set], wb[set][4], ah[1]);
        SCHED();
    };
    auto fold = [&](int i, int e) {
        const int nt = i >> 2, mt = i & 3;
        float t;
        if (i < 16) t = __builtin_fmaf(mr[mt], csq[nt][e], acc[mt][nt][e]);
        else if (i < 24) t = __builtin_fmaf(m4[mt][e], csv[nt - 4], acc[mt][nt][e]);
        else if (i == 24) t = __builtin_fmaf(mh, csq[2][e], ah[0][e]);
        else t = __builtin_fmaf(mh4[e], csv[0], ah[1][e]);
        asm volatile("" : "+v"(t));
        if (i < 24) acc[mt][nt][e] = t; else ah[i - 24][e] = t;
    };
    constexpr Tail<HALO> TL{};
    constexpr int NP = HALO ? 6 : 2, FN = 4 * NM;
    static_assert(TL.v[3][TL.n[3]] == NP + FN + (HALO ? 2 : 0) && KT % NQA == 1 && KT % NQW == 0, "tail");
    s16x4 wl[6], wh[6];
    auto frd = [&](int set, int k, int ao) {
        if (k < 4) xa[set][k] = *(const LAS bf16x8*)(lds(set ? pa1 : pa0) + ao + k * 2048);
        else if (k == 16) xh[set] = *(const LAS bf16x8*)(lds(set ? ph1 : ph0) + ao);
        else {
            const int nt = (k - 4) >> 1;
            const LAS char* pb = lds((nt & 1) ? pb1 : pb0) + (nt >> 1) * 8192 + set * 4096;
            if (k & 1) { wh[nt] = tr_read(pb + 512); wb[set][nt] = __builtin_shufflevector(wl[nt], wh[nt], 0, 1, 2, 3, 4, 5, 6, 7); }
            else wl[nt] = tr_read(pb);
        }
    };
    constexpr int RS = M_RSTD - M_MEAN;
    unsigned vl, vg, tb, xr, xh_, b0a, qb0, qb1;
    f32x4 bprev, bcur, bprev0; u32x4 ps; u32x2 ko; float ninf;
    auto rdop = [&](int h, int k) {
        const int j = k - NH;
        if (h == 0) {
            if (k < 2) { mr[2 * k] = r1(sa0, k * 128); mr[2 * k + 1] = r1(sa0, k * 128 + 64); }
            else if (k < 4) csq[k - 2] = r4_((k & 1) ? sa3 : sa2, 0);
            else if (j < 4) frd(0, k - 4, 2 * QA_SLOT);
            else csq[j - 2] = r4_((j & 1) ? sa3 : sa2, 256);
        } else if (j < 0) frd(h & 1, k, h == 1 ? 2 * QA_SLOT : 0);
        else if (h == 1) {
            if (j < 2) csv[j] = r1(j ? sa7 : sa6, 0);
            else if (HALO && j == 2) mh = r1(sa4, 0);
            else if (HALO && j == 3) mh4 = r4_(sa5, 0);
            else { const int mt = HALO ? 7 - j : j - 2; m4[mt] = r4_(sa1, mt * 64); }
        } else if (h == 2) {
            if (j == 0) m4[2] = r4_(sa1, 128);
            if (j == 1) ko = *(const LAS u32x2*)(lds(tb) + 3072);
            if (j == 2) { rr[0] = r1(sa0, RS); rr[1] = r1(sa0, 64 + RS); }
            if (j == 3) { rr[2] = r1(sa0, 128 + RS); rr[3] = r1(sa0, 192 + RS); }
            if (j >= 4) m4[5 - j] = r4_(sa1, (5 - j) * 64);
        } else {
            if (j == 0) bprev = r4_(tb, 0);
            if (j == 1) bcur = r4_(tb, 1024);
            if (j == 2) ps = *(const LAS u32x4*)(lds(tb) + 2048);
            if (j == 3) bprev0 = r4_(b0a, 0);
        }
    };
    auto vop = [&](int k) {
        if (k == 0) { asm volatile("" : "+v"(boff[0])); qb0 = boff[0] + QW_TOP; asm volatile("" : "+v"(qb0)); }
        else if (k == 1) { asm volatile("" : "+v"(boff[1])); qb1 = boff[1] + QW_TOP; asm volatile("" : "+v"(qb1)); }
        else if (k < NP) {
            if (k == 2) { vl = (unsigned)(lane * 16); asm volatile("" : "+v"(vl)); }
            if (k == 3) { tb = vl + M_TAB; asm volatile("" : "+v"(tb)); }
            if (k == 4) { b0a = tb | b0m; asm volatile("" : "+v"(b0a)); }
            if (k == 5) { xr = xrs + vl; asm volatile("" : "+v"(xr)); }
        } else if (k < NP + FN) fold(TL.f[(k - NP) >> 2], (k - NP) & 3);
        else {
            if (k == NP + FN) { xh_ = xhs + vl; asm volatile("" : "+v"(xh_)); }
            if (k == NP + FN + 1) { ninf = -INFINITY; asm volatile("" : "+v"(ninf)); }
        }
    };
    unsigned wa, wq;
    int wr2 = wr, wc2 = wc;
#pragma unroll
    for (int i = 0; i < NM; ++i) {
        mf(0, TL.t[0][i]);
        if (TL.t[0][i] < 24) asm volatile("" : "+v"(acc[TL.t[0][i] & 3][TL.t[0][i] >> 2])); else asm volatile("" : "+v"(ah[TL.t[0][i] & 1]));
        FENCE();
        if (i < NH) frd(1, i, 0);
        if (!HALO && i == NH + 3) { asm volatile("" : "+s"(wr2), "+s"(wc2)); vl = (unsigned)(lane * 16); wq = (unsigned)(AT_Q + (wc2 * 8 + wr2 * 4) * 1024) + vl; asm volatile("" : "+v"(vl), "+v"(wq)); }
        if (!HALO && i == NH + 4) { asm volatile("" : "+s"(wr2), "+s"(wc2)); wa = (unsigned)(AT_OWN + wc2 * 16384 + wr2 * 4096) + vl; asm volatile("" : "+v"(wa)); }
        if (i == NH + 1) { asm volatile("" : "+v"(boff[0]), "+v"(boff[1])); pb0 = boff[0] + 2 * QW_SLOT; pb1 = boff[1] + 2 * QW_SLOT; asm volatile("" : "+v"(pb0), "+v"(pb1)); }
        if (i == NH + 2) { pa0 = aoff0; pa1 = aoff0 ^ xm; asm volatile("" : "+v"(pa1)); if (HALO) { ph0 = hoff0; ph1 = hoff0 ^ xm; asm volatile("" : "+v"(ph1)); } }
        SCHED();
    }
    WAIT_L0();
    BAR();
    FENCE();
    SCHED();
    auto frags = [&](int h) {
        static_assert(TL.d[0][NM] == NH + 6 && TL.d[1][NM] == NH + (HALO ? 5 : 6) && TL.d[2][NM] == NH + (HALO ? 6 : 0) && TL.d[3][TL.n[3]] == NH + (HALO ? 4 : 0), "reads");
        if (h == 0) __builtin_amdgcn_s_waitcnt(0xC27F);
        if (h == 1) __builtin_amdgcn_s_waitcnt(HALO ? 0xC57F : 0xC67F);
        if (h == 2) __builtin_amdgcn_s_waitcnt(HALO ? 0xC67F : 0xC07F);
        if (h == 3) __builtin_amdgcn_s_waitcnt(HALO ? 0xC47F : 0xC07F);
    };
#pragma unroll
    for (int h = 0; h < 4; ++h) {
        if (h == 2) { pb0 = qb0; pb1 = qb1; }
#pragma unroll
        for (int i = 0; i < TL.n[h]; ++i) {
            if (!(h & 1) && i == TL.n[h] - 1) { frags(h); SCHED(); if (h) QSTAMP(4); }
            mf(~h & 1, TL.t[h][i]);
#pragma unroll
            for (int k = TL.d[h][i]; k < TL.d[h][i + 1]; ++k) { FENCE(); rdop(h, k); }
            SCHED();
#pragma unroll
            for (int k = TL.v[h][i]; k < TL.v[h][i + 1]; ++k) vop(k);
            SCHED();
        }
        if (h & 1) {
            frags(h);
            if (h == 1) QSTAMP(2); else QSTAMP(5);
            BAR();
            FENCE();
            SCHED();
            if (h == 1) QSTAMP(3); else QSTAMP(6);
        }
    }
    if (!HALO) {
        f32x4 r4[4];
        mf(1, 8);
#pragma unroll
        for (int mt = 0; mt < 4; ++mt) rr[mt] = r1(sa0, mt * 64 + RS);
        SCHED();
        mf(1, 12);
#pragma unroll
        for (int mt = 0; mt < 4; ++mt) r4[mt] = r4_(sa1, mt * 64 + RS);
        SCHED();
        mf(1, 9);
        vg = HK ? (unsigned)hv<HK>(lane) : vl;
        if (HK) asm volatile("" : "+v"(vg));
        SCHED();
        u32x4 pk[8];
        auto op = [&](int g, int k) {
            const int ta = g < 4 ? g : (g - 4) & 2, tb = g < 4 ? g : ta + 1, na = g < 4 ? 2 : 4 + (g & 1), nb = g < 4 ? 3 : na;
            f32x4& A = acc[ta][na]; f32x4& Bv = acc[tb][nb];
            if (k < 12) {
                const int h = k / 6, kk = k % 6;
                f32x4& X = h ? Bv : A;
                const int tx = h ? tb : ta;
                if (kk < 4) { float t = X[kk] * (g < 4 ? rr[tx] : r4[tx][kk]); asm volatile("" : "+v"(t)); X[kk] = t; }
                else { unsigned t = cvt_pk_bf16(X[2 * (kk - 4)], X[2 * (kk - 4) + 1]); asm volatile("" : "+v"(t)); pk[g][2 * h + kk - 4] = t; }
            } else if (k == 12) __builtin_amdgcn_raw_buffer_store_b128(pk[g], rso, vg + hp<HK>(g & 3), (g >> 2) * (8192 << HK), 16);
            else *(LAS u32x4*)(lds(wa) + (g >> 2) * 8192 + (g & 3) * 1024) = pk[g];
            SCHED();
        };
        auto pm = [&](int i) {
            if (i < 16) mf(1, TL.h3[i]);
            else if (i < 32) mf(((i - 16) >> 1) & 1, ((i - 16) >> 2) + 4 * (i & 1));
        };
        pm(3);
#pragma unroll
        for (int g = 0; g < 8; ++g) {
#pragma unroll
            for (int k = 0; k < 6; ++k) op(g, k);
            pm(2 * g + 4);
            if (g > 0) op(g - 1, 12);
#pragma unroll
            for (int k = 6; k < 12; ++k) op(g, k);
            pm(2 * g + 5);
            if (g > 0 && g <= LWA) op(g - 1, 13);
        }
        op(7, 12);
        QSTAMP(7);
        u32x4 qk;
        auto qop = [&](int mt, int k) {
            if (k == 0) { rr[mt] *= QSCALE; asm volatile("" : "+v"(rr[mt])); }
            else if (k < 13) {
                const int h = (k - 1) / 6, kk = (k - 1) % 6;
                f32x4& X = acc[mt][h];
                if (kk < 4) { float t = X[kk] * rr[mt]; asm volatile("" : "+v"(t)); X[kk] = t; }
                else { unsigned t = cvt_pk_bf16(X[2 * (kk - 4)], X[2 * (kk - 4) + 1]); asm volatile("" : "+v"(t)); qk[2 * h + kk - 4] = t; }
            } else *(LAS u32x4*)(lds(wq) + mt * 1024) = qk;
            SCHED();
        };
#pragma unroll
        for (int mt = 0; mt < 3; ++mt) {
            pm(20 + 4 * mt); if (3 * mt + LWA < 8) op(3 * mt + LWA, 13); qop(mt, 0); qop(mt, 1); qop(mt, 2);
            pm(21 + 4 * mt); if (3 * mt + 1 + LWA < 8) op(3 * mt + 1 + LWA, 13); qop(mt, 3); qop(mt, 4); qop(mt, 5);
            pm(22 + 4 * mt); if (3 * mt + 2 + LWA < 8) op(3 * mt + 2 + LWA, 13); qop(mt, 6); qop(mt, 7); qop(mt, 8);
            pm(23 + 4 * mt); qop(mt, 9); qop(mt, 10); qop(mt, 11); qop(mt, 12); qop(mt, 13);
        }
        if (c.wid == 0) STAMP_AT(l * 8 + 1);
        QSTAMP(8);
        {
            const u32x2 q0 = pack4(acc[3][0] * (rr[3] * QSCALE)), q1 = pack4(acc[3][1] * (rr[3] * QSCALE));
            *(LAS u32x4*)(lds(wq) + 3 * 1024) = (u32x4){q0[0], q0[1], q1[0], q1[1]};
        }
        QSTAMP(9);
        QSTORE();
        if (K0E) {
            SCHED();
            WAIT_L0();
            BAR();
            FENCE();
            SCHED();
            if (KSP == 4) {
                publish<4, 0>(smem, lane, c.ws, (unsigned)WS_FLAGQ + (c.pq * 16 + c.jq) * FQS, lane == 0, (unsigned)(4 * l + 2));
                if (c.wid == 0) STAMP_AT(l * 8 + 2);
                SCHED();
            }
        }
    } else {
        typedef unsigned u32x10 __attribute__((ext_vector_type(10)));
        u32x4 kp[4], qp[4];
        u32x10 vw[2];
        float ks[2][4], cs[2][4];
        auto pk8 = [&](const f32x4& x, const f32x4& y) { const u32x2 a = pack4(x), b = pack4(y); u32x4 r = {a[0], a[1], b[0], b[1]}; asm volatile("" : "+v"(r)); SCHED(); return r; };
        auto pkv = [&](int mt, int cc) { const u32x2 a = pack4(acc[mt][4 + cc]); vw[cc][2 * mt + 2] = a[0]; vw[cc][2 * mt + 3] = a[1]; };
        auto fr8 = [](const u32x4& x) { return __builtin_bit_cast(bf16x8, x); };
        mf(1, 24);
        unsigned ka0 = ko[0] + wr * 256, ka1 = ko[1] + wr * 256;
        asm volatile("" : "+v"(ka0), "+v"(ka1));
        SCHED();
        mf(1, 25);
#pragma unroll
        for (int mt = 0; mt < 4; ++mt) ks[0][mt] = r1(ka0, mt * 64);
        rr[0] *= QSCALE; asm volatile("" : "+v"(rr[0]));
        SCHED();
        mf(1, 11);
#pragma unroll
        for (int mt = 0; mt < 4; ++mt) ks[1][mt] = r1(ka1, mt * 64);
        rr[1] *= QSCALE; asm volatile("" : "+v"(rr[1]));
        SCHED();
        mf(1, 15);
        rr[2] *= QSCALE; asm volatile("" : "+v"(rr[2]));
        rr[3] *= QSCALE; asm volatile("" : "+v"(rr[3]));
        SCHED();
        mf(1, 19);
        const u32x4 hh = pk8(ah[0], ah[1]);
        mf(1, 23);
        *(LAS u32x2*)lds(xh_) = (u32x2){hh[0], hh[1]}; *(LAS u32x2*)(lds(xh_) + 1024) = (u32x2){hh[2], hh[3]};
        SCHED();
        mf(1, 10);
        kp[3] = pk8(acc[3][2], acc[3][3]);
        mf(1, 14);
        *(LAS u32x4*)(lds(xr) + 2048) = kp[3];
        pkv(3, 0); asm volatile("" : "+v"(vw[0]));
        *(LAS u32x2*)(lds(xr) + 3072) = (u32x2){vw[0][8], vw[0][9]};
        SCHED();
        mf(1, 3);
        pkv(3, 1); asm volatile("" : "+v"(vw[1]));
        *(LAS u32x2*)(lds(xr) + 3072 + 8) = (u32x2){vw[1][8], vw[1][9]};
        SCHED();
        mf(1, 7);
        mf(1, 9);
        kp[2] = pk8(acc[2][2], acc[2][3]);
        mf(1, 13);
        qp[3] = pk8(acc[3][0], acc[3][1]);
        mf(1, 2);
        f32x4 s0[4], s1[4];
        u32x4 pk;
        auto sc = [&](int mt, int h) {
            if (h) { s1[mt] = MFMA(fr8(kp[mt]), fr8(qp[mt]), bcur); asm volatile("" : "+v"(s1[mt])); }
            else { s0[mt] = MFMA(fr8(mt ? kp[mt - (mt > 0)] : pk), fr8(qp[mt]), mt == 0 ? bprev0 : bprev); asm volatile("" : "+v"(s0[mt])); }
            SCHED();
        };
        auto csm = [&](int mt) { cs[0][mt] = ks[0][mt] * rr[mt]; asm volatile("" : "+v"(cs[0][mt])); cs[1][mt] = ks[1][mt] * rr[mt]; asm volatile("" : "+v"(cs[1][mt])); };
        float u[2][4], mx[4], iv[4];
        auto sv = [](float x) { asm volatile("" : "+v"(x)); return x; };
        auto mx2 = [&](float a, float b) { return max3(ninf, a, b); };
        auto pick = [&](int mt, int h) { u[h][mt] = sv(max3(mx2(s0[mt][2 * h], s0[mt][2 * h + 1]), s1[mt][2 * h], s1[mt][2 * h + 1]) * cs[h][mt]); };
        auto vf = [&](int i) { const int mt = 2 - (i >> 2), nt = 4 + (i & 1), set = (i >> 1) & 1; SCHED(); acc[mt][nt] = MFMA(xa[set][mt], wb[set][nt], acc[mt][nt]); asm volatile("" : "+v"(acc[mt][nt])); SCHED(); };
        mf(1, 6);
        kp[1] = pk8(acc[1][2], acc[1][3]);
        mf(1, 8);
        WAIT_L0();
        QSTAMP(7);
        BAR();
        FENCE();
        SCHED();
        QSTAMP(8);
        mf(1, 12);
        {
            const u32x2 k0 = *(const LAS u32x2*)(lds(xr) + 8);
            FENCE(); SCHED();
            const u32x2 k1 = *(const LAS u32x2*)lds(xr);
            FENCE(); SCHED();
            const u32x2 a = *(const LAS u32x2*)(lds(xr) + 1024 + 8);
            FENCE(); SCHED();
            const u32x2 b = *(const LAS u32x2*)(lds(xr) + 1024);
            pk = (u32x4){k0[0], k0[1], k1[0], k1[1]};
            vw[0][0] = a[0]; vw[0][1] = a[1]; vw[1][0] = b[0]; vw[1][1] = b[1];
        }
        SCHED();
        auto sw16 = [](float& a, float& b) { auto r = __builtin_amdgcn_permlane16_swap(__float_as_uint(a), __float_as_uint(b), false, false); a = __uint_as_float(r[0]); b = __uint_as_float(r[1]); };
        auto sw32 = [](float& a, float& b) { auto r = __builtin_amdgcn_permlane32_swap(__float_as_uint(a), __float_as_uint(b), false, false); a = __uint_as_float(r[0]); b = __uint_as_float(r[1]); };
        auto ldr = [&](int i) { };
        auto top = [&](int mt) { mx[mt] = sv(mx2(u[0][mt], u[1][mt])); };
        auto pv1 = [&](int mt, int k) {
            SCHED();
            vw[k >> 1][2 * mt + 2 + (k & 1)] = cvt_pk_bf16(acc[mt][4 + (k >> 1)][2 * (k & 1)], acc[mt][4 + (k >> 1)][2 * (k & 1) + 1]);
            asm volatile("" : "+v"(vw[k >> 1]));
            SCHED();
        };
        auto uk = [&](int mt) { SCHED(); u[0][mt] = sv(u[0][mt] * ks[0][mt]); u[1][mt] = sv(u[1][mt] * ks[1][mt]); SCHED(); };
        mf(1, 1);
        qp[2] = pk8(acc[2][0], acc[2][1]);
        mf(1, 5);
        csm(3); SCHED();
        mf(1, 0);
        kp[0] = pk8(acc[0][2], acc[0][3]);
        mf(1, 4);
        csm(2); SCHED();
        sc(3, 1);
        qp[1] = pk8(acc[1][0], acc[1][1]);
        sc(3, 0);
        csm(1); ldr(0); SCHED();
        sc(2, 1);
        qp[0] = pk8(acc[0][0], acc[0][1]);
        sc(2, 0);
        csm(0); pick(3, 0); SCHED();
        sc(1, 1);
        pick(3, 1); ldr(1); SCHED();
        sc(1, 0);
        pick(2, 0); top(3); SCHED();
        sc(0, 1);
        pick(2, 1); ldr(2); SCHED();
        sc(0, 0);
        if (c.wid == 0) STAMP_AT(l * 8 + 1);
        pick(1, 0); top(2);
        vf(0);
        pick(1, 1); ldr(3);
        vf(1);
        sw16(mx[2], mx[3]);
        pick(0, 0);
        vf(2);
        pick(0, 1); top(1);
        vf(3);
        top(0); mx[2] = sv(mx2(mx[2], mx[3]));
        vf(4);
        sw16(mx[0], mx[1]); mx[0] = sv(mx2(mx[0], mx[1]));
        vf(5); pv1(2, 0);
        sw32(mx[0], mx[2]);
        { const float a = mx[0], b = mx[2]; mx[0] = sv(mx2(a, b)); mx[2] = sv(mx2(b, a)); }
        vf(6); pv1(2, 1);
        sw32(mx[0], mx[2]);
        mx[3] = sv(mx[2]); mx[1] = sv(mx[0]);
        vf(7);
        sw16(mx[2], mx[3]); sw16(mx[0], mx[1]);
        SCHED();
#pragma unroll
        for (int mm = 0; mm < 4; ++mm) { const int mt = 3 - mm; u[0][mt] = sv(u[0][mt] - mx[mt]); u[1][mt] = sv(u[1][mt] - mx[mt]); if (mm == 1) vf(8); }
        vf(9);
#pragma unroll
        for (int mm = 0; mm < 4; ++mm) {
            const int mt = 3 - mm;
            u[0][mt] = sv(__builtin_amdgcn_exp2f(u[0][mt])); u[1][mt] = sv(__builtin_amdgcn_exp2f(u[1][mt]));
            if (mm > 0) iv[mt + 1] = sv(u[0][mt + 1] + u[1][mt + 1]);
            if (mm < 2) vf(10 + mm);
        }
        SCHED();
        sw16(iv[2], iv[3]);
        iv[0] = sv(u[0][0] + u[1][0]);
        iv[2] = sv(iv[2] + iv[3]);
        pv1(2, 2);
        sw16(iv[0], iv[1]); iv[0] = sv(iv[0] + iv[1]);
        uk(3);
        sw32(iv[0], iv[2]);
        iv[0] = sv(iv[0] + iv[2]);
        pv1(2, 3);
        iv[0] = sv(__builtin_amdgcn_rcpf(iv[0]));
        pv1(1, 0);
        iv[2] = sv(iv[0]);
        uk(2);
        sw32(iv[0], iv[2]);
        iv[3] = sv(iv[2]); iv[1] = sv(iv[0]);
        pv1(1, 1);
        sw16(iv[2], iv[3]); sw16(iv[0], iv[1]);
        SCHED();
        f32x4 o[4][2];
        u32x4 pf[4];
        auto nrm = [&](int mt) { u[0][mt] = sv(u[0][mt] * iv[mt]); u[1][mt] = sv(u[1][mt] * iv[mt]); SCHED(); };
        auto prb = [&](int mt) {
            const unsigned p = cvt_pk_bf16(u[0][mt], u[1][mt]);
            pf[mt] = (u32x4){__builtin_amdgcn_perm(0u, p, ps[0]), __builtin_amdgcn_perm(0u, p, ps[1]), __builtin_amdgcn_perm(0u, p, ps[2]), __builtin_amdgcn_perm(0u, p, ps[3])};
            asm volatile("" : "+v"(pf[mt]));
            SCHED();
        };
        auto pvm = [&](int mt, int cc) {
            o[mt][cc] = MFMA(fr8((u32x4){vw[cc][2 * mt], vw[cc][2 * mt + 1], vw[cc][2 * mt + 2], vw[cc][2 * mt + 3]}), fr8(pf[mt]), ((f32x4){0.f, 0.f, 0.f, 0.f}));
            asm volatile("" : "+v"(o[mt][cc]));
            SCHED();
        };
        auto out = [&](int mt) {
            const u32x2 lo = pack4(o[mt][0]), hi = pack4(o[mt][1]);
            __builtin_amdgcn_raw_buffer_store_b128((u32x4){lo[0], lo[1], hi[0], hi[1]}, rso, vg + hp<HO>(mt), 0, 16);
            SCHED();
        };
        vg = (unsigned)hv<HO>(lane);
        asm volatile("" : "+v"(vg));
        nrm(3); nrm(2); prb(3);
        pv1(1, 2); pv1(1, 3);
        pvm(3, 0); prb(2); pvm(3, 1);
        uk(1); uk(0);
        pvm(2, 0); nrm(1); nrm(0); pvm(2, 1);
        prb(1); pv1(0, 0); pv1(0, 1);
        pvm(1, 0); pv1(0, 2); pv1(0, 3); prb(0);
        pvm(1, 1); out(3); pvm(0, 0); out(2); pvm(0, 1); out(1); out(0);
        SCHED();
        QSTAMP(9);
        if (c.wid == 0) STAMP_AT(l * 8 + 2);
        QSTORE();
    }
}

#define MFMA32(a, b, c) __builtin_amdgcn_mfma_f32_32x32x16_bf16(__builtin_bit_cast(bf16x8, a), __builtin_bit_cast(bf16x8, b), (c), 0, 0, 0)
DI float max16(const f32x16& s) {
    float m = max3(s[0], s[1], s[2]), n = max3(s[3], s[4], s[5]);
    m = max3(m, s[6], s[7]); n = max3(n, s[8], s[9]);
    m = max3(m, s[10], s[11]); n = max3(n, s[12], s[13]);
    m = max3(max3(m, s[14], s[15]), n, n);
    auto b = __builtin_amdgcn_permlane32_swap(__float_as_uint(m), __float_as_uint(m), false, false);
    return __builtin_amdgcn_fmed3f(__uint_as_float(b[0]), __uint_as_float(b[1]), INFINITY);
}
template <bool HI> DI void attn_a(const Ctx& c0, int l, const char* Wo, KP kp) {
    const Ctx c = fresh(c0);
    asm volatile("" : "+s"(Wo));
    LAS char* smem = c.smem;
    const int lane = c.lane, w = c.wid;
    constexpr int H = HI ? 0 : 1;
    int qt = HI ? w - 4 : 3 - w;
    asm volatile("" : "+s"(qt));
    const float NEG = -INFINITY;
    int zq = (lane & 31) - 4 * (lane >> 5);
    unsigned lb = (unsigned)(((lane >> 4) & 1) * 1024 + ((lane >> 5) * 16 + (lane & 15)) * 16);
    unsigned qa = (unsigned)(AT_Q + H * 8192 + qt * 2048) + lb, ta = (unsigned)(AT_OWN + H * 16384 - 3 * 2048 + qt * 2048) + lb;
    asm volatile("" : "+v"(lb), "+v"(qa), "+v"(ta));
    auto rd = [&](unsigned base, int off) { return *(const LAS u32x4*)(lds(base) + off); };
    f32x16 ce;
    u32x4 sel;
    unsigned tm = (unsigned)(AT_N + lane * 16);
    auto masks = [&]() {
        if (HI) {
#pragma unroll
            for (int r = 0; r < 16; ++r) ce[r] = 8 * (r >> 2) + (r & 3) <= zq ? 0.f : NEG;
            asm volatile("" : "+v"(ce));
        }
        const unsigned s1 = ((lane >> 4) ^ (lane >> 2)) & 1 ? 0u : 0x3f803f80u;
        sel = (u32x4){s1, s1, s1, s1};
        asm volatile("" : "+v"(sel));
    };
    u64 ep, pa_, kvv, nkv, kact;
    unsigned po, pk_, cnta = M_CNT, one = 1u;
    char* ob;
    const char *kvb, *s1_, *s2_;
    unsigned vk, dst, fo;
    auto setup = [&]() {
        if (HI) {
            const u32x2 e = *(const LAS u32x2*)(smem + M_EP);
            ep = ((u64)(unsigned)__builtin_amdgcn_readfirstlane(e[1]) << 32) | (unsigned)__builtin_amdgcn_readfirstlane(e[0]);
            po = o_post(c);
            pk_ = pick(lane < 2, (unsigned)(WS_KBOX + ((c.pq + 1 + lane) * 16 + c.jq) * 256 + lane * 8), (unsigned)(WS_FLAGQ + (c.pq * 16 + c.jq) * FQS));
            pa_ = c.pq < 14 ? 7ull : (c.pq < 15 ? 5ull : 4ull);
            fo = (unsigned)(WS_KBOX + (c.pq * 16 + c.jq) * 256 + lane * 8);
            asm volatile("" : "+v"(po), "+v"(pk_), "+v"(fo));
            asm volatile("" : "+s"(ep), "+s"(pa_));
        }
        ob = c.ws + WS_O + (size_t)l * (8u << 20) + (size_t)(unsigned)(((c.jq * 2 + H) * 16 + c.pq) * 8192 + qt * 2048);
        kvb = c.ws + WS_KV + (size_t)(unsigned)((l >> 1) * (16u << 20) + c.jq * 32768);
        asm volatile("" : "+s"(ob), "+s"(kvb));
    };
    auto setup2 = [&]() {
        const int p1 = c.pq >= 1 ? c.pq - 1 : c.pq, p2 = c.pq >= 2 ? c.pq - 2 : c.pq;
        s1_ = kvb + (size_t)(unsigned)(p1 * 524288);
        s2_ = kvb + (size_t)(unsigned)(p2 * 524288);
        vk = (unsigned)((w & 3) * 1024 + lane * 16); dst = (unsigned)(AT_N + (w & 3) * 1024);
        asm volatile("" : "+s"(s1_), "+s"(dst));
        if (!HI) asm volatile("" : "+s"(s2_));
        asm volatile("" : "+v"(vk));
    };
    auto arrive = [&](auto cc, unsigned msk, unsigned val) {
        unsigned old = 0;
        if (lane == 0) asm volatile("ds_add_rtn_u32 %0, %1, %2 offset:%3\n\ts_waitcnt lgkmcnt(0)" : "+v"(old) : "v"(cnta), "v"(one), "i"(decltype(cc)::value * 4) : "memory");
        if ((__builtin_amdgcn_readfirstlane(old) & msk) == msk) { if (lane < 16) post(c.ws, po, ep + (4 * l + val)); }
    };
    static_assert((MBOX & 2) && !HO && !HK, "layout");
    int tv = 0;
    auto kvs = [&]() {
        if (!HI) {
            SCHED();
            LSTAMP(2);
            asm volatile("s_mov_b64 exec, 1\n\ts_waitcnt vmcnt(0)\n\tds_add_u32 %0, %1\n\ts_mov_b64 exec, -1" :: "v"(cnta), "v"(one) : "memory");
            LSTAMP(3);
            SCHED();
        }
    };
    asm volatile("" : "+v"(cnta), "+v"(one), "+v"(tm));
    if (HI) {
        masks(); setup(); setup2();
        if (w == 5) {
#pragma unroll
            for (int i = 0; i < 4; ++i) *(LAS f32x4*)(lds(tm) + i * 1024) = (f32x4){ce[4 * i], ce[4 * i + 1], ce[4 * i + 2], ce[4 * i + 3]};
        }
        kvv = ep + (4 * l + 2); nkv = 0 - kvv;
        kact = __builtin_amdgcn_ballot_w64(w == KPW && lane < 2 && lane < c.pq);
        asm volatile("" : "+v"(kvv), "+v"(nkv));
        asm volatile("" : "+s"(kact));
        WAIT_L0();
        WAIT_V0();
        LSTAMP(0);
        if (w == 4) __builtin_amdgcn_s_setprio(HP4);
        BAR();
        FENCE();
    }
    auto kvp = [&]() {
        if (HI && w == 4) {
            __builtin_amdgcn_s_setprio(3);
#pragma unroll 1
            for (unsigned spins = 0; spins < (1u << 24); ++spins) {
                if (__builtin_amdgcn_readfirstlane(*(volatile LAS int*)lds(cnta)) == 8 * l + 4) break;
            }
            if (__builtin_amdgcn_inverse_ballot_w64(pa_)) post(c.ws, pk_, kvv);
            STAMP_AT(l * 8 + 2);
            __builtin_amdgcn_s_setprio(HP4);
        }
        SCHED();
    };
    if (KPP == -1) kvp();
    FENCE();
    SCHED();
    LSTAMP(1);
    u32x4 q0, q1, k0, k1, va, vp, pa, pb;
    f32x16 cm, s0, s1, o;
    f32x4 ls;
    auto stage = [&](f32x16& sc, f32x16& sn, const f32x16& cn, bool pend, bool zero, unsigned kb, int ko, unsigned vb, int vo1, unsigned nb, int vo0) {
        auto ex = [&](int i) { float x = __builtin_amdgcn_exp2f(sc[i]); asm volatile("" : "+v"(x)); sc[i] = x; SCHED(); };
        auto cv = [&](u32x4& p, int j, int i) { unsigned x = cvt_pk_bf16(sc[i], sc[i + 1]); asm volatile("" : "+v"(x)); p[j] = x; SCHED(); };
        sn = MFMA32(k0, q0, cn); SCHED();
        k0 = rd(kb, ko); SCHED();
        ex(0); ex(1); ex(2);
        if (pend) { o = MFMA32(vp, pb, o); SCHED(); }
        ex(3); ex(8); ex(9);
        sn = MFMA32(k1, q1, sn); SCHED();
        k1 = rd(kb, ko + 512); SCHED();
        ex(10); ex(11); cv(pa, 0, 0); cv(pa, 1, 2);
        if (pend) { ls = MFMA(__builtin_bit_cast(bf16x8, sel), __builtin_bit_cast(bf16x8, pb), ls); SCHED(); }
        vp = rd(vb, 8192 + vo1 + 512); SCHED();
        cv(pa, 2, 8); cv(pa, 3, 10); ex(4); ex(5);
        o = MFMA32(va, pa, zero ? (f32x16)(0.f) : o); SCHED();
        va = rd(nb, 8192 + vo0); SCHED();
        ex(6); ex(7); ex(12);
        ls = MFMA(__builtin_bit_cast(bf16x8, sel), __builtin_bit_cast(bf16x8, pa), zero ? (f32x4)(0.f) : ls); SCHED();
        ex(13); ex(14); ex(15); cv(pb, 0, 4); cv(pb, 1, 6); cv(pb, 2, 12); cv(pb, 3, 14);
    };
    auto flush = [&]() {
        ls = MFMA(__builtin_bit_cast(bf16x8, sel), __builtin_bit_cast(bf16x8, pb), ls); SCHED();
        o = MFMA32(vp, pb, o); SCHED();
    };
    float nm;
    {
        if (!HI) {
#pragma unroll
            for (int i = 0; i < 4; ++i) {
                const f32x4 t = *(const LAS f32x4*)(lds(tm) + i * 1024);
                ce[4 * i] = t[0]; ce[4 * i + 1] = t[1]; ce[4 * i + 2] = t[2]; ce[4 * i + 3] = t[3];
            }
        }
        q0 = rd(qa, 0);
        const u32x4 d0 = rd(ta, 6144);
        q1 = rd(qa, 512);
        const u32x4 d1 = rd(ta, 6144 + 512);
        k0 = rd(ta, 4096); k1 = rd(ta, 4096 + 512); va = rd(ta, 8192 + 6144);
        SCHED();
        if (KSP == -1) kvs();
        if (!HI) { masks(); setup(); SCHED(); }
        s0 = MFMA32(d0, q0, ce);
        s0 = MFMA32(d1, q1, s0);
        SCHED();
        if (!HI) { setup2(); SCHED(); }
        if (KSP == 0) kvs();
        if (KPP == 0) kvp();
        nm = -max16(s0);
        asm volatile("" : "+v"(nm));
#pragma unroll
        for (int r = 0; r < 16; ++r) { float t; asm("v_mov_b32 %0, %1" : "=v"(t) : "v"(nm), "i"(r)); cm[r] = t; }
        asm volatile("" : "+v"(cm));
        SCHED();
#pragma unroll
        for (int r = 0; r < 16; ++r) s0[r] = addf(s0[r], nm);
        asm volatile("" : "+v"(s0));
        SCHED();
        if (KSP == 1) kvs();
        if (KPP == 1) kvp();
        stage(s0, s1, cm, false, true, ta, 2048, ta, 6144, ta, 4096);
        if (KSP == 2) kvs();
        if (KPP == 2) kvp();
        if (qt > 0) {
            stage(s1, s0, cm, true, false, ta, 0, ta, 4096, ta, 2048);
            if (KSP == 3) kvs();
            if (qt > 1) {
                stage(s0, s1, cm, true, false, ta, 0, ta, 2048, ta, 0);
                if (qt > 2) stage(s1, s0, cm, true, false, ta, 0, ta, 0, ta, 0);
            }
        } else if (KSP == 3) kvs();
        flush();
    }
    LSTAMP(4);
    f32x16 c2;
    unsigned na, nb;
    auto rso = __builtin_amdgcn_make_buffer_rsrc(ob, 0, 2048, 0x00020000);
    auto starts = [&]() {
        const float n1 = c.pq >= 1 ? nm : NEG, n2 = c.pq >= 2 ? nm : NEG;
        asm volatile("" : "+v"(zq));
#pragma unroll
        for (int r = 0; r < 16; ++r) { cm[r] = n1; c2[r] = n2; ce[r] = 8 * (r >> 2) + (r & 3) <= zq ? NEG : n2; }
        na = (unsigned)(AT_N + H * 32768) + lb; nb = na + (unsigned)(16384 + qt * 2048);
        asm volatile("" : "+v"(cm), "+v"(c2), "+v"(ce), "+v"(na), "+v"(nb));
        asm volatile("" : "+s"(rso));
        SCHED();
    };
    {
        auto piece = [&](int b, int i) { dma16<true>(dst + b * 16384 + i * 4096, vk, ((b & 1) ? s2_ : s1_) + (b >> 1) * 16384 + i * 4096); };
        auto block = [&](int b) {
#pragma unroll
            for (int i = 0; i < 4; ++i) piece(b, i);
        };
        Ask t;
        if (HI) {
            poll_rmw<true>(t, c.ws, fo, __builtin_amdgcn_inverse_ballot_w64(kact), nkv, 62 - 4 * l);
            if (w == KPW) STAMP_AT(32 + l * 8 + 1);
        }
        WAIT_L0();
        LSTAMP(5);
        BAR();
        FENCE();
        LSTAMP(6);
        piece(0, H); piece(0, 2 + H);
        if (HI) {
            starts();
            if (APRIO) __builtin_amdgcn_s_setprio(APRIO);
            WAIT_V0();
            keep(t);
            LSTAMP(7);
            BAR();
            FENCE();
            if (w == 4) STAMP_AT(l * 8 + 3);
        } else {
            if (AB2 >= 0) block(1);
            if (AB2 > 0) block(2);
            starts();
            Ctx c2 = c;
            if (ELO) c2.wid = 0;
            unsigned vw = wo_w_off(c2);
            int qs = c.cw == 3 ? 1024 : 8 * D_ * 2;
            asm volatile("" : "+v"(vw));
            asm volatile("" : "+s"(qs));
            SCHED();
            wait_v<4 * AB2 + 4>();
            LSTAMP(7);
            BAR();
            if (AB2 < 0) block(1);
            if (AB2 <= 0) block(2);
            block(3);
            wait_v<8>();
            BAR();
            if (!ELO) {
                wo_w_issue<WA0, WA1>(c, Wo, vw);
                wait_v<WA1 - WA0>();
            } else WAIT_V0();
            LSTAMP(9);
            BAR();
            FENCE();
            if (!ELO) wo_w_issue<WA1, 26>(c, Wo, vw);
            else {
                const int s0 = w == 0 ? WA0 : (w == 1 ? WA0 + EN0 : WA0 + EN0 + EN1), s1 = w == 0 ? WA0 + EN0 : (w == 1 ? WA0 + EN0 + EN1 : (w == 2 ? 22 : 0));
#pragma unroll 1
                for (int sl = s0; sl < s1; ++sl) {
                    const char* b = Wo + (size_t)(unsigned)((2 * (sl & 15) + (sl >> 4)) * 65536);
#pragma unroll
                    for (int q = 0; q < 4; ++q) dma16<true>(sl * 4096 + q * 1024, vw, b + q * qs);
                }
            }
            if (LSL) { for (int i = 0; i < qt; ++i) __builtin_amdgcn_s_sleep(LSL); }
        }
    }
    SCHED();
    LSTAMP(8);
    {
        const u32x4 d0 = rd(na, 0), d1 = rd(na, 512);
        k0 = rd(na, 2048); k1 = rd(na, 2048 + 512); va = rd(na, 8192);
        SCHED();
        s0 = MFMA32(d0, q0, cm);
        s0 = MFMA32(d1, q1, s0);
        SCHED();
        stage(s0, s1, cm, false, false, na, 4096, na, 0, na, 2048);
        stage(s1, s0, cm, true, false, na, 6144, na, 2048, na, 4096);
        if (HI) { BAR(); FENCE(); SCHED(); }
        stage(s0, s1, cm, true, false, nb, 0, na, 4096, na, 6144);
        if (HI && CPOS == 3) { BAR(); SCHED(); }
        stage(s1, s0, ce, true, false, nb, 2048, na, 6144, nb, 0);
        if (HI && CPOS == 4) { WAIT_L0(); BAR(); SCHED(); }
        stage(s0, s1, c2, true, false, nb, 4096, nb, 0, nb, 2048);
        if (HI && CPOS == 5) { BAR(); SCHED(); }
        if (qt < 3) {
            stage(s1, s0, c2, true, false, nb, 6144, nb, 2048, nb, 4096);
            if (qt < 2) {
                stage(s0, s1, c2, true, false, nb, 0, nb, 4096, nb, 6144);
                if (qt < 1) stage(s1, s0, c2, true, false, nb, 0, nb, 6144, nb, 0);
            }
        }
        flush();
    }
    LSTAMP(11);
    auto out = [&]() {
        const float iv = __builtin_amdgcn_rcpf(ls[0]);
#pragma unroll
        for (int u = 0; u < 2; ++u) {
            u32x4 t;
#pragma unroll
            for (int i = 0; i < 4; ++i) { const int r = 4 * u + (i >> 1) * 8 + (i & 1) * 2; t[i] = cvt_pk_bf16(o[r] * iv, o[r + 1] * iv); }
            __builtin_amdgcn_raw_buffer_store_b128(t, rso, lb + u * 512, 0, 16);
        }
        SCHED();
    };
    out();
    if (__builtin_expect(__any(!(ls[0] < SAFE_SUM)), 0)) {
        auto rk = __builtin_amdgcn_make_buffer_rsrc((void*)(kvb + H * 16384), 0, 16u << 20, 0x00020000);
        const int g0 = c.pq * 4 + qt - 8;
        float m = NEG;
#pragma unroll 1
        for (int ps = 0; ps < 2; ++ps) {
            o = (f32x16)(0.f); ls = (f32x4)(0.f);
#pragma unroll 1
            for (int j = g0 < 0 ? -g0 : 0; j < 9; ++j) {
                const int so = ((g0 + j) >> 2) * 524288 + ((g0 + j) & 3) * 2048;
                u32x4 f[4];
#pragma unroll
                for (int i = 0; i < 4; ++i) f[i] = __builtin_amdgcn_raw_buffer_load_b128(rk, lb + (i & 1) * 512, so + (i >> 1) * 8192, 16);
                f32x16 s;
                const float sb = ps ? -m : 0.f;
                float sa = j == 0 ? NEG : sb, sz = j == 8 ? NEG : sb;
                asm volatile("" : "+v"(zq), "+v"(sa), "+v"(sz));
#pragma unroll
                for (int r = 0; r < 16; ++r) s[r] = 8 * (r >> 2) + (r & 3) <= zq ? sa : sz;
                s = MFMA32(f[0], q0, s);
                s = MFMA32(f[1], q1, s);
                if (ps == 0) m = fmaxf(m, max16(s));
                else {
                    u32x4 p[2];
#pragma unroll
                    for (int r = 0; r < 16; r += 2) p[(r >> 2) & 1][(r >> 3) * 2 + ((r >> 1) & 1)] = cvt_pk_bf16(__builtin_amdgcn_exp2f(s[r]), __builtin_amdgcn_exp2f(s[r + 1]));
#pragma unroll
                    for (int u = 0; u < 2; ++u) {
                        o = MFMA32(f[2 + u], p[u], o);
                        ls = MFMA(__builtin_bit_cast(bf16x8, sel), __builtin_bit_cast(bf16x8, p[u]), ls);
                    }
                }
            }
        }
        __builtin_amdgcn_s_waitcnt(0x0F70);
        WAIT_V0();
        out();
    }
    SCHED();
    if (w == 3 || w == 4) STAMP_AT(32 + l * 8 + 3 + w);
    LSTAMP(12);
    if (HI) {
        WAIT_V0();
        arrive(std::integral_constant<int, 1>{}, 3u, 3u);
        if (w == 4) STAMP_AT(32 + l * 8 + 4);
        LSTAMP(13);
        if (APRIO) __builtin_amdgcn_s_setprio(0);
        if (lane == 0) asm volatile("ds_add_u32 %0, %1 offset:12" :: "v"(cnta), "v"(one) : "memory");
        WAIT_V0();
        arrive(std::integral_constant<int, 2>{}, 7u, 4u);
        if (w == 5 || w == 6) {
            Ctx c2 = c;
            c2.wid = 2 * w - 10;
            unsigned vw = wo_w_off(c2), vw2 = vw + (c.cw == 3 ? 1024u : 8u * D_ * 2);
            asm volatile("" : "+v"(vw), "+v"(vw2));
#pragma unroll 1
            for (unsigned spins = 0; spins < (1u << 22); ++spins) {
                if (__hip_atomic_load((LAS unsigned*)(smem + M_CNT + 12), __ATOMIC_RELAXED, __HIP_MEMORY_SCOPE_WORKGROUP) == (unsigned)(2 * l + 4)) break;
                __builtin_amdgcn_s_sleep(1);
            }
            FENCE();
            wo_w_issue<ELO ? 22 : 26, 30>(c2, Wo, vw);
            c2.wid += 1;
            wo_w_issue<ELO ? 22 : 26, 30>(c2, Wo, vw2);
        }
    }
    LSTAMP(23);
    ASTORE();
}

DI void wo_load(const Ctx& c0, int l, const char* Wn, const char* Wo) {
    const Ctx c = fresh(c0);
    asm volatile("" : "+s"(Wn), "+s"(Wo));
    const u64 ep = lds_epoch(c.smem);
    const unsigned fo = o_find(c);
    if ((CP2 & 1) && l == 0 && c.wid == 4) code_again(c);
    if ((CP2 & 2) && l == 1 && c.wid == 4) code_again(c);
        if (l == 0 && c.wid == 6) {
            const int fr = c.lane & 15, od = fr & 1, j0 = 4 * (c.lane >> 4) + od;
            const bool p0 = j0 > fr, p1 = j0 + 2 > fr;
            const unsigned N = 0xff800000u, Z = 0x0c0c0c0cu, lo = od ? 0x01000c0cu : 0x0c0c0100u, hi = od ? 0x03020c0cu : 0x0c0c0302u;
            LAS u32x4* t = (LAS u32x4*)(c.smem + M_TAB + c.lane * 16);
            t[0] = (u32x4){p0 && !od ? 0u : N, p0 && od ? 0u : N, p1 && !od ? 0u : N, p1 && od ? 0u : N};
            t[64] = (u32x4){!p0 && !od ? 0u : N, !p0 && od ? 0u : N, !p1 && !od ? 0u : N, !p1 && od ? 0u : N};
            t[128] = (u32x4){p0 ? lo : Z, p1 ? hi : Z, p0 ? Z : lo, p1 ? Z : hi};
            t[192] = (u32x4){(unsigned)(M_RSTD + j0 * 4 + (p0 ? 0 : 64)), (unsigned)(M_RSTD + j0 * 4 + 8 + (p1 ? 0 : 64)), 0u, 0u};
        }
    if (CPY && l == 0 && c.wid == CKW) {
#pragma unroll 1
        for (int i = 0; i < 2; ++i) {
            const int j = i ? c.jw : c.jq, g = i ? 2 - 2 * (j >> 3) : 1 + 2 * (j >> 3);
            if ((j & 7) == 3) poll_flags(c.ws, ep, (unsigned)WS_FLAGQ + ((c.lane >> 2) * 16 + 4 * g + (c.lane & 3)) * FQS, true, 2u);
        }
    }
    Ask t, t2;
    if (!(l & 1)) {
        Ctx c2 = c;
        c2.wid = 2 * c.wid - 10;
        unsigned vw = wo_w_off(c2), vw2 = vw + (c.cw == 3 ? 1024u : 8u * D_ * 2);
        asm volatile("" : "+v"(vw), "+v"(vw2));
        poll_rmw(t, c.ws, fo, c.wid == 7 && c.lane < 16, ep + (4 * l + 3), 61 - 4 * l);
        if (c.wid == 7) STAMP_AT(32 + l * 8 + 2);
        BAR();
        if (c.wid == 5 || c.wid == 6) {
            __builtin_amdgcn_s_sleep(W30S);
            wo_w_issue<30, 32>(c2, Wo, vw);
            c2.wid += 1;
            wo_w_issue<30, 32>(c2, Wo, vw2);
        }
        if (c.wid != 7) WAIT_V0();
    } else {
        asm volatile("" : "=v"(t.v[0]), "=v"(t.v[1]), "=v"(t.v[2]), "=v"(t.v[3]));
        WAIT_V0();
    }
    poll_rmw(t2, c.ws, fo, c.wid == 7 && c.lane < 16, ep + (4 * l + 4), 60 - 4 * l);
    if (c.wid == 7) STAMP_AT(32 + l * 8 + 2 + !(l & 1));
    BAR();
    WAIT_V0();
    keep(t); keep(t2);
    if (l == 3 && c.bid == 0 && c.wid == 7) {
#pragma unroll 1
        for (int i = 0; i < 4; ++i) poll_flags(c.ws, ep, (unsigned)WS_FLAGQ + (i * 64 + c.lane) * FQS, true, 1u);
        if (c.lane == 0) __hip_atomic_store((GAS u64*)(c.ws + WS_EPOCH), ep + 64, __ATOMIC_RELAXED, __HIP_MEMORY_SCOPE_AGENT);
    }
    if (RS0 && l == 3) BAR();
}

DI void wo_consume(const bool B, const Ctx& c0, const bf16_t* xin, bf16_t* xout, int l) {
    u32x4 res[2][2];
    const Ctx c = fresh(c0);
    LAS char* smem = c.smem;
    const int lane = c.lane, w = c.wid, fr = lane & 15, fq = lane >> 4;
    const char* obase = c.ws + WS_O + (size_t)l * (8u << 20);
    int ovo = (c.pw * 8 + w * 2) * 1024 + lane * 16;
    asm volatile("" : "+v"(ovo));
    constexpr int NP = WNQ, R = 3;
    static_assert(!HO && !HX && !HS && !RLD && BPR == 1, "layout");
    auto ro = __builtin_amdgcn_make_buffer_rsrc((void*)obase, 0, 4u << 20, 0x00020000);
    asm volatile("" : "+s"(ro));
    u32x4 xa[NP][2];
    s16x4 wf[R][8];
    f32x4 acc[2][4];
    unsigned bw[8];
    int coff, rmo, spr;
    u32x4 on = {0x3f803f80u, 0x3f803f80u, 0x3f803f80u, 0x3f803f80u};
    unsigned sto, xpo, coff1, cnta = M_CNT, one = 1;
    u64 xv, b0, b1, am;
    auto rs = __builtin_amdgcn_make_buffer_rsrc(xout, 0, S_ * D_ * 2, 0x00020000);
    auto rst = __builtin_amdgcn_make_buffer_rsrc(c.ws + WS_STATS + (size_t)((l + 1) & 3) * 524288, 0, 524288, 0x00020000);
    auto prep2 = [&]() {
        sto = fq == (fr >> 2) && l < 3 ? (unsigned)((c.pw * 16 + c.jw) * 1024 + (w * 32 + fr) * 8) : 0x80000000u;
        xpo = x_post(c);
        coff = (int)pick(l == 3, (unsigned)rmo, (unsigned)coff);
        coff1 = coff + (l == 3 ? xrow<0>(0, 16) : 2048);
        spr = l == 3 ? 64 : 1024;
        asm volatile("" : "+s"(spr));
        b0 = __builtin_amdgcn_ballot_w64(fr & 1); b1 = __builtin_amdgcn_ballot_w64(fr & 2); am = __builtin_amdgcn_ballot_w64(x_act(c, !B));
        asm volatile("" : "+v"(on), "+v"(sto), "+v"(xpo), "+v"(coff), "+v"(coff1), "+v"(xv), "+v"(cnta), "+v"(one));
        asm volatile("" : "+s"(rs), "+s"(rst), "+s"(b0), "+s"(b1), "+s"(am));
    };
    auto prep = [&]() {
        int ln = lane;
        asm volatile("" : "+v"(ln));
        const int fr = ln & 15, fq = ln >> 4, q4 = fr >> 2, p4 = ln & 3;
        coff = (c.pw * 16 + c.jw) * 16384 + w * 4096 + (((fq & 1) * 2 + (fq >> 1)) * 16 + fr) * 16;
        rmo = xrow<0>(c.pw, w * 32 + fr) + xcol<0>(c.jw) + (fq & 1) * 32 + (fq >> 1) * 16;
        asm volatile("" : "+v"(coff), "+v"(rmo));
        const int fb = (q4 & 2) | ((fq & 1) << 2);
        const unsigned swp = (B && w >= 2) ? 2048u : 0u;
#pragma unroll
        for (int cc = 0; cc < 4; ++cc) {
            const unsigned b = (unsigned)((4 * fq + q4) * 128 + (((2 * cc + (p4 >> 1)) ^ fb) << 4) + (p4 & 1) * 8);
            bw[2 * cc] = b + swp; bw[2 * cc + 1] = b + (swp ^ 2048u);
            asm volatile("" : "+v"(bw[2 * cc]), "+v"(bw[2 * cc + 1]));
        }
    };
    auto start = [&](int mt, int pr) {
            {
                auto r0 = __builtin_amdgcn_permlane16_swap(res[mt][pr][0], res[mt][pr][2], false, false);
                auto r1 = __builtin_amdgcn_permlane16_swap(res[mt][pr][1], res[mt][pr][3], false, false);
#pragma unroll
                for (int h = 0; h < 2; ++h) {
                    acc[mt][2 * pr + h] = (f32x4){bf_lo(r0[h]), bf_hi(r0[h]), bf_lo(r1[h]), bf_hi(r1[h])};
                    asm volatile("" : "+v"(acc[mt][2 * pr + h]));
                }
            }
    };
    auto rdw = [&](auto ic) {
        constexpr int i = decltype(ic)::value, h = i >> 3, k = i & 7;
        wf[h % R][k] = tr_read(lds(bw[k]) + (h & 15) * 4096);
        if constexpr (h == 15) { bw[k] += 65536; asm volatile("" : "+v"(bw[k])); }
    };
    {
        auto resid = [&]() {
            const unsigned roff = pick(l != 0, (unsigned)coff, (unsigned)rmo);
            const char* xp = (const char*)xin;
#pragma unroll
            for (int i = 0; i < 4; ++i) aload4c(res[i >> 1][i & 1], roff, xp + (i >> 1) * (l ? 2048 : xrow<0>(0, 16)) + (i & 1) * (l ? 1024 : 64));
            SCHED();
        };
        unsigned opo = o_post(c), oca = B ? M_CNT : M_CNT + 8, old = 0, msk = B ? 3u : 7u;
        xv = lds_epoch(smem) + (unsigned)(l + 2);
        u64 ov = xv + (unsigned)(3 * l + 2);
        prep();
        asm volatile("" : "+v"(opo), "+v"(oca), "+v"(ov), "+v"(old), "+v"(one));
        asm volatile("" : "+s"(msk));
        SCHED();
        if (RBP) resid();
        static_for<0, 16>([&](auto ic) { rdw(ic); SCHED(); });
        if (!P2A) prep2();
        SCHED();
        wait_v<RBP ? 4 : 0>();
        if (lane == 0) asm volatile("ds_add_rtn_u32 %0, %1, %2\n\ts_waitcnt lgkmcnt(0)" : "+v"(old) : "v"(oca), "v"(one) : "memory");
        if ((__builtin_amdgcn_readfirstlane(old) & msk) == msk) { if (lane < 16) post(c.ws, opo, ov); }
        if (w == 0) STAMP_AT(l * 8 + 4);
        SCHED();
        if (!RBP) resid();
        if (P2A == 1) prep2();
        SCHED();
        if (w == 3) STAMP_AT(32 + l * 8 + 5);
        BAR();
    }
    FENCE();
    SCHED();
    if (w == 0) STAMP_AT(l * 8 + 5);
    int so = 0;
    auto next = [&](int j) { so += j == 15 ? 131072 - 15 * 262144 : 262144; asm volatile("" : "+s"(so)); };
    auto ldo = [&](int j, int mt) { xa[j % NP][mt] = __builtin_amdgcn_raw_buffer_load_b128(ro, ovo + mt * 1024, so, 16); };
#pragma unroll
    for (int h = 0; h < NP; ++h) {
        ldo(h, 0);
        SCHED();
        if (h > SH0 && h <= SH0 + 2) { start(h - SH0 - 1, 0); SCHED(); }
        ldo(h, 1);
        if (!WAL || h + 1 < NP) next(h);
        SCHED();
        if (P2A >= 2 && h == P2A - 2) { prep2(); SCHED(); }
        if (h == SH0) {
            wait_v<2 * SH0 + 2>();
            asm volatile("" : "+v"(res[0][0]), "+v"(res[0][1]), "+v"(res[1][0]), "+v"(res[1][1]));
        }
        if (h > SH0 && h <= SH0 + 2) start(h - SH0 - 1, 1);
        SCHED();
    }
    int d0 = 262144, d1 = 131072 - 15 * 262144;
    asm volatile("" : "+s"(d0), "+s"(d1));
    static_assert(WEP || !WAL, "last step");
    static_for<0, WEP ? 31 : 32>([&](auto jc) {
        constexpr int j = decltype(jc)::value;
        constexpr int c0 = j < 30 ? 16 + 8 * j : 256, c1 = j < 30 ? c0 + 8 : 256;
        if constexpr (j + NP == 16) {
            static_assert(c0 <= 8 * 20, "weights that landed before the attention");
            if (!B) BAR();
            FENCE();
            asm volatile("" : "+v"(ovo));
            SCHED();
        }
        if constexpr (j == 0 || j + NP == 16) ALIGN8();
        constexpr int nv = WAL == 1 || j == 0 || j + NP > 32 ? 2 * (NP - 1 < 31 - j ? NP - 1 : 31 - j) : 2 * NP - 3;
        __builtin_amdgcn_s_waitcnt(0x0870 | nv);
        const int sp = so;
        static_assert(!RS0 || (RS0 + NP >= 32 && RS0 < 31 && QW_SLOT <= (RS0 - 1) * 4096), "R0");
        if constexpr (RS0 && j == RS0) BAR();
        else asm volatile("s_add_i32 %0, %1, %2" : "=&s"(so) : "s"(sp), "s"(j + NP == 16 ? d1 : d0) : "scc");
        SCHED();
        static_for<0, 8>([&](auto mc) {
            constexpr int m = decltype(mc)::value, mt = m >> 2, nt = mt ? 3 - (m & 3) : m & 3;
            const bf16x8 wv = __builtin_shufflevector(wf[j % R][2 * nt], wf[j % R][2 * nt + 1], 0, 1, 2, 3, 4, 5, 6, 7);
            if constexpr (j < 31) asm volatile("v_mfma_f32_16x16x32_bf16 %0, %1, %2, %0" : "+v"(acc[mt][nt]) : "v"(wv), "v"(xa[j % NP][mt]));
            else acc[mt][nt] = MFMA(wv, __builtin_bit_cast(bf16x8, xa[j % NP][mt]), acc[mt][nt]);
            SCHED();
            if constexpr (c0 < c1 && m < 7) { rdw(std::integral_constant<int, c0 + m>{}); SCHED(); }
            if constexpr (c0 < c1 && m == 6) rdw(std::integral_constant<int, c0 + 7>{});
            if constexpr (m == WL1 && j > 0 && j - 1 + NP < 32) xa[(j + NP - 1) % NP][1] = __builtin_amdgcn_raw_buffer_load_b128(ro, ovo + 1024, sp, 16);
            if constexpr (m == WL1 + 4 && j + NP < 32) ldo(j, 0);
            SCHED();
        });
    });
    const bf16x8 ones = __builtin_bit_cast(bf16x8, on);
    u32x4 o[2][2];
    f32x4 sm[2], g[2];
    const f32x4 zero = {0.f, 0.f, 0.f, 0.f};
    auto sel = [&](float a, float b, u64 m) { return __builtin_amdgcn_inverse_ballot_w64(m) ? b : a; };
    auto stats = [&](int mt) {
        float qa = sel(g[mt][0], g[mt][1], b0), qb = sel(g[mt][2], g[mt][3], b0);
        asm volatile("" : "+v"(qa), "+v"(qb));
        SCHED();
        if (WEP) asm volatile("" :: "v"(sm[1][1]), "v"(sm[1][2]), "v"(sm[1][3]), "v"(o[1][0]), "v"(o[1][1]), "v"(on));
        __builtin_amdgcn_raw_buffer_store_b64((u32x2){__float_as_uint(sm[mt][0]), __float_as_uint(sel(qa, qb, b1))}, rst, sto + mt * 128, 0, 16);
        SCHED();
    };
    u32x2 pk[2];
    auto last = [&](int mt, int nt) {
        acc[mt][nt] = MFMA(__builtin_shufflevector(wf[31 % R][2 * nt], wf[31 % R][2 * nt + 1], 0, 1, 2, 3, 4, 5, 6, 7), __builtin_bit_cast(bf16x8, xa[31 % NP][mt]), acc[mt][nt]);
        SCHED();
    };
    auto cv = [&](int mt, int pr) {
        pk[0] = pack4(acc[mt][2 * pr]); pk[1] = pack4(acc[mt][2 * pr + 1]);
        asm volatile("" : "+v"(pk[0]), "+v"(pk[1]));
        SCHED();
    };
    auto st = [&](int mt, int pr) {
        o[mt][pr] = widen(pk[0], pk[1]);
        __builtin_amdgcn_raw_buffer_store_b128(o[mt][pr], rs, mt ? coff1 : coff, pr ? spr : 0, 16);
        SCHED();
    };
    auto gq = [&](int mt, int pr, bool f) { const bf16x8 of = __builtin_bit_cast(bf16x8, o[mt][pr]); g[mt] = MFMA(of, of, f ? zero : g[mt]); SCHED(); };
    auto gs = [&](int mt, int pr, bool f) { sm[mt] = MFMA(ones, __builtin_bit_cast(bf16x8, o[mt][pr]), f ? zero : sm[mt]); SCHED(); };
    WAIT_L0();
    last(0, 0); last(0, 1); last(0, 2); last(0, 3);
    last(1, 3); cv(0, 0);
    last(1, 2); st(0, 0);
    last(1, 1); cv(0, 1);
    last(1, 0); st(0, 1);
    if (w == 0) STAMP_AT(l * 8 + 6);
    gq(0, 0, true); cv(1, 1);
    gs(0, 0, true); st(1, 1);
    gq(0, 1, false); cv(1, 0);
    gs(0, 1, false); st(1, 0);
    gq(1, 1, true);
    gs(1, 1, true);
    gq(1, 0, false);
    gs(1, 0, false);
    stats(0);
    stats(1);
    if (l < 3) {
        unsigned old = 0;
        if (!RS0 || RN1 > RN0) BAR();
        wait_v<XE2>();
        if (lane == 0) asm volatile("ds_add_rtn_u32 %0, %1, %2\n\ts_waitcnt lgkmcnt(0)" : "+v"(old) : "v"(cnta), "v"(one) : "memory");
        if ((__builtin_amdgcn_readfirstlane(old) & 3) == 3) {
            if (__builtin_amdgcn_inverse_ballot_w64(am)) post(c.ws, xpo, xv);
        }
    }
    if (w == 0) STAMP_AT(l * 8 + 7);
}

__global__ __launch_bounds__(512, 2) __attribute__((aligned(4096))) void k_main(Params P) {
    extern __shared__ __attribute__((aligned(16))) char smem_[];
    KP kp = (KP)__builtin_amdgcn_kernarg_segment_ptr();
    const char *x0, *w0, *wo;
    Ctx c;
    c.smem = (LAS char*)smem_;
    c.lane = threadIdx.x & 63;
    c.wid = __builtin_amdgcn_readfirstlane(threadIdx.x >> 6);
    c.bid = blockIdx.x;
    const bool loader = c.wid >= 4;
    unsigned va, vw;
    auto enter = [&](const bool LD) {
        if (LD) asm volatile("s_load_dwordx2 %0, %4, 0x0\n\ts_load_dwordx2 %1, %4, 0x8\n\ts_load_dwordx2 %2, %4, 0x10\n\ts_load_dwordx2 %3, %4, 0x20 ; loader" : "=&s"(x0), "=&s"(w0), "=&s"(c.ws), "=&s"(wo) : "s"(kp));
        else asm volatile("s_load_dwordx2 %0, %4, 0x0\n\ts_load_dwordx2 %1, %4, 0x8\n\ts_load_dwordx2 %2, %4, 0x10\n\ts_load_dwordx2 %3, %4, 0x20" : "=&s"(x0), "=&s"(w0), "=&s"(c.ws), "=&s"(wo) : "s"(kp));
        if (LD) asm volatile("; loader" : "+s"(c.bid), "+s"(c.wid)); else asm volatile("; consumer" : "+s"(c.bid), "+s"(c.wid));
        if (LD) asm volatile("; loader" : "+v"(c.lane)); else asm volatile("; consumer" : "+v"(c.lane));
        static_assert(QXM == 8 && WXM == 4 && ALT == 1, "map");
        c.pq = (((c.bid & 1) * 8) ^ PQX) + ((c.bid >> 3) & 7); c.jq = (c.bid & 6) * 2 + (c.bid >> 6);
        const int w = c.wid, lr = c.lane >> 3, lc = c.lane & 7;
        va = (unsigned)((c.pq * 128 + 8 * (w & 3) + lr) * (D_ * 2) + ((lc ^ lr) << 4));
        vw = (unsigned)((8 * (w & 3) + lr) * 6144 + c.jq * 128 + ((lc ^ ((lr & 2) | ((w & 1) << 2))) << 4));
        asm volatile("" : "+v"(va), "+v"(vw));
        c.pw = ((c.pq & 8) ^ 8) + ((c.bid >> 1) & 1) * 4 + ((c.bid >> 3) & 3); c.jw = (c.bid & 4) * 2 + (c.bid >> 5); c.cw = 0;
        if (LD) {
            asm volatile("" : "+s"(c.pw), "+s"(c.jw));
            asm volatile("s_waitcnt lgkmcnt(0)" : "+s"(x0), "+s"(w0), "+s"(c.ws), "+s"(wo));
            if (ESL) __builtin_amdgcn_s_sleep(ESL);
        } else {
            int t0 = first_tile(first_order(c.pq, c.jq), 0);
            asm volatile("" : "+s"(t0));
            asm volatile("s_waitcnt lgkmcnt(0) ; c" : "+s"(x0), "+s"(w0), "+s"(c.ws), "+s"(wo));
            if (ST0 && c.wid == 3) STAMP_AT(41);
            const char* xb = x0 + (unsigned)(t0 * 128);
            const char* wb = w0 + (unsigned)(t0 * (64 * 6144));
#pragma unroll
            for (int h = 0; h < 2; ++h) {
#pragma unroll
                for (int j = 0; j < 2; ++j) dma16<false>(QA_OFF + (j * 8 + h * 4 + w) * 1024, va, xb + (j * 64 + h * 32) * (D_ * 2));
#pragma unroll
                for (int j = 0; j < 3; ++j) dma16<false>((j * 8 + h * 4 + w) * 1024, vw, wb + h * (32 * 6144) + j * 2048);
            }
            if (ST0 && c.wid == 1) STAMP_AT(43);
            if (w == 0 && c.lane < 2) touch(M_EP, (unsigned)(WS_EPOCH + c.lane * 4), c.ws);
            if (w == 1 && c.lane < 5) *(LAS unsigned*)(c.smem + (c.lane < 4 ? M_CNT + c.lane * 4 : M_GO)) = 0u;
        }
        asm volatile("" : "+s"(kp), "+s"(c.bid), "+s"(c.ws));
    };
    if (c.wid == CPW) {
        const char* pc; int len;
        asm volatile("s_getpc_b64 %0\n.Lcpf_a:\n\ts_mov_b32 %1, .Lcpf_end-.Lcpf_a-4" : "=s"(pc), "=s"(len));
        const int o = min((int)(((c.bid >> 3) & 7) * 64 + c.lane) * 128, len);
        asm volatile("s_mov_b32 m0, %0\n\ts_nop 3\n\tglobal_load_lds_dword %1, %2" ::"s"(M_TCH), "v"(o), "s"(pc) : "memory");
    }
    const char* xi;
    auto step = [&](int l) {
        asm volatile("" : "+s"(kp), "+s"(c.ws), "+s"(c.jw), "+s"(c.bid));
        c.pq ^= 8; c.pw ^= 8; c.cw = CPY ? c.jw & 7 : 0;
        xi = c.ws + WS_X + (unsigned)(l * (8u << 20));
        wo = (const char*)((l & 1) ? kp->wo0 : kp->wo1);
        if (c.cw == 3) wo = c.ws + WS_CO + (((l & 1) ? 2 : 0) + (c.jw >> 3)) * 4096;
    };
    if (loader) {
        enter(true);
        xi = x0;
            qkv_load<false, true>(c, xi, w0, wo, 0, kp, va, vw);
#pragma unroll 1
        for (int l = 0;;) {
            asm volatile("" : "+s"(l));
            if (!(l & 1)) attn_a<true>(c, l, wo, kp);
            wo_load(c, l, nullptr, wo);
            if (l == 3) break;
            step(l);
            ++l;
            if (l == 2) { qkv_load<false, false>(c, xi, (const char*)kp->wqkv0, wo, l); }
            else { qkv_load<true, false>(c, xi, (const char*)kp->wqkv1, wo, l); }
        }
    } else {
        enter(false);
        xi = x0;
        bf16_t* xo = (bf16_t*)(c.ws + WS_X);
#pragma unroll 1
        for (int l = 0; l < 4; ++l) {
            if (l & 1) { qkv_consume<true>(c, l, kp); }
            else {
                qkv_consume<false>(c, l, kp, va, vw);
                attn_a<false>(c, l, wo, kp);
            }
            wo_consume(l & 1, c, (const bf16_t*)xi, xo, l);
            step(l);
            xo = l == 2 ? kp->out : (bf16_t*)(c.ws + WS_X + (unsigned)((l + 1) * (8u << 20)));
            asm volatile("" : "=v"(va), "=v"(vw));
        }
    }
    asm volatile(".Lcpf_end:");
}

extern "C" void kernel_launch(void* const* d_in, const int* in_sizes, int n_in, void* d_out, int out_size, void* d_ws, size_t ws_size,
                              hipStream_t stream) {
    Params P;
    P.x0 = (const bf16_t*)d_in[0];
    P.wqkv0 = (const bf16_t*)d_in[1];
    P.wo0 = (const bf16_t*)d_in[2];
    P.wqkv1 = (const bf16_t*)d_in[4];
    P.wo1 = (const bf16_t*)d_in[5];
    P.out = (bf16_t*)d_out;
    P.ws = (char*)d_ws;
    (void)hipFuncSetAttribute((const void*)k_main, hipFuncAttributeMaxDynamicSharedMemorySize, LDS_TOTAL);
    k_main<<<256, 512, LDS_TOTAL, stream>>>(P);
}
```

```cpp
#include <hip/hip_runtime.h>
#include <stdint.h>
#include <type_traits>

typedef unsigned short bf16_t;
typedef short bf16x8 __attribute__((ext_vector_type(8)));
typedef short s16x4 __attribute__((ext_vector_type(4)));
typedef float f32x4 __attribute__((ext_vector_type(4)));
typedef float f32x2 __attribute__((ext_vector_type(2)));
typedef float f32x16 __attribute__((ext_vector_type(16)));
typedef unsigned u32x4 __attribute__((ext_vector_type(4)));
typedef unsigned u32x2 __attribute__((ext_vector_type(2)));
#define DI __device__ __forceinline__
#define LAS __attribute__((address_space(3)))
#define GAS __attribute__((address_space(1)))
#define STAMP 3
#define SA 63
#define SB 63
#define SM 1
#define SX 100

constexpr int S_ = 2048, D_ = 1024, KT = 16;
constexpr float LN_EPS = 1e-5f;
constexpr float QSCALE = 0.17677669529663687f * 1.4426950408889634f;

#define FQO 0
#define XBO 524288
#define HX 0
#define HO 0
#define HK 0
#define HS 0
#define HM 1
constexpr size_t WS_FLAGQ = FQO;
constexpr size_t WS_FLAGW = 98304;
constexpr size_t WS_EPOCH = 65536;
constexpr size_t WS_CSUM = 131072;
constexpr size_t WS_XBOX = XBO;
constexpr size_t WS_OBOX = 655360;
constexpr size_t WS_KBOX = 720896;
constexpr size_t WS_STATS = 1u << 20;
constexpr size_t WS_X = 4u << 20;
constexpr size_t WS_O = 96u << 20;
constexpr size_t WS_KV = 28u << 20;
constexpr size_t WS_CQ = 36u << 20;
constexpr size_t WS_CO = 42u << 20;
constexpr size_t WS_STAMP = 64u << 20;

template <int H> DI constexpr int hp(int c) { return !H ? c * 1024 : (c >> 2) * 8192 + (HM == 1 ? (c & 3) * 256 : (c & 1) * 128 + (c & 2) * 256); }
template <int H> DI constexpr int hl(int u) { return !H ? u : (HM == 1 ? u + (u & ~127) : u + (u & ~255)); }
template <int H> DI constexpr int hv(int lane) { return !H ? lane * 16 : (lane >> 3) * 1024 + (lane & 7) * 16; }
template <int H> DI constexpr int xrow(int p, int r) { return H == 0 ? (p * 128 + r) * 2048 : (H == 1 ? (p * 128 + r) * 4096 : (H == 2 ? p * 524288 + r * 256 : p * 262144 + r * 128)); }
template <int H> DI constexpr int xcol(int t) { return H == 0 ? t * 128 : (H == 1 ? hl<1>(t * 128) : (H == 2 ? t * 32768 : t * 16384)); }
#define WTC 2
#define WT0 1
#define SPR 1
#define SL 4
#define R4X 0
#define R5X 0
#define TOPJ 0x2000
#define WV0 1
#define C2X 13
#define FQS 256
#define SNK 1
#define WBE 1
#define WAE 1
#define EXF 0
#define RW0 6
#define KSP 0
#define K0E 1
#define TOPS 4
#define BPR 1
#define RLD 0
#define RCK 1
#define CPF 2
#define WN1 20
#define WLO 3
#define FPJ 2
#define WPJ 2
#define WB14 0
#define WB15 5
#define RESP 1
#define WT13 2
#define RPOS 2
#define CPOLL 1
#define ENT 1
#define MBOX 3
#define OBS 256
#define WTCH 0
#define WTDA 0
#define WTDB 4
#define WTG 0
#define PQX 8
#define ALT 1
#define RESA 1
#define CKP 1
#define ALN 1
#define EPH 2
#define NQA_ 3
#define NQW_ 4
#define WNP 8
#define LPRIO 3
#define TOUCH 1
#define TSTEP 13
#define XS_ 9
#define WROT 0
#define WROTD 1
#define JQX 0
#define ROTM 0
#define ROT0 0
#define WPRE 3
#define PSLEEP 1
#define XPRE 2
#define WE0 1
#define EPJ 3
#define TW0 5
#define TW1 99
#define KPJ 1
#define PKSUB 0
#define APRIO 1
#define QIL 0
#define QXM 8
#define WNQ 5
#define WRG 2
#define WMG 1
#define WB2 1
#define WBS 1
#define WEP 1
#define WAL 2
#define WL1 0
#define RS0 27
#define RN0 6
#define RN1 6
#define AH0 0
#define RSL 1
#define WXM 4
#define CPOS 5
#define LSL 0
#define SAFE_SUM 0x1p80f
#define AB2 0
#define W30S 8
#define STC 0
#define NOR 1
#define NRH 0
#define CPY 1
#define AGG 1
#define HT0 0
#define DEF 0
#define XST 2
#define CKW 4
#define XSP 1
#define EPOLL 1
#define ORD 2
#define RSLP 1
#define APOLL 1
#define APOST 1
#define ASLP 2
#define APIPE 4
#define RBP 0
#define P2A 1
#define SH0 2
#define SL1 0
#define KPW 4
#define LWA 7
#define WA0 12
#define WA1 22
#define KPP 1
#define HP4 1
#define TWQ 1
#define CP2 0
#define PADN -1
#define ERL 1
#define ESL 0
#define DM 0
#define DS 0
#define ELO 0
#define EN0 3
#define EN1 2
#define ST0 0
#define NC 1
#define NCM 1
#define NCB 4
#define AD1 3
#define AD2 0
#define AD3 4
#define WB0 3
#define WBN 6
#define TWO 1
#define XSB 10
#define TL0 0
#define TL1 12
#define FDM 1
#define TSP 0
#define CPW 4
#define EXS 1
#define NCH 2
#define XE1 0
#define XE2 0
#define OTB 0
#define OTJ 8
#define OTA 0
constexpr int QA_SLOT = 18432, NQA = NQA_, QW_SLOT = 24576, NQW = NQW_, QA_OFF = 3 * QW_SLOT, QW_TOP = 131072;
DI constexpr int qw_slot(int s) { return s == 3 ? QW_TOP : s * QW_SLOT; }
constexpr int AT_N = 90112, AT_OWN = AT_N + 32768, AT_Q = 73728;
constexpr int MISC = 155648, M_MEAN = MISC, M_RSTD = MISC + 640, M_CS = MISC + 1280, M_CNT = MISC + 2816, M_EP = MISC + 2832, M_GO = MISC + 2848, M_TCH = MISC + 3072, M_TAB = MISC + 4096, M_XCH = 131072;
constexpr int LDS_TOTAL = 163840;
static_assert(QA_OFF + NQA * QA_SLOT <= QW_TOP && QW_TOP + QW_SLOT <= MISC && AT_N + 65536 <= MISC && AT_Q + 16384 <= AT_N, "lds");

DI unsigned cvt_pk_bf16(float lo, float hi) {
    typedef __bf16 bf2 __attribute__((ext_vector_type(2)));
    bf2 r; r[0] = (__bf16)lo; r[1] = (__bf16)hi;
    return __builtin_bit_cast(unsigned, r);
}
DI u32x2 pack4(const f32x4& v) { return (u32x2){cvt_pk_bf16(v[0], v[1]), cvt_pk_bf16(v[2], v[3])}; }
DI bf16x8 join(const u32x2& a, const u32x2& b) { return __builtin_bit_cast(bf16x8, (u32x4){a[0], a[1], b[0], b[1]}); }
DI float bf_lo(unsigned u) { return __uint_as_float(u << 16); }
DI float bf_hi(unsigned u) { return __uint_as_float(u & 0xffff0000u); }
DI float red_max4(float x) {
    auto a = __builtin_amdgcn_permlane16_swap(__float_as_uint(x), __float_as_uint(x), false, false);
    x = fmaxf(__uint_as_float(a[0]), __uint_as_float(a[1]));
    auto b = __builtin_amdgcn_permlane32_swap(__float_as_uint(x), __float_as_uint(x), false, false);
    return fmaxf(__uint_as_float(b[0]), __uint_as_float(b[1]));
}
DI float red_sum4(float x) {
    auto a = __builtin_amdgcn_permlane16_swap(__float_as_uint(x), __float_as_uint(x), false, false);
    x = __uint_as_float(a[0]) + __uint_as_float(a[1]);
    auto b = __builtin_amdgcn_permlane32_swap(__float_as_uint(x), __float_as_uint(x), false, false);
    return __uint_as_float(b[0]) + __uint_as_float(b[1]);
}
DI u32x4 widen(const u32x2& a, const u32x2& b) {
    auto r0 = __builtin_amdgcn_permlane16_swap(a[0], b[0], false, false);
    auto r1 = __builtin_amdgcn_permlane16_swap(a[1], b[1], false, false);
    return (u32x4){r0[0], r1[0], r0[1], r1[1]};
}
#define MFMA(a, b, c) __builtin_amdgcn_mfma_f32_16x16x32_bf16((a), (b), (c), 0, 0, 0)
#define WAIT_L0() __builtin_amdgcn_s_waitcnt(0xC07F)
#define WAIT_V0() asm volatile("s_waitcnt vmcnt(0)" ::: "memory")
#define BAR() __builtin_amdgcn_s_barrier()
#define SCHED() __builtin_amdgcn_sched_barrier(0)
template <int N> DI void wait_v() { asm volatile("s_waitcnt vmcnt(%0)" ::"i"(N) : "memory"); }
template <int I, int N, class F> DI void static_for(F&& f) { if constexpr (I < N) { f(std::integral_constant<int, I>{}); static_for<I + 1, N>(f); } }

template <bool SC1> DI void dma16(unsigned lds, unsigned voff, const char* sbase) {
    if (SC1) asm volatile("s_mov_b32 m0, %0\n\ts_nop 3\n\tglobal_load_lds_dwordx4 %1, %2 sc1" ::"s"(lds), "v"(voff), "s"(sbase) : "memory");
    else asm volatile("s_mov_b32 m0, %0\n\ts_nop 3\n\tglobal_load_lds_dwordx4 %1, %2" ::"s"(lds), "v"(voff), "s"(sbase) : "memory");
}
DI void touch(unsigned lds, unsigned voff, const char* sbase) {
    asm volatile("s_mov_b32 m0, %0\n\ts_nop 3\n\tglobal_load_lds_dword %1, %2 sc1" ::"s"(lds), "v"(voff), "s"(sbase) : "memory");
}
DI s16x4 tr_read(const LAS char* p) {
    typedef short v4i16_t __attribute__((ext_vector_type(4)));
    return __builtin_bit_cast(s16x4, __builtin_amdgcn_ds_read_tr16_b64_v4i16((LAS v4i16_t*)p));
}
DI LAS char* lds(unsigned off) { return (LAS char*)(uintptr_t)off; }
DI unsigned long long rstamp() { unsigned long long t; asm volatile("s_memrealtime %0\n\ts_waitcnt lgkmcnt(0)" : "=s"(t)::"memory"); return t; }

template <int NAI, int NWI, bool FIRST> struct Sched {
    int w[KT];
    constexpr Sched() : w{} {
        int endA[KT] = {}, endW[KT] = {};
        int n = 0;
        if (FIRST) {
            if (ENT == 0) { n += 2; endA[0] = n; n += 3; endW[0] = n; }
            n += NAI; endA[1] = n; n += NWI; endW[1] = n;
            if (ERL >= 5) { n += NAI; endA[2] = n; }
            if (ERL == 2 || ERL == 3 || ERL == 5) { n += NWI; endW[2] = n; }
        } else {
            for (int s = 0; s < WPRE; ++s) { n += NWI; endW[s] = n; }
            for (int s = 0; s < 2; ++s) { n += NAI; endA[s] = n; }
        }
        for (int kt = 0; kt < KT; ++kt) {
            const int need = endA[kt] > endW[kt] ? endA[kt] : endW[kt];
            w[kt] = (n - need) > 63 ? 63 : (n - need);
            if (kt + 2 < KT && !(FIRST && ERL >= 5 && kt == 0)) { n += NAI; endA[kt + 2] = n; }
            if (FIRST && (ERL == 4 || ERL == 6) && kt == 0) { n += NWI; endW[2] = n; }
            if (FIRST && ERL < 2 && kt < 5) {
                if (kt < 4) { n += NWI; endW[kt + 2] = n; }
                if (kt >= 3) n += NWI / 2;
                if (kt == 4) { endW[6] = n; n += NWI; endW[7] = n; }
                continue;
            }
            if (!FIRST && WPRE == 2 && kt < 2) { n += NWI; endW[kt + 2] = n; }
            if ((FIRST || WPRE == 3) && kt == 0) { n += NWI; endW[3] = n; }
            if (kt >= 1 && kt + 3 < KT) { n += NWI; endW[kt + 3] = n; }
            if (kt == 13) n += WT13;
            if (kt == 14) n += (NAI == 5 || WA0 > 12 ? 12 : WA0) - WT13 + (NAI == 5 ? WB14 : 0);
        }
    }
};

struct Params {
    const bf16_t* x0;
    const bf16_t* wqkv0;
    char* ws;
    const bf16_t* wqkv1;
    const bf16_t* wo0;
    const bf16_t* wo1;
    bf16_t* out;
};
typedef const __attribute__((address_space(4))) Params* KP;
DI int rot_first(int pq, int jq) {
    const int pr = 2 * (pq & 7) + (pq >> 3);
    if (ROT0 == 6) return 0;
    if (ROT0 == 7) return (pq & 1) * 8;
    if (ROT0 == 8) return (pq & 3) * 4;
    if (ROT0 == 9) return pq;
    if (ROT0 == 4) return (pq + (jq ^ 3)) & 15;
    if (ROT0 == 5) return (pr + (jq ^ 3)) & 15;
    return (ROT0 == 0 ? pq + jq : (ROT0 == 1 ? pr + jq : (ROT0 == 2 ? pr + 4 * (jq & 3) + (jq >> 2) : pq + 4 * (jq & 3) + (jq >> 2)))) & 15;
}

struct Order { int base, r, re; };
template <int N> DI Order make_order(int base, int rot) { Order o; o.base = base; o.r = (rot - base) & 15; o.re = o.r > N ? o.r : N; return o; }
DI Order first_order(int pq, int jq) { return make_order<NC>((pq & NCM) * (16 / (NCM + 1)) + NCB, rot_first(pq, jq)); }
template <int N = NC> DI int first_tile(const Order& o, int s) {
    const int v = o.re + s - N;
    if (s < N) { int a = N + o.base, b = s + o.base; if (N - s == 1) asm volatile("" : "+s"(a)); return (o.r == s ? a : b) & 15; }
    return ((s == N ? o.r : (v > 15 ? v - (16 - N) : v)) + o.base) & 15;
}

typedef unsigned long long u64;
DI u64 lds_epoch(LAS char* smem) { return *(const LAS u64*)(smem + M_EP); }
DI bool reached(u64 v, u64 ep, unsigned target) { return (v - ep - target) < (u64)(64u - target); }
DI void poll_flags(const char* ws, u64 ep, unsigned off, bool active, unsigned target) {
    const GAS u64* f = (const GAS u64*)(ws + off);
#pragma unroll 1
    for (unsigned spins = 0; spins < (1u << 22); ++spins) {
        const u64 v = __hip_atomic_load(f, __ATOMIC_RELAXED, __HIP_MEMORY_SCOPE_AGENT);
        if (__all(!active || reached(v, ep, target))) break;
        __builtin_amdgcn_s_sleep(PSLEEP);
    }
    asm volatile("" ::: "memory");
}
struct Ask { u64 v[4]; };
DI void keep(Ask& t) { asm volatile("" : "+v"(t.v[0]), "+v"(t.v[1]), "+v"(t.v[2]), "+v"(t.v[3])); }
#define ASK(r) "global_atomic_or_x2 " r ", %[off], %[z], %[ws] sc0 sc1\n\t"
#define CHK(r) "s_waitcnt vmcnt(%[n])\n\tv_lshl_add_u64 %[d], " r ", 0, %[nb]\n\tv_cmp_gt_u64 vcc, %[span], %[d]\n\ts_cmp_eq_u64 vcc, exec\n\ts_cbranch_scc1 .Lpe%=\n\ts_sleep %[sl]\n\t"
template <bool NEG = false> DI void poll_rmw(Ask& t, const char* ws, unsigned off, bool act, u64 base, unsigned span) {
    u64 z, d, nb = NEG ? base : 0 - base, sp = span;
    unsigned cnt;
    asm volatile("" : "=v"(t.v[0]), "=v"(t.v[1]), "=v"(t.v[2]), "=v"(t.v[3]));
    if (act) {
        if (APIPE == 4)
            asm volatile("v_mov_b64 %[z], 0\n\ts_mov_b32 %[cnt], 0x80000\n\ts_nop 3\n\t" ASK("%[a]") "s_sleep %[sl]\n\t" ASK("%[b]") "s_sleep %[sl]\n\t" ASK("%[c]") "s_sleep %[sl]\n"
                         ".Lpl%=:\n\t" ASK("%[e]") CHK("%[a]") ASK("%[a]") CHK("%[b]") ASK("%[b]") CHK("%[c]") ASK("%[c]") CHK("%[e]")
                         "s_sub_u32 %[cnt], %[cnt], 1\n\ts_cbranch_scc0 .Lpl%=\n.Lpe%=:"
                         : [a] "+v"(t.v[0]), [b] "+v"(t.v[1]), [c] "+v"(t.v[2]), [e] "+v"(t.v[3]), [d] "=&v"(d), [z] "=&v"(z), [cnt] "=&s"(cnt)
                         : [off] "v"(off), [ws] "s"(ws), [nb] "v"(nb), [span] "s"(sp), [sl] "i"(ASLP), [n] "i"(3) : "vcc", "scc", "memory");
        else if (APIPE == 2)
            asm volatile("v_mov_b64 %[z], 0\n\ts_mov_b32 %[cnt], 0x100000\n\ts_nop 3\n\t" ASK("%[a]") "s_sleep %[sl]\n"
                         ".Lpl%=:\n\t" ASK("%[b]") CHK("%[a]") ASK("%[a]") CHK("%[b]")
                         "s_sub_u32 %[cnt], %[cnt], 1\n\ts_cbranch_scc0 .Lpl%=\n.Lpe%=:"
                         : [a] "+v"(t.v[0]), [b] "+v"(t.v[1]), [d] "=&v"(d), [z] "=&v"(z), [cnt] "=&s"(cnt)
                         : [off] "v"(off), [ws] "s"(ws), [nb] "v"(nb), [span] "s"(sp), [sl] "i"(ASLP), [n] "i"(1) : "vcc", "scc", "memory");
        else
            asm volatile("v_mov_b64 %[z], 0\n\ts_mov_b32 %[cnt], 0x200000\n\ts_nop 3\n"
                         ".Lpl%=:\n\t" ASK("%[a]") CHK("%[a]")
                         "s_sub_u32 %[cnt], %[cnt], 1\n\ts_cbranch_scc0 .Lpl%=\n.Lpe%=:"
                         : [a] "+v"(t.v[0]), [d] "=&v"(d), [z] "=&v"(z), [cnt] "=&s"(cnt)
                         : [off] "v"(off), [ws] "s"(ws), [nb] "v"(nb), [span] "s"(sp), [sl] "i"(ASLP), [n] "i"(0) : "vcc", "scc", "memory");
    }
}
DI void post(char* ws, unsigned off, u64 v) {
    if (APOST) asm volatile("s_nop 4\n\tglobal_atomic_swap_x2 %0, %1, %2 sc1" :: "v"(off), "v"(v), "s"(ws) : "memory");
    else __hip_atomic_store((GAS u64*)(ws + off), v, __ATOMIC_RELAXED, __HIP_MEMORY_SCOPE_AGENT);
}
template <int NW, int CNT, int NV = 0> DI void publish(LAS char* smem, int lane, char* ws, unsigned off, bool act, unsigned val) {
    const u64 ep = lds_epoch(smem) + val;
    unsigned old = 0;
    asm volatile("" :: "v"(ep), "v"(off));
    wait_v<NV>();
    if (lane == 0) asm volatile("ds_add_rtn_u32 %0, %1, %2\n\ts_waitcnt lgkmcnt(0)" : "+v"(old) : "v"(M_CNT + CNT * 4), "v"(1u) : "memory");
    if ((__builtin_amdgcn_readfirstlane(old) & (NW - 1)) == NW - 1) {
        if (act) post(ws, off, ep);
    }
}
typedef __bf16 bf16x2_t __attribute__((ext_vector_type(2)));
DI float dot2(unsigned a, unsigned b, float c) { return __builtin_amdgcn_fdot2_f32_bf16(__builtin_bit_cast(bf16x2_t, a), __builtin_bit_cast(bf16x2_t, b), c, false); }
DI u32x2 tile_stats(const u32x4& a, const u32x4& b) {
    float s = 0.f, q = 0.f;
#pragma unroll
    for (int j = 0; j < 4; ++j) { s = dot2(a[j], 0x3f803f80u, s); q = dot2(a[j], a[j], q); }
#pragma unroll
    for (int j = 0; j < 4; ++j) { s = dot2(b[j], 0x3f803f80u, s); q = dot2(b[j], b[j], q); }
    return (u32x2){__float_as_uint(red_sum4(s)), __float_as_uint(red_sum4(q))};
}

struct Ctx {
    LAS char* smem;
    char* ws;
    int lane, wid;
    int pq, jq;
    int pw, jw;
    int bid;
    int cw;
};
DI Ctx fresh(const Ctx& c0) {
    Ctx c = c0;
    asm volatile("" : "+v"(c.lane));
    asm volatile("" : "+s"(c.wid), "+s"(c.pq), "+s"(c.jq), "+s"(c.pw), "+s"(c.jw), "+s"(c.ws), "+s"(c.cw));
    return c;
}
DI unsigned o_post(const Ctx& c) { return (MBOX & 2) ? (unsigned)WS_OBOX + (c.pq * 16 + (c.lane & 15)) * OBS + c.jq * 8 : (unsigned)WS_FLAGQ + (c.pq * 16 + c.jq) * FQS; }
DI bool o_act(const Ctx& c) { return c.lane < ((MBOX & 2) ? 16 : 1); }
DI unsigned o_find(const Ctx& c) { return (MBOX & 2) ? (unsigned)WS_OBOX + (c.pw * 16 + c.jw) * OBS + (c.lane & 15) * 8 : (unsigned)WS_FLAGQ + (c.pw * 16 + (c.lane & 15)) * FQS; }
DI unsigned x_post(const Ctx& c) { return (MBOX & 1) ? (unsigned)WS_XBOX + (c.pw * 16 + (c.lane & 31)) * (2 * FQS) + (c.lane & 16) * (FQS / 16) + c.jw * 8 : (unsigned)WS_FLAGW + (c.pw * 16 + c.jw) * 128; }
DI bool x_act(const Ctx& c, bool halo) { return c.lane < ((MBOX & 1) ? (halo && c.pw < 15 ? 32 : 16) : 1); }
DI unsigned x_find(const Ctx& c) {
    const int pp = c.pq - (c.lane >> 4);
    return (MBOX & 1) ? (unsigned)WS_XBOX + (c.pq * 16 + c.jq) * (2 * FQS) + (c.lane & 16) * (FQS / 16) + (c.lane & 15) * 8 : (unsigned)WS_FLAGW + ((pp < 0 ? 0 : pp) * 16 + (c.lane & 15)) * 128;
}
#define STAMP_AT(slot) do { if ((slot) == SA || (slot) == SB) { if (c.lane == 0) *(LAS unsigned long long*)lds(MISC + 3328 + (slot) * 8) = rstamp(); } } while (0)

#define LSTAMP(i) do { } while (0)
#define LSTORE(ph) do { } while (0)
#define LMARK() do { } while (0)
#define LPUT(i) do { } while (0)
#define ASTORE() do { } while (0)
#define QSTAMP(i) do { } while (0)
#define QSTORE() do { } while (0)
#define FENCE() asm volatile("" ::: "memory")
#define ALIGN8() do { if (ALN) { asm volatile(".p2align 3"); SCHED(); } } while (0)
DI float addf(float a, float b) { float r; asm("v_add_f32 %0, %1, %2" : "=v"(r) : "v"(a), "v"(b)); return r; }
DI f32x4 add4x4(const u32x4& a, const u32x4& b, const u32x4& c, const u32x4& d) {
    f32x4 r;
#pragma unroll
    for (int i = 0; i < 4; ++i) r[i] = addf(addf(__uint_as_float(a[i]), __uint_as_float(b[i])), addf(__uint_as_float(c[i]), __uint_as_float(d[i])));
    asm volatile("s_nop 1" : "+v"(r));
    return r;
}
DI float fold16(float a, float b) {
    auto r = __builtin_amdgcn_permlane16_swap(__float_as_uint(a), __float_as_uint(b), false, false);
    float x = __uint_as_float(r[0]) + __uint_as_float(r[1]);
    asm volatile("" : "+v"(x));
    return x;
}
DI float fold32(float a, float b) {
    auto r = __builtin_amdgcn_permlane32_swap(__float_as_uint(a), __float_as_uint(b), false, false);
    float x = __uint_as_float(r[0]) + __uint_as_float(r[1]);
    asm volatile("" : "+v"(x));
    return x;
}
DI void aload4(u32x4& d, unsigned voff, const char* sb) { asm volatile("s_nop 4\n\tglobal_load_dwordx4 %0, %1, %2" : "=v"(d) : "v"(voff), "s"(sb) : "memory"); }
DI void aload4p(u32x4& d, unsigned voff, const char* sb) { asm volatile("s_nop 4\n\tglobal_load_dwordx4 %0, %1, %2" : "+v"(d) : "v"(voff), "s"(sb) : "memory"); }
DI void aload4c(u32x4& d, unsigned voff, const char* sb) { asm volatile("s_nop 4\n\tglobal_load_dwordx4 %0, %1, %2 sc1" : "=v"(d) : "v"(voff), "s"(sb) : "memory"); }
DI void aload2c(u32x2& d, unsigned voff, const char* sb) { asm volatile("s_nop 4\n\tglobal_load_dwordx2 %0, %1, %2 sc1" : "=v"(d) : "v"(voff), "s"(sb) : "memory"); }
DI unsigned wo_src(int q, int lane, int jw) { return (unsigned)((8 * q + (lane >> 3)) * (D_ * 2) + jw * 128 + (((lane & 7) ^ ((lane >> 3) & 6)) << 4)); }
DI unsigned pick(bool f, unsigned a, unsigned b) { asm volatile("" : "+v"(a), "+v"(b)); return f ? a : b; }
DI unsigned wo_w_off(const Ctx& c) { return pick(c.cw == 3, (unsigned)((c.wid & 3) * 1024 + c.lane * 16), wo_src(c.wid & 3, c.lane, c.jw)); }
DI unsigned wq_src(int ib, int lane, int jq) {
    const int seg = ib >> 3, ii = ib & 7, lr = lane >> 3;
    return (unsigned)((8 * ii + lr) * 6144 + seg * 2048 + jq * 128 + (((lane & 7) ^ ((lr & 2) | ((ii & 1) << 2))) << 4));
}
template <int S0, int S1> DI void wo_w_issue(const Ctx& c, const char* W, unsigned vw) {
    const int lw = c.wid & 3;
#pragma unroll
    for (int s = S0; s < S1; ++s) dma16<CPY != 0>(s * 4096 + lw * 1024, vw, W + (size_t)((2 * (s & 15) + (s >> 4)) * 32) * (D_ * 2));
}

DI void code_again(const Ctx& c) {
    const char* pc; int len, back;
    asm volatile("s_getpc_b64 %0\n.Lcpb%=:\n\ts_mov_b32 %2, .Lcpb%=-.Lcpf_a\n\ts_mov_b32 %1, .Lcpf_end-.Lcpf_a-4" : "=s"(pc), "=s"(len), "=s"(back));
    pc -= back;
    const int o = min((((c.bid >> 3) & 7) * 64 + c.lane) * 128, len);
    asm volatile("s_mov_b32 m0, %0\n\ts_nop 3\n\tglobal_load_lds_dword %1, %2" ::"s"(M_TCH), "v"(o), "s"(pc) : "memory");
}
template <bool HALO, bool FIRST> DI void qkv_load(const Ctx& c0, const char* xin, const char* W, const char* Wo, int l, KP kp = nullptr, unsigned va0 = 0, unsigned vw0 = 0) {
    const Ctx c = fresh(c0);
    asm volatile("" : "+s"(xin), "+s"(W), "+s"(Wo));
    LAS char* smem = c.smem;
    constexpr int NAI = HALO ? 5 : 4, NWI = 6, XS = FIRST ? XS_ : XSB, NR = HALO ? 144 : 128;
    constexpr Sched<NAI, NWI, FIRST> SC{};
    static_assert(NQA == 3 && NQW == 4, "schedule");
    const int lw = c.wid - 4, lane = c.lane, lr = lane >> 3, lc = lane & 7, fr = lane & 15, fq = lane >> 4;
    const int row0 = c.pq * 128;
    const bool cq_ = CPY && !FIRST && (c.jq & 7) == 3;
    if (cq_) W = c.ws + WS_CQ + ((HALO ? 0 : 2) + (c.jq >> 3)) * 24576;
    asm volatile("" : "+s"(W));
    constexpr int SP = FIRST ? NC : (HALO ? NCH : 0);
    static_assert(FIRST || SP + DS <= TL0 + 2 + TSP, "one flag for both kinds of sums");
    const int rot = FIRST ? rot_first(c.pq, c.jq) : (l == 1 ? c.pq : SP);
    Order od = FIRST ? first_order(c.pq, c.jq) : make_order<SP>(0, rot);
    if (FIRST) asm volatile("" : "+s"(od.base));
    if (FIRST || HALO) asm volatile("" : "+s"(od.r), "+s"(od.re));
    auto tile = [&](int s) { return FIRST || HALO ? first_tile<SP>(od, s) : s; };
    unsigned va[5], vw[NWI];
#pragma unroll
    for (int j = 0; j < 4; ++j) va[j] = (unsigned)(FIRST ? va0 + j * 32 * (D_ * 2) : c.pq * 262144 + (j * 4 + lw) * 1024 + lane * 16);
    if (HALO) va[4] = (unsigned)((c.pq > 0 ? (c.pq - 1) * 262144 + 14336 : 0) + lw * 512 + lane * 16);
#pragma unroll
    for (int j = 0; j < NWI; ++j) vw[j] = FIRST ? vw0 + (j & 1) * (32 * 6144) + (j >> 1) * 2048 : pick(cq_, (unsigned)((j * 4 + lw) * 1024 + lane * 16), wq_src(j * 4 + lw, lane, c.jq));
    auto issueA = [&](int s, int slot) {
        const char* b = xin + (unsigned)(tile(s) * (FIRST ? 128 : 16384));
#pragma unroll
        for (int j = 0; j < 4; ++j) dma16<true>(QA_OFF + slot * QA_SLOT + (j * 4 + lw) * 1024, va[j], b);
        if (HALO) { if (lane < 32) dma16<true>(QA_OFF + slot * QA_SLOT + 16384 + lw * 512, va[4], b); }
    };
    auto issueW = [&](int s, int slot, int j0 = 0, int j1 = NWI) {
        const char* b = W + (unsigned)(tile(s) * (64 * 6144));
#pragma unroll
        for (int j = j0; j < j1; ++j) dma16<CPY && !FIRST>(qw_slot(slot) + (j * 4 + lw) * 1024, vw[j], b);
    };
    auto ahead = [&]() {
        const int r3 = (lane * 43) >> 7;
        const unsigned o = (unsigned)((((rot - (c.pq & 7) + 10) * 64 + (c.pq & 7) * 48 + lw * 12 + r3) & 1023) * 6144 + (lane - 3 * r3) * 2048 + c.jq * 128);
        if (lane < 36 && (WTC != 2 || (c.jq & 7) == 3)) asm volatile("s_mov_b32 m0, %0\n\ts_nop 3\n\tglobal_load_lds_dword %1, %2" ::"s"(M_TCH), "v"(o), "s"(W) : "memory");
    };
    if (ST0 && FIRST && lw == 1) STAMP_AT(47);
    if (FIRST && ERL) {
        SCHED();
        issueA(1, 1); issueW(1, 1);
        SCHED();
    }
    const bool docs = FIRST || (HALO && l == 1);
    const int t = lw * 64 + lane;
    static_assert(MBOX & 1, "residual flag");
    unsigned foff, ftg, so, ho, fo_, co, l3, l4, vwo, wt[3], cso, xt, xt1, xso, tp, tl;
    u32x4 on;
    auto addr1 = [&]() {
        int lane = c.lane;
        asm volatile("" : "+v"(lane));
        const int fr = lane & 15, fq = lane >> 4;
        const int ph = c.pq > 0 ? c.pq - 1 : 0;
        foff = pick(lane != 32, (unsigned)WS_FLAGQ + pick(lane < 16, c.pq * 16 + lane, pick(lane < 32, fr * 16 + c.jq, ph * 16 + 14 + (lane & 1))) * FQS, (unsigned)WS_XBOX + (c.pw * 16 + c.jw) * (2 * FQS) + c.jw * 8);
        ftg = lane != 32 ? (unsigned)(4 * l + 1) : (unsigned)(l + 1);
        so = FIRST ? (unsigned)((c.pq * 16 + fq * 4) * 1024 + (lw * 32 + fr * 2) * 8) : (unsigned)((c.pq * 16 + (lane >> 2)) * 1024 + (c.jq * 8 + lw * 2 + (lane & 1)) * 8);
        ho = FDM ? (unsigned)(262144 + ((lw * 36 + lane < 16 ? ph * 128 + 128 : c.pq * 128) + lw * 36 + lane - 16) * 8)
                 : (unsigned)(262144 + pick(lane < 32, c.pq * 128 + lw * 32 + lane, ph * 128 + 112 + lw * 4 + (lane & 3)) * 8);
        fo_ = pick(lane < 2, (unsigned)(262144 + (c.pq * 128 + c.jq * 8 + lw * 2 + lane) * 8), 0x80000000u);
        const int cq = lw * 12 + (fr < 12 ? fr : 11);
        co = (unsigned)((fq * 4 * 3072 + (cq >> 4) * 1024 + c.jq * 64 + (cq & 15) * 4) * 4);
        l3 = (unsigned)(M_CS + (HALO ? 768 : 0) + (cq * 4 + fq) * 4);
        l4 = (unsigned)(M_MEAN + pick(lane < 32, 16 + lw * 32 + (FIRST ? fr * 2 + fq : lane), lw * 4 + (lane & 3)) * 4);
        asm volatile("" : "+v"(foff), "+v"(ftg), "+v"(so), "+v"(co), "+v"(l3), "+v"(l4));
        if (!FIRST) asm volatile("" : "+v"(ho), "+v"(fo_));
        SCHED();
    };
    auto addr2 = [&]() {
        int lane = c.lane;
        asm volatile("" : "+v"(lane));
        const int fr = lane & 15, fq = lane >> 4;
        const int q4 = fr >> 2, p4 = lane & 3, fb = (q4 & 2) | ((fq & 1) << 2);
#pragma unroll
        for (int i = 0; i < 3; ++i) {
            const int T = 3 * lw + i;
            wt[i] = (unsigned)(qw_slot(SP % NQW) + (T >> 2) * 8192 + (8 * fq + q4) * 128 + (((2 * (T & 3) + (p4 >> 1)) ^ fb) << 4) + (p4 & 1) * 8);
        }
        const int T = 3 * lw + fq;
        cso = (unsigned)((((HALO ? 16 : 0) + rot) * 3072 + (T >> 2) * 1024 + c.jq * 64 + (T & 3) * 16 + fr) * 4);
        xt = (unsigned)(QA_OFF + (SP % NQA) * QA_SLOT + (lw * 32 + fr) * 128 + ((fq ^ (fr & 7)) << 4));
        xt1 = xt ^ 64;
        xso = (unsigned)hl<HS>((c.pq * 16 + rot) * 1024 + (lw * 32 + fr) * 8);
        on = (u32x4){0x3f803f80u, 0x3f803f80u, 0x3f803f80u, 0x3f803f80u};
        if (FIRST || HALO) asm volatile("" : "+v"(wt[0]), "+v"(wt[1]), "+v"(wt[2]), "+v"(cso), "+v"(on));
        if (FIRST) asm volatile("" : "+v"(xt), "+v"(xt1), "+v"(xso));
        SCHED();
    };
    auto addr3 = [&]() {
        Ctx c2 = c;
        asm volatile("" : "+v"(c2.lane));
        asm volatile("" : "+s"(c2.bid));
        vwo = wo_w_off(c2);
        tp = (unsigned)(pick(c2.lane < 30, (c2.bid >> 3) * 30 + c2.lane, (c2.bid >> 3) * 16 + c2.lane - 30 + (int)(WS_O >> 16)) * 65536 + 65536 - 128);
        tl = (unsigned)(lw == 1 ? ((c2.bid >> 3) * 2 + c2.lane) * 65536 : c2.bid * 8192 + (TWO && !(EXS && (c.jq & 7) == 3) ? c2.lane * 128 : 0));
        asm volatile("" : "+v"(vwo));
        if (FIRST) asm volatile("" : "+v"(tp), "+v"(tl));
        SCHED();
    };
    if (!FIRST || AD1 < 0) addr1();
    if (FIRST && ERL >= 5) { issueA(2, 2); SCHED(); }
    if (FIRST && (ERL == 2 || ERL == 5)) { issueW(2, 2); SCHED(); }
    if (!FIRST || AD2 < 0) addr2();
    if (!FIRST || AD3 < 0) addr3();
#pragma unroll
    for (int j = 0; j < NAI; ++j) asm volatile("" : "+v"(va[j]));
#pragma unroll
    for (int j = 0; j < NWI; ++j) asm volatile("" : "+v"(vw[j]));
    SCHED();
    int tv = 0; unsigned long long lt = 0;
    if (FIRST) {
        LSTAMP(55);
    } else {
        static_assert(CPY == 1 && RS0 > 0 && RN0 == NWI && WPRE >= 3 && (FIRST || SC.w[0] == NAI), "order");
        unsigned vx[2], xfo = (unsigned)WS_XBOX + (c.pq * 16 + c.jq) * (2 * FQS) + (HALO && c.pq > 0 ? (lane & 16) * (FQS / 16) : 0) + fr * 8, go = M_GO;
        u64 xv = lds_epoch(smem) + (unsigned)(l + 1);
#pragma unroll
        for (int i = 0; i < 2; ++i) vx[i] = pick(cq_, (unsigned)(((2 * lw + i) * 4 + 3) * 1024 + lane * 16), wq_src((2 * lw + i) * 4 + 3, lane, c.jq));
        asm volatile("" : "+v"(vx[0]), "+v"(vx[1]), "+v"(xfo), "+v"(xv), "+v"(go));
        BAR();
        issueW(0, 0);
        auto word = [&](int off) { return __builtin_amdgcn_readfirstlane(*(volatile LAS int*)(lds(go) + off)); };
        auto until = [&](int off, int v) {
#pragma unroll 1
            for (unsigned spins = 0; spins < (1u << 24); ++spins) { if (word(off) == v) break; }
            FENCE();
        };
        static_assert(WPRE == 3, "stages");
        int t1 = tile(1), t2 = tile(2);
        asm volatile("" : "+s"(t1), "+s"(t2));
        auto stage = [&](int s) {
            const char* b = W + (unsigned)((s == 1 ? t1 : t2) * (64 * 6144));
            unsigned d = (unsigned)((s == 3 ? QW_TOP : s * QW_SLOT) + lw * 1024), dx = d + (unsigned)(lw * 7168 + 3072);
            asm volatile("" : "+s"(d), "+s"(dx), "+s"(b));
#pragma unroll
            for (int j = 0; j < NWI; ++j) dma16<true>(d + j * 4096, vw[j], b);
#pragma unroll
            for (int i = 0; i < 2; ++i) dma16<true>(dx + i * 4096, vx[i], b);
        };
        auto first2 = [&]() { __builtin_amdgcn_s_setprio(LPRIO); issueA(0, 0); issueA(1, 1); };
        if (lw == 3) {
            if (!EPOLL) until(M_CNT - M_GO, 8 * l);
            Ask t;
            poll_rmw(t, c.ws, xfo, lane < (HALO && c.pq > 0 ? 32 : 16), xv, 63 - l);
            *(volatile LAS int*)lds(go) = l;
            until(M_CNT - M_GO, 8 * l);
            first2();
            STAMP_AT(32 + l * 8 + 0);
            wait_v<NAI>();
            keep(t);
        } else {
            until(M_CNT - M_GO, 8 * l);
            if (RSLP) __builtin_amdgcn_s_sleep(RSLP);
            int ns = 1;
#pragma unroll 1
            for (; ns < WPRE; ++ns) {
                if (word(0) == l) break;
                stage(ns);
            }
            until(0, l);
            first2();
            const int left = WPRE - ns;
#pragma unroll 1
            for (; ns < WPRE; ++ns) stage(ns);
            if (left == 0) wait_v<NAI>();
            else if (left == 1) wait_v<NAI + 8>();
            else if (left == 2) wait_v<NAI + 16>();
            else wait_v<NAI + 24>();
        }
        FENCE();
    }
    if (FIRST && ERL == 3) issueW(2, 2);
    if (ST0 && FIRST && lw == 2) STAMP_AT(57);
    if (FIRST && !ERL) BAR();
    __builtin_amdgcn_s_setprio(LPRIO);
    if (FIRST && !ERL) { LSTAMP(57); issueA(1, 1); issueW(1, 1); LSTAMP(58); }
    u32x4 st[4], cs[4]; u32x2 sh, fl; u64 fv = 0; float rs_, rq_, hs;
    f32x4 d[3], sm[2], g[2];
    bf16x8 wf[3][2], xf[2][2];
    const bool fact = FIRST ? lane < 32 : ((lane < 16 || lane >= (docs ? 16 : 32)) && lane <= (HALO ? 34 : 32));
    static_assert(XS + 3 <= (FIRST ? 12 : 13) && SPR == 1, "aux schedule");
    auto side = [&](auto ktc) {
        constexpr int kt = decltype(ktc)::value;
        {
            constexpr int C0 = FIRST ? 12 : XS + 2, C1 = FIRST ? 13 : XS + 3, C2 = 13;
            auto acc4 = [](u32x4& a, const u32x4& b) {
#pragma unroll
                for (int i = 0; i < 4; ++i) a[i] = __float_as_uint(addf(__uint_as_float(a[i]), __uint_as_float(b[i])));
                asm volatile("" : "+v"(a));
            };
            auto fin = [&]() {
                rs_ *= -1.f / 1024.f;
                asm volatile("" : "+v"(rs_));
                rq_ = __builtin_amdgcn_rsqf(fmaxf(rq_ * (1.f / 1024.f) - rs_ * rs_, 0.f) + LN_EPS);
                asm volatile("" : "+v"(rq_));
            };
            if constexpr (FIRST && kt == XS + 2) {
                asm volatile("" : "+v"(st[0]), "+v"(st[1]), "+v"(st[2]));
                acc4(st[0], st[1]); acc4(st[0], st[2]);
            }
            if constexpr (FIRST && kt == XS + 3) { asm volatile("" : "+v"(st[3])); acc4(st[0], st[3]); }
            if constexpr (kt == C0) {
                if (docs) { asm volatile("" : "+v"(cs[0]), "+v"(cs[1])); acc4(cs[0], cs[1]); }
            }
            if constexpr (FIRST && kt == 12) {
                asm volatile("s_nop 1" : "+v"(st[0]));
                rs_ = fold16(__uint_as_float(st[0][0]), __uint_as_float(st[0][2])); rq_ = fold16(__uint_as_float(st[0][1]), __uint_as_float(st[0][3]));
                rs_ = fold32(rs_, rs_); rq_ = fold32(rq_, rq_);
            }
            if constexpr (FIRST && kt == 13) fin();
            if constexpr (!FIRST && kt == TL0 + 2) {
                asm volatile("" : "+v"(sh));
                hs = fold16(__uint_as_float(sh[0]), __uint_as_float(sh[1]));
                hs = fold32(hs, hs);
                hs += __uint_as_float(__builtin_amdgcn_update_dpp(0u, __float_as_uint(hs), 0x124, 0xf, 0xf, false));
                hs += __uint_as_float(__builtin_amdgcn_update_dpp(0u, __float_as_uint(hs), 0x128, 0xf, 0xf, false));
                asm volatile("" : "+v"(hs));
            }
            if constexpr (!FIRST && kt == TL0 + 2 + TSP) {
                auto hh = __builtin_amdgcn_permlane16_swap(__float_as_uint(hs), __float_as_uint(hs), false, false);
                rs_ = __uint_as_float(hh[0]); rq_ = __uint_as_float(hh[1]);
                asm volatile("" : "+v"(rs_), "+v"(rq_));
                fin();
                auto rf = __builtin_amdgcn_make_buffer_rsrc(c.ws + WS_STATS + (size_t)l * 524288, 0, 524288, 0x00020000);
                __builtin_amdgcn_raw_buffer_store_b64((u32x2){__float_as_uint(rs_), __float_as_uint(rq_)}, rf, fo_, 0, 16);
            }
            if constexpr (kt == C1) {
                if (docs) { asm volatile("" : "+v"(cs[2]), "+v"(cs[3])); acc4(cs[2], cs[3]); acc4(cs[0], cs[2]); }
            }
            if constexpr (kt == C2) {
                if (docs) {
                    asm volatile("s_nop 1" : "+v"(cs[0]));
                    hs = fold32(fold16(__uint_as_float(cs[0][0]), __uint_as_float(cs[0][1])), fold16(__uint_as_float(cs[0][2]), __uint_as_float(cs[0][3])));
                }
            }
            if constexpr (kt == 13) {
                static_assert(WV0 == 1, "wait");
                if (FIRST || !FDM || docs) WAIT_V0();
                if (!FIRST && !FDM) { asm volatile("" : "+v"(sh)); rs_ = __uint_as_float(sh[0]); rq_ = __uint_as_float(sh[1]); }
                if ((FIRST || !FDM) && lane < (HALO ? 36 : 32)) {
                    *(LAS float*)lds(l4) = rs_;
                    *(LAS float*)(lds(l4) + (M_RSTD - M_MEAN)) = rq_;
                }
                if (docs) *(LAS float*)lds(l3) = hs;
                FENCE();
            }
        }
    };
    static_for<0, KT>([&](auto ktc) {
        constexpr int kt = decltype(ktc)::value;
        LSTAMP(3 * kt);
        if constexpr (FIRST || kt > 0) wait_v<SC.w[kt]>();
        LSTAMP(3 * kt + 1);
        if constexpr (kt == XS) {
            if (lw == 0) {
                asm volatile("" : "+v"(fl));
                const u64 ep = lds_epoch(smem);
                if (!__all(!fact || reached(__builtin_bit_cast(u64, fl), ep, ftg))) poll_flags(c.ws, ep, foff, fact, ftg);
            }
        }
        if constexpr (kt == 14) asm volatile("s_waitcnt lgkmcnt(0)" ::: "memory");
        BAR();
        FENCE();
        LMARK();
        LSTAMP(3 * kt + 2);
        if constexpr (TOPJ & (1 << kt)) side(ktc);
        if constexpr (kt == (FIRST ? SP + DS : TL0 + 2 + TSP) + 3) {
            if (lw == 0 && lane == 0) __hip_atomic_store((GAS u64*)(c.ws + WS_FLAGQ + (c.pq * 16 + c.jq) * FQS), fv, __ATOMIC_RELAXED, __HIP_MEMORY_SCOPE_AGENT);
        }
        if constexpr (kt == XS + 1 - NQA) {
            if (lw == 0) { if (fact) aload2c(fl, foff, c.ws); }
        }
        {
            const char* sb = c.ws + WS_STATS + (size_t)l * 524288;
            const char* cb = c.ws + WS_CSUM + (HALO ? 16 * 3072 * 4 : 0);
            constexpr int sa = 3 * (kt - XS), ca = FIRST ? 2 * (kt - XS - 1) : 2 * (kt - XS), cn = 2;
            if constexpr (FIRST) {
#pragma unroll
                for (int i = sa; i < sa + 3; ++i) if (i >= 0 && i < 4) aload4c(st[i], so, sb + i * 1024);
            }
            if constexpr (!FIRST && kt == TL0) aload2c(sh, so, sb);
            if constexpr (!FIRST && kt == TL1) {
                static_assert(TL1 <= 12, "covered by the wait of step 14");
                if (FDM) { if (lane < 36) { touch(M_MEAN + lw * 144, ho, sb); touch(M_RSTD + lw * 144, ho, sb + 4); } }
                else aload2c(sh, ho, sb);
            }
            if constexpr (ca >= 0 && ca < 4) {
                if (docs) {
#pragma unroll
                    for (int i = ca; i < ca + cn; ++i) aload4c(cs[i], co, cb + i * 12288);
                }
            }
        }
        if constexpr (kt == SP) {
            if (docs) {
#pragma unroll
                for (int i = 0; i < 3; ++i)
#pragma unroll
                    for (int ks = 0; ks < 2; ++ks) {
                        const s16x4 lo = tr_read(lds(wt[i]) + ks * 4096), hi = tr_read(lds(wt[i]) + ks * 4096 + 512);
                        wf[i][ks] = __builtin_shufflevector(lo, hi, 0, 1, 2, 3, 4, 5, 6, 7);
                    }
                if (FIRST) {
#pragma unroll
                    for (int mt = 0; mt < 2; ++mt)
#pragma unroll
                        for (int ks = 0; ks < 2; ++ks) xf[mt][ks] = *(const LAS bf16x8*)(lds(ks ? xt1 : xt) + mt * 2048);
                }
            }
            SCHED();
        }
        if constexpr (kt + 2 < KT && !(FIRST && ERL >= 5 && kt == 0)) issueA(kt + 2, (kt + 2) % NQA);
        LPUT(3 * kt + 2);
        if constexpr (kt == SP + DM) {
            SCHED();
            if (docs) {
                const bf16x8 ones = __builtin_bit_cast(bf16x8, on);
                const f32x4 zero = {0.f, 0.f, 0.f, 0.f};
#pragma unroll
                for (int ks = 0; ks < 2; ++ks) {
#pragma unroll
                    for (int i = 0; i < 3; ++i) d[i] = MFMA(ones, wf[i][ks], ks ? d[i] : zero);
                    if (FIRST) {
#pragma unroll
                        for (int mt = 0; mt < 2; ++mt) { sm[mt] = MFMA(ones, xf[mt][ks], ks ? sm[mt] : zero); g[mt] = MFMA(xf[mt][ks], xf[mt][ks], ks ? g[mt] : zero); }
                    }
                }
            }
            SCHED();
        }
        if constexpr (FIRST && ERL < 2 && kt < 5) {
            if constexpr (kt < 4) issueW(kt + 2, (kt + 2) % NQW);
            if constexpr (kt == 3) issueW(6, 2, 0, NWI / 2);
            if constexpr (kt == 4) { issueW(6, 2, NWI / 2, NWI); issueW(7, 3); }
        } else {
            if constexpr (WPRE == 2 && kt < 2) issueW(kt + 2, kt + 2);
            if constexpr (FIRST && (ERL == 4 || ERL == 6) && kt == 0) issueW(2, 2);
            if constexpr ((FIRST || WPRE == 3) && kt == 0) issueW(3, 3);
            if constexpr (kt >= 1 && kt + 3 < KT) issueW(kt + 3, (kt + 3) % NQW);
        }
        if constexpr (FIRST && kt == WT0) ahead();
        if constexpr ((CP2 & 4) && FIRST && kt == 8) { if (lw == 2) code_again(c); }
        if constexpr ((CP2 & 8) && HALO && kt == 8) { if (lw == 2 && l == 1) code_again(c); }
        if constexpr (FIRST && kt == TSTEP) {
            if (lw == 1 && lane < 46) touch(M_TCH, tp, c.ws);
        }
        if constexpr (FIRST && kt == TW0) {
            if (lw == 0) touch(M_TCH, tl, Wo);
            else if (lw == 1) { asm volatile("" : "+s"(kp)); const char* outp = (const char*)kp->out; if (lane < 2) touch(M_TCH, tl, outp); }
        }
        if constexpr (kt == SP && DM > 0) {
            SCHED();
            if (docs) {
                asm volatile("" : "+v"(wf[0][0]), "+v"(wf[0][1]), "+v"(wf[1][0]), "+v"(wf[1][1]), "+v"(wf[2][0]), "+v"(wf[2][1]));
                if (FIRST) asm volatile("" : "+v"(xf[0][0]), "+v"(xf[0][1]), "+v"(xf[1][0]), "+v"(xf[1][1]));
            }
        }
        if constexpr (kt == SP + DS) {
            SCHED();
            if (docs) {
                if (FIRST) {
                    auto rst = __builtin_amdgcn_make_buffer_rsrc(c.ws + WS_STATS, 0, 524288, 0x00020000);
                    const bool b0 = fr & 1, b1 = fr & 2;
                    u32x2 sv[2];
#pragma unroll
                    for (int mt = 0; mt < 2; ++mt) {
                        float qa = b0 ? g[mt][1] : g[mt][0], qb = b0 ? g[mt][3] : g[mt][2];
                        asm volatile("" : "+v"(qa), "+v"(qb));
                        sv[mt] = (u32x2){__float_as_uint(sm[mt][0]), __float_as_uint(b1 ? qb : qa)};
                    }
                    if (fq == (fr >> 2)) {
                        __builtin_amdgcn_raw_buffer_store_b64(sv[0], rst, xso, 0, 16);
                        __builtin_amdgcn_raw_buffer_store_b64(sv[1], rst, xso + hl<HS>(128), 0, 16);
                    }
                }
                float d0 = d[0][0], d1 = d[1][0], d2 = d[2][0];
                asm volatile("" : "+v"(d0), "+v"(d1), "+v"(d2));
                const float v = fq == 0 ? d0 : (fq == 1 ? d1 : d2);
                auto rc = __builtin_amdgcn_make_buffer_rsrc(c.ws + WS_CSUM, 0, 2 * 16 * 3072 * 4, 0x00020000);
                if (lane < 48) __builtin_amdgcn_raw_buffer_store_b32(__float_as_uint(v), rc, cso, 0, 16);
                asm volatile("s_waitcnt lgkmcnt(0)" ::: "memory");
            }
        }
        if constexpr (kt == 1) {
            if (lw == 0) {
                const u32x2 e = *(const LAS u32x2*)(smem + M_EP);
                const u64 es = ((u64)(unsigned)__builtin_amdgcn_readfirstlane(e[1]) << 32) | (unsigned)__builtin_amdgcn_readfirstlane(e[0]);
                fv = es + (unsigned)(4 * l + 1);
                asm volatile("" : "+s"(fv));
            }
        }
        if constexpr (FIRST && kt == AD1) addr1();
        if constexpr (FIRST && kt == AD2) addr2();
        if constexpr (FIRST && kt == AD3) addr3();
        if constexpr (!(TOPJ & (1 << kt))) side(ktc);
        if constexpr (kt == 13) wo_w_issue<0, WT13>(c, Wo, vwo);
        if constexpr (kt == 14) { wo_w_issue<WT13, (HALO || WA0 > 12) ? 12 : WA0>(c, Wo, vwo); if (HALO) wo_w_issue<23, 23 + WB14>(c, Wo, vwo); }
        if constexpr (kt == 15) {
            if (HALO ? WBE < 2 : WA0 > 12) wo_w_issue<12, HALO ? 18 : WA0>(c, Wo, vwo);
            if (HALO) wo_w_issue<23 + WB14, 23 + WB14 + WB15>(c, Wo, vwo);
        }
    });
    __builtin_amdgcn_s_setprio(0);
    BAR();
    LMARK(); LPUT(48);
    LSTAMP(48);
    LSTORE(0);
    if (HALO ? WBE < 1 : WAE < 1) wo_w_issue<18, HALO ? 23 : 20>(c, Wo, vwo);
    if (HALO) {
        static_assert(!WBS || (WBE == 1 && WB14 == 0 && WRG <= 12), "Wo weights of the first twelve steps are older than the last 6 + WB15");
        if (WBS) wait_v<6 + WB15>();
        BAR();
        if (WBE) wo_w_issue<WBE < 2 ? 18 : 12, 23>(c, Wo, vwo);
        wo_w_issue<23 + WB14 + WB15, 32>(c, Wo, vwo);
    }
}

DI float max3(float a, float b, float c) { return fmaxf(fmaxf(a, b), c); }
#define SGB(m, n) __builtin_amdgcn_sched_group_barrier(m, n, 0)
template <bool HALO> struct Tail {
    int f[26], n[4], t[4][26], d[4][27], v[4][27], h3[32], s3[32], o3[33], q3[112];
    constexpr Tail() : f{}, n{}, t{}, d{}, v{}, h3{}, s3{}, o3{}, q3{} {
        const int FB[26] = {0, 1, 2, 3, 4, 5, 6, 7, 8, 9, 10, 11, 12, 13, 14, 15, 24, 25, 19, 23, 18, 22, 17, 21, 16, 20};
        const int FA[24] = {0, 1, 2, 3, 4, 5, 6, 7, 8, 9, 10, 11, 12, 13, 14, 15, 16, 17, 20, 21, 18, 19, 22, 23};
        const int TA[4][26] = {{0, 1, 2, 3, 7, 6, 5, 4, 8, 9, 10, 11, 15, 14, 13, 12, 16, 17, 18, 19, 23, 22, 21, 20, 0, 0}, {4, 8, 9, 10, 11, 15, 14, 13, 12, 16, 17, 18, 19, 23, 22, 21, 20, 0, 1, 2, 3, 7, 6, 5, 0, 0}, {16, 17, 18, 19, 23, 22, 21, 20, 0, 1, 2, 3, 7, 6, 5, 4, 8, 9, 10, 11, 15, 14, 13, 12, 0, 0}, {17, 18, 19, 23, 22, 21, 20, 8, 9, 10, 11, 15, 14, 13, 12, 16, 0, 0, 0, 0, 0, 0, 0, 0, 0, 0}};
        const int DA[4][26] = {{1, 1, 1, 1, 1, 1, 1, 1, 1, 1, 1, 1, 1, 1, 1, 1, 1, 1, 1, 1, 1, 1, 0, 0, 0, 0}, {1, 1, 1, 1, 1, 1, 1, 1, 1, 1, 1, 1, 1, 1, 1, 1, 1, 1, 1, 1, 1, 1, 0, 0, 0, 0}, {1, 1, 1, 1, 1, 1, 1, 1, 1, 1, 1, 1, 1, 1, 1, 1, 0, 0, 0, 0, 0, 0, 0, 0, 0, 0}, {2, 2, 2, 2, 1, 1, 1, 1, 1, 1, 1, 1, 0, 0, 0, 0, 0, 0, 0, 0, 0, 0, 0, 0, 0, 0}};
        const int VA[4][26] = {{1, 1, 0, 0, 0, 0, 1, 1, 1, 1, 1, 1, 1, 1, 1, 1, 1, 1, 1, 1, 1, 1, 2, 2, 0, 0}, {1, 1, 1, 1, 1, 1, 1, 1, 1, 1, 1, 1, 1, 1, 1, 1, 1, 1, 1, 1, 1, 1, 2, 2, 0, 0}, {1, 1, 1, 1, 1, 1, 1, 1, 1, 1, 1, 1, 1, 1, 1, 1, 2, 2, 2, 2, 2, 3, 3, 2, 0, 0}, {0, 0, 0, 0, 1, 1, 1, 1, 1, 1, 1, 1, 2, 2, 2, 2, 0, 0, 0, 0, 0, 0, 0, 0, 0, 0}};
        const int TB[4][26] = {{0, 1, 2, 3, 7, 6, 5, 4, 24, 8, 9, 10, 11, 15, 14, 13, 12, 25, 16, 17, 18, 19, 23, 22, 21, 20}, {24, 8, 9, 10, 11, 15, 14, 13, 12, 25, 16, 17, 18, 19, 23, 22, 21, 20, 0, 1, 2, 3, 7, 6, 5, 4}, {25, 16, 17, 18, 19, 23, 22, 21, 20, 0, 1, 2, 3, 7, 6, 5, 4, 24, 8, 9, 10, 11, 15, 14, 13, 12}, {0, 1, 2, 3, 7, 6, 5, 4, 24, 8, 9, 10, 11, 15, 14, 13, 12, 25, 19, 23, 0, 0, 0, 0, 0, 0}};
        const int DB[4][26] = {{1, 1, 1, 1, 1, 1, 1, 1, 1, 1, 1, 1, 1, 1, 1, 1, 1, 1, 1, 1, 1, 1, 1, 0, 0, 0}, {1, 1, 1, 1, 1, 1, 1, 1, 1, 1, 1, 1, 1, 1, 1, 1, 1, 1, 1, 1, 1, 1, 0, 0, 0, 0}, {1, 1, 1, 1, 1, 1, 1, 1, 1, 1, 1, 1, 1, 1, 1, 1, 1, 1, 1, 1, 1, 1, 1, 0, 0, 0}, {2, 2, 1, 1, 1, 1, 1, 1, 1, 1, 1, 1, 1, 1, 1, 1, 1, 1, 1, 0, 0, 0, 0, 0, 0, 0}};
        const int VB[4][26] = {{1, 1, 1, 1, 1, 1, 1, 1, 1, 1, 1, 1, 1, 1, 1, 1, 1, 1, 1, 1, 1, 1, 1, 2, 2, 2}, {1, 1, 1, 1, 1, 1, 1, 1, 1, 1, 1, 1, 1, 1, 1, 1, 1, 1, 1, 1, 1, 1, 3, 3, 3, 2}, {1, 1, 1, 1, 1, 1, 1, 1, 1, 1, 1, 1, 1, 1, 1, 1, 1, 1, 1, 1, 1, 1, 1, 3, 3, 2}, {0, 0, 1, 1, 1, 1, 1, 1, 1, 1, 1, 1, 1, 1, 1, 1, 1, 1, 1, 2, 0, 0, 0, 0, 0, 0}};
        const int G[32] = {8, 12, 9, 13, 10, 14, 11, 15, 16, 17, 20, 21, 18, 19, 22, 23, 0, 4, 1, 5, 2, 6, 3, 7, 0, 4, 1, 5, 2, 6, 3, 7};
        const int NM = HALO ? 26 : 24;
        n[0] = n[1] = n[2] = NM; n[3] = HALO ? 20 : 16;
        for (int i = 0; i < NM; ++i) f[i] = HALO ? FB[i] : FA[i];
        int vv = 0;
        for (int h = 0; h < 4; ++h) {
            v[h][0] = vv;
            for (int i = 0; i < n[h]; ++i) {
                t[h][i] = HALO ? TB[h][i] : TA[h][i];
                d[h][i + 1] = d[h][i] + (HALO ? DB[h][i] : DA[h][i]);
                vv += HALO ? VB[h][i] : VA[h][i];
                v[h][i + 1] = vv;
            }
        }
        for (int i = 0; i < 32; ++i) { h3[i] = G[i]; s3[i] = i >= 16 && i < 24 ? 0 : 1; }
        for (int i = 3; i < 32; ++i) o3[i + 1] = o3[i] + TOPS;
        int nv = 0, ns = 0;
        for (int k = 0; k < 8 * 14; ++k) {
            const bool top = k % TOPS < 2 || nv == 96;
            if (top && ns < 16 && nv >= 12 * (ns / 2 + 1) && (nv == 96 || k % TOPS == (ns & 1))) { q3[k] = (ns / 2) * 14 + 13 - (ns & 1); ++ns; }
            else { q3[k] = (nv / 12) * 14 + nv % 12; ++nv; }
        }
    }
};
template <bool HALO> DI void qkv_consume(const Ctx& c0, int l, KP kp, unsigned va0 = 0, unsigned vw0 = 0) {
    const Ctx c = fresh(c0);
    if (ST0 && l == 0 && c.wid == 2) STAMP_AT(59);
    LAS char* smem = c.smem;
    const int lane = c.lane, wr = c.wid >> 1, wc = c.wid & 1, fr = lane & 15, fq = lane >> 4;
    const int sw = HALO ? wr : 0;
    f32x4 acc[4][6], ah[2];
#pragma unroll
    for (int mt = 0; mt < 4; ++mt)
#pragma unroll
        for (int nt = 0; nt < 6; ++nt) { acc[mt][nt] = (f32x4){0.f, 0.f, 0.f, 0.f}; asm volatile("" : "+v"(acc[mt][nt])); }
    ah[0] = ah[1] = (f32x4){0.f, 0.f, 0.f, 0.f};
    if (HALO) asm volatile("" : "+v"(ah[0]), "+v"(ah[1]));
    const unsigned aoff0 = (unsigned)(QA_OFF + wr * 8192 + (HALO || l ? lane * 16 : fr * 128 + ((fq ^ (fr & 7)) << 4)));
    const unsigned hoff0 = (unsigned)(QA_OFF + 16384 + lane * 16);
    unsigned xm = HALO || l ? 1024u : 64u;
    asm volatile("" : "+s"(xm));
    static_assert(((QA_OFF | QA_SLOT) & 2047) == 0, "halves");
    const int q4 = fr >> 2, p4 = lane & 3;
    const int fb = (q4 & 2) | ((fq & 1) << 2);
    unsigned boff[2];
#pragma unroll
    for (int cc = 0; cc < 2; ++cc)
        boff[cc] = (unsigned)((8 * fq + q4) * 128 + (((2 * (2 * wc + (cc ^ sw)) + (p4 >> 1)) ^ fb) << 4) + (p4 & 1) * 8);
    bf16x8 xa[2][4], xh[2], wb[2][6];
    auto ldfrag = [&](int set, unsigned pa, unsigned ph, unsigned pb0, unsigned pb1, int ks) {
#pragma unroll
        for (int mt = 0; mt < 4; ++mt) xa[set][mt] = *(const LAS bf16x8*)(lds(pa) + mt * 2048);
        if (HALO) xh[set] = *(const LAS bf16x8*)lds(ph);
#pragma unroll
        for (int nt = 0; nt < 6; ++nt) {
            const LAS char* pb = lds((nt & 1) ? pb1 : pb0) + (nt >> 1) * 8192 + ks * 4096;
            const s16x4 lo = tr_read(pb), hi = tr_read(pb + 512);
            wb[set][nt] = __builtin_shufflevector(lo, hi, 0, 1, 2, 3, 4, 5, 6, 7);
        }
    };
    constexpr int NM = HALO ? 26 : 24, NH = HALO ? 17 : 16;
    auto mm = [&](int set, int i0, int i1) {
#pragma unroll
        for (int ii = i0; ii < i1; ++ii) {
            const int i = !HALO || ii < 8 ? ii : (!SNK ? (ii < 10 ? ii + 16 : ii - 2) : (ii == 8 ? 24 : (ii < 17 ? ii - 1 : (ii == 17 ? 25 : ii - 2))));
            const int nt = i >> 2, mt = SNK && (nt & 1) ? 3 - (i & 3) : i & 3;
            if (i < 16) acc[mt][nt] = MFMA(wb[set][nt], xa[set][mt], acc[mt][nt]);
            else if (i < 24) acc[mt][nt] = MFMA(xa[set][mt], wb[set][nt], acc[mt][nt]);
            else if (i == 24) ah[0] = MFMA(wb[set][2], xh[set], ah[0]);
            else ah[1] = MFMA(xh[set], wb[set][4], ah[1]);
        }
    };
    auto interleave = [&]() {
#pragma unroll
        for (int i = 0; i < NH; ++i) { SGB(0x008, 1); SGB(0x100, 1); }
        SCHED();
    };
    unsigned sa = 0, sb = 0;
    unsigned pa0 = aoff0, pa1 = aoff0 ^ xm, ph0 = hoff0, ph1 = hoff0 ^ xm, pb0 = boff[0], pb1 = boff[1];
    asm volatile("" : "+v"(pa0), "+v"(pa1), "+v"(pb0), "+v"(pb1));
    if (HALO) asm volatile("" : "+v"(ph0), "+v"(ph1));
    constexpr int M_CSH = M_CS + (HALO ? 768 : 0);
    unsigned sa0 = (unsigned)(M_MEAN + 64 + (wr * 64 + fr) * 4), sa1 = (unsigned)(M_MEAN + 64 + wr * 256 + fq * 16);
    unsigned sa2 = (unsigned)(M_CSH + wc * 128 + sw * 64 + fq * 16), sa6 = (unsigned)(M_CSH + 512 + wc * 128 + sw * 64 + fr * 4);
    unsigned sa3 = sa2 ^ 64, sa7 = sa6 ^ 64, sa4 = (unsigned)(M_MEAN + fr * 4), sa5 = (unsigned)(M_MEAN + fq * 16);
    asm volatile("" : "+v"(sa0), "+v"(sa1), "+v"(sa2), "+v"(sa6), "+v"(sa3), "+v"(sa7));
    if (HALO) asm volatile("" : "+v"(sa4), "+v"(sa5));
    const int head = c.jq * 2 + wc;
    auto rso = __builtin_amdgcn_make_buffer_rsrc(HALO ? c.ws + WS_O + (size_t)l * (8u << 20) + (size_t)((head * 16 + c.pq) * 8 + wr * 4) * (1024 << HO)
                                                      : c.ws + WS_KV + (size_t)(l >> 1) * (16u << 20) + (size_t)((c.pq * 32 + head) * 4 + wr) * (4096 << HK), 0, HALO ? 4096 << HO : 12288 << HK, 0x00020000);
    unsigned xrs = (unsigned)(M_XCH + wc * 6144 + wr * 2048), xhs = (unsigned)(M_XCH + wc * 6144 + 8 - wr * 8);
    unsigned b0m = (c.pq == 0 && wr == 0) ? 15u * 16u : 0u;
    asm volatile("" : "+s"(rso));
    if (HALO) asm volatile("" : "+s"(xrs), "+s"(xhs), "+s"(b0m));
    u32x4 cpy[3];
    unsigned cv, vt, cdst;
    const char *cs_, *tw, *cd_;
    int more = !HALO && l == 0 ? KT - 3 - XST - (c.pq & 7) : 0, three = c.jq & 4;
    if (!HALO) {
        static_assert(CPY + TOUCH == 2 && XSP == 1, "extra");
        asm volatile("" : "+s"(kp));
        const int g = c.jq >> 2, n = c.pq * 4 + (c.jq & 3), m = n >> 5, kt = (n >> 1) & 15;
        const char* src = *(const char* const __attribute__((address_space(4)))*)((const __attribute__((address_space(4))) char*)kp + (three ? (m ? 8 : 24) : (m ? 32 : 40)));
        const char* tsr = *(const char* const __attribute__((address_space(4)))*)((const __attribute__((address_space(4))) char*)kp + (c.wid < 3 ? 24 : 40));
        int o1 = kt * (64 * 6144) + (n & 1) * (32 * 6144) + (3 + 4 * (g & 2) - c.jq) * 128 + 2048, o2 = (n & 31) * 65536 + (11 - 4 * g) * 128 - c.pq * 128 * (D_ * 2) + 2048;
        int d1 = (int)WS_CQ + kt * (64 * 6144) + (m * 2 + (g >> 1)) * 24576 + (n & 1) * 4096, d2 = (int)WS_CO + (n & 31) * 65536 + (m * 2 + 1 - (g >> 1)) * 4096;
        asm volatile("" : "+s"(o1), "+s"(o2), "+s"(d1), "+s"(d2));
        cs_ = src + (three ? o1 : o2);
        tw = tsr + (unsigned)((c.wid < 3 ? c.wid : 0) * (2u << 20) + c.bid * 8192);
        cd_ = c.ws + (unsigned)(three ? d1 : d2);
        cv = pick(three, vw0, va0 ^ ((lane & 8) << 1));
        cdst = (unsigned)(c.wid * 1024 + lane * 16);
        vt = TWQ ? (unsigned)(((lane & 7) == 3 ? lane - 1 : lane) * 128) : 0u;
        if (EXS) vt &= (c.jq & 7) == 3 ? 0u : ~0u;
        asm volatile("" : "+s"(more), "+s"(three), "+s"(cs_), "+s"(tw), "+s"(cd_));
        asm volatile("" : "+v"(cv), "+v"(cdst), "+v"(vt));
        asm volatile("" : "=v"(cpy[0]), "=v"(cpy[1]), "=v"(cpy[2]));
    }
    unsigned hto = HT0 == 2 ? 0u : 0x80000000u;
    const char* W0 = (const char*)kp->wqkv0;
    if (!HALO && HT0) {
        const int n = ((c.pq & 7) * 3 + (c.jq & 3)) * 64 + c.wid * 16 + fr, r3 = (n * 43691) >> 17;
        const int t0 = rot_first(c.pq & 8, c.jq | 3) + 8;
        if (l == 0 && !(c.jq & 4) && (c.jq & 3) != 3 && lane < 16) hto = (unsigned)(((t0 * 64 + r3) & 1023) * 6144 + (n - 3 * r3) * 2048 + (c.jq | 3) * 128);
        asm volatile("" : "+v"(hto));
        asm volatile("" : "+s"(W0));
    }
    if (!HALO && HT0 == 2) { asm volatile("s_mov_b32 m0, %0\n\ts_nop 3\n\tglobal_load_lds_dword %1, %2" ::"s"(M_TCH), "v"(hto), "s"(W0) : "memory"); }
    if (!HALO && !ERL && l == 0) BAR();
    if (ST0 && l == 0 && c.wid == 1) STAMP_AT(44);
    if (!HALO) { if (HT0 == 2) wait_v<1>(); else WAIT_V0(); }
    if (ST0 && l == 0 && c.wid == 1) STAMP_AT(46);
    BAR();
    if (!HALO && HT0 == 1) { if (hto < 0x80000000u) asm volatile("s_mov_b32 m0, %0\n\ts_nop 3\n\tglobal_load_lds_dword %1, %2" ::"s"(M_TCH), "v"(hto), "s"(W0) : "memory"); }
    if (c.wid == 0) STAMP_AT(l * 8 + 0);
    ldfrag(0, pa0, ph0, pb0, pb1, 0);
    SCHED();
    if (ALN) WAIT_L0();
    SCHED();
    int cnt = KT - 3 - more, tv = 0;
    QSTAMP(0);
    auto half0 = [&]() {
        ldfrag(1, pa1, ph1, pb0, pb1, 1);
        mm(0, 0, NH);
        interleave();
        mm(0, NH, NH + 1); SCHED();
        asm volatile("s_addk_i32 %0, %2\n\ts_addk_i32 %1, %3" : "+s"(sa), "+s"(sb) : "i"(QA_SLOT), "i"(QW_SLOT) : "scc"); SCHED();
        mm(0, NH + 1, NH + 2); SCHED();
        asm volatile("s_cmpk_lg_u32 %0, %2\n\ts_cselect_b32 %0, %0, 0\n\ts_cmp_lg_u32 %1, %3\n\ts_cselect_b32 %1, %1, %4" : "+s"(sa), "+s"(sb) : "i"(NQA * QA_SLOT), "i"(3 * QW_SLOT), "i"(QW_TOP) : "scc"); SCHED();
        mm(0, NH + 2, NH + 3); SCHED();
        asm volatile("s_cmp_lg_u32 %0, %2\n\ts_cselect_b32 %0, %0, 0\n\ts_addk_i32 %1, -1" : "+s"(sb), "+s"(cnt) : "i"(QW_TOP + QW_SLOT) : "scc"); SCHED();
        mm(0, NH + 3, NH + 4); SCHED();
        pa0 = aoff0 + sa; pb0 = boff[0] + sb;
        asm volatile("" : "+v"(pa0), "+v"(pb0)); SCHED();
        mm(0, NH + 4, NH + 5); SCHED();
        pb1 = boff[1] + sb; pa1 = pa0 ^ xm;
        asm volatile("" : "+v"(pa1), "+v"(pb1)); SCHED();
        mm(0, NH + 5, NH + 6); SCHED();
        if (HALO) { ph0 = hoff0 + sa; ph1 = ph0 ^ xm; asm volatile("" : "+v"(ph0), "+v"(ph1)); SCHED(); }
        mm(0, NH + 6, NM);
        SCHED();
        WAIT_L0();
        BAR();
        SCHED();
    };
    asm volatile("" : "+s"(sa), "+s"(sb), "+s"(cnt));
    do {
    ALIGN8();
    do {
        half0();
        ldfrag(0, pa0, ph0, pb0, pb1, 0);
        mm(1, 0, NH);
        interleave();
        mm(1, NH, NM - 1);
        SCHED();
        WAIT_L0();
        asm volatile("s_nop 0");
        SCHED();
        mm(1, NM - 1, NM);
        SCHED();
    } while (cnt != 0);
    if (!HALO) {
        asm volatile("s_cmp_eq_u32 %[more], 0\n\ts_cbranch_scc1 .Lex%=\n\ts_mov_b32 m0, %[tch]\n\ts_nop 3\n\tglobal_load_lds_dword %[vt], %[tw] sc1\n\t"
                     "global_load_dwordx4 %[c0], %[v0], %[cs] offset:-2048\n\ts_cmp_eq_u32 %[three], 0\n\ts_cbranch_scc1 .Lex%=\n\t"
                     "global_load_dwordx4 %[c1], %[v0], %[cs]\n\tglobal_load_dwordx4 %[c2], %[v0], %[cs] offset:2048\n.Lex%=:"
                     : [c0] "+v"(cpy[0]), [c1] "+v"(cpy[1]), [c2] "+v"(cpy[2])
                     : [more] "s"(more), [tch] "i"(M_TCH), [vt] "v"(vt), [tw] "s"(tw), [v0] "v"(cv), [cs] "s"(cs_), [three] "s"(three) : "scc", "memory");
        cnt = more; more = 0;
        asm volatile("" : "+s"(cnt), "+s"(more));
        SCHED();
    }
    } while (cnt != 0);
    if (!HALO) {
        asm volatile("s_cmp_lg_u32 %[l], 0\n\ts_cbranch_scc1 .Lex%=\n\ts_waitcnt vmcnt(0)\n\ts_nop 3\n\tglobal_store_dwordx4 %[d], %[c0], %[ws] sc1\n\ts_cmp_eq_u32 %[three], 0\n\ts_cbranch_scc1 .Lex%=\n\t"
                     "v_add_u32 %[d], 0x2000, %[d]\n\tglobal_store_dwordx4 %[d], %[c1], %[ws] sc1\n\tv_add_u32 %[d], 0x2000, %[d]\n\tglobal_store_dwordx4 %[d], %[c2], %[ws] sc1\n.Lex%=:\n\ts_nop 1"
                     : [d] "+v"(cdst) : [c0] "v"(cpy[0]), [c1] "v"(cpy[1]), [c2] "v"(cpy[2]), [l] "s"(l), [ws] "s"(cd_), [three] "s"(three) : "scc", "memory");
    }
    QSTAMP(1);
    FENCE();
    SCHED();

    f32x4 csq[4], m4[4], mh4; float csv[2], mr[4], rr[4], mh;
    auto r1 = [&](unsigned b, int off) { return *(const LAS float*)(lds(b) + off); };
    auto r4_ = [&](unsigned b, int off) { return *(const LAS f32x4*)(lds(b) + off); };
    auto mf = [&](int set, int i) {
        const int nt = i >> 2, mt = i & 3;
        if (i < 16) acc[mt][nt] = MFMA(wb[set][nt], xa[set][mt], acc[mt][nt]);
        else if (i < 24) acc[mt][nt] = MFMA(xa[set][mt], wb[set][nt], acc[mt][nt]);
        else if (i == 24) ah[0] = MFMA(wb[set][2], xh[set], ah[0]);
        else ah[1] = MFMA(xh[set], wb[set][4], ah[1]);
        SCHED();
    };
    auto fold = [&](int i, int e) {
        const int nt = i >> 2, mt = i & 3;
        float t;
        if (i < 16) t = __builtin_fmaf(mr[mt], csq[nt][e], acc[mt][nt][e]);
        else if (i < 24) t = __builtin_fmaf(m4[mt][e], csv[nt - 4], acc[mt][nt][e]);
        else if (i == 24) t = __builtin_fmaf(mh, csq[2][e], ah[0][e]);
        else t = __builtin_fmaf(mh4[e], csv[0], ah[1][e]);
        asm volatile("" : "+v"(t));
        if (i < 24) acc[mt][nt][e] = t; else ah[i - 24][e] = t;
    };
    constexpr Tail<HALO> TL{};
    constexpr int NP = HALO ? 6 : 2, FN = 4 * NM;
    static_assert(TL.v[3][TL.n[3]] == NP + FN + (HALO ? 2 : 0) && KT % NQA == 1 && KT % NQW == 0, "tail");
    s16x4 wl[6], wh[6];
    auto frd = [&](int set, int k, int ao) {
        if (k < 4) xa[set][k] = *(const LAS bf16x8*)(lds(set ? pa1 : pa0) + ao + k * 2048);
        else if (k == 16) xh[set] = *(const LAS bf16x8*)(lds(set ? ph1 : ph0) + ao);
        else {
            const int nt = (k - 4) >> 1;
            const LAS char* pb = lds((nt & 1) ? pb1 : pb0) + (nt >> 1) * 8192 + set * 4096;
            if (k & 1) { wh[nt] = tr_read(pb + 512); wb[set][nt] = __builtin_shufflevector(wl[nt], wh[nt], 0, 1, 2, 3, 4, 5, 6, 7); }
            else wl[nt] = tr_read(pb);
        }
    };
    constexpr int RS = M_RSTD - M_MEAN;
    unsigned vl, vg, tb, xr, xh_, b0a, qb0, qb1;
    f32x4 bprev, bcur, bprev0; u32x4 ps; u32x2 ko; float ninf;
    auto rdop = [&](int h, int k) {
        const int j = k - NH;
        if (h == 0) {
            if (k < 2) { mr[2 * k] = r1(sa0, k * 128); mr[2 * k + 1] = r1(sa0, k * 128 + 64); }
            else if (k < 4) csq[k - 2] = r4_((k & 1) ? sa3 : sa2, 0);
            else if (j < 4) frd(0, k - 4, 2 * QA_SLOT);
            else csq[j - 2] = r4_((j & 1) ? sa3 : sa2, 256);
        } else if (j < 0) frd(h & 1, k, h == 1 ? 2 * QA_SLOT : 0);
        else if (h == 1) {
            if (j < 2) csv[j] = r1(j ? sa7 : sa6, 0);
            else if (HALO && j == 2) mh = r1(sa4, 0);
            else if (HALO && j == 3) mh4 = r4_(sa5, 0);
            else { const int mt = HALO ? 7 - j : j - 2; m4[mt] = r4_(sa1, mt * 64); }
        } else if (h == 2) {
            if (j == 0) m4[2] = r4_(sa1, 128);
            if (j == 1) ko = *(const LAS u32x2*)(lds(tb) + 3072);
            if (j == 2) { rr[0] = r1(sa0, RS); rr[1] = r1(sa0, 64 + RS); }
            if (j == 3) { rr[2] = r1(sa0, 128 + RS); rr[3] = r1(sa0, 192 + RS); }
            if (j >= 4) m4[5 - j] = r4_(sa1, (5 - j) * 64);
        } else {
            if (j == 0) bprev = r4_(tb, 0);
            if (j == 1) bcur = r4_(tb, 1024);
            if (j == 2) ps = *(const LAS u32x4*)(lds(tb) + 2048);
            if (j == 3) bprev0 = r4_(b0a, 0);
        }
    };
    auto vop = [&](int k) {
        if (k == 0) { asm volatile("" : "+v"(boff[0])); qb0 = boff[0] + QW_TOP; asm volatile("" : "+v"(qb0)); }
        else if (k == 1) { asm volatile("" : "+v"(boff[1])); qb1 = boff[1] + QW_TOP; asm volatile("" : "+v"(qb1)); }
        else if (k < NP) {
            if (k == 2) { vl = (unsigned)(lane * 16); asm volatile("" : "+v"(vl)); }
            if (k == 3) { tb = vl + M_TAB; asm volatile("" : "+v"(tb)); }
            if (k == 4) { b0a = tb | b0m; asm volatile("" : "+v"(b0a)); }
            if (k == 5) { xr = xrs + vl; asm volatile("" : "+v"(xr)); }
        } else if (k < NP + FN) fold(TL.f[(k - NP) >> 2], (k - NP) & 3);
        else {
            if (k == NP + FN) { xh_ = xhs + vl; asm volatile("" : "+v"(xh_)); }
            if (k == NP + FN + 1) { ninf = -INFINITY; asm volatile("" : "+v"(ninf)); }
        }
    };
    unsigned wa, wq;
    int wr2 = wr, wc2 = wc;
#pragma unroll
    for (int i = 0; i < NM; ++i) {
        mf(0, TL.t[0][i]);
        if (TL.t[0][i] < 24) asm volatile("" : "+v"(acc[TL.t[0][i] & 3][TL.t[0][i] >> 2])); else asm volatile("" : "+v"(ah[TL.t[0][i] & 1]));
        FENCE();
        if (i < NH) frd(1, i, 0);
        if (!HALO && i == NH + 3) { asm volatile("" : "+s"(wr2), "+s"(wc2)); vl = (unsigned)(lane * 16); wq = (unsigned)(AT_Q + (wc2 * 8 + wr2 * 4) * 1024) + vl; asm volatile("" : "+v"(vl), "+v"(wq)); }
        if (!HALO && i == NH + 4) { asm volatile("" : "+s"(wr2), "+s"(wc2)); wa = (unsigned)(AT_OWN + wc2 * 16384 + wr2 * 4096) + vl; asm volatile("" : "+v"(wa)); }
        if (i == NH + 1) { asm volatile("" : "+v"(boff[0]), "+v"(boff[1])); pb0 = boff[0] + 2 * QW_SLOT; pb1 = boff[1] + 2 * QW_SLOT; asm volatile("" : "+v"(pb0), "+v"(pb1)); }
        if (i == NH + 2) { pa0 = aoff0; pa1 = aoff0 ^ xm; asm volatile("" : "+v"(pa1)); if (HALO) { ph0 = hoff0; ph1 = hoff0 ^ xm; asm volatile("" : "+v"(ph1)); } }
        SCHED();
    }
    WAIT_L0();
    BAR();
    FENCE();
    SCHED();
    auto frags = [&](int h) {
        static_assert(TL.d[0][NM] == NH + 6 && TL.d[1][NM] == NH + (HALO ? 5 : 6) && TL.d[2][NM] == NH + (HALO ? 6 : 0) && TL.d[3][TL.n[3]] == NH + (HALO ? 4 : 0), "reads");
        if (h == 0) __builtin_amdgcn_s_waitcnt(0xC27F);
        if (h == 1) __builtin_amdgcn_s_waitcnt(HALO ? 0xC57F : 0xC67F);
        if (h == 2) __builtin_amdgcn_s_waitcnt(HALO ? 0xC67F : 0xC07F);
        if (h == 3) __builtin_amdgcn_s_waitcnt(HALO ? 0xC47F : 0xC07F);
    };
#pragma unroll
    for (int h = 0; h < 4; ++h) {
        if (h == 2) { pb0 = qb0; pb1 = qb1; }
#pragma unroll
        for (int i = 0; i < TL.n[h]; ++i) {
            if (!(h & 1) && i == TL.n[h] - 1) { frags(h); SCHED(); if (h) QSTAMP(4); }
            mf(~h & 1, TL.t[h][i]);
#pragma unroll
            for (int k = TL.d[h][i]; k < TL.d[h][i + 1]; ++k) { FENCE(); rdop(h, k); }
            SCHED();
#pragma unroll
            for (int k = TL.v[h][i]; k < TL.v[h][i + 1]; ++k) vop(k);
            SCHED();
        }
        if (h & 1) {
            frags(h);
            if (h == 1) QSTAMP(2); else QSTAMP(5);
            BAR();
            FENCE();
            SCHED();
            if (h == 1) QSTAMP(3); else QSTAMP(6);
        }
    }
    if (!HALO) {
        f32x4 r4[4];
        mf(1, 8);
#pragma unroll
        for (int mt = 0; mt < 4; ++mt) rr[mt] = r1(sa0, mt * 64 + RS);
        SCHED();
        mf(1, 12);
#pragma unroll
        for (int mt = 0; mt < 4; ++mt) r4[mt] = r4_(sa1, mt * 64 + RS);
        SCHED();
        mf(1, 9);
        vg = HK ? (unsigned)hv<HK>(lane) : vl;
        if (HK) asm volatile("" : "+v"(vg));
        SCHED();
        u32x4 pk[8];
        auto op = [&](int g, int k) {
            const int ta = g < 4 ? g : (g - 4) & 2, tb = g < 4 ? g : ta + 1, na = g < 4 ? 2 : 4 + (g & 1), nb = g < 4 ? 3 : na;
            f32x4& A = acc[ta][na]; f32x4& Bv = acc[tb][nb];
            if (k < 12) {
                const int h = k / 6, kk = k % 6;
                f32x4& X = h ? Bv : A;
                const int tx = h ? tb : ta;
                if (kk < 4) { float t = X[kk] * (g < 4 ? rr[tx] : r4[tx][kk]); asm volatile("" : "+v"(t)); X[kk] = t; }
                else { unsigned t = cvt_pk_bf16(X[2 * (kk - 4)], X[2 * (kk - 4) + 1]); asm volatile("" : "+v"(t)); pk[g][2 * h + kk - 4] = t; }
            } else if (k == 12) __builtin_amdgcn_raw_buffer_store_b128(pk[g], rso, vg + hp<HK>(g & 3), (g >> 2) * (8192 << HK), 16);
            else *(LAS u32x4*)(lds(wa) + (g >> 2) * 8192 + (g & 3) * 1024) = pk[g];
            SCHED();
        };
        auto pm = [&](int i) {
            if (i < 16) mf(1, TL.h3[i]);
            else if (i < 32) mf(((i - 16) >> 1) & 1, ((i - 16) >> 2) + 4 * (i & 1));
        };
        pm(3);
#pragma unroll
        for (int g = 0; g < 8; ++g) {
#pragma unroll
            for (int k = 0; k < 6; ++k) op(g, k);
            pm(2 * g + 4);
            if (g > 0) op(g - 1, 12);
#pragma unroll
            for (int k = 6; k < 12; ++k) op(g, k);
            pm(2 * g + 5);
            if (g > 0 && g <= LWA) op(g - 1, 13);
        }
        op(7, 12);
        QSTAMP(7);
        u32x4 qk;
        auto qop = [&](int mt, int k) {
            if (k == 0) { rr[mt] *= QSCALE; asm volatile("" : "+v"(rr[mt])); }
            else if (k < 13) {
                const int h = (k - 1) / 6, kk = (k - 1) % 6;
                f32x4& X = acc[mt][h];
                if (kk < 4) { float t = X[kk] * rr[mt]; asm volatile("" : "+v"(t)); X[kk] = t; }
                else { unsigned t = cvt_pk_bf16(X[2 * (kk - 4)], X[2 * (kk - 4) + 1]); asm volatile("" : "+v"(t)); qk[2 * h + kk - 4] = t; }
            } else *(LAS u32x4*)(lds(wq) + mt * 1024) = qk;
            SCHED();
        };
#pragma unroll
        for (int mt = 0; mt < 3; ++mt) {
            pm(20 + 4 * mt); if (3 * mt + LWA < 8) op(3 * mt + LWA, 13); qop(mt, 0); qop(mt, 1); qop(mt, 2);
            pm(21 + 4 * mt); if (3 * mt + 1 + LWA < 8) op(3 * mt + 1 + LWA, 13); qop(mt, 3); qop(mt, 4); qop(mt, 5);
            pm(22 + 4 * mt); if (3 * mt + 2 + LWA < 8) op(3 * mt + 2 + LWA, 13); qop(mt, 6); qop(mt, 7); qop(mt, 8);
            pm(23 + 4 * mt); qop(mt, 9); qop(mt, 10); qop(mt, 11); qop(mt, 12); qop(mt, 13);
        }
        if (c.wid == 0) STAMP_AT(l * 8 + 1);
        QSTAMP(8);
        {
            const u32x2 q0 = pack4(acc[3][0] * (rr[3] * QSCALE)), q1 = pack4(acc[3][1] * (rr[3] * QSCALE));
            *(LAS u32x4*)(lds(wq) + 3 * 1024) = (u32x4){q0[0], q0[1], q1[0], q1[1]};
        }
        QSTAMP(9);
        QSTORE();
        if (K0E) {
            SCHED();
            WAIT_L0();
            BAR();
            FENCE();
            SCHED();
            if (KSP == 4) {
                publish<4, 0>(smem, lane, c.ws, (unsigned)WS_FLAGQ + (c.pq * 16 + c.jq) * FQS, lane == 0, (unsigned)(4 * l + 2));
                if (c.wid == 0) STAMP_AT(l * 8 + 2);
                SCHED();
            }
        }
    } else {
        typedef unsigned u32x10 __attribute__((ext_vector_type(10)));
        u32x4 kp[4], qp[4];
        u32x10 vw[2];
        float ks[2][4], cs[2][4];
        auto pk8 = [&](const f32x4& x, const f32x4& y) { const u32x2 a = pack4(x), b = pack4(y); u32x4 r = {a[0], a[1], b[0], b[1]}; asm volatile("" : "+v"(r)); SCHED(); return r; };
        auto pkv = [&](int mt, int cc) { const u32x2 a = pack4(acc[mt][4 + cc]); vw[cc][2 * mt + 2] = a[0]; vw[cc][2 * mt + 3] = a[1]; };
        auto fr8 = [](const u32x4& x) { return __builtin_bit_cast(bf16x8, x); };
        mf(1, 24);
        unsigned ka0 = ko[0] + wr * 256, ka1 = ko[1] + wr * 256;
        asm volatile("" : "+v"(ka0), "+v"(ka1));
        SCHED();
        mf(1, 25);
#pragma unroll
        for (int mt = 0; mt < 4; ++mt) ks[0][mt] = r1(ka0, mt * 64);
        rr[0] *= QSCALE; asm volatile("" : "+v"(rr[0]));
        SCHED();
        mf(1, 11);
#pragma unroll
        for (int mt = 0; mt < 4; ++mt) ks[1][mt] = r1(ka1, mt * 64);
        rr[1] *= QSCALE; asm volatile("" : "+v"(rr[1]));
        SCHED();
        mf(1, 15);
        rr[2] *= QSCALE; asm volatile("" : "+v"(rr[2]));
        rr[3] *= QSCALE; asm volatile("" : "+v"(rr[3]));
        SCHED();
        mf(1, 19);
        const u32x4 hh = pk8(ah[0], ah[1]);
        mf(1, 23);
        *(LAS u32x2*)lds(xh_) = (u32x2){hh[0], hh[1]}; *(LAS u32x2*)(lds(xh_) + 1024) = (u32x2){hh[2], hh[3]};
        SCHED();
        mf(1, 10);
        kp[3] = pk8(acc[3][2], acc[3][3]);
        mf(1, 14);
        *(LAS u32x4*)(lds(xr) + 2048) = kp[3];
        pkv(3, 0); asm volatile("" : "+v"(vw[0]));
        *(LAS u32x2*)(lds(xr) + 3072) = (u32x2){vw[0][8], vw[0][9]};
        SCHED();
        mf(1, 3);
        pkv(3, 1); asm volatile("" : "+v"(vw[1]));
        *(LAS u32x2*)(lds(xr) + 3072 + 8) = (u32x2){vw[1][8], vw[1][9]};
        SCHED();
        mf(1, 7);
        mf(1, 9);
        kp[2] = pk8(acc[2][2], acc[2][3]);
        mf(1, 13);
        qp[3] = pk8(acc[3][0], acc[3][1]);
        mf(1, 2);
        f32x4 s0[4], s1[4];
        u32x4 pk;
        auto sc = [&](int mt, int h) {
            if (h) { s1[mt] = MFMA(fr8(kp[mt]), fr8(qp[mt]), bcur); asm volatile("" : "+v"(s1[mt])); }
            else { s0[mt] = MFMA(fr8(mt ? kp[mt - (mt > 0)] : pk), fr8(qp[mt]), mt == 0 ? bprev0 : bprev); asm volatile("" : "+v"(s0[mt])); }
            SCHED();
        };
        auto csm = [&](int mt) { cs[0][mt] = ks[0][mt] * rr[mt]; asm volatile("" : "+v"(cs[0][mt])); cs[1][mt] = ks[1][mt] * rr[mt]; asm volatile("" : "+v"(cs[1][mt])); };
        float u[2][4], mx[4], iv[4];
        auto sv = [](float x) { asm volatile("" : "+v"(x)); return x; };
        auto mx2 = [&](float a, float b) { return max3(ninf, a, b); };
        auto pick = [&](int mt, int h) { u[h][mt] = sv(max3(mx2(s0[mt][2 * h], s0[mt][2 * h + 1]), s1[mt][2 * h], s1[mt][2 * h + 1]) * cs[h][mt]); };
        auto vf = [&](int i) { const int mt = 2 - (i >> 2), nt = 4 + (i & 1), set = (i >> 1) & 1; SCHED(); acc[mt][nt] = MFMA(xa[set][mt], wb[set][nt], acc[mt][nt]); asm volatile("" : "+v"(acc[mt][nt])); SCHED(); };
        mf(1, 6);
        kp[1] = pk8(acc[1][2], acc[1][3]);
        mf(1, 8);
        WAIT_L0();
        QSTAMP(7);
        BAR();
        FENCE();
        SCHED();
        QSTAMP(8);
        mf(1, 12);
        {
            const u32x2 k0 = *(const LAS u32x2*)(lds(xr) + 8);
            FENCE(); SCHED();
            const u32x2 k1 = *(const LAS u32x2*)lds(xr);
            FENCE(); SCHED();
            const u32x2 a = *(const LAS u32x2*)(lds(xr) + 1024 + 8);
            FENCE(); SCHED();
            const u32x2 b = *(const LAS u32x2*)(lds(xr) + 1024);
            pk = (u32x4){k0[0], k0[1], k1[0], k1[1]};
            vw[0][0] = a[0]; vw[0][1] = a[1]; vw[1][0] = b[0]; vw[1][1] = b[1];
        }
        SCHED();
        auto sw16 = [](float& a, float& b) { auto r = __builtin_amdgcn_permlane16_swap(__float_as_uint(a), __float_as_uint(b), false, false); a = __uint_as_float(r[0]); b = __uint_as_float(r[1]); };
        auto sw32 = [](float& a, float& b) { auto r = __builtin_amdgcn_permlane32_swap(__float_as_uint(a), __float_as_uint(b), false, false); a = __uint_as_float(r[0]); b = __uint_as_float(r[1]); };
        auto ldr = [&](int i) { };
        auto top = [&](int mt) { mx[mt] = sv(mx2(u[0][mt], u[1][mt])); };
        auto pv1 = [&](int mt, int k) {
            SCHED();
            vw[k >> 1][2 * mt + 2 + (k & 1)] = cvt_pk_bf16(acc[mt][4 + (k >> 1)][2 * (k & 1)], acc[mt][4 + (k >> 1)][2 * (k & 1) + 1]);
            asm volatile("" : "+v"(vw[k >> 1]));
            SCHED();
        };
        auto uk = [&](int mt) { SCHED(); u[0][mt] = sv(u[0][mt] * ks[0][mt]); u[1][mt] = sv(u[1][mt] * ks[1][mt]); SCHED(); };
        mf(1, 1);
        qp[2] = pk8(acc[2][0], acc[2][1]);
        mf(1, 5);
        csm(3); SCHED();
        mf(1, 0);
        kp[0] = pk8(acc[0][2], acc[0][3]);
        mf(1, 4);
        csm(2); SCHED();
        sc(3, 1);
        qp[1] = pk8(acc[1][0], acc[1][1]);
        sc(3, 0);
        csm(1); ldr(0); SCHED();
        sc(2, 1);
        qp[0] = pk8(acc[0][0], acc[0][1]);
        sc(2, 0);
        csm(0); pick(3, 0); SCHED();
        sc(1, 1);
        pick(3, 1); ldr(1); SCHED();
        sc(1, 0);
        pick(2, 0); top(3); SCHED();
        sc(0, 1);
        pick(2, 1); ldr(2); SCHED();
        sc(0, 0);
        if (c.wid == 0) STAMP_AT(l * 8 + 1);
        pick(1, 0); top(2);
        vf(0);
        pick(1, 1); ldr(3);
        vf(1);
        sw16(mx[2], mx[3]);
        pick(0, 0);
        vf(2);
        pick(0, 1); top(1);
        vf(3);
        top(0); mx[2] = sv(mx2(mx[2], mx[3]));
        vf(4);
        sw16(mx[0], mx[1]); mx[0] = sv(mx2(mx[0], mx[1]));
        vf(5); pv1(2, 0);
        sw32(mx[0], mx[2]);
        { const float a = mx[0], b = mx[2]; mx[0] = sv(mx2(a, b)); mx[2] = sv(mx2(b, a)); }
        vf(6); pv1(2, 1);
        sw32(mx[0], mx[2]);
        mx[3] = sv(mx[2]); mx[1] = sv(mx[0]);
        vf(7);
        sw16(mx[2], mx[3]); sw16(mx[0], mx[1]);
        SCHED();
#pragma unroll
        for (int mm = 0; mm < 4; ++mm) { const int mt = 3 - mm; u[0][mt] = sv(u[0][mt] - mx[mt]); u[1][mt] = sv(u[1][mt] - mx[mt]); if (mm == 1) vf(8); }
        vf(9);
#pragma unroll
        for (int mm = 0; mm < 4; ++mm) {
            const int mt = 3 - mm;
            u[0][mt] = sv(__builtin_amdgcn_exp2f(u[0][mt])); u[1][mt] = sv(__builtin_amdgcn_exp2f(u[1][mt]));
            if (mm > 0) iv[mt + 1] = sv(u[0][mt + 1] + u[1][mt + 1]);
            if (mm < 2) vf(10 + mm);
        }
        SCHED();
        sw16(iv[2], iv[3]);
        iv[0] = sv(u[0][0] + u[1][0]);
        iv[2] = sv(iv[2] + iv[3]);
        pv1(2, 2);
        sw16(iv[0], iv[1]); iv[0] = sv(iv[0] + iv[1]);
        uk(3);
        sw32(iv[0], iv[2]);
        iv[0] = sv(iv[0] + iv[2]);
        pv1(2, 3);
        iv[0] = sv(__builtin_amdgcn_rcpf(iv[0]));
        pv1(1, 0);
        iv[2] = sv(iv[0]);
        uk(2);
        sw32(iv[0], iv[2]);
        iv[3] = sv(iv[2]); iv[1] = sv(iv[0]);
        pv1(1, 1);
        sw16(iv[2], iv[3]); sw16(iv[0], iv[1]);
        SCHED();
        f32x4 o[4][2];
        u32x4 pf[4];
        auto nrm = [&](int mt) { u[0][mt] = sv(u[0][mt] * iv[mt]); u[1][mt] = sv(u[1][mt] * iv[mt]); SCHED(); };
        auto prb = [&](int mt) {
            const unsigned p = cvt_pk_bf16(u[0][mt], u[1][mt]);
            pf[mt] = (u32x4){__builtin_amdgcn_perm(0u, p, ps[0]), __builtin_amdgcn_perm(0u, p, ps[1]), __builtin_amdgcn_perm(0u, p, ps[2]), __builtin_amdgcn_perm(0u, p, ps[3])};
            asm volatile("" : "+v"(pf[mt]));
            SCHED();
        };
        auto pvm = [&](int mt, int cc) {
            o[mt][cc] = MFMA(fr8((u32x4){vw[cc][2 * mt], vw[cc][2 * mt + 1], vw[cc][2 * mt + 2], vw[cc][2 * mt + 3]}), fr8(pf[mt]), ((f32x4){0.f, 0.f, 0.f, 0.f}));
            asm volatile("" : "+v"(o[mt][cc]));
            SCHED();
        };
        auto out = [&](int mt) {
            const u32x2 lo = pack4(o[mt][0]), hi = pack4(o[mt][1]);
            __builtin_amdgcn_raw_buffer_store_b128((u32x4){lo[0], lo[1], hi[0], hi[1]}, rso, vg + hp<HO>(mt), 0, 16);
            SCHED();
        };
        vg = (unsigned)hv<HO>(lane);
        asm volatile("" : "+v"(vg));
        nrm(3); nrm(2); prb(3);
        pv1(1, 2); pv1(1, 3);
        pvm(3, 0); prb(2); pvm(3, 1);
        uk(1); uk(0);
        pvm(2, 0); nrm(1); nrm(0); pvm(2, 1);
        prb(1); pv1(0, 0); pv1(0, 1);
        pvm(1, 0); pv1(0, 2); pv1(0, 3); prb(0);
        pvm(1, 1); out(3); pvm(0, 0); out(2); pvm(0, 1); out(1); out(0);
        SCHED();
        QSTAMP(9);
        if (c.wid == 0) STAMP_AT(l * 8 + 2);
        QSTORE();
    }
}

#define MFMA32(a, b, c) __builtin_amdgcn_mfma_f32_32x32x16_bf16(__builtin_bit_cast(bf16x8, a), __builtin_bit_cast(bf16x8, b), (c), 0, 0, 0)
DI float max16(const f32x16& s) {
    float m = max3(s[0], s[1], s[2]), n = max3(s[3], s[4], s[5]);
    m = max3(m, s[6], s[7]); n = max3(n, s[8], s[9]);
    m = max3(m, s[10], s[11]); n = max3(n, s[12], s[13]);
    m = max3(max3(m, s[14], s[15]), n, n);
    auto b = __builtin_amdgcn_permlane32_swap(__float_as_uint(m), __float_as_uint(m), false, false);
    return __builtin_amdgcn_fmed3f(__uint_as_float(b[0]), __uint_as_float(b[1]), INFINITY);
}
template <bool HI> DI void attn_a(const Ctx& c0, int l, const char* Wo, KP kp) {
    const Ctx c = fresh(c0);
    asm volatile("" : "+s"(Wo));
    LAS char* smem = c.smem;
    const int lane = c.lane, w = c.wid;
    constexpr int H = HI ? 0 : 1;
    int qt = HI ? w - 4 : 3 - w;
    asm volatile("" : "+s"(qt));
    const float NEG = -INFINITY;
    int zq = (lane & 31) - 4 * (lane >> 5);
    unsigned lb = (unsigned)(((lane >> 4) & 1) * 1024 + ((lane >> 5) * 16 + (lane & 15)) * 16);
    unsigned qa = (unsigned)(AT_Q + H * 8192 + qt * 2048) + lb, ta = (unsigned)(AT_OWN + H * 16384 - 3 * 2048 + qt * 2048) + lb;
    asm volatile("" : "+v"(lb), "+v"(qa), "+v"(ta));
    auto rd = [&](unsigned base, int off) { return *(const LAS u32x4*)(lds(base) + off); };
    f32x16 ce;
    u32x4 sel;
    unsigned tm = (unsigned)(AT_N + lane * 16);
    auto masks = [&]() {
        if (HI) {
#pragma unroll
            for (int r = 0; r < 16; ++r) ce[r] = 8 * (r >> 2) + (r & 3) <= zq ? 0.f : NEG;
            asm volatile("" : "+v"(ce));
        }
        const unsigned s1 = ((lane >> 4) ^ (lane >> 2)) & 1 ? 0u : 0x3f803f80u;
        sel = (u32x4){s1, s1, s1, s1};
        asm volatile("" : "+v"(sel));
    };
    u64 ep, pa_, kvv, nkv, kact;
    unsigned po, pk_, cnta = M_CNT, one = 1u;
    char* ob;
    const char *kvb, *s1_, *s2_;
    unsigned vk, dst, fo;
    auto setup = [&]() {
        if (HI) {
            const u32x2 e = *(const LAS u32x2*)(smem + M_EP);
            ep = ((u64)(unsigned)__builtin_amdgcn_readfirstlane(e[1]) << 32) | (unsigned)__builtin_amdgcn_readfirstlane(e[0]);
            po = o_post(c);
            pk_ = pick(lane < 2, (unsigned)(WS_KBOX + ((c.pq + 1 + lane) * 16 + c.jq) * 256 + lane * 8), (unsigned)(WS_FLAGQ + (c.pq * 16 + c.jq) * FQS));
            pa_ = c.pq < 14 ? 7ull : (c.pq < 15 ? 5ull : 4ull);
            fo = (unsigned)(WS_KBOX + (c.pq * 16 + c.jq) * 256 + lane * 8);
            asm volatile("" : "+v"(po), "+v"(pk_), "+v"(fo));
            asm volatile("" : "+s"(ep), "+s"(pa_));
        }
        ob = c.ws + WS_O + (size_t)l * (8u << 20) + (size_t)(unsigned)(((c.jq * 2 + H) * 16 + c.pq) * 8192 + qt * 2048);
        kvb = c.ws + WS_KV + (size_t)(unsigned)((l >> 1) * (16u << 20) + c.jq * 32768);
        asm volatile("" : "+s"(ob), "+s"(kvb));
    };
    auto setup2 = [&]() {
        const int p1 = c.pq >= 1 ? c.pq - 1 : c.pq, p2 = c.pq >= 2 ? c.pq - 2 : c.pq;
        s1_ = kvb + (size_t)(unsigned)(p1 * 524288);
        s2_ = kvb + (size_t)(unsigned)(p2 * 524288);
        vk = (unsigned)((w & 3) * 1024 + lane * 16); dst = (unsigned)(AT_N + (w & 3) * 1024);
        asm volatile("" : "+s"(s1_), "+s"(dst));
        if (!HI) asm volatile("" : "+s"(s2_));
        asm volatile("" : "+v"(vk));
    };
    auto arrive = [&](auto cc, unsigned msk, unsigned val) {
        unsigned old = 0;
        if (lane == 0) asm volatile("ds_add_rtn_u32 %0, %1, %2 offset:%3\n\ts_waitcnt lgkmcnt(0)" : "+v"(old) : "v"(cnta), "v"(one), "i"(decltype(cc)::value * 4) : "memory");
        if ((__builtin_amdgcn_readfirstlane(old) & msk) == msk) { if (lane < 16) post(c.ws, po, ep + (4 * l + val)); }
    };
    static_assert((MBOX & 2) && !HO && !HK, "layout");
    int tv = 0;
    auto kvs = [&]() {
        if (!HI) {
            SCHED();
            LSTAMP(2);
            asm volatile("s_mov_b64 exec, 1\n\ts_waitcnt vmcnt(0)\n\tds_add_u32 %0, %1\n\ts_mov_b64 exec, -1" :: "v"(cnta), "v"(one) : "memory");
            LSTAMP(3);
            SCHED();
        }
    };
    asm volatile("" : "+v"(cnta), "+v"(one), "+v"(tm));
    if (HI) {
        masks(); setup(); setup2();
        if (w == 5) {
#pragma unroll
            for (int i = 0; i < 4; ++i) *(LAS f32x4*)(lds(tm) + i * 1024) = (f32x4){ce[4 * i], ce[4 * i + 1], ce[4 * i + 2], ce[4 * i + 3]};
        }
        kvv = ep + (4 * l + 2); nkv = 0 - kvv;
        kact = __builtin_amdgcn_ballot_w64(w == KPW && lane < 2 && lane < c.pq);
        asm volatile("" : "+v"(kvv), "+v"(nkv));
        asm volatile("" : "+s"(kact));
        WAIT_L0();
        WAIT_V0();
        LSTAMP(0);
        if (w == 4) __builtin_amdgcn_s_setprio(HP4);
        BAR();
        FENCE();
    }
    auto kvp = [&]() {
        if (HI && w == 4) {
            __builtin_amdgcn_s_setprio(3);
#pragma unroll 1
            for (unsigned spins = 0; spins < (1u << 24); ++spins) {
                if (__builtin_amdgcn_readfirstlane(*(volatile LAS int*)lds(cnta)) == 8 * l + 4) break;
            }
            if (__builtin_amdgcn_inverse_ballot_w64(pa_)) post(c.ws, pk_, kvv);
            STAMP_AT(l * 8 + 2);
            __builtin_amdgcn_s_setprio(HP4);
        }
        SCHED();
    };
    if (KPP == -1) kvp();
    FENCE();
    SCHED();
    LSTAMP(1);
    u32x4 q0, q1, k0, k1, va, vp, pa, pb;
    f32x16 cm, s0, s1, o;
    f32x4 ls;
    auto stage = [&](f32x16& sc, f32x16& sn, const f32x16& cn, bool pend, bool zero, unsigned kb, int ko, unsigned vb, int vo1, unsigned nb, int vo0) {
        auto ex = [&](int i) { float x = __builtin_amdgcn_exp2f(sc[i]); asm volatile("" : "+v"(x)); sc[i] = x; SCHED(); };
        auto cv = [&](u32x4& p, int j, int i) { unsigned x = cvt_pk_bf16(sc[i], sc[i + 1]); asm volatile("" : "+v"(x)); p[j] = x; SCHED(); };
        sn = MFMA32(k0, q0, cn); SCHED();
        k0 = rd(kb, ko); SCHED();
        ex(0); ex(1); ex(2);
        if (pend) { o = MFMA32(vp, pb, o); SCHED(); }
        ex(3); ex(8); ex(9);
        sn = MFMA32(k1, q1, sn); SCHED();
        k1 = rd(kb, ko + 512); SCHED();
        ex(10); ex(11); cv(pa, 0, 0); cv(pa, 1, 2);
        if (pend) { ls = MFMA(__builtin_bit_cast(bf16x8, sel), __builtin_bit_cast(bf16x8, pb), ls); SCHED(); }
        vp = rd(vb, 8192 + vo1 + 512); SCHED();
        cv(pa, 2, 8); cv(pa, 3, 10); ex(4); ex(5);
        o = MFMA32(va, pa, zero ? (f32x16)(0.f) : o); SCHED();
        va = rd(nb, 8192 + vo0); SCHED();
        ex(6); ex(7); ex(12);
        ls = MFMA(__builtin_bit_cast(bf16x8, sel), __builtin_bit_cast(bf16x8, pa), zero ? (f32x4)(0.f) : ls); SCHED();
        ex(13); ex(14); ex(15); cv(pb, 0, 4); cv(pb, 1, 6); cv(pb, 2, 12); cv(pb, 3, 14);
    };
    auto flush = [&]() {
        ls = MFMA(__builtin_bit_cast(bf16x8, sel), __builtin_bit_cast(bf16x8, pb), ls); SCHED();
        o = MFMA32(vp, pb, o); SCHED();
    };
    float nm;
    {
        if (!HI) {
#pragma unroll
            for (int i = 0; i < 4; ++i) {
                const f32x4 t = *(const LAS f32x4*)(lds(tm) + i * 1024);
                ce[4 * i] = t[0]; ce[4 * i + 1] = t[1]; ce[4 * i + 2] = t[2]; ce[4 * i + 3] = t[3];
            }
        }
        q0 = rd(qa, 0);
        const u32x4 d0 = rd(ta, 6144);
        q1 = rd(qa, 512);
        const u32x4 d1 = rd(ta, 6144 + 512);
        k0 = rd(ta, 4096); k1 = rd(ta, 4096 + 512); va = rd(ta, 8192 + 6144);
        SCHED();
        if (KSP == -1) kvs();
        if (!HI) { masks(); setup(); SCHED(); }
        s0 = MFMA32(d0, q0, ce);
        s0 = MFMA32(d1, q1, s0);
        SCHED();
        if (!HI) { setup2(); SCHED(); }
        if (KSP == 0) kvs();
        if (KPP == 0) kvp();
        nm = -max16(s0);
        asm volatile("" : "+v"(nm));
#pragma unroll
        for (int r = 0; r < 16; ++r) { float t; asm("v_mov_b32 %0, %1" : "=v"(t) : "v"(nm), "i"(r)); cm[r] = t; }
        asm volatile("" : "+v"(cm));
        SCHED();
#pragma unroll
        for (int r = 0; r < 16; ++r) s0[r] = addf(s0[r], nm);
        asm volatile("" : "+v"(s0));
        SCHED();
        if (KSP == 1) kvs();
        if (KPP == 1) kvp();
        stage(s0, s1, cm, false, true, ta, 2048, ta, 6144, ta, 4096);
        if (KSP == 2) kvs();
        if (KPP == 2) kvp();
        if (qt > 0) {
            stage(s1, s0, cm, true, false, ta, 0, ta, 4096, ta, 2048);
            if (KSP == 3) kvs();
            if (qt > 1) {
                stage(s0, s1, cm, true, false, ta, 0, ta, 2048, ta, 0);
                if (qt > 2) stage(s1, s0, cm, true, false, ta, 0, ta, 0, ta, 0);
            }
        } else if (KSP == 3) kvs();
        flush();
    }
    LSTAMP(4);
    f32x16 c2;
    unsigned na, nb;
    auto rso = __builtin_amdgcn_make_buffer_rsrc(ob, 0, 2048, 0x00020000);
    auto starts = [&]() {
        const float n1 = c.pq >= 1 ? nm : NEG, n2 = c.pq >= 2 ? nm : NEG;
        asm volatile("" : "+v"(zq));
#pragma unroll
        for (int r = 0; r < 16; ++r) { cm[r] = n1; c2[r] = n2; ce[r] = 8 * (r >> 2) + (r & 3) <= zq ? NEG : n2; }
        na = (unsigned)(AT_N + H * 32768) + lb; nb = na + (unsigned)(16384 + qt * 2048);
        asm volatile("" : "+v"(cm), "+v"(c2), "+v"(ce), "+v"(na), "+v"(nb));
        asm volatile("" : "+s"(rso));
        SCHED();
    };
    {
        auto piece = [&](int b, int i) { dma16<true>(dst + b * 16384 + i * 4096, vk, ((b & 1) ? s2_ : s1_) + (b >> 1) * 16384 + i * 4096); };
        auto block = [&](int b) {
#pragma unroll
            for (int i = 0; i < 4; ++i) piece(b, i);
        };
        Ask t;
        if (HI) {
            poll_rmw<true>(t, c.ws, fo, __builtin_amdgcn_inverse_ballot_w64(kact), nkv, 62 - 4 * l);
            if (w == KPW) STAMP_AT(32 + l * 8 + 1);
        }
        WAIT_L0();
        LSTAMP(5);
        BAR();
        FENCE();
        LSTAMP(6);
        piece(0, H); piece(0, 2 + H);
        if (HI) {
            starts();
            if (APRIO) __builtin_amdgcn_s_setprio(APRIO);
            WAIT_V0();
            keep(t);
            LSTAMP(7);
            BAR();
            FENCE();
            if (w == 4) STAMP_AT(l * 8 + 3);
        } else {
            if (AB2 >= 0) block(1);
            if (AB2 > 0) block(2);
            starts();
            Ctx c2 = c;
            if (ELO) c2.wid = 0;
            unsigned vw = wo_w_off(c2);
            int qs = c.cw == 3 ? 1024 : 8 * D_ * 2;
            asm volatile("" : "+v"(vw));
            asm volatile("" : "+s"(qs));
            SCHED();
            wait_v<4 * AB2 + 4>();
            LSTAMP(7);
            BAR();
            if (AB2 < 0) block(1);
            if (AB2 <= 0) block(2);
            block(3);
            wait_v<8>();
            BAR();
            if (!ELO) {
                wo_w_issue<WA0, WA1>(c, Wo, vw);
                wait_v<WA1 - WA0>();
            } else WAIT_V0();
            LSTAMP(9);
            BAR();
            FENCE();
            if (!ELO) wo_w_issue<WA1, 26>(c, Wo, vw);
            else {
                const int s0 = w == 0 ? WA0 : (w == 1 ? WA0 + EN0 : WA0 + EN0 + EN1), s1 = w == 0 ? WA0 + EN0 : (w == 1 ? WA0 + EN0 + EN1 : (w == 2 ? 22 : 0));
#pragma unroll 1
                for (int sl = s0; sl < s1; ++sl) {
                    const char* b = Wo + (size_t)(unsigned)((2 * (sl & 15) + (sl >> 4)) * 65536);
#pragma unroll
                    for (int q = 0; q < 4; ++q) dma16<true>(sl * 4096 + q * 1024, vw, b + q * qs);
                }
            }
            if (LSL) { for (int i = 0; i < qt; ++i) __builtin_amdgcn_s_sleep(LSL); }
        }
    }
    SCHED();
    LSTAMP(8);
    {
        const u32x4 d0 = rd(na, 0), d1 = rd(na, 512);
        k0 = rd(na, 2048); k1 = rd(na, 2048 + 512); va = rd(na, 8192);
        SCHED();
        s0 = MFMA32(d0, q0, cm);
        s0 = MFMA32(d1, q1, s0);
        SCHED();
        stage(s0, s1, cm, false, false, na, 4096, na, 0, na, 2048);
        stage(s1, s0, cm, true, false, na, 6144, na, 2048, na, 4096);
        if (HI) { BAR(); FENCE(); SCHED(); }
        stage(s0, s1, cm, true, false, nb, 0, na, 4096, na, 6144);
        if (HI && CPOS == 3) { BAR(); SCHED(); }
        stage(s1, s0, ce, true, false, nb, 2048, na, 6144, nb, 0);
        if (HI && CPOS == 4) { WAIT_L0(); BAR(); SCHED(); }
        stage(s0, s1, c2, true, false, nb, 4096, nb, 0, nb, 2048);
        if (HI && CPOS == 5) { BAR(); SCHED(); }
        if (qt < 3) {
            stage(s1, s0, c2, true, false, nb, 6144, nb, 2048, nb, 4096);
            if (qt < 2) {
                stage(s0, s1, c2, true, false, nb, 0, nb, 4096, nb, 6144);
                if (qt < 1) stage(s1, s0, c2, true, false, nb, 0, nb, 6144, nb, 0);
            }
        }
        flush();
    }
    LSTAMP(11);
    auto out = [&]() {
        const float iv = __builtin_amdgcn_rcpf(ls[0]);
#pragma unroll
        for (int u = 0; u < 2; ++u) {
            u32x4 t;
#pragma unroll
            for (int i = 0; i < 4; ++i) { const int r = 4 * u + (i >> 1) * 8 + (i & 1) * 2; t[i] = cvt_pk_bf16(o[r] * iv, o[r + 1] * iv); }
            __builtin_amdgcn_raw_buffer_store_b128(t, rso, lb + u * 512, 0, 16);
        }
        SCHED();
    };
    out();
    if (__builtin_expect(__any(!(ls[0] < SAFE_SUM)), 0)) {
        auto rk = __builtin_amdgcn_make_buffer_rsrc((void*)(kvb + H * 16384), 0, 16u << 20, 0x00020000);
        const int g0 = c.pq * 4 + qt - 8;
        float m = NEG;
#pragma unroll 1
        for (int ps = 0; ps < 2; ++ps) {
            o = (f32x16)(0.f); ls = (f32x4)(0.f);
#pragma unroll 1
            for (int j = g0 < 0 ? -g0 : 0; j < 9; ++j) {
                const int so = ((g0 + j) >> 2) * 524288 + ((g0 + j) & 3) * 2048;
                u32x4 f[4];
#pragma unroll
                for (int i = 0; i < 4; ++i) f[i] = __builtin_amdgcn_raw_buffer_load_b128(rk, lb + (i & 1) * 512, so + (i >> 1) * 8192, 16);
                f32x16 s;
                const float sb = ps ? -m : 0.f;
                float sa = j == 0 ? NEG : sb, sz = j == 8 ? NEG : sb;
                asm volatile("" : "+v"(zq), "+v"(sa), "+v"(sz));
#pragma unroll
                for (int r = 0; r < 16; ++r) s[r] = 8 * (r >> 2) + (r & 3) <= zq ? sa : sz;
                s = MFMA32(f[0], q0, s);
                s = MFMA32(f[1], q1, s);
                if (ps == 0) m = fmaxf(m, max16(s));
                else {
                    u32x4 p[2];
#pragma unroll
                    for (int r = 0; r < 16; r += 2) p[(r >> 2) & 1][(r >> 3) * 2 + ((r >> 1) & 1)] = cvt_pk_bf16(__builtin_amdgcn_exp2f(s[r]), __builtin_amdgcn_exp2f(s[r + 1]));
#pragma unroll
                    for (int u = 0; u < 2; ++u) {
                        o = MFMA32(f[2 + u], p[u], o);
                        ls = MFMA(__builtin_bit_cast(bf16x8, sel), __builtin_bit_cast(bf16x8, p[u]), ls);
                    }
                }
            }
        }
        __builtin_amdgcn_s_waitcnt(0x0F70);
        WAIT_V0();
        out();
    }
    SCHED();
    if (w == 3 || w == 4) STAMP_AT(32 + l * 8 + 3 + w);
    LSTAMP(12);
    if (HI) {
        WAIT_V0();
        arrive(std::integral_constant<int, 1>{}, 3u, 3u);
        if (w == 4) STAMP_AT(32 + l * 8 + 4);
        LSTAMP(13);
        if (APRIO) __builtin_amdgcn_s_setprio(0);
        if (lane == 0) asm volatile("ds_add_u32 %0, %1 offset:12" :: "v"(cnta), "v"(one) : "memory");
        WAIT_V0();
        arrive(std::integral_constant<int, 2>{}, 7u, 4u);
        if (w == 5 || w == 6) {
            Ctx c2 = c;
            c2.wid = 2 * w - 10;
            unsigned vw = wo_w_off(c2), vw2 = vw + (c.cw == 3 ? 1024u : 8u * D_ * 2);
            asm volatile("" : "+v"(vw), "+v"(vw2));
#pragma unroll 1
            for (unsigned spins = 0; spins < (1u << 22); ++spins) {
                if (__hip_atomic_load((LAS unsigned*)(smem + M_CNT + 12), __ATOMIC_RELAXED, __HIP_MEMORY_SCOPE_WORKGROUP) == (unsigned)(2 * l + 4)) break;
                __builtin_amdgcn_s_sleep(1);
            }
            FENCE();
            wo_w_issue<ELO ? 22 : 26, 30>(c2, Wo, vw);
            c2.wid += 1;
            wo_w_issue<ELO ? 22 : 26, 30>(c2, Wo, vw2);
        }
    }
    LSTAMP(23);
    ASTORE();
}

DI void wo_load(const Ctx& c0, int l, const char* Wn, const char* Wo) {
    const Ctx c = fresh(c0);
    asm volatile("" : "+s"(Wn), "+s"(Wo));
    const u64 ep = lds_epoch(c.smem);
    const unsigned fo = o_find(c);
    if ((CP2 & 1) && l == 0 && c.wid == 4) code_again(c);
    if ((CP2 & 2) && l == 1 && c.wid == 4) code_again(c);
        if (l == 0 && c.wid == 6) {
            const int fr = c.lane & 15, od = fr & 1, j0 = 4 * (c.lane >> 4) + od;
            const bool p0 = j0 > fr, p1 = j0 + 2 > fr;
            const unsigned N = 0xff800000u, Z = 0x0c0c0c0cu, lo = od ? 0x01000c0cu : 0x0c0c0100u, hi = od ? 0x03020c0cu : 0x0c0c0302u;
            LAS u32x4* t = (LAS u32x4*)(c.smem + M_TAB + c.lane * 16);
            t[0] = (u32x4){p0 && !od ? 0u : N, p0 && od ? 0u : N, p1 && !od ? 0u : N, p1 && od ? 0u : N};
            t[64] = (u32x4){!p0 && !od ? 0u : N, !p0 && od ? 0u : N, !p1 && !od ? 0u : N, !p1 && od ? 0u : N};
            t[128] = (u32x4){p0 ? lo : Z, p1 ? hi : Z, p0 ? Z : lo, p1 ? Z : hi};
            t[192] = (u32x4){(unsigned)(M_RSTD + j0 * 4 + (p0 ? 0 : 64)), (unsigned)(M_RSTD + j0 * 4 + 8 + (p1 ? 0 : 64)), 0u, 0u};
        }
    if (CPY && l == 0 && c.wid == CKW) {
#pragma unroll 1
        for (int i = 0; i < 2; ++i) {
            const int j = i ? c.jw : c.jq, g = i ? 2 - 2 * (j >> 3) : 1 + 2 * (j >> 3);
            if ((j & 7) == 3) poll_flags(c.ws, ep, (unsigned)WS_FLAGQ + ((c.lane >> 2) * 16 + 4 * g + (c.lane & 3)) * FQS, true, 2u);
        }
    }
    Ask t, t2;
    if (!(l & 1)) {
        Ctx c2 = c;
        c2.wid = 2 * c.wid - 10;
        unsigned vw = wo_w_off(c2), vw2 = vw + (c.cw == 3 ? 1024u : 8u * D_ * 2);
        asm volatile("" : "+v"(vw), "+v"(vw2));
        poll_rmw(t, c.ws, fo, c.wid == 7 && c.lane < 16, ep + (4 * l + 3), 61 - 4 * l);
        if (c.wid == 7) STAMP_AT(32 + l * 8 + 2);
        BAR();
        if (c.wid == 5 || c.wid == 6) {
            __builtin_amdgcn_s_sleep(W30S);
            wo_w_issue<30, 32>(c2, Wo, vw);
            c2.wid += 1;
            wo_w_issue<30, 32>(c2, Wo, vw2);
        }
        if (c.wid != 7) WAIT_V0();
    } else {
        asm volatile("" : "=v"(t.v[0]), "=v"(t.v[1]), "=v"(t.v[2]), "=v"(t.v[3]));
        WAIT_V0();
    }
    poll_rmw(t2, c.ws, fo, c.wid == 7 && c.lane < 16, ep + (4 * l + 4), 60 - 4 * l);
    if (c.wid == 7) STAMP_AT(32 + l * 8 + 2 + !(l & 1));
    BAR();
    WAIT_V0();
    keep(t); keep(t2);
    if (l == 3 && c.bid == 0 && c.wid == 7) {
#pragma unroll 1
        for (int i = 0; i < 4; ++i) poll_flags(c.ws, ep, (unsigned)WS_FLAGQ + (i * 64 + c.lane) * FQS, true, 1u);
        if (c.lane == 0) __hip_atomic_store((GAS u64*)(c.ws + WS_EPOCH), ep + 64, __ATOMIC_RELAXED, __HIP_MEMORY_SCOPE_AGENT);
    }
    if (RS0 && l == 3) BAR();
}

DI void wo_consume(const bool B, const Ctx& c0, const bf16_t* xin, bf16_t* xout, int l) {
    u32x4 res[2][2];
    const Ctx c = fresh(c0);
    LAS char* smem = c.smem;
    const int lane = c.lane, w = c.wid, fr = lane & 15, fq = lane >> 4;
    const char* obase = c.ws + WS_O + (size_t)l * (8u << 20);
    int ovo = (c.pw * 8 + w * 2) * 1024 + lane * 16;
    asm volatile("" : "+v"(ovo));
    constexpr int NP = WNQ, R = 3;
    static_assert(!HO && !HX && !HS && !RLD && BPR == 1, "layout");
    auto ro = __builtin_amdgcn_make_buffer_rsrc((void*)obase, 0, 4u << 20, 0x00020000);
    asm volatile("" : "+s"(ro));
    u32x4 xa[NP][2];
    s16x4 wf[R][8];
    f32x4 acc[2][4];
    unsigned bw[8];
    int coff, rmo, spr;
    u32x4 on = {0x3f803f80u, 0x3f803f80u, 0x3f803f80u, 0x3f803f80u};
    unsigned sto, xpo, coff1, cnta = M_CNT, one = 1;
    u64 xv, b0, b1, am;
    auto rs = __builtin_amdgcn_make_buffer_rsrc(xout, 0, S_ * D_ * 2, 0x00020000);
    auto rst = __builtin_amdgcn_make_buffer_rsrc(c.ws + WS_STATS + (size_t)((l + 1) & 3) * 524288, 0, 524288, 0x00020000);
    auto prep2 = [&]() {
        sto = fq == (fr >> 2) && l < 3 ? (unsigned)((c.pw * 16 + c.jw) * 1024 + (w * 32 + fr) * 8) : 0x80000000u;
        xpo = x_post(c);
        coff = (int)pick(l == 3, (unsigned)rmo, (unsigned)coff);
        coff1 = coff + (l == 3 ? xrow<0>(0, 16) : 2048);
        spr = l == 3 ? 64 : 1024;
        asm volatile("" : "+s"(spr));
        b0 = __builtin_amdgcn_ballot_w64(fr & 1); b1 = __builtin_amdgcn_ballot_w64(fr & 2); am = __builtin_amdgcn_ballot_w64(x_act(c, !B));
        asm volatile("" : "+v"(on), "+v"(sto), "+v"(xpo), "+v"(coff), "+v"(coff1), "+v"(xv), "+v"(cnta), "+v"(one));
        asm volatile("" : "+s"(rs), "+s"(rst), "+s"(b0), "+s"(b1), "+s"(am));
    };
    auto prep = [&]() {
        int ln = lane;
        asm volatile("" : "+v"(ln));
        const int fr = ln & 15, fq = ln >> 4, q4 = fr >> 2, p4 = ln & 3;
        coff = (c.pw * 16 + c.jw) * 16384 + w * 4096 + (((fq & 1) * 2 + (fq >> 1)) * 16 + fr) * 16;
        rmo = xrow<0>(c.pw, w * 32 + fr) + xcol<0>(c.jw) + (fq & 1) * 32 + (fq >> 1) * 16;
        asm volatile("" : "+v"(coff), "+v"(rmo));
        const int fb = (q4 & 2) | ((fq & 1) << 2);
        const unsigned swp = (B && w >= 2) ? 2048u : 0u;
#pragma unroll
        for (int cc = 0; cc < 4; ++cc) {
            const unsigned b = (unsigned)((4 * fq + q4) * 128 + (((2 * cc + (p4 >> 1)) ^ fb) << 4) + (p4 & 1) * 8);
            bw[2 * cc] = b + swp; bw[2 * cc + 1] = b + (swp ^ 2048u);
            asm volatile("" : "+v"(bw[2 * cc]), "+v"(bw[2 * cc + 1]));
        }
    };
    auto start = [&](int mt, int pr) {
            {
                auto r0 = __builtin_amdgcn_permlane16_swap(res[mt][pr][0], res[mt][pr][2], false, false);
                auto r1 = __builtin_amdgcn_permlane16_swap(res[mt][pr][1], res[mt][pr][3], false, false);
#pragma unroll
                for (int h = 0; h < 2; ++h) {
                    acc[mt][2 * pr + h] = (f32x4){bf_lo(r0[h]), bf_hi(r0[h]), bf_lo(r1[h]), bf_hi(r1[h])};
                    asm volatile("" : "+v"(acc[mt][2 * pr + h]));
                }
            }
    };
    auto rdw = [&](auto ic) {
        constexpr int i = decltype(ic)::value, h = i >> 3, k = i & 7;
        wf[h % R][k] = tr_read(lds(bw[k]) + (h & 15) * 4096);
        if constexpr (h == 15) { bw[k] += 65536; asm volatile("" : "+v"(bw[k])); }
    };
    {
        auto resid = [&]() {
            const unsigned roff = pick(l != 0, (unsigned)coff, (unsigned)rmo);
            const char* xp = (const char*)xin;
#pragma unroll
            for (int i = 0; i < 4; ++i) aload4c(res[i >> 1][i & 1], roff, xp + (i >> 1) * (l ? 2048 : xrow<0>(0, 16)) + (i & 1) * (l ? 1024 : 64));
            SCHED();
        };
        unsigned opo = o_post(c), oca = B ? M_CNT : M_CNT + 8, old = 0, msk = B ? 3u : 7u;
        xv = lds_epoch(smem) + (unsigned)(l + 2);
        u64 ov = xv + (unsigned)(3 * l + 2);
        prep();
        asm volatile("" : "+v"(opo), "+v"(oca), "+v"(ov), "+v"(old), "+v"(one));
        asm volatile("" : "+s"(msk));
        SCHED();
        if (RBP) resid();
        static_for<0, 16>([&](auto ic) { rdw(ic); SCHED(); });
        if (!P2A) prep2();
        SCHED();
        wait_v<RBP ? 4 : 0>();
        if (lane == 0) asm volatile("ds_add_rtn_u32 %0, %1, %2\n\ts_waitcnt lgkmcnt(0)" : "+v"(old) : "v"(oca), "v"(one) : "memory");
        if ((__builtin_amdgcn_readfirstlane(old) & msk) == msk) { if (lane < 16) post(c.ws, opo, ov); }
        if (w == 0) STAMP_AT(l * 8 + 4);
        SCHED();
        if (!RBP) resid();
        if (P2A == 1) prep2();
        SCHED();
        if (w == 3) STAMP_AT(32 + l * 8 + 5);
        BAR();
    }
    FENCE();
    SCHED();
    if (w == 0) STAMP_AT(l * 8 + 5);
    int so = 0;
    auto next = [&](int j) { so += j == 15 ? 131072 - 15 * 262144 : 262144; asm volatile("" : "+s"(so)); };
    auto ldo = [&](int j, int mt) { xa[j % NP][mt] = __builtin_amdgcn_raw_buffer_load_b128(ro, ovo + mt * 1024, so, 16); };
#pragma unroll
    for (int h = 0; h < NP; ++h) {
        ldo(h, 0);
        SCHED();
        if (h > SH0 && h <= SH0 + 2) { start(h - SH0 - 1, 0); SCHED(); }
        ldo(h, 1);
        if (!WAL || h + 1 < NP) next(h);
        SCHED();
        if (P2A >= 2 && h == P2A - 2) { prep2(); SCHED(); }
        if (h == SH0) {
            wait_v<2 * SH0 + 2>();
            asm volatile("" : "+v"(res[0][0]), "+v"(res[0][1]), "+v"(res[1][0]), "+v"(res[1][1]));
        }
        if (h > SH0 && h <= SH0 + 2) start(h - SH0 - 1, 1);
        SCHED();
    }
    int d0 = 262144, d1 = 131072 - 15 * 262144;
    asm volatile("" : "+s"(d0), "+s"(d1));
    static_assert(WEP || !WAL, "last step");
    static_for<0, WEP ? 31 : 32>([&](auto jc) {
        constexpr int j = decltype(jc)::value;
        constexpr int c0 = j < 30 ? 16 + 8 * j : 256, c1 = j < 30 ? c0 + 8 : 256;
        if constexpr (j + NP == 16) {
            static_assert(c0 <= 8 * 20, "weights that landed before the attention");
            if (!B) BAR();
            FENCE();
            asm volatile("" : "+v"(ovo));
            SCHED();
        }
        if constexpr (j == 0 || j + NP == 16) ALIGN8();
        constexpr int nv = WAL == 1 || j == 0 || j + NP > 32 ? 2 * (NP - 1 < 31 - j ? NP - 1 : 31 - j) : 2 * NP - 3;
        __builtin_amdgcn_s_waitcnt(0x0870 | nv);
        const int sp = so;
        static_assert(!RS0 || (RS0 + NP >= 32 && RS0 < 31 && QW_SLOT <= (RS0 - 1) * 4096), "R0");
        if constexpr (RS0 && j == RS0) BAR();
        else asm volatile("s_add_i32 %0, %1, %2" : "=&s"(so) : "s"(sp), "s"(j + NP == 16 ? d1 : d0) : "scc");
        SCHED();
        static_for<0, 8>([&](auto mc) {
            constexpr int m = decltype(mc)::value, mt = m >> 2, nt = mt ? 3 - (m & 3) : m & 3;
            const bf16x8 wv = __builtin_shufflevector(wf[j % R][2 * nt], wf[j % R][2 * nt + 1], 0, 1, 2, 3, 4, 5, 6, 7);
            if constexpr (j < 31) asm volatile("v_mfma_f32_16x16x32_bf16 %0, %1, %2, %0" : "+v"(acc[mt][nt]) : "v"(wv), "v"(xa[j % NP][mt]));
            else acc[mt][nt] = MFMA(wv, __builtin_bit_cast(bf16x8, xa[j % NP][mt]), acc[mt][nt]);
            SCHED();
            if constexpr (c0 < c1 && m < 7) { rdw(std::integral_constant<int, c0 + m>{}); SCHED(); }
            if constexpr (c0 < c1 && m == 6) rdw(std::integral_constant<int, c0 + 7>{});
            if constexpr (m == WL1 && j > 0 && j - 1 + NP < 32) xa[(j + NP - 1) % NP][1] = __builtin_amdgcn_raw_buffer_load_b128(ro, ovo + 1024, sp, 16);
            if constexpr (m == WL1 + 4 && j + NP < 32) ldo(j, 0);
            SCHED();
        });
    });
    const bf16x8 ones = __builtin_bit_cast(bf16x8, on);
    u32x4 o[2][2];
    f32x4 sm[2], g[2];
    const f32x4 zero = {0.f, 0.f, 0.f, 0.f};
    auto sel = [&](float a, float b, u64 m) { return __builtin_amdgcn_inverse_ballot_w64(m) ? b : a; };
    auto stats = [&](int mt) {
        float qa = sel(g[mt][0], g[mt][1], b0), qb = sel(g[mt][2], g[mt][3], b0);
        asm volatile("" : "+v"(qa), "+v"(qb));
        SCHED();
        if (WEP) asm volatile("" :: "v"(sm[1][1]), "v"(sm[1][2]), "v"(sm[1][3]), "v"(o[1][0]), "v"(o[1][1]), "v"(on));
        __builtin_amdgcn_raw_buffer_store_b64((u32x2){__float_as_uint(sm[mt][0]), __float_as_uint(sel(qa, qb, b1))}, rst, sto + mt * 128, 0, 16);
        SCHED();
    };
    u32x2 pk[2];
    auto last = [&](int mt, int nt) {
        acc[mt][nt] = MFMA(__builtin_shufflevector(wf[31 % R][2 * nt], wf[31 % R][2 * nt + 1], 0, 1, 2, 3, 4, 5, 6, 7), __builtin_bit_cast(bf16x8, xa[31 % NP][mt]), acc[mt][nt]);
        SCHED();
    };
    auto cv = [&](int mt, int pr) {
        pk[0] = pack4(acc[mt][2 * pr]); pk[1] = pack4(acc[mt][2 * pr + 1]);
        asm volatile("" : "+v"(pk[0]), "+v"(pk[1]));
        SCHED();
    };
    auto st = [&](int mt, int pr) {
        o[mt][pr] = widen(pk[0], pk[1]);
        __builtin_amdgcn_raw_buffer_store_b128(o[mt][pr], rs, mt ? coff1 : coff, pr ? spr : 0, 16);
        SCHED();
    };
    auto gq = [&](int mt, int pr, bool f) { const bf16x8 of = __builtin_bit_cast(bf16x8, o[mt][pr]); g[mt] = MFMA(of, of, f ? zero : g[mt]); SCHED(); };
    auto gs = [&](int mt, int pr, bool f) { sm[mt] = MFMA(ones, __builtin_bit_cast(bf16x8, o[mt][pr]), f ? zero : sm[mt]); SCHED(); };
    WAIT_L0();
    last(0, 0); last(0, 1); last(0, 2); last(0, 3);
    last(1, 3); cv(0, 0);
    last(1, 2); st(0, 0);
    last(1, 1); cv(0, 1);
    last(1, 0); st(0, 1);
    if (w == 0) STAMP_AT(l * 8 + 6);
    gq(0, 0, true); cv(1, 1);
    gs(0, 0, true); st(1, 1);
    gq(0, 1, false); cv(1, 0);
    gs(0, 1, false); st(1, 0);
    gq(1, 1, true);
    gs(1, 1, true);
    gq(1, 0, false);
    gs(1, 0, false);
    stats(0);
    stats(1);
    if (l < 3) {
        unsigned old = 0;
        if (!RS0 || RN1 > RN0) BAR();
        wait_v<XE2>();
        if (lane == 0) asm volatile("ds_add_rtn_u32 %0, %1, %2\n\ts_waitcnt lgkmcnt(0)" : "+v"(old) : "v"(cnta), "v"(one) : "memory");
        if ((__builtin_amdgcn_readfirstlane(old) & 3) == 3) {
            if (__builtin_amdgcn_inverse_ballot_w64(am)) post(c.ws, xpo, xv);
        }
    }
    if (w == 0) STAMP_AT(l * 8 + 7);
}

__global__ __launch_bounds__(512, 2) __attribute__((aligned(4096))) void k_main(Params P) {
    extern __shared__ __attribute__((aligned(16))) char smem_[];
    KP kp = (KP)__builtin_amdgcn_kernarg_segment_ptr();
    unsigned long long tstart = rstamp();
    const char *x0, *w0, *wo;
    Ctx c;
    c.smem = (LAS char*)smem_;
    c.lane = threadIdx.x & 63;
    c.wid = __builtin_amdgcn_readfirstlane(threadIdx.x >> 6);
    c.bid = blockIdx.x;
    const bool loader = c.wid >= 4;
    unsigned va, vw;
    auto enter = [&](const bool LD) {
        if (LD) asm volatile("s_load_dwordx2 %0, %4, 0x0\n\ts_load_dwordx2 %1, %4, 0x8\n\ts_load_dwordx2 %2, %4, 0x10\n\ts_load_dwordx2 %3, %4, 0x20 ; loader" : "=&s"(x0), "=&s"(w0), "=&s"(c.ws), "=&s"(wo) : "s"(kp));
        else asm volatile("s_load_dwordx2 %0, %4, 0x0\n\ts_load_dwordx2 %1, %4, 0x8\n\ts_load_dwordx2 %2, %4, 0x10\n\ts_load_dwordx2 %3, %4, 0x20" : "=&s"(x0), "=&s"(w0), "=&s"(c.ws), "=&s"(wo) : "s"(kp));
        if (LD) asm volatile("; loader" : "+s"(c.bid), "+s"(c.wid)); else asm volatile("; consumer" : "+s"(c.bid), "+s"(c.wid));
        if (LD) asm volatile("; loader" : "+v"(c.lane)); else asm volatile("; consumer" : "+v"(c.lane));
        static_assert(QXM == 8 && WXM == 4 && ALT == 1, "map");
        c.pq = (((c.bid & 1) * 8) ^ PQX) + ((c.bid >> 3) & 7); c.jq = (c.bid & 6) * 2 + (c.bid >> 6);
        const int w = c.wid, lr = c.lane >> 3, lc = c.lane & 7;
        va = (unsigned)((c.pq * 128 + 8 * (w & 3) + lr) * (D_ * 2) + ((lc ^ lr) << 4));
        vw = (unsigned)((8 * (w & 3) + lr) * 6144 + c.jq * 128 + ((lc ^ ((lr & 2) | ((w & 1) << 2))) << 4));
        asm volatile("" : "+v"(va), "+v"(vw));
        c.pw = ((c.pq & 8) ^ 8) + ((c.bid >> 1) & 1) * 4 + ((c.bid >> 3) & 3); c.jw = (c.bid & 4) * 2 + (c.bid >> 5); c.cw = 0;
        if (LD) {
            asm volatile("" : "+s"(c.pw), "+s"(c.jw));
            asm volatile("s_waitcnt lgkmcnt(0)" : "+s"(x0), "+s"(w0), "+s"(c.ws), "+s"(wo));
            if (ESL) __builtin_amdgcn_s_sleep(ESL);
        } else {
            int t0 = first_tile(first_order(c.pq, c.jq), 0);
            asm volatile("" : "+s"(t0));
            asm volatile("s_waitcnt lgkmcnt(0) ; c" : "+s"(x0), "+s"(w0), "+s"(c.ws), "+s"(wo));
            if (ST0 && c.wid == 3) STAMP_AT(41);
            const char* xb = x0 + (unsigned)(t0 * 128);
            const char* wb = w0 + (unsigned)(t0 * (64 * 6144));
#pragma unroll
            for (int h = 0; h < 2; ++h) {
#pragma unroll
                for (int j = 0; j < 2; ++j) dma16<false>(QA_OFF + (j * 8 + h * 4 + w) * 1024, va, xb + (j * 64 + h * 32) * (D_ * 2));
#pragma unroll
                for (int j = 0; j < 3; ++j) dma16<false>((j * 8 + h * 4 + w) * 1024, vw, wb + h * (32 * 6144) + j * 2048);
            }
            if (ST0 && c.wid == 1) STAMP_AT(43);
            if (w == 0 && c.lane < 2) touch(M_EP, (unsigned)(WS_EPOCH + c.lane * 4), c.ws);
            if (w == 1 && c.lane < 5) *(LAS unsigned*)(c.smem + (c.lane < 4 ? M_CNT + c.lane * 4 : M_GO)) = 0u;
            if (c.wid == 0 && c.lane == 0) *(LAS unsigned long long*)lds(MISC + 3328 + 63 * 8) = tstart;
        }
        asm volatile("" : "+s"(kp), "+s"(c.bid), "+s"(c.ws));
    };
    if (c.wid == CPW) {
        const char* pc; int len;
        asm volatile("s_getpc_b64 %0\n.Lcpf_a:\n\ts_mov_b32 %1, .Lcpf_end-.Lcpf_a-4" : "=s"(pc), "=s"(len));
        const int o = min((int)(((c.bid >> 3) & 7) * 64 + c.lane) * 128, len);
        asm volatile("s_mov_b32 m0, %0\n\ts_nop 3\n\tglobal_load_lds_dword %1, %2" ::"s"(M_TCH), "v"(o), "s"(pc) : "memory");
    }
    const char* xi;
    auto step = [&](int l) {
        asm volatile("" : "+s"(kp), "+s"(c.ws), "+s"(c.jw), "+s"(c.bid));
        c.pq ^= 8; c.pw ^= 8; c.cw = CPY ? c.jw & 7 : 0;
        xi = c.ws + WS_X + (unsigned)(l * (8u << 20));
        wo = (const char*)((l & 1) ? kp->wo0 : kp->wo1);
        if (c.cw == 3) wo = c.ws + WS_CO + (((l & 1) ? 2 : 0) + (c.jw >> 3)) * 4096;
    };
    if (loader) {
        enter(true);
        xi = x0;
            qkv_load<false, true>(c, xi, w0, wo, 0, kp, va, vw);
#pragma unroll 1
        for (int l = 0;;) {
            asm volatile("" : "+s"(l));
            if (!(l & 1)) attn_a<true>(c, l, wo, kp);
            wo_load(c, l, nullptr, wo);
            if (l == 3) break;
            step(l);
            ++l;
            if (l == 2) { qkv_load<false, false>(c, xi, (const char*)kp->wqkv0, wo, l); }
            else { qkv_load<true, false>(c, xi, (const char*)kp->wqkv1, wo, l); }
        }
    } else {
        enter(false);
        xi = x0;
        bf16_t* xo = (bf16_t*)(c.ws + WS_X);
#pragma unroll 1
        for (int l = 0; l < 4; ++l) {
            if (l & 1) { qkv_consume<true>(c, l, kp); }
            else {
                qkv_consume<false>(c, l, kp, va, vw);
                attn_a<false>(c, l, wo, kp);
            }
            wo_consume(l & 1, c, (const bf16_t*)xi, xo, l);
            step(l);
            xo = l == 2 ? kp->out : (bf16_t*)(c.ws + WS_X + (unsigned)((l + 1) * (8u << 20)));
            asm volatile("" : "=v"(va), "=v"(vw));
        }
    }
    asm volatile(".Lcpf_end:");
    if (c.wid == 0) {
        WAIT_V0();
        const unsigned long long te = rstamp();
        if (c.lane == 0) *(LAS unsigned long long*)lds(MISC + 3328 + 62 * 8) = te;
        asm volatile("s_waitcnt lgkmcnt(0)" ::: "memory");
        const unsigned long long t0 = *(volatile LAS unsigned long long*)lds(MISC + 3328 + 63 * 8);
        unsigned long long ta = *(volatile LAS unsigned long long*)lds(MISC + 3328 + (SA < 64 ? SA : 63) * 8), tb = *(volatile LAS unsigned long long*)lds(MISC + 3328 + SB * 8);
        if (SA == 65) {
            if (c.bid == SX) {
                u64 m = 0;
                for (int i = 0; i < 4; ++i) { const u64 v = __hip_atomic_load((GAS u64*)(c.ws + WS_STAMP + 4096 + (i * 64 + c.lane) * 8), __ATOMIC_RELAXED, __HIP_MEMORY_SCOPE_AGENT); m = v > m ? v : m; }
                for (int o = 1; o < 64; o <<= 1) { const u64 v = __shfl_xor(m, o); m = v > m ? v : m; }
                ta = m;
            }
        } else if (SA == 66) {
            if (c.lane == 0) __hip_atomic_store((GAS u64*)(c.ws + WS_STAMP + c.bid * 8), te, __ATOMIC_RELAXED, __HIP_MEMORY_SCOPE_AGENT);
            return;
        } else if (SA >= 64) {
            if (c.lane == 0) __hip_atomic_store((GAS u64*)(c.ws + WS_STAMP + c.bid * 8), t0, __ATOMIC_RELAXED, __HIP_MEMORY_SCOPE_AGENT);
            if (c.bid == SX) {
                while (rstamp() - te < 500) { }
                u64 m = ~0ull;
                for (int i = 0; i < 4; ++i) { const u64 v = __hip_atomic_load((GAS u64*)(c.ws + WS_STAMP + (i * 64 + c.lane) * 8), __ATOMIC_RELAXED, __HIP_MEMORY_SCOPE_AGENT); m = v < m ? v : m; }
                for (int o = 1; o < 64; o <<= 1) { const u64 v = __shfl_xor(m, o); m = v < m ? v : m; }
                ta = m;
                if (SA == 67) {
                    u64 x = 0;
                    for (int i = 0; i < 4; ++i) { const u64 v = __hip_atomic_load((GAS u64*)(c.ws + WS_STAMP + (i * 64 + c.lane) * 8), __ATOMIC_RELAXED, __HIP_MEMORY_SCOPE_AGENT); x = v > x ? v : x; }
                    for (int o = 1; o < 64; o <<= 1) { const u64 v = __shfl_xor(x, o); x = v > x ? v : x; }
                    tb = x;
                }
            }
        }
        if (c.bid == SX) {
            const unsigned long long d = (tb - ta) * SM + 1000;
            while (rstamp() - te < d && d < 100000) { }
        }
    }
}

__global__ void k_pre(char* ws) {
    if (threadIdx.x == 0) __hip_atomic_store((GAS u64*)(ws + WS_STAMP + 4096 + blockIdx.x * 8), rstamp(), __ATOMIC_RELAXED, __HIP_MEMORY_SCOPE_AGENT);
}
__global__ void k_post(char* ws) {
    const u64 t0 = rstamp();
    if (blockIdx.x == SX) {
        u64 m = 0;
        for (int i = 0; i < 4; ++i) { const u64 v = __hip_atomic_load((GAS u64*)(ws + WS_STAMP + (i * 64 + threadIdx.x) * 8), __ATOMIC_RELAXED, __HIP_MEMORY_SCOPE_AGENT); m = v > m ? v : m; }
        for (int o = 1; o < 64; o <<= 1) { const u64 v = __shfl_xor(m, o); m = v > m ? v : m; }
        const u64 d = (t0 - m) * SM + 1000;
        while (rstamp() - t0 < d && d < 100000) { }
    }
}
extern "C" void kernel_launch(void* const* d_in, const int* in_sizes, int n_in, void* d_out, int out_size, void* d_ws, size_t ws_size,
                              hipStream_t stream) {
    Params P;
    P.x0 = (const bf16_t*)d_in[0];
    P.wqkv0 = (const bf16_t*)d_in[1];
    P.wo0 = (const bf16_t*)d_in[2];
    P.wqkv1 = (const bf16_t*)d_in[4];
    P.wo1 = (const bf16_t*)d_in[5];
    P.out = (bf16_t*)d_out;
    P.ws = (char*)d_ws;
    (void)hipFuncSetAttribute((const void*)k_main, hipFuncAttributeMaxDynamicSharedMemorySize, LDS_TOTAL);
    if (SA == 65) k_pre<<<256, 64, 0, stream>>>(P.ws);
    k_main<<<256, 512, LDS_TOTAL, stream>>>(P);
    if (SA == 66) k_post<<<256, 64, 0, stream>>>(P.ws);
}
```

```cpp
#include <hip/hip_runtime.h>
#include <stdint.h>
#include <type_traits>

typedef unsigned short bf16_t;
typedef short bf16x8 __attribute__((ext_vector_type(8)));
typedef short s16x4 __attribute__((ext_vector_type(4)));
typedef float f32x4 __attribute__((ext_vector_type(4)));
typedef float f32x2 __attribute__((ext_vector_type(2)));
typedef float f32x16 __attribute__((ext_vector_type(16)));
typedef unsigned u32x4 __attribute__((ext_vector_type(4)));
typedef unsigned u32x2 __attribute__((ext_vector_type(2)));
#define DI __device__ __forceinline__
#define LAS __attribute__((address_space(3)))
#define GAS __attribute__((address_space(1)))
#define STAMP 0

constexpr int S_ = 2048, D_ = 1024, KT = 16;
constexpr float LN_EPS = 1e-5f;
constexpr float QSCALE = 0.17677669529663687f * 1.4426950408889634f;

#define FQO 0
#define XBO 524288
#define HX 0
#define HO 0
#define HK 0
#define HS 0
#define HM 1
constexpr size_t WS_FLAGQ = FQO;
constexpr size_t WS_FLAGW = 98304;
constexpr size_t WS_EPOCH = 65536;
constexpr size_t WS_CSUM = 131072;
constexpr size_t WS_XBOX = XBO;
constexpr size_t WS_OBOX = 655360;
constexpr size_t WS_KBOX = 720896;
constexpr size_t WS_STATS = 1u << 20;
constexpr size_t WS_X = 4u << 20;
constexpr size_t WS_O = 96u << 20;
constexpr size_t WS_KV = 28u << 20;
constexpr size_t WS_CQ = 36u << 20;
constexpr size_t WS_CO = 42u << 20;
constexpr size_t WS_STAMP = 64u << 20;

template <int H> DI constexpr int hp(int c) { return !H ? c * 1024 : (c >> 2) * 8192 + (HM == 1 ? (c & 3) * 256 : (c & 1) * 128 + (c & 2) * 256); }
template <int H> DI constexpr int hl(int u) { return !H ? u : (HM == 1 ? u + (u & ~127) : u + (u & ~255)); }
template <int H> DI constexpr int hv(int lane) { return !H ? lane * 16 : (lane >> 3) * 1024 + (lane & 7) * 16; }
template <int H> DI constexpr int xrow(int p, int r) { return H == 0 ? (p * 128 + r) * 2048 : (H == 1 ? (p * 128 + r) * 4096 : (H == 2 ? p * 524288 + r * 256 : p * 262144 + r * 128)); }
template <int H> DI constexpr int xcol(int t) { return H == 0 ? t * 128 : (H == 1 ? hl<1>(t * 128) : (H == 2 ? t * 32768 : t * 16384)); }
#define WTC 2
#define WT0 1
#define SPR 1
#define TOPJ 0x2000
#define WV0 1
#define FQS 256
#define SNK 1
#define WBE 1
#define WAE 1
#define KSP 0
#define K0E 1
#define TOPS 4
#define BPR 1
#define RLD 0
#define CPF 2
#define WB14 0
#define WB15 5
#define WT13 2
#define ENT 1
#define MBOX 3
#define OBS 256
#define PQX 8
#define ALT 1
#define ALN 1
#define NQA_ 3
#define NQW_ 4
#define LPRIO 3
#define TOUCH 1
#define TSTEP 13
#define XS_ 9
#define ROT0 0
#define WPRE 3
#define PSLEEP 1
#define TW0 5
#define APRIO 1
#define QXM 8
#define WNQ 5
#define WRG 2
#define WBS 1
#define WEP 1
#define WAL 2
#define WL1 0
#define RS0 27
#define RN0 6
#define RN1 6
#define WXM 4
#define CPOS 5
#define LSL 0
#define SAFE_SUM 0x1p80f
#define AB2 0
#define W30S 8
#define CPY 1
#define HT0 0
#define XST 2
#define CKW 4
#define XSP 1
#define EPOLL 1
#define RSLP 1
#define APOST 1
#define ASLP 2
#define APIPE 4
#define RBP 0
#define P2A 1
#define SH0 2
#define KPW 4
#define LWA 7
#define WA0 12
#define WA1 22
#define KPP 1
#define HP4 1
#define TWQ 1
#define ERL 1
#define ESL 0
#define DM 0
#define DS 0
#define ELO 0
#define EN0 3
#define EN1 2
#define ST0 0
#define NC 1
#define NCM 1
#define NCB 4
#define AD1 3
#define AD2 0
#define AD3 4
#define TWO 1
#define XSB 10
#define TL0 0
#define TL1 12
#define FDM 1
#define TSP 0
#define CPW 4
#define EXS 1
#define NCH 2
#define XE2 0
constexpr int QA_SLOT = 18432, NQA = NQA_, QW_SLOT = 24576, NQW = NQW_, QA_OFF = 3 * QW_SLOT, QW_TOP = 131072;
DI constexpr int qw_slot(int s) { return s == 3 ? QW_TOP : s * QW_SLOT; }
constexpr int AT_N = 90112, AT_OWN = AT_N + 32768, AT_Q = 73728;
constexpr int MISC = 155648, M_MEAN = MISC, M_RSTD = MISC + 640, M_CS = MISC + 1280, M_CNT = MISC + 2816, M_EP = MISC + 2832, M_GO = MISC + 2848, M_TCH = MISC + 3072, M_TAB = MISC + 4096, M_XCH = 131072;
constexpr int LDS_TOTAL = 163840;
static_assert(QA_OFF + NQA * QA_SLOT <= QW_TOP && QW_TOP + QW_SLOT <= MISC && AT_N + 65536 <= MISC && AT_Q + 16384 <= AT_N, "lds");

DI unsigned cvt_pk_bf16(float lo, float hi) {
    typedef __bf16 bf2 __attribute__((ext_vector_type(2)));
    bf2 r; r[0] = (__bf16)lo; r[1] = (__bf16)hi;
    return __builtin_bit_cast(unsigned, r);
}
DI u32x2 pack4(const f32x4& v) { return (u32x2){cvt_pk_bf16(v[0], v[1]), cvt_pk_bf16(v[2], v[3])}; }
DI float bf_lo(unsigned u) { return __uint_as_float(u << 16); }
DI float bf_hi(unsigned u) { return __uint_as_float(u & 0xffff0000u); }
DI u32x4 widen(const u32x2& a, const u32x2& b) {
    auto r0 = __builtin_amdgcn_permlane16_swap(a[0], b[0], false, false);
    auto r1 = __builtin_amdgcn_permlane16_swap(a[1], b[1], false, false);
    return (u32x4){r0[0], r1[0], r0[1], r1[1]};
}
#define MFMA(a, b, c) __builtin_amdgcn_mfma_f32_16x16x32_bf16((a), (b), (c), 0, 0, 0)
#define WAIT_L0() __builtin_amdgcn_s_waitcnt(0xC07F)
#define WAIT_V0() asm volatile("s_waitcnt vmcnt(0)" ::: "memory")
#define BAR() __builtin_amdgcn_s_barrier()
#define SCHED() __builtin_amdgcn_sched_barrier(0)
template <int N> DI void wait_v() { asm volatile("s_waitcnt vmcnt(%0)" ::"i"(N) : "memory"); }
template <int I, int N, class F> DI void static_for(F&& f) { if constexpr (I < N) { f(std::integral_constant<int, I>{}); static_for<I + 1, N>(f); } }

template <bool SC1> DI void dma16(unsigned lds, unsigned voff, const char* sbase) {
    if (SC1) asm volatile("s_mov_b32 m0, %0\n\ts_nop 3\n\tglobal_load_lds_dwordx4 %1, %2 sc1" ::"s"(lds), "v"(voff), "s"(sbase) : "memory");
    else asm volatile("s_mov_b32 m0, %0\n\ts_nop 3\n\tglobal_load_lds_dwordx4 %1, %2" ::"s"(lds), "v"(voff), "s"(sbase) : "memory");
}
DI void touch(unsigned lds, unsigned voff, const char* sbase) {
    asm volatile("s_mov_b32 m0, %0\n\ts_nop 3\n\tglobal_load_lds_dword %1, %2 sc1" ::"s"(lds), "v"(voff), "s"(sbase) : "memory");
}
DI s16x4 tr_read(const LAS char* p) {
    typedef short v4i16_t __attribute__((ext_vector_type(4)));
    return __builtin_bit_cast(s16x4, __builtin_amdgcn_ds_read_tr16_b64_v4i16((LAS v4i16_t*)p));
}
DI LAS char* lds(unsigned off) { return (LAS char*)(uintptr_t)off; }
DI unsigned long long rstamp() { unsigned long long t; asm volatile("s_memrealtime %0\n\ts_waitcnt lgkmcnt(0)" : "=s"(t)::"memory"); return t; }

template <int NAI, int NWI, bool FIRST> struct Sched {
    int w[KT];
    constexpr Sched() : w{} {
        int endA[KT] = {}, endW[KT] = {};
        int n = 0;
        if (FIRST) {
            if (ENT == 0) { n += 2; endA[0] = n; n += 3; endW[0] = n; }
            n += NAI; endA[1] = n; n += NWI; endW[1] = n;
            if (ERL >= 5) { n += NAI; endA[2] = n; }
            if (ERL == 2 || ERL == 3 || ERL == 5) { n += NWI; endW[2] = n; }
        } else {
            for (int s = 0; s < WPRE; ++s) { n += NWI; endW[s] = n; }
            for (int s = 0; s < 2; ++s) { n += NAI; endA[s] = n; }
        }
        for (int kt = 0; kt < KT; ++kt) {
            const int need = endA[kt] > endW[kt] ? endA[kt] : endW[kt];
            w[kt] = (n - need) > 63 ? 63 : (n - need);
            if (kt + 2 < KT && !(FIRST && ERL >= 5 && kt == 0)) { n += NAI; endA[kt + 2] = n; }
            if (FIRST && (ERL == 4 || ERL == 6) && kt == 0) { n += NWI; endW[2] = n; }
            if (FIRST && ERL < 2 && kt < 5) {
                if (kt < 4) { n += NWI; endW[kt + 2] = n; }
                if (kt >= 3) n += NWI / 2;
                if (kt == 4) { endW[6] = n; n += NWI; endW[7] = n; }
                continue;
            }
            if (!FIRST && WPRE == 2 && kt < 2) { n += NWI; endW[kt + 2] = n; }
            if ((FIRST || WPRE == 3) && kt == 0) { n += NWI; endW[3] = n; }
            if (kt >= 1 && kt + 3 < KT) { n += NWI; endW[kt + 3] = n; }
            if (kt == 13) n += WT13;
            if (kt == 14) n += (NAI == 5 || WA0 > 12 ? 12 : WA0) - WT13 + (NAI == 5 ? WB14 : 0);
        }
    }
};

struct Params {
    const bf16_t* x0;
    const bf16_t* wqkv0;
    char* ws;
    const bf16_t* wqkv1;
    const bf16_t* wo0;
    const bf16_t* wo1;
    bf16_t* out;
};
typedef const __attribute__((address_space(4))) Params* KP;
DI int rot_first(int pq, int jq) {
    const int pr = 2 * (pq & 7) + (pq >> 3);
    if (ROT0 == 6) return 0;
    if (ROT0 == 7) return (pq & 1) * 8;
    if (ROT0 == 8) return (pq & 3) * 4;
    if (ROT0 == 9) return pq;
    if (ROT0 == 4) return (pq + (jq ^ 3)) & 15;
    if (ROT0 == 5) return (pr + (jq ^ 3)) & 15;
    return (ROT0 == 0 ? pq + jq : (ROT0 == 1 ? pr + jq : (ROT0 == 2 ? pr + 4 * (jq & 3) + (jq >> 2) : pq + 4 * (jq & 3) + (jq >> 2)))) & 15;
}

struct Order { int base, r, re; };
template <int N> DI Order make_order(int base, int rot) { Order o; o.base = base; o.r = (rot - base) & 15; o.re = o.r > N ? o.r : N; return o; }
DI Order first_order(int pq, int jq) { return make_order<NC>((pq & NCM) * (16 / (NCM + 1)) + NCB, rot_first(pq, jq)); }
template <int N = NC> DI int first_tile(const Order& o, int s) {
    const int v = o.re + s - N;
    if (s < N) { int a = N + o.base, b = s + o.base; if (N - s == 1) asm volatile("" : "+s"(a)); return (o.r == s ? a : b) & 15; }
    return ((s == N ? o.r : (v > 15 ? v - (16 - N) : v)) + o.base) & 15;
}

typedef unsigned long long u64;
DI u64 lds_epoch(LAS char* smem) { return *(const LAS u64*)(smem + M_EP); }
DI bool reached(u64 v, u64 ep, unsigned target) { return (v - ep - target) < (u64)(64u - target); }
DI void poll_flags(const char* ws, u64 ep, unsigned off, bool active, unsigned target) {
    const GAS u64* f = (const GAS u64*)(ws + off);
#pragma unroll 1
    for (unsigned spins = 0; spins < (1u << 22); ++spins) {
        const u64 v = __hip_atomic_load(f, __ATOMIC_RELAXED, __HIP_MEMORY_SCOPE_AGENT);
        if (__all(!active || reached(v, ep, target))) break;
        __builtin_amdgcn_s_sleep(PSLEEP);
    }
    asm volatile("" ::: "memory");
}
struct Ask { u64 v[4]; };
DI void keep(Ask& t) { asm volatile("" : "+v"(t.v[0]), "+v"(t.v[1]), "+v"(t.v[2]), "+v"(t.v[3])); }
#define ASK(r) "global_atomic_or_x2 " r ", %[off], %[z], %[ws] sc0 sc1\n\t"
#define CHK(r) "s_waitcnt vmcnt(%[n])\n\tv_lshl_add_u64 %[d], " r ", 0, %[nb]\n\tv_cmp_gt_u64 vcc, %[span], %[d]\n\ts_cmp_eq_u64 vcc, exec\n\ts_cbranch_scc1 .Lpe%=\n\ts_sleep %[sl]\n\t"
template <bool NEG = false> DI void poll_rmw(Ask& t, const char* ws, unsigned off, bool act, u64 base, unsigned span) {
    u64 z, d, nb = NEG ? base : 0 - base, sp = span;
    unsigned cnt;
    asm volatile("" : "=v"(t.v[0]), "=v"(t.v[1]), "=v"(t.v[2]), "=v"(t.v[3]));
    if (act) {
        if (APIPE == 4)
            asm volatile("v_mov_b64 %[z], 0\n\ts_mov_b32 %[cnt], 0x80000\n\ts_nop 3\n\t" ASK("%[a]") "s_sleep %[sl]\n\t" ASK("%[b]") "s_sleep %[sl]\n\t" ASK("%[c]") "s_sleep %[sl]\n"
                         ".Lpl%=:\n\t" ASK("%[e]") CHK("%[a]") ASK("%[a]") CHK("%[b]") ASK("%[b]") CHK("%[c]") ASK("%[c]") CHK("%[e]")
                         "s_sub_u32 %[cnt], %[cnt], 1\n\ts_cbranch_scc0 .Lpl%=\n.Lpe%=:"
                         : [a] "+v"(t.v[0]), [b] "+v"(t.v[1]), [c] "+v"(t.v[2]), [e] "+v"(t.v[3]), [d] "=&v"(d), [z] "=&v"(z), [cnt] "=&s"(cnt)
                         : [off] "v"(off), [ws] "s"(ws), [nb] "v"(nb), [span] "s"(sp), [sl] "i"(ASLP), [n] "i"(3) : "vcc", "scc", "memory");
        else if (APIPE == 2)
            asm volatile("v_mov_b64 %[z], 0\n\ts_mov_b32 %[cnt], 0x100000\n\ts_nop 3\n\t" ASK("%[a]") "s_sleep %[sl]\n"
                         ".Lpl%=:\n\t" ASK("%[b]") CHK("%[a]") ASK("%[a]") CHK("%[b]")
                         "s_sub_u32 %[cnt], %[cnt], 1\n\ts_cbranch_scc0 .Lpl%=\n.Lpe%=:"
                         : [a] "+v"(t.v[0]), [b] "+v"(t.v[1]), [d] "=&v"(d), [z] "=&v"(z), [cnt] "=&s"(cnt)
                         : [off] "v"(off), [ws] "s"(ws), [nb] "v"(nb), [span] "s"(sp), [sl] "i"(ASLP), [n] "i"(1) : "vcc", "scc", "memory");
        else
            asm volatile("v_mov_b64 %[z], 0\n\ts_mov_b32 %[cnt], 0x200000\n\ts_nop 3\n"
                         ".Lpl%=:\n\t" ASK("%[a]") CHK("%[a]")
                         "s_sub_u32 %[cnt], %[cnt], 1\n\ts_cbranch_scc0 .Lpl%=\n.Lpe%=:"
                         : [a] "+v"(t.v[0]), [d] "=&v"(d), [z] "=&v"(z), [cnt] "=&s"(cnt)
                         : [off] "v"(off), [ws] "s"(ws), [nb] "v"(nb), [span] "s"(sp), [sl] "i"(ASLP), [n] "i"(0) : "vcc", "scc", "memory");
    }
}
DI void post(char* ws, unsigned off, u64 v) {
    if (APOST) asm volatile("s_nop 4\n\tglobal_atomic_swap_x2 %0, %1, %2 sc1" :: "v"(off), "v"(v), "s"(ws) : "memory");
    else __hip_atomic_store((GAS u64*)(ws + off), v, __ATOMIC_RELAXED, __HIP_MEMORY_SCOPE_AGENT);
}
struct Ctx {
    LAS char* smem;
    char* ws;
    int lane, wid;
    int pq, jq;
    int pw, jw;
    int bid;
    int cw;
};
DI Ctx fresh(const Ctx& c0) {
    Ctx c = c0;
    asm volatile("" : "+v"(c.lane));
    asm volatile("" : "+s"(c.wid), "+s"(c.pq), "+s"(c.jq), "+s"(c.pw), "+s"(c.jw), "+s"(c.ws), "+s"(c.cw));
    return c;
}
DI unsigned o_post(const Ctx& c) { return (MBOX & 2) ? (unsigned)WS_OBOX + (c.pq * 16 + (c.lane & 15)) * OBS + c.jq * 8 : (unsigned)WS_FLAGQ + (c.pq * 16 + c.jq) * FQS; }
DI unsigned o_find(const Ctx& c) { return (MBOX & 2) ? (unsigned)WS_OBOX + (c.pw * 16 + c.jw) * OBS + (c.lane & 15) * 8 : (unsigned)WS_FLAGQ + (c.pw * 16 + (c.lane & 15)) * FQS; }
DI unsigned x_post(const Ctx& c) { return (MBOX & 1) ? (unsigned)WS_XBOX + (c.pw * 16 + (c.lane & 31)) * (2 * FQS) + (c.lane & 16) * (FQS / 16) + c.jw * 8 : (unsigned)WS_FLAGW + (c.pw * 16 + c.jw) * 128; }
DI bool x_act(const Ctx& c, bool halo) { return c.lane < ((MBOX & 1) ? (halo && c.pw < 15 ? 32 : 16) : 1); }
#define STAMP_AT(slot) do { } while (0)

#define LSTAMP(i) do { } while (0)
#define LSTORE(ph) do { } while (0)
#define LMARK() do { } while (0)
#define LPUT(i) do { } while (0)
#define ASTORE() do { } while (0)
#define QSTAMP(i) do { } while (0)
#define QSTORE() do { } while (0)
#define FENCE() asm volatile("" ::: "memory")
#define ALIGN8() do { if (ALN) { asm volatile(".p2align 3"); SCHED(); } } while (0)
DI float addf(float a, float b) { float r; asm("v_add_f32 %0, %1, %2" : "=v"(r) : "v"(a), "v"(b)); return r; }
DI float fold16(float a, float b) {
    auto r = __builtin_amdgcn_permlane16_swap(__float_as_uint(a), __float_as_uint(b), false, false);
    float x = __uint_as_float(r[0]) + __uint_as_float(r[1]);
    asm volatile("" : "+v"(x));
    return x;
}
DI float fold32(float a, float b) {
    auto r = __builtin_amdgcn_permlane32_swap(__float_as_uint(a), __float_as_uint(b), false, false);
    float x = __uint_as_float(r[0]) + __uint_as_float(r[1]);
    asm volatile("" : "+v"(x));
    return x;
}
DI void aload4c(u32x4& d, unsigned voff, const char* sb) { asm volatile("s_nop 4\n\tglobal_load_dwordx4 %0, %1, %2 sc1" : "=v"(d) : "v"(voff), "s"(sb) : "memory"); }
DI void aload2c(u32x2& d, unsigned voff, const char* sb) { asm volatile("s_nop 4\n\tglobal_load_dwordx2 %0, %1, %2 sc1" : "=v"(d) : "v"(voff), "s"(sb) : "memory"); }
DI unsigned wo_src(int q, int lane, int jw) { return (unsigned)((8 * q + (lane >> 3)) * (D_ * 2) + jw * 128 + (((lane & 7) ^ ((lane >> 3) & 6)) << 4)); }
DI unsigned pick(bool f, unsigned a, unsigned b) { asm volatile("" : "+v"(a), "+v"(b)); return f ? a : b; }
DI unsigned wo_w_off(const Ctx& c) { return pick(c.cw == 3, (unsigned)((c.wid & 3) * 1024 + c.lane * 16), wo_src(c.wid & 3, c.lane, c.jw)); }
DI unsigned wq_src(int ib, int lane, int jq) {
    const int seg = ib >> 3, ii = ib & 7, lr = lane >> 3;
    return (unsigned)((8 * ii + lr) * 6144 + seg * 2048 + jq * 128 + (((lane & 7) ^ ((lr & 2) | ((ii & 1) << 2))) << 4));
}
template <int S0, int S1> DI void wo_w_issue(const Ctx& c, const char* W, unsigned vw) {
    const int lw = c.wid & 3;
#pragma unroll
    for (int s = S0; s < S1; ++s) dma16<CPY != 0>(s * 4096 + lw * 1024, vw, W + (size_t)((2 * (s & 15) + (s >> 4)) * 32) * (D_ * 2));
}

template <bool HALO, bool FIRST> DI void qkv_load(const Ctx& c0, const char* xin, const char* W, const char* Wo, int l, KP kp = nullptr, unsigned va0 = 0, unsigned vw0 = 0) {
    const Ctx c = fresh(c0);
    asm volatile("" : "+s"(xin), "+s"(W), "+s"(Wo));
    LAS char* smem = c.smem;
    constexpr int NAI = HALO ? 5 : 4, NWI = 6, XS = FIRST ? XS_ : XSB, NR = HALO ? 144 : 128;
    constexpr Sched<NAI, NWI, FIRST> SC{};
    static_assert(NQA == 3 && NQW == 4, "schedule");
    const int lw = c.wid - 4, lane = c.lane, lr = lane >> 3, lc = lane & 7, fr = lane & 15, fq = lane >> 4;
    const int row0 = c.pq * 128;
    const bool cq_ = CPY && !FIRST && (c.jq & 7) == 3;
    if (cq_) W = c.ws + WS_CQ + ((HALO ? 0 : 2) + (c.jq >> 3)) * 24576;
    asm volatile("" : "+s"(W));
    constexpr int SP = FIRST ? NC : (HALO ? NCH : 0);
    static_assert(FIRST || SP + DS <= TL0 + 2 + TSP, "one flag for both kinds of sums");
    const int rot = FIRST ? rot_first(c.pq, c.jq) : (l == 1 ? c.pq : SP);
    Order od = FIRST ? first_order(c.pq, c.jq) : make_order<SP>(0, rot);
    if (FIRST) asm volatile("" : "+s"(od.base));
    if (FIRST || HALO) asm volatile("" : "+s"(od.r), "+s"(od.re));
    auto tile = [&](int s) { return FIRST || HALO ? first_tile<SP>(od, s) : s; };
    unsigned va[5], vw[NWI];
#pragma unroll
    for (int j = 0; j < 4; ++j) va[j] = (unsigned)(FIRST ? va0 + j * 32 * (D_ * 2) : c.pq * 262144 + (j * 4 + lw) * 1024 + lane * 16);
    if (HALO) va[4] = (unsigned)((c.pq > 0 ? (c.pq - 1) * 262144 + 14336 : 0) + lw * 512 + lane * 16);
#pragma unroll
    for (int j = 0; j < NWI; ++j) vw[j] = FIRST ? vw0 + (j & 1) * (32 * 6144) + (j >> 1) * 2048 : pick(cq_, (unsigned)((j * 4 + lw) * 1024 + lane * 16), wq_src(j * 4 + lw, lane, c.jq));
    auto issueA = [&](int s, int slot) {
        const char* b = xin + (unsigned)(tile(s) * (FIRST ? 128 : 16384));
#pragma unroll
        for (int j = 0; j < 4; ++j) dma16<true>(QA_OFF + slot * QA_SLOT + (j * 4 + lw) * 1024, va[j], b);
        if (HALO) { if (lane < 32) dma16<true>(QA_OFF + slot * QA_SLOT + 16384 + lw * 512, va[4], b); }
    };
    auto issueW = [&](int s, int slot, int j0 = 0, int j1 = NWI) {
        const char* b = W + (unsigned)(tile(s) * (64 * 6144));
#pragma unroll
        for (int j = j0; j < j1; ++j) dma16<CPY && !FIRST>(qw_slot(slot) + (j * 4 + lw) * 1024, vw[j], b);
    };
    auto ahead = [&]() {
        const int r3 = (lane * 43) >> 7;
        const unsigned o = (unsigned)((((rot - (c.pq & 7) + 10) * 64 + (c.pq & 7) * 48 + lw * 12 + r3) & 1023) * 6144 + (lane - 3 * r3) * 2048 + c.jq * 128);
        if (lane < 36 && (WTC != 2 || (c.jq & 7) == 3)) asm volatile("s_mov_b32 m0, %0\n\ts_nop 3\n\tglobal_load_lds_dword %1, %2" ::"s"(M_TCH), "v"(o), "s"(W) : "memory");
    };
    if (ST0 && FIRST && lw == 1) STAMP_AT(47);
    if (FIRST && ERL) {
        SCHED();
        issueA(1, 1); issueW(1, 1);
        SCHED();
    }
    const bool docs = FIRST || (HALO && l == 1);
    const int t = lw * 64 + lane;
    static_assert(MBOX & 1, "residual flag");
    unsigned foff, ftg, so, ho, fo_, co, l3, l4, vwo, wt[3], cso, xt, xt1, xso, tp, tl;
    u32x4 on;
    auto addr1 = [&]() {
        int lane = c.lane;
        asm volatile("" : "+v"(lane));
        const int fr = lane & 15, fq = lane >> 4;
        const int ph = c.pq > 0 ? c.pq - 1 : 0;
        foff = pick(lane != 32, (unsigned)WS_FLAGQ + pick(lane < 16, c.pq * 16 + lane, pick(lane < 32, fr * 16 + c.jq, ph * 16 + 14 + (lane & 1))) * FQS, (unsigned)WS_XBOX + (c.pw * 16 + c.jw) * (2 * FQS) + c.jw * 8);
        ftg = lane != 32 ? (unsigned)(4 * l + 1) : (unsigned)(l + 1);
        so = FIRST ? (unsigned)((c.pq * 16 + fq * 4) * 1024 + (lw * 32 + fr * 2) * 8) : (unsigned)((c.pq * 16 + (lane >> 2)) * 1024 + (c.jq * 8 + lw * 2 + (lane & 1)) * 8);
        ho = FDM ? (unsigned)(262144 + ((lw * 36 + lane < 16 ? ph * 128 + 128 : c.pq * 128) + lw * 36 + lane - 16) * 8)
                 : (unsigned)(262144 + pick(lane < 32, c.pq * 128 + lw * 32 + lane, ph * 128 + 112 + lw * 4 + (lane & 3)) * 8);
        fo_ = pick(lane < 2, (unsigned)(262144 + (c.pq * 128 + c.jq * 8 + lw * 2 + lane) * 8), 0x80000000u);
        const int cq = lw * 12 + (fr < 12 ? fr : 11);
        co = (unsigned)((fq * 4 * 3072 + (cq >> 4) * 1024 + c.jq * 64 + (cq & 15) * 4) * 4);
        l3 = (unsigned)(M_CS + (HALO ? 768 : 0) + (cq * 4 + fq) * 4);
        l4 = (unsigned)(M_MEAN + pick(lane < 32, 16 + lw * 32 + (FIRST ? fr * 2 + fq : lane), lw * 4 + (lane & 3)) * 4);
        asm volatile("" : "+v"(foff), "+v"(ftg), "+v"(so), "+v"(co), "+v"(l3), "+v"(l4));
        if (!FIRST) asm volatile("" : "+v"(ho), "+v"(fo_));
        SCHED();
    };
    auto addr2 = [&]() {
        int lane = c.lane;
        asm volatile("" : "+v"(lane));
        const int fr = lane & 15, fq = lane >> 4;
        const int q4 = fr >> 2, p4 = lane & 3, fb = (q4 & 2) | ((fq & 1) << 2);
#pragma unroll
        for (int i = 0; i < 3; ++i) {
            const int T = 3 * lw + i;
            wt[i] = (unsigned)(qw_slot(SP % NQW) + (T >> 2) * 8192 + (8 * fq + q4) * 128 + (((2 * (T & 3) + (p4 >> 1)) ^ fb) << 4) + (p4 & 1) * 8);
        }
        const int T = 3 * lw + fq;
        cso = (unsigned)((((HALO ? 16 : 0) + rot) * 3072 + (T >> 2) * 1024 + c.jq * 64 + (T & 3) * 16 + fr) * 4);
        xt = (unsigned)(QA_OFF + (SP % NQA) * QA_SLOT + (lw * 32 + fr) * 128 + ((fq ^ (fr & 7)) << 4));
        xt1 = xt ^ 64;
        xso = (unsigned)hl<HS>((c.pq * 16 + rot) * 1024 + (lw * 32 + fr) * 8);
        on = (u32x4){0x3f803f80u, 0x3f803f80u, 0x3f803f80u, 0x3f803f80u};
        if (FIRST || HALO) asm volatile("" : "+v"(wt[0]), "+v"(wt[1]), "+v"(wt[2]), "+v"(cso), "+v"(on));
        if (FIRST) asm volatile("" : "+v"(xt), "+v"(xt1), "+v"(xso));
        SCHED();
    };
    auto addr3 = [&]() {
        Ctx c2 = c;
        asm volatile("" : "+v"(c2.lane));
        asm volatile("" : "+s"(c2.bid));
        vwo = wo_w_off(c2);
        tp = (unsigned)(pick(c2.lane < 30, (c2.bid >> 3) * 30 + c2.lane, (c2.bid >> 3) * 16 + c2.lane - 30 + (int)(WS_O >> 16)) * 65536 + 65536 - 128);
        tl = (unsigned)(lw == 1 ? ((c2.bid >> 3) * 2 + c2.lane) * 65536 : c2.bid * 8192 + (TWO && !(EXS && (c.jq & 7) == 3) ? c2.lane * 128 : 0));
        asm volatile("" : "+v"(vwo));
        if (FIRST) asm volatile("" : "+v"(tp), "+v"(tl));
        SCHED();
    };
    if (!FIRST || AD1 < 0) addr1();
    if (FIRST && ERL >= 5) { issueA(2, 2); SCHED(); }
    if (FIRST && (ERL == 2 || ERL == 5)) { issueW(2, 2); SCHED(); }
    if (!FIRST || AD2 < 0) addr2();
    if (!FIRST || AD3 < 0) addr3();
#pragma unroll
    for (int j = 0; j < NAI; ++j) asm volatile("" : "+v"(va[j]));
#pragma unroll
    for (int j = 0; j < NWI; ++j) asm volatile("" : "+v"(vw[j]));
    SCHED();
    int tv = 0; unsigned long long lt = 0;
    if (FIRST) {
        LSTAMP(55);
    } else {
        static_assert(CPY == 1 && RS0 > 0 && RN0 == NWI && WPRE >= 3 && (FIRST || SC.w[0] == NAI), "order");
        unsigned vx[2], xfo = (unsigned)WS_XBOX + (c.pq * 16 + c.jq) * (2 * FQS) + (HALO && c.pq > 0 ? (lane & 16) * (FQS / 16) : 0) + fr * 8, go = M_GO;
        u64 xv = lds_epoch(smem) + (unsigned)(l + 1);
#pragma unroll
        for (int i = 0; i < 2; ++i) vx[i] = pick(cq_, (unsigned)(((2 * lw + i) * 4 + 3) * 1024 + lane * 16), wq_src((2 * lw + i) * 4 + 3, lane, c.jq));
        asm volatile("" : "+v"(vx[0]), "+v"(vx[1]), "+v"(xfo), "+v"(xv), "+v"(go));
        BAR();
        issueW(0, 0);
        auto word = [&](int off) { return __builtin_amdgcn_readfirstlane(*(volatile LAS int*)(lds(go) + off)); };
        auto until = [&](int off, int v) {
#pragma unroll 1
            for (unsigned spins = 0; spins < (1u << 24); ++spins) { if (word(off) == v) break; }
            FENCE();
        };
        static_assert(WPRE == 3, "stages");
        int t1 = tile(1), t2 = tile(2);
        asm volatile("" : "+s"(t1), "+s"(t2));
        auto stage = [&](int s) {
            const char* b = W + (unsigned)((s == 1 ? t1 : t2) * (64 * 6144));
            unsigned d = (unsigned)((s == 3 ? QW_TOP : s * QW_SLOT) + lw * 1024), dx = d + (unsigned)(lw * 7168 + 3072);
            asm volatile("" : "+s"(d), "+s"(dx), "+s"(b));
#pragma unroll
            for (int j = 0; j < NWI; ++j) dma16<true>(d + j * 4096, vw[j], b);
#pragma unroll
            for (int i = 0; i < 2; ++i) dma16<true>(dx + i * 4096, vx[i], b);
        };
        auto first2 = [&]() { __builtin_amdgcn_s_setprio(LPRIO); issueA(0, 0); issueA(1, 1); };
        if (lw == 3) {
            if (!EPOLL) until(M_CNT - M_GO, 8 * l);
            Ask t;
            poll_rmw(t, c.ws, xfo, lane < (HALO && c.pq > 0 ? 32 : 16), xv, 63 - l);
            *(volatile LAS int*)lds(go) = l;
            until(M_CNT - M_GO, 8 * l);
            first2();
            STAMP_AT(32 + l * 8 + 0);
            wait_v<NAI>();
            keep(t);
        } else {
            until(M_CNT - M_GO, 8 * l);
            if (RSLP) __builtin_amdgcn_s_sleep(RSLP);
            int ns = 1;
#pragma unroll 1
            for (; ns < WPRE; ++ns) {
                if (word(0) == l) break;
                stage(ns);
            }
            until(0, l);
            first2();
            const int left = WPRE - ns;
#pragma unroll 1
            for (; ns < WPRE; ++ns) stage(ns);
            if (left == 0) wait_v<NAI>();
            else if (left == 1) wait_v<NAI + 8>();
            else if (left == 2) wait_v<NAI + 16>();
            else wait_v<NAI + 24>();
        }
        FENCE();
    }
    if (FIRST && ERL == 3) issueW(2, 2);
    if (ST0 && FIRST && lw == 2) STAMP_AT(57);
    if (FIRST && !ERL) BAR();
    __builtin_amdgcn_s_setprio(LPRIO);
    if (FIRST && !ERL) { LSTAMP(57); issueA(1, 1); issueW(1, 1); LSTAMP(58); }
    u32x4 st[4], cs[4]; u32x2 sh, fl; u64 fv = 0; float rs_, rq_, hs;
    f32x4 d[3], sm[2], g[2];
    bf16x8 wf[3][2], xf[2][2];
    const bool fact = FIRST ? lane < 32 : ((lane < 16 || lane >= (docs ? 16 : 32)) && lane <= (HALO ? 34 : 32));
    static_assert(XS + 3 <= (FIRST ? 12 : 13) && SPR == 1, "aux schedule");
    auto side = [&](auto ktc) {
        constexpr int kt = decltype(ktc)::value;
        {
            constexpr int C0 = FIRST ? 12 : XS + 2, C1 = FIRST ? 13 : XS + 3, C2 = 13;
            auto acc4 = [](u32x4& a, const u32x4& b) {
#pragma unroll
                for (int i = 0; i < 4; ++i) a[i] = __float_as_uint(addf(__uint_as_float(a[i]), __uint_as_float(b[i])));
                asm volatile("" : "+v"(a));
            };
            auto fin = [&]() {
                rs_ *= -1.f / 1024.f;
                asm volatile("" : "+v"(rs_));
                rq_ = __builtin_amdgcn_rsqf(fmaxf(rq_ * (1.f / 1024.f) - rs_ * rs_, 0.f) + LN_EPS);
                asm volatile("" : "+v"(rq_));
            };
            if constexpr (FIRST && kt == XS + 2) {
                asm volatile("" : "+v"(st[0]), "+v"(st[1]), "+v"(st[2]));
                acc4(st[0], st[1]); acc4(st[0], st[2]);
            }
            if constexpr (FIRST && kt == XS + 3) { asm volatile("" : "+v"(st[3])); acc4(st[0], st[3]); }
            if constexpr (kt == C0) {
                if (docs) { asm volatile("" : "+v"(cs[0]), "+v"(cs[1])); acc4(cs[0], cs[1]); }
            }
            if constexpr (FIRST && kt == 12) {
                asm volatile("s_nop 1" : "+v"(st[0]));
                rs_ = fold16(__uint_as_float(st[0][0]), __uint_as_float(st[0][2])); rq_ = fold16(__uint_as_float(st[0][1]), __uint_as_float(st[0][3]));
                rs_ = fold32(rs_, rs_); rq_ = fold32(rq_, rq_);
            }
            if constexpr (FIRST && kt == 13) fin();
            if constexpr (!FIRST && kt == TL0 + 2) {
                asm volatile("" : "+v"(sh));
                hs = fold16(__uint_as_float(sh[0]), __uint_as_float(sh[1]));
                hs = fold32(hs, hs);
                hs += __uint_as_float(__builtin_amdgcn_update_dpp(0u, __float_as_uint(hs), 0x124, 0xf, 0xf, false));
                hs += __uint_as_float(__builtin_amdgcn_update_dpp(0u, __float_as_uint(hs), 0x128, 0xf, 0xf, false));
                asm volatile("" : "+v"(hs));
            }
            if constexpr (!FIRST && kt == TL0 + 2 + TSP) {
                auto hh = __builtin_amdgcn_permlane16_swap(__float_as_uint(hs), __float_as_uint(hs), false, false);
                rs_ = __uint_as_float(hh[0]); rq_ = __uint_as_float(hh[1]);
                asm volatile("" : "+v"(rs_), "+v"(rq_));
                fin();
                auto rf = __builtin_amdgcn_make_buffer_rsrc(c.ws + WS_STATS + (size_t)l * 524288, 0, 524288, 0x00020000);
                __builtin_amdgcn_raw_buffer_store_b64((u32x2){__float_as_uint(rs_), __float_as_uint(rq_)}, rf, fo_, 0, 16);
            }
            if constexpr (kt == C1) {
                if (docs) { asm volatile("" : "+v"(cs[2]), "+v"(cs[3])); acc4(cs[2], cs[3]); acc4(cs[0], cs[2]); }
            }
            if constexpr (kt == C2) {
                if (docs) {
                    asm volatile("s_nop 1" : "+v"(cs[0]));
                    hs = fold32(fold16(__uint_as_float(cs[0][0]), __uint_as_float(cs[0][1])), fold16(__uint_as_float(cs[0][2]), __uint_as_float(cs[0][3])));
                }
            }
            if constexpr (kt == 13) {
                static_assert(WV0 == 1, "wait");
                if (FIRST || !FDM || docs) WAIT_V0();
                if (!FIRST && !FDM) { asm volatile("" : "+v"(sh)); rs_ = __uint_as_float(sh[0]); rq_ = __uint_as_float(sh[1]); }
                if ((FIRST || !FDM) && lane < (HALO ? 36 : 32)) {
                    *(LAS float*)lds(l4) = rs_;
                    *(LAS float*)(lds(l4) + (M_RSTD - M_MEAN)) = rq_;
                }
                if (docs) *(LAS float*)lds(l3) = hs;
                FENCE();
            }
        }
    };
    static_for<0, KT>([&](auto ktc) {
        constexpr int kt = decltype(ktc)::value;
        LSTAMP(3 * kt);
        if constexpr (FIRST || kt > 0) wait_v<SC.w[kt]>();
        LSTAMP(3 * kt + 1);
        if constexpr (kt == XS) {
            if (lw == 0) {
                asm volatile("" : "+v"(fl));
                const u64 ep = lds_epoch(smem);
                if (!__all(!fact || reached(__builtin_bit_cast(u64, fl), ep, ftg))) poll_flags(c.ws, ep, foff, fact, ftg);
            }
        }
        if constexpr (kt == 14) asm volatile("s_waitcnt lgkmcnt(0)" ::: "memory");
        BAR();
        FENCE();
        LMARK();
        LSTAMP(3 * kt + 2);
        if constexpr (TOPJ & (1 << kt)) side(ktc);
        if constexpr (kt == (FIRST ? SP + DS : TL0 + 2 + TSP) + 3) {
            if (lw == 0 && lane == 0) __hip_atomic_store((GAS u64*)(c.ws + WS_FLAGQ + (c.pq * 16 + c.jq) * FQS), fv, __ATOMIC_RELAXED, __HIP_MEMORY_SCOPE_AGENT);
        }
        if constexpr (kt == XS + 1 - NQA) {
            if (lw == 0) { if (fact) aload2c(fl, foff, c.ws); }
        }
        {
            const char* sb = c.ws + WS_STATS + (size_t)l * 524288;
            const char* cb = c.ws + WS_CSUM + (HALO ? 16 * 3072 * 4 : 0);
            constexpr int sa = 3 * (kt - XS), ca = FIRST ? 2 * (kt - XS - 1) : 2 * (kt - XS), cn = 2;
            if constexpr (FIRST) {
#pragma unroll
                for (int i = sa; i < sa + 3; ++i) if (i >= 0 && i < 4) aload4c(st[i], so, sb + i * 1024);
            }
            if constexpr (!FIRST && kt == TL0) aload2c(sh, so, sb);
            if constexpr (!FIRST && kt == TL1) {
                static_assert(TL1 <= 12, "covered by the wait of step 14");
                if (FDM) { if (lane < 36) { touch(M_MEAN + lw * 144, ho, sb); touch(M_RSTD + lw * 144, ho, sb + 4); } }
                else aload2c(sh, ho, sb);
            }
            if constexpr (ca >= 0 && ca < 4) {
                if (docs) {
#pragma unroll
                    for (int i = ca; i < ca + cn; ++i) aload4c(cs[i], co, cb + i * 12288);
                }
            }
        }
        if constexpr (kt == SP) {
            if (docs) {
#pragma unroll
                for (int i = 0; i < 3; ++i)
#pragma unroll
                    for (int ks = 0; ks < 2; ++ks) {
                        const s16x4 lo = tr_read(lds(wt[i]) + ks * 4096), hi = tr_read(lds(wt[i]) + ks * 4096 + 512);
                        wf[i][ks] = __builtin_shufflevector(lo, hi, 0, 1, 2, 3, 4, 5, 6, 7);
                    }
                if (FIRST) {
#pragma unroll
                    for (int mt = 0; mt < 2; ++mt)
#pragma unroll
                        for (int ks = 0; ks < 2; ++ks) xf[mt][ks] = *(const LAS bf16x8*)(lds(ks ? xt1 : xt) + mt * 2048);
                }
            }
            SCHED();
        }
        if constexpr (kt + 2 < KT && !(FIRST && ERL >= 5 && kt == 0)) issueA(kt + 2, (kt + 2) % NQA);
        LPUT(3 * kt + 2);
        if constexpr (kt == SP + DM) {
            SCHED();
            if (docs) {
                const bf16x8 ones = __builtin_bit_cast(bf16x8, on);
                const f32x4 zero = {0.f, 0.f, 0.f, 0.f};
#pragma unroll
                for (int ks = 0; ks < 2; ++ks) {
#pragma unroll
                    for (int i = 0; i < 3; ++i) d[i] = MFMA(ones, wf[i][ks], ks ? d[i] : zero);
                    if (FIRST) {
#pragma unroll
                        for (int mt = 0; mt < 2; ++mt) { sm[mt] = MFMA(ones, xf[mt][ks], ks ? sm[mt] : zero); g[mt] = MFMA(xf[mt][ks], xf[mt][ks], ks ? g[mt] : zero); }
                    }
                }
            }
            SCHED();
        }
        if constexpr (FIRST && ERL < 2 && kt < 5) {
            if constexpr (kt < 4) issueW(kt + 2, (kt + 2) % NQW);
            if constexpr (kt == 3) issueW(6, 2, 0, NWI / 2);
            if constexpr (kt == 4) { issueW(6, 2, NWI / 2, NWI); issueW(7, 3); }
        } else {
            if constexpr (WPRE == 2 && kt < 2) issueW(kt + 2, kt + 2);
            if constexpr (FIRST && (ERL == 4 || ERL == 6) && kt == 0) issueW(2, 2);
            if constexpr ((FIRST || WPRE == 3) && kt == 0) issueW(3, 3);
            if constexpr (kt >= 1 && kt + 3 < KT) issueW(kt + 3, (kt + 3) % NQW);
        }
        if constexpr (FIRST && kt == WT0) ahead();
        if constexpr (FIRST && kt == TSTEP) {
            if (lw == 1 && lane < 46) touch(M_TCH, tp, c.ws);
        }
        if constexpr (FIRST && kt == TW0) {
            if (lw == 0) touch(M_TCH, tl, Wo);
            else if (lw == 1) { asm volatile("" : "+s"(kp)); const char* outp = (const char*)kp->out; if (lane < 2) touch(M_TCH, tl, outp); }
        }
        if constexpr (kt == SP && DM > 0) {
            SCHED();
            if (docs) {
                asm volatile("" : "+v"(wf[0][0]), "+v"(wf[0][1]), "+v"(wf[1][0]), "+v"(wf[1][1]), "+v"(wf[2][0]), "+v"(wf[2][1]));
                if (FIRST) asm volatile("" : "+v"(xf[0][0]), "+v"(xf[0][1]), "+v"(xf[1][0]), "+v"(xf[1][1]));
            }
        }
        if constexpr (kt == SP + DS) {
            SCHED();
            if (docs) {
                if (FIRST) {
                    auto rst = __builtin_amdgcn_make_buffer_rsrc(c.ws + WS_STATS, 0, 524288, 0x00020000);
                    const bool b0 = fr & 1, b1 = fr & 2;
                    u32x2 sv[2];
#pragma unroll
                    for (int mt = 0; mt < 2; ++mt) {
                        float qa = b0 ? g[mt][1] : g[mt][0], qb = b0 ? g[mt][3] : g[mt][2];
                        asm volatile("" : "+v"(qa), "+v"(qb));
                        sv[mt] = (u32x2){__float_as_uint(sm[mt][0]), __float_as_uint(b1 ? qb : qa)};
                    }
                    if (fq == (fr >> 2)) {
                        __builtin_amdgcn_raw_buffer_store_b64(sv[0], rst, xso, 0, 16);
                        __builtin_amdgcn_raw_buffer_store_b64(sv[1], rst, xso + hl<HS>(128), 0, 16);
                    }
                }
                float d0 = d[0][0], d1 = d[1][0], d2 = d[2][0];
                asm volatile("" : "+v"(d0), "+v"(d1), "+v"(d2));
                const float v = fq == 0 ? d0 : (fq == 1 ? d1 : d2);
                auto rc = __builtin_amdgcn_make_buffer_rsrc(c.ws + WS_CSUM, 0, 2 * 16 * 3072 * 4, 0x00020000);
                if (lane < 48) __builtin_amdgcn_raw_buffer_store_b32(__float_as_uint(v), rc, cso, 0, 16);
                asm volatile("s_waitcnt lgkmcnt(0)" ::: "memory");
            }
        }
        if constexpr (kt == 1) {
            if (lw == 0) {
                const u32x2 e = *(const LAS u32x2*)(smem + M_EP);
                const u64 es = ((u64)(unsigned)__builtin_amdgcn_readfirstlane(e[1]) << 32) | (unsigned)__builtin_amdgcn_readfirstlane(e[0]);
                fv = es + (unsigned)(4 * l + 1);
                asm volatile("" : "+s"(fv));
            }
        }
        if constexpr (FIRST && kt == AD1) addr1();
        if constexpr (FIRST && kt == AD2) addr2();
        if constexpr (FIRST && kt == AD3) addr3();
        if constexpr (!(TOPJ & (1 << kt))) side(ktc);
        if constexpr (kt == 13) wo_w_issue<0, WT13>(c, Wo, vwo);
        if constexpr (kt == 14) { wo_w_issue<WT13, (HALO || WA0 > 12) ? 12 : WA0>(c, Wo, vwo); if (HALO) wo_w_issue<23, 23 + WB14>(c, Wo, vwo); }
        if constexpr (kt == 15) {
            if (HALO ? WBE < 2 : WA0 > 12) wo_w_issue<12, HALO ? 18 : WA0>(c, Wo, vwo);
            if (HALO) wo_w_issue<23 + WB14, 23 + WB14 + WB15>(c, Wo, vwo);
        }
    });
    __builtin_amdgcn_s_setprio(0);
    BAR();
    LMARK(); LPUT(48);
    LSTAMP(48);
    LSTORE(0);
    if (HALO ? WBE < 1 : WAE < 1) wo_w_issue<18, HALO ? 23 : 20>(c, Wo, vwo);
    if (HALO) {
        static_assert(!WBS || (WBE == 1 && WB14 == 0 && WRG <= 12), "Wo weights of the first twelve steps are older than the last 6 + WB15");
        if (WBS) wait_v<6 + WB15>();
        BAR();
        if (WBE) wo_w_issue<WBE < 2 ? 18 : 12, 23>(c, Wo, vwo);
        wo_w_issue<23 + WB14 + WB15, 32>(c, Wo, vwo);
    }
}

DI float max3(float a, float b, float c) { return fmaxf(fmaxf(a, b), c); }
#define SGB(m, n) __builtin_amdgcn_sched_group_barrier(m, n, 0)
template <bool HALO> struct Tail {
    int f[26], n[4], t[4][26], d[4][27], v[4][27], h3[32], s3[32], o3[33], q3[112];
    constexpr Tail() : f{}, n{}, t{}, d{}, v{}, h3{}, s3{}, o3{}, q3{} {
        const int FB[26] = {0, 1, 2, 3, 4, 5, 6, 7, 8, 9, 10, 11, 12, 13, 14, 15, 24, 25, 19, 23, 18, 22, 17, 21, 16, 20};
        const int FA[24] = {0, 1, 2, 3, 4, 5, 6, 7, 8, 9, 10, 11, 12, 13, 14, 15, 16, 17, 20, 21, 18, 19, 22, 23};
        const int TA[4][26] = {{0, 1, 2, 3, 7, 6, 5, 4, 8, 9, 10, 11, 15, 14, 13, 12, 16, 17, 18, 19, 23, 22, 21, 20, 0, 0}, {4, 8, 9, 10, 11, 15, 14, 13, 12, 16, 17, 18, 19, 23, 22, 21, 20, 0, 1, 2, 3, 7, 6, 5, 0, 0}, {16, 17, 18, 19, 23, 22, 21, 20, 0, 1, 2, 3, 7, 6, 5, 4, 8, 9, 10, 11, 15, 14, 13, 12, 0, 0}, {17, 18, 19, 23, 22, 21, 20, 8, 9, 10, 11, 15, 14, 13, 12, 16, 0, 0, 0, 0, 0, 0, 0, 0, 0, 0}};
        const int DA[4][26] = {{1, 1, 1, 1, 1, 1, 1, 1, 1, 1, 1, 1, 1, 1, 1, 1, 1, 1, 1, 1, 1, 1, 0, 0, 0, 0}, {1, 1, 1, 1, 1, 1, 1, 1, 1, 1, 1, 1, 1, 1, 1, 1, 1, 1, 1, 1, 1, 1, 0, 0, 0, 0}, {1, 1, 1, 1, 1, 1, 1, 1, 1, 1, 1, 1, 1, 1, 1, 1, 0, 0, 0, 0, 0, 0, 0, 0, 0, 0}, {2, 2, 2, 2, 1, 1, 1, 1, 1, 1, 1, 1, 0, 0, 0, 0, 0, 0, 0, 0, 0, 0, 0, 0, 0, 0}};
        const int VA[4][26] = {{1, 1, 0, 0, 0, 0, 1, 1, 1, 1, 1, 1, 1, 1, 1, 1, 1, 1, 1, 1, 1, 1, 2, 2, 0, 0}, {1, 1, 1, 1, 1, 1, 1, 1, 1, 1, 1, 1, 1, 1, 1, 1, 1, 1, 1, 1, 1, 1, 2, 2, 0, 0}, {1, 1, 1, 1, 1, 1, 1, 1, 1, 1, 1, 1, 1, 1, 1, 1, 2, 2, 2, 2, 2, 3, 3, 2, 0, 0}, {0, 0, 0, 0, 1, 1, 1, 1, 1, 1, 1, 1, 2, 2, 2, 2, 0, 0, 0, 0, 0, 0, 0, 0, 0, 0}};
        const int TB[4][26] = {{0, 1, 2, 3, 7, 6, 5, 4, 24, 8, 9, 10, 11, 15, 14, 13, 12, 25, 16, 17, 18, 19, 23, 22, 21, 20}, {24, 8, 9, 10, 11, 15, 14, 13, 12, 25, 16, 17, 18, 19, 23, 22, 21, 20, 0, 1, 2, 3, 7, 6, 5, 4}, {25, 16, 17, 18, 19, 23, 22, 21, 20, 0, 1, 2, 3, 7, 6, 5, 4, 24, 8, 9, 10, 11, 15, 14, 13, 12}, {0, 1, 2, 3, 7, 6, 5, 4, 24, 8, 9, 10, 11, 15, 14, 13, 12, 25, 19, 23, 0, 0, 0, 0, 0, 0}};
        const int DB[4][26] = {{1, 1, 1, 1, 1, 1, 1, 1, 1, 1, 1, 1, 1, 1, 1, 1, 1, 1, 1, 1, 1, 1, 1, 0, 0, 0}, {1, 1, 1, 1, 1, 1, 1, 1, 1, 1, 1, 1, 1, 1, 1, 1, 1, 1, 1, 1, 1, 1, 0, 0, 0, 0}, {1, 1, 1, 1, 1, 1, 1, 1, 1, 1, 1, 1, 1, 1, 1, 1, 1, 1, 1, 1, 1, 1, 1, 0, 0, 0}, {2, 2, 1, 1, 1, 1, 1, 1, 1, 1, 1, 1, 1, 1, 1, 1, 1, 1, 1, 0, 0, 0, 0, 0, 0, 0}};
        const int VB[4][26] = {{1, 1, 1, 1, 1, 1, 1, 1, 1, 1, 1, 1, 1, 1, 1, 1, 1, 1, 1, 1, 1, 1, 1, 2, 2, 2}, {1, 1, 1, 1, 1, 1, 1, 1, 1, 1, 1, 1, 1, 1, 1, 1, 1, 1, 1, 1, 1, 1, 3, 3, 3, 2}, {1, 1, 1, 1, 1, 1, 1, 1, 1, 1, 1, 1, 1, 1, 1, 1, 1, 1, 1, 1, 1, 1, 1, 3, 3, 2}, {0, 0, 1, 1, 1, 1, 1, 1, 1, 1, 1, 1, 1, 1, 1, 1, 1, 1, 1, 2, 0, 0, 0, 0, 0, 0}};
        const int G[32] = {8, 12, 9, 13, 10, 14, 11, 15, 16, 17, 20, 21, 18, 19, 22, 23, 0, 4, 1, 5, 2, 6, 3, 7, 0, 4, 1, 5, 2, 6, 3, 7};
        const int NM = HALO ? 26 : 24;
        n[0] = n[1] = n[2] = NM; n[3] = HALO ? 20 : 16;
        for (int i = 0; i < NM; ++i) f[i] = HALO ? FB[i] : FA[i];
        int vv = 0;
        for (int h = 0; h < 4; ++h) {
            v[h][0] = vv;
            for (int i = 0; i < n[h]; ++i) {
                t[h][i] = HALO ? TB[h][i] : TA[h][i];
                d[h][i + 1] = d[h][i] + (HALO ? DB[h][i] : DA[h][i]);
                vv += HALO ? VB[h][i] : VA[h][i];
                v[h][i + 1] = vv;
            }
        }
        for (int i = 0; i < 32; ++i) { h3[i] = G[i]; s3[i] = i >= 16 && i < 24 ? 0 : 1; }
        for (int i = 3; i < 32; ++i) o3[i + 1] = o3[i] + TOPS;
        int nv = 0, ns = 0;
        for (int k = 0; k < 8 * 14; ++k) {
            const bool top = k % TOPS < 2 || nv == 96;
            if (top && ns < 16 && nv >= 12 * (ns / 2 + 1) && (nv == 96 || k % TOPS == (ns & 1))) { q3[k] = (ns / 2) * 14 + 13 - (ns & 1); ++ns; }
            else { q3[k] = (nv / 12) * 14 + nv % 12; ++nv; }
        }
    }
};
template <bool HALO> DI void qkv_consume(const Ctx& c0, int l, KP kp, unsigned va0 = 0, unsigned vw0 = 0) {
    const Ctx c = fresh(c0);
    if (ST0 && l == 0 && c.wid == 2) STAMP_AT(59);
    LAS char* smem = c.smem;
    const int lane = c.lane, wr = c.wid >> 1, wc = c.wid & 1, fr = lane & 15, fq = lane >> 4;
    const int sw = HALO ? wr : 0;
    f32x4 acc[4][6], ah[2];
#pragma unroll
    for (int mt = 0; mt < 4; ++mt)
#pragma unroll
        for (int nt = 0; nt < 6; ++nt) { acc[mt][nt] = (f32x4){0.f, 0.f, 0.f, 0.f}; asm volatile("" : "+v"(acc[mt][nt])); }
    ah[0] = ah[1] = (f32x4){0.f, 0.f, 0.f, 0.f};
    if (HALO) asm volatile("" : "+v"(ah[0]), "+v"(ah[1]));
    const unsigned aoff0 = (unsigned)(QA_OFF + wr * 8192 + (HALO || l ? lane * 16 : fr * 128 + ((fq ^ (fr & 7)) << 4)));
    const unsigned hoff0 = (unsigned)(QA_OFF + 16384 + lane * 16);
    unsigned xm = HALO || l ? 1024u : 64u;
    asm volatile("" : "+s"(xm));
    static_assert(((QA_OFF | QA_SLOT) & 2047) == 0, "halves");
    const int q4 = fr >> 2, p4 = lane & 3;
    const int fb = (q4 & 2) | ((fq & 1) << 2);
    unsigned boff[2];
#pragma unroll
    for (int cc = 0; cc < 2; ++cc)
        boff[cc] = (unsigned)((8 * fq + q4) * 128 + (((2 * (2 * wc + (cc ^ sw)) + (p4 >> 1)) ^ fb) << 4) + (p4 & 1) * 8);
    bf16x8 xa[2][4], xh[2], wb[2][6];
    auto ldfrag = [&](int set, unsigned pa, unsigned ph, unsigned pb0, unsigned pb1, int ks) {
#pragma unroll
        for (int mt = 0; mt < 4; ++mt) xa[set][mt] = *(const LAS bf16x8*)(lds(pa) + mt * 2048);
        if (HALO) xh[set] = *(const LAS bf16x8*)lds(ph);
#pragma unroll
        for (int nt = 0; nt < 6; ++nt) {
            const LAS char* pb = lds((nt & 1) ? pb1 : pb0) + (nt >> 1) * 8192 + ks * 4096;
            const s16x4 lo = tr_read(pb), hi = tr_read(pb + 512);
            wb[set][nt] = __builtin_shufflevector(lo, hi, 0, 1, 2, 3, 4, 5, 6, 7);
        }
    };
    constexpr int NM = HALO ? 26 : 24, NH = HALO ? 17 : 16;
    auto mm = [&](int set, int i0, int i1) {
#pragma unroll
        for (int ii = i0; ii < i1; ++ii) {
            const int i = !HALO || ii < 8 ? ii : (!SNK ? (ii < 10 ? ii + 16 : ii - 2) : (ii == 8 ? 24 : (ii < 17 ? ii - 1 : (ii == 17 ? 25 : ii - 2))));
            const int nt = i >> 2, mt = SNK && (nt & 1) ? 3 - (i & 3) : i & 3;
            if (i < 16) acc[mt][nt] = MFMA(wb[set][nt], xa[set][mt], acc[mt][nt]);
            else if (i < 24) acc[mt][nt] = MFMA(xa[set][mt], wb[set][nt], acc[mt][nt]);
            else if (i == 24) ah[0] = MFMA(wb[set][2], xh[set], ah[0]);
            else ah[1] = MFMA(xh[set], wb[set][4], ah[1]);
        }
    };
    auto interleave = [&]() {
#pragma unroll
        for (int i = 0; i < NH; ++i) { SGB(0x008, 1); SGB(0x100, 1); }
        SCHED();
    };
    unsigned sa = 0, sb = 0;
    unsigned pa0 = aoff0, pa1 = aoff0 ^ xm, ph0 = hoff0, ph1 = hoff0 ^ xm, pb0 = boff[0], pb1 = boff[1];
    asm volatile("" : "+v"(pa0), "+v"(pa1), "+v"(pb0), "+v"(pb1));
    if (HALO) asm volatile("" : "+v"(ph0), "+v"(ph1));
    constexpr int M_CSH = M_CS + (HALO ? 768 : 0);
    unsigned sa0 = (unsigned)(M_MEAN + 64 + (wr * 64 + fr) * 4), sa1 = (unsigned)(M_MEAN + 64 + wr * 256 + fq * 16);
    unsigned sa2 = (unsigned)(M_CSH + wc * 128 + sw * 64 + fq * 16), sa6 = (unsigned)(M_CSH + 512 + wc * 128 + sw * 64 + fr * 4);
    unsigned sa3 = sa2 ^ 64, sa7 = sa6 ^ 64, sa4 = (unsigned)(M_MEAN + fr * 4), sa5 = (unsigned)(M_MEAN + fq * 16);
    asm volatile("" : "+v"(sa0), "+v"(sa1), "+v"(sa2), "+v"(sa6), "+v"(sa3), "+v"(sa7));
    if (HALO) asm volatile("" : "+v"(sa4), "+v"(sa5));
    const int head = c.jq * 2 + wc;
    auto rso = __builtin_amdgcn_make_buffer_rsrc(HALO ? c.ws + WS_O + (size_t)l * (8u << 20) + (size_t)((head * 16 + c.pq) * 8 + wr * 4) * (1024 << HO)
                                                      : c.ws + WS_KV + (size_t)(l >> 1) * (16u << 20) + (size_t)((c.pq * 32 + head) * 4 + wr) * (4096 << HK), 0, HALO ? 4096 << HO : 12288 << HK, 0x00020000);
    unsigned xrs = (unsigned)(M_XCH + wc * 6144 + wr * 2048), xhs = (unsigned)(M_XCH + wc * 6144 + 8 - wr * 8);
    unsigned b0m = (c.pq == 0 && wr == 0) ? 15u * 16u : 0u;
    asm volatile("" : "+s"(rso));
    if (HALO) asm volatile("" : "+s"(xrs), "+s"(xhs), "+s"(b0m));
    u32x4 cpy[3];
    unsigned cv, vt, cdst;
    const char *cs_, *tw, *cd_;
    int more = !HALO && l == 0 ? KT - 3 - XST - (c.pq & 7) : 0, three = c.jq & 4;
    if (!HALO) {
        static_assert(CPY + TOUCH == 2 && XSP == 1, "extra");
        asm volatile("" : "+s"(kp));
        const int g = c.jq >> 2, n = c.pq * 4 + (c.jq & 3), m = n >> 5, kt = (n >> 1) & 15;
        const char* src = *(const char* const __attribute__((address_space(4)))*)((const __attribute__((address_space(4))) char*)kp + (three ? (m ? 8 : 24) : (m ? 32 : 40)));
        const char* tsr = *(const char* const __attribute__((address_space(4)))*)((const __attribute__((address_space(4))) char*)kp + (c.wid < 3 ? 24 : 40));
        int o1 = kt * (64 * 6144) + (n & 1) * (32 * 6144) + (3 + 4 * (g & 2) - c.jq) * 128 + 2048, o2 = (n & 31) * 65536 + (11 - 4 * g) * 128 - c.pq * 128 * (D_ * 2) + 2048;
        int d1 = (int)WS_CQ + kt * (64 * 6144) + (m * 2 + (g >> 1)) * 24576 + (n & 1) * 4096, d2 = (int)WS_CO + (n & 31) * 65536 + (m * 2 + 1 - (g >> 1)) * 4096;
        asm volatile("" : "+s"(o1), "+s"(o2), "+s"(d1), "+s"(d2));
        cs_ = src + (three ? o1 : o2);
        tw = tsr + (unsigned)((c.wid < 3 ? c.wid : 0) * (2u << 20) + c.bid * 8192);
        cd_ = c.ws + (unsigned)(three ? d1 : d2);
        cv = pick(three, vw0, va0 ^ ((lane & 8) << 1));
        cdst = (unsigned)(c.wid * 1024 + lane * 16);
        vt = TWQ ? (unsigned)(((lane & 7) == 3 ? lane - 1 : lane) * 128) : 0u;
        if (EXS) vt &= (c.jq & 7) == 3 ? 0u : ~0u;
        asm volatile("" : "+s"(more), "+s"(three), "+s"(cs_), "+s"(tw), "+s"(cd_));
        asm volatile("" : "+v"(cv), "+v"(cdst), "+v"(vt));
        asm volatile("" : "=v"(cpy[0]), "=v"(cpy[1]), "=v"(cpy[2]));
    }
    unsigned hto = HT0 == 2 ? 0u : 0x80000000u;
    const char* W0 = (const char*)kp->wqkv0;
    if (!HALO && HT0) {
        const int n = ((c.pq & 7) * 3 + (c.jq & 3)) * 64 + c.wid * 16 + fr, r3 = (n * 43691) >> 17;
        const int t0 = rot_first(c.pq & 8, c.jq | 3) + 8;
        if (l == 0 && !(c.jq & 4) && (c.jq & 3) != 3 && lane < 16) hto = (unsigned)(((t0 * 64 + r3) & 1023) * 6144 + (n - 3 * r3) * 2048 + (c.jq | 3) * 128);
        asm volatile("" : "+v"(hto));
        asm volatile("" : "+s"(W0));
    }
    if (!HALO && HT0 == 2) { asm volatile("s_mov_b32 m0, %0\n\ts_nop 3\n\tglobal_load_lds_dword %1, %2" ::"s"(M_TCH), "v"(hto), "s"(W0) : "memory"); }
    if (!HALO && !ERL && l == 0) BAR();
    if (ST0 && l == 0 && c.wid == 1) STAMP_AT(44);
    if (!HALO) { if (HT0 == 2) wait_v<1>(); else WAIT_V0(); }
    if (ST0 && l == 0 && c.wid == 1) STAMP_AT(46);
    BAR();
    if (!HALO && HT0 == 1) { if (hto < 0x80000000u) asm volatile("s_mov_b32 m0, %0\n\ts_nop 3\n\tglobal_load_lds_dword %1, %2" ::"s"(M_TCH), "v"(hto), "s"(W0) : "memory"); }
    if (c.wid == 0) STAMP_AT(l * 8 + 0);
    ldfrag(0, pa0, ph0, pb0, pb1, 0);
    SCHED();
    if (ALN) WAIT_L0();
    SCHED();
    int cnt = KT - 3 - more, tv = 0;
    QSTAMP(0);
    auto half0 = [&]() {
        ldfrag(1, pa1, ph1, pb0, pb1, 1);
        mm(0, 0, NH);
        interleave();
        mm(0, NH, NH + 1); SCHED();
        asm volatile("s_addk_i32 %0, %2\n\ts_addk_i32 %1, %3" : "+s"(sa), "+s"(sb) : "i"(QA_SLOT), "i"(QW_SLOT) : "scc"); SCHED();
        mm(0, NH + 1, NH + 2); SCHED();
        asm volatile("s_cmpk_lg_u32 %0, %2\n\ts_cselect_b32 %0, %0, 0\n\ts_cmp_lg_u32 %1, %3\n\ts_cselect_b32 %1, %1, %4" : "+s"(sa), "+s"(sb) : "i"(NQA * QA_SLOT), "i"(3 * QW_SLOT), "i"(QW_TOP) : "scc"); SCHED();
        mm(0, NH + 2, NH + 3); SCHED();
        asm volatile("s_cmp_lg_u32 %0, %2\n\ts_cselect_b32 %0, %0, 0\n\ts_addk_i32 %1, -1" : "+s"(sb), "+s"(cnt) : "i"(QW_TOP + QW_SLOT) : "scc"); SCHED();
        mm(0, NH + 3, NH + 4); SCHED();
        pa0 = aoff0 + sa; pb0 = boff[0] + sb;
        asm volatile("" : "+v"(pa0), "+v"(pb0)); SCHED();
        mm(0, NH + 4, NH + 5); SCHED();
        pb1 = boff[1] + sb; pa1 = pa0 ^ xm;
        asm volatile("" : "+v"(pa1), "+v"(pb1)); SCHED();
        mm(0, NH + 5, NH + 6); SCHED();
        if (HALO) { ph0 = hoff0 + sa; ph1 = ph0 ^ xm; asm volatile("" : "+v"(ph0), "+v"(ph1)); SCHED(); }
        mm(0, NH + 6, NM);
        SCHED();
        WAIT_L0();
        BAR();
        SCHED();
    };
    asm volatile("" : "+s"(sa), "+s"(sb), "+s"(cnt));
    do {
    ALIGN8();
    do {
        half0();
        ldfrag(0, pa0, ph0, pb0, pb1, 0);
        mm(1, 0, NH);
        interleave();
        mm(1, NH, NM - 1);
        SCHED();
        WAIT_L0();
        asm volatile("s_nop 0");
        SCHED();
        mm(1, NM - 1, NM);
        SCHED();
    } while (cnt != 0);
    if (!HALO) {
        asm volatile("s_cmp_eq_u32 %[more], 0\n\ts_cbranch_scc1 .Lex%=\n\ts_mov_b32 m0, %[tch]\n\ts_nop 3\n\tglobal_load_lds_dword %[vt], %[tw] sc1\n\t"
                     "global_load_dwordx4 %[c0], %[v0], %[cs] offset:-2048\n\ts_cmp_eq_u32 %[three], 0\n\ts_cbranch_scc1 .Lex%=\n\t"
                     "global_load_dwordx4 %[c1], %[v0], %[cs]\n\tglobal_load_dwordx4 %[c2], %[v0], %[cs] offset:2048\n.Lex%=:"
                     : [c0] "+v"(cpy[0]), [c1] "+v"(cpy[1]), [c2] "+v"(cpy[2])
                     : [more] "s"(more), [tch] "i"(M_TCH), [vt] "v"(vt), [tw] "s"(tw), [v0] "v"(cv), [cs] "s"(cs_), [three] "s"(three) : "scc", "memory");
        cnt = more; more = 0;
        asm volatile("" : "+s"(cnt), "+s"(more));
        SCHED();
    }
    } while (cnt != 0);
    if (!HALO) {
        asm volatile("s_cmp_lg_u32 %[l], 0\n\ts_cbranch_scc1 .Lex%=\n\ts_waitcnt vmcnt(0)\n\ts_nop 3\n\tglobal_store_dwordx4 %[d], %[c0], %[ws] sc1\n\ts_cmp_eq_u32 %[three], 0\n\ts_cbranch_scc1 .Lex%=\n\t"
                     "v_add_u32 %[d], 0x2000, %[d]\n\tglobal_store_dwordx4 %[d], %[c1], %[ws] sc1\n\tv_add_u32 %[d], 0x2000, %[d]\n\tglobal_store_dwordx4 %[d], %[c2], %[ws] sc1\n.Lex%=:\n\ts_nop 1"
                     : [d] "+v"(cdst) : [c0] "v"(cpy[0]), [c1] "v"(cpy[1]), [c2] "v"(cpy[2]), [l] "s"(l), [ws] "s"(cd_), [three] "s"(three) : "scc", "memory");
    }
    QSTAMP(1);
    FENCE();
    SCHED();

    f32x4 csq[4], m4[4], mh4; float csv[2], mr[4], rr[4], mh;
    auto r1 = [&](unsigned b, int off) { return *(const LAS float*)(lds(b) + off); };
    auto r4_ = [&](unsigned b, int off) { return *(const LAS f32x4*)(lds(b) + off); };
    auto mf = [&](int set, int i) {
        const int nt = i >> 2, mt = i & 3;
        if (i < 16) acc[mt][nt] = MFMA(wb[set][nt], xa[set][mt], acc[mt][nt]);
        else if (i < 24) acc[mt][nt] = MFMA(xa[set][mt], wb[set][nt], acc[mt][nt]);
        else if (i == 24) ah[0] = MFMA(wb[set][2], xh[set], ah[0]);
        else ah[1] = MFMA(xh[set], wb[set][4], ah[1]);
        SCHED();
    };
    auto fold = [&](int i, int e) {
        const int nt = i >> 2, mt = i & 3;
        float t;
        if (i < 16) t = __builtin_fmaf(mr[mt], csq[nt][e], acc[mt][nt][e]);
        else if (i < 24) t = __builtin_fmaf(m4[mt][e], csv[nt - 4], acc[mt][nt][e]);
        else if (i == 24) t = __builtin_fmaf(mh, csq[2][e], ah[0][e]);
        else t = __builtin_fmaf(mh4[e], csv[0], ah[1][e]);
        asm volatile("" : "+v"(t));
        if (i < 24) acc[mt][nt][e] = t; else ah[i - 24][e] = t;
    };
    constexpr Tail<HALO> TL{};
    constexpr int NP = HALO ? 6 : 2, FN = 4 * NM;
    static_assert(TL.v[3][TL.n[3]] == NP + FN + (HALO ? 2 : 0) && KT % NQA == 1 && KT % NQW == 0, "tail");
    s16x4 wl[6], wh[6];
    auto frd = [&](int set, int k, int ao) {
        if (k < 4) xa[set][k] = *(const LAS bf16x8*)(lds(set ? pa1 : pa0) + ao + k * 2048);
        else if (k == 16) xh[set] = *(const LAS bf16x8*)(lds(set ? ph1 : ph0) + ao);
        else {
            const int nt = (k - 4) >> 1;
            const LAS char* pb = lds((nt & 1) ? pb1 : pb0) + (nt >> 1) * 8192 + set * 4096;
            if (k & 1) { wh[nt] = tr_read(pb + 512); wb[set][nt] = __builtin_shufflevector(wl[nt], wh[nt], 0, 1, 2, 3, 4, 5, 6, 7); }
            else wl[nt] = tr_read(pb);
        }
    };
    constexpr int RS = M_RSTD - M_MEAN;
    unsigned vl, vg, tb, xr, xh_, b0a, qb0, qb1;
    f32x4 bprev, bcur, bprev0; u32x4 ps; u32x2 ko; float ninf;
    auto rdop = [&](int h, int k) {
        const int j = k - NH;
        if (h == 0) {
            if (k < 2) { mr[2 * k] = r1(sa0, k * 128); mr[2 * k + 1] = r1(sa0, k * 128 + 64); }
            else if (k < 4) csq[k - 2] = r4_((k & 1) ? sa3 : sa2, 0);
            else if (j < 4) frd(0, k - 4, 2 * QA_SLOT);
            else csq[j - 2] = r4_((j & 1) ? sa3 : sa2, 256);
        } else if (j < 0) frd(h & 1, k, h == 1 ? 2 * QA_SLOT : 0);
        else if (h == 1) {
            if (j < 2) csv[j] = r1(j ? sa7 : sa6, 0);
            else if (HALO && j == 2) mh = r1(sa4, 0);
            else if (HALO && j == 3) mh4 = r4_(sa5, 0);
            else { const int mt = HALO ? 7 - j : j - 2; m4[mt] = r4_(sa1, mt * 64); }
        } else if (h == 2) {
            if (j == 0) m4[2] = r4_(sa1, 128);
            if (j == 1) ko = *(const LAS u32x2*)(lds(tb) + 3072);
            if (j == 2) { rr[0] = r1(sa0, RS); rr[1] = r1(sa0, 64 + RS); }
            if (j == 3) { rr[2] = r1(sa0, 128 + RS); rr[3] = r1(sa0, 192 + RS); }
            if (j >= 4) m4[5 - j] = r4_(sa1, (5 - j) * 64);
        } else {
            if (j == 0) bprev = r4_(tb, 0);
            if (j == 1) bcur = r4_(tb, 1024);
            if (j == 2) ps = *(const LAS u32x4*)(lds(tb) + 2048);
            if (j == 3) bprev0 = r4_(b0a, 0);
        }
    };
    auto vop = [&](int k) {
        if (k == 0) { asm volatile("" : "+v"(boff[0])); qb0 = boff[0] + QW_TOP; asm volatile("" : "+v"(qb0)); }
        else if (k == 1) { asm volatile("" : "+v"(boff[1])); qb1 = boff[1] + QW_TOP; asm volatile("" : "+v"(qb1)); }
        else if (k < NP) {
            if (k == 2) { vl = (unsigned)(lane * 16); asm volatile("" : "+v"(vl)); }
            if (k == 3) { tb = vl + M_TAB; asm volatile("" : "+v"(tb)); }
            if (k == 4) { b0a = tb | b0m; asm volatile("" : "+v"(b0a)); }
            if (k == 5) { xr = xrs + vl; asm volatile("" : "+v"(xr)); }
        } else if (k < NP + FN) fold(TL.f[(k - NP) >> 2], (k - NP) & 3);
        else {
            if (k == NP + FN) { xh_ = xhs + vl; asm volatile("" : "+v"(xh_)); }
            if (k == NP + FN + 1) { ninf = -INFINITY; asm volatile("" : "+v"(ninf)); }
        }
    };
    unsigned wa, wq;
    int wr2 = wr, wc2 = wc;
#pragma unroll
    for (int i = 0; i < NM; ++i) {
        mf(0, TL.t[0][i]);
        if (TL.t[0][i] < 24) asm volatile("" : "+v"(acc[TL.t[0][i] & 3][TL.t[0][i] >> 2])); else asm volatile("" : "+v"(ah[TL.t[0][i] & 1]));
        FENCE();
        if (i < NH) frd(1, i, 0);
        if (!HALO && i == NH + 3) { asm volatile("" : "+s"(wr2), "+s"(wc2)); vl = (unsigned)(lane * 16); wq = (unsigned)(AT_Q + (wc2 * 8 + wr2 * 4) * 1024) + vl; asm volatile("" : "+v"(vl), "+v"(wq)); }
        if (!HALO && i == NH + 4) { asm volatile("" : "+s"(wr2), "+s"(wc2)); wa = (unsigned)(AT_OWN + wc2 * 16384 + wr2 * 4096) + vl; asm volatile("" : "+v"(wa)); }
        if (i == NH + 1) { asm volatile("" : "+v"(boff[0]), "+v"(boff[1])); pb0 = boff[0] + 2 * QW_SLOT; pb1 = boff[1] + 2 * QW_SLOT; asm volatile("" : "+v"(pb0), "+v"(pb1)); }
        if (i == NH + 2) { pa0 = aoff0; pa1 = aoff0 ^ xm; asm volatile("" : "+v"(pa1)); if (HALO) { ph0 = hoff0; ph1 = hoff0 ^ xm; asm volatile("" : "+v"(ph1)); } }
        SCHED();
    }
    WAIT_L0();
    BAR();
    FENCE();
    SCHED();
    auto frags = [&](int h) {
        static_assert(TL.d[0][NM] == NH + 6 && TL.d[1][NM] == NH + (HALO ? 5 : 6) && TL.d[2][NM] == NH + (HALO ? 6 : 0) && TL.d[3][TL.n[3]] == NH + (HALO ? 4 : 0), "reads");
        if (h == 0) __builtin_amdgcn_s_waitcnt(0xC27F);
        if (h == 1) __builtin_amdgcn_s_waitcnt(HALO ? 0xC57F : 0xC67F);
        if (h == 2) __builtin_amdgcn_s_waitcnt(HALO ? 0xC67F : 0xC07F);
        if (h == 3) __builtin_amdgcn_s_waitcnt(HALO ? 0xC47F : 0xC07F);
    };
#pragma unroll
    for (int h = 0; h < 4; ++h) {
        if (h == 2) { pb0 = qb0; pb1 = qb1; }
#pragma unroll
        for (int i = 0; i < TL.n[h]; ++i) {
            if (!(h & 1) && i == TL.n[h] - 1) { frags(h); SCHED(); if (h) QSTAMP(4); }
            mf(~h & 1, TL.t[h][i]);
#pragma unroll
            for (int k = TL.d[h][i]; k < TL.d[h][i + 1]; ++k) { FENCE(); rdop(h, k); }
            SCHED();
#pragma unroll
            for (int k = TL.v[h][i]; k < TL.v[h][i + 1]; ++k) vop(k);
            SCHED();
        }
        if (h & 1) {
            frags(h);
            if (h == 1) QSTAMP(2); else QSTAMP(5);
            BAR();
            FENCE();
            SCHED();
            if (h == 1) QSTAMP(3); else QSTAMP(6);
        }
    }
    if (!HALO) {
        f32x4 r4[4];
        mf(1, 8);
#pragma unroll
        for (int mt = 0; mt < 4; ++mt) rr[mt] = r1(sa0, mt * 64 + RS);
        SCHED();
        mf(1, 12);
#pragma unroll
        for (int mt = 0; mt < 4; ++mt) r4[mt] = r4_(sa1, mt * 64 + RS);
        SCHED();
        mf(1, 9);
        vg = HK ? (unsigned)hv<HK>(lane) : vl;
        if (HK) asm volatile("" : "+v"(vg));
        SCHED();
        u32x4 pk[8];
        auto op = [&](int g, int k) {
            const int ta = g < 4 ? g : (g - 4) & 2, tb = g < 4 ? g : ta + 1, na = g < 4 ? 2 : 4 + (g & 1), nb = g < 4 ? 3 : na;
            f32x4& A = acc[ta][na]; f32x4& Bv = acc[tb][nb];
            if (k < 12) {
                const int h = k / 6, kk = k % 6;
                f32x4& X = h ? Bv : A;
                const int tx = h ? tb : ta;
                if (kk < 4) { float t = X[kk] * (g < 4 ? rr[tx] : r4[tx][kk]); asm volatile("" : "+v"(t)); X[kk] = t; }
                else { unsigned t = cvt_pk_bf16(X[2 * (kk - 4)], X[2 * (kk - 4) + 1]); asm volatile("" : "+v"(t)); pk[g][2 * h + kk - 4] = t; }
            } else if (k == 12) __builtin_amdgcn_raw_buffer_store_b128(pk[g], rso, vg + hp<HK>(g & 3), (g >> 2) * (8192 << HK), 16);
            else *(LAS u32x4*)(lds(wa) + (g >> 2) * 8192 + (g & 3) * 1024) = pk[g];
            SCHED();
        };
        auto pm = [&](int i) {
            if (i < 16) mf(1, TL.h3[i]);
            else if (i < 32) mf(((i - 16) >> 1) & 1, ((i - 16) >> 2) + 4 * (i & 1));
        };
        pm(3);
#pragma unroll
        for (int g = 0; g < 8; ++g) {
#pragma unroll
            for (int k = 0; k < 6; ++k) op(g, k);
            pm(2 * g + 4);
            if (g > 0) op(g - 1, 12);
#pragma unroll
            for (int k = 6; k < 12; ++k) op(g, k);
            pm(2 * g + 5);
            if (g > 0 && g <= LWA) op(g - 1, 13);
        }
        op(7, 12);
        QSTAMP(7);
        u32x4 qk;
        auto qop = [&](int mt, int k) {
            if (k == 0) { rr[mt] *= QSCALE; asm volatile("" : "+v"(rr[mt])); }
            else if (k < 13) {
                const int h = (k - 1) / 6, kk = (k - 1) % 6;
                f32x4& X = acc[mt][h];
                if (kk < 4) { float t = X[kk] * rr[mt]; asm volatile("" : "+v"(t)); X[kk] = t; }
                else { unsigned t = cvt_pk_bf16(X[2 * (kk - 4)], X[2 * (kk - 4) + 1]); asm volatile("" : "+v"(t)); qk[2 * h + kk - 4] = t; }
            } else *(LAS u32x4*)(lds(wq) + mt * 1024) = qk;
            SCHED();
        };
#pragma unroll
        for (int mt = 0; mt < 3; ++mt) {
            pm(20 + 4 * mt); if (3 * mt + LWA < 8) op(3 * mt + LWA, 13); qop(mt, 0); qop(mt, 1); qop(mt, 2);
            pm(21 + 4 * mt); if (3 * mt + 1 + LWA < 8) op(3 * mt + 1 + LWA, 13); qop(mt, 3); qop(mt, 4); qop(mt, 5);
            pm(22 + 4 * mt); if (3 * mt + 2 + LWA < 8) op(3 * mt + 2 + LWA, 13); qop(mt, 6); qop(mt, 7); qop(mt, 8);
            pm(23 + 4 * mt); qop(mt, 9); qop(mt, 10); qop(mt, 11); qop(mt, 12); qop(mt, 13);
        }
        if (c.wid == 0) STAMP_AT(l * 8 + 1);
        QSTAMP(8);
        {
            const u32x2 q0 = pack4(acc[3][0] * (rr[3] * QSCALE)), q1 = pack4(acc[3][1] * (rr[3] * QSCALE));
            *(LAS u32x4*)(lds(wq) + 3 * 1024) = (u32x4){q0[0], q0[1], q1[0], q1[1]};
        }
        QSTAMP(9);
        QSTORE();
        if (K0E) {
            SCHED();
            WAIT_L0();
            BAR();
            FENCE();
            SCHED();
        }
    } else {
        typedef unsigned u32x10 __attribute__((ext_vector_type(10)));
        u32x4 kp[4], qp[4];
        u32x10 vw[2];
        float ks[2][4], cs[2][4];
        auto pk8 = [&](const f32x4& x, const f32x4& y) { const u32x2 a = pack4(x), b = pack4(y); u32x4 r = {a[0], a[1], b[0], b[1]}; asm volatile("" : "+v"(r)); SCHED(); return r; };
        auto pkv = [&](int mt, int cc) { const u32x2 a = pack4(acc[mt][4 + cc]); vw[cc][2 * mt + 2] = a[0]; vw[cc][2 * mt + 3] = a[1]; };
        auto fr8 = [](const u32x4& x) { return __builtin_bit_cast(bf16x8, x); };
        mf(1, 24);
        unsigned ka0 = ko[0] + wr * 256, ka1 = ko[1] + wr * 256;
        asm volatile("" : "+v"(ka0), "+v"(ka1));
        SCHED();
        mf(1, 25);
#pragma unroll
        for (int mt = 0; mt < 4; ++mt) ks[0][mt] = r1(ka0, mt * 64);
        rr[0] *= QSCALE; asm volatile("" : "+v"(rr[0]));
        SCHED();
        mf(1, 11);
#pragma unroll
        for (int mt = 0; mt < 4; ++mt) ks[1][mt] = r1(ka1, mt * 64);
        rr[1] *= QSCALE; asm volatile("" : "+v"(rr[1]));
        SCHED();
        mf(1, 15);
        rr[2] *= QSCALE; asm volatile("" : "+v"(rr[2]));
        rr[3] *= QSCALE; asm volatile("" : "+v"(rr[3]));
        SCHED();
        mf(1, 19);
        const u32x4 hh = pk8(ah[0], ah[1]);
        mf(1, 23);
        *(LAS u32x2*)lds(xh_) = (u32x2){hh[0], hh[1]}; *(LAS u32x2*)(lds(xh_) + 1024) = (u32x2){hh[2], hh[3]};
        SCHED();
        mf(1, 10);
        kp[3] = pk8(acc[3][2], acc[3][3]);
        mf(1, 14);
        *(LAS u32x4*)(lds(xr) + 2048) = kp[3];
        pkv(3, 0); asm volatile("" : "+v"(vw[0]));
        *(LAS u32x2*)(lds(xr) + 3072) = (u32x2){vw[0][8], vw[0][9]};
        SCHED();
        mf(1, 3);
        pkv(3, 1); asm volatile("" : "+v"(vw[1]));
        *(LAS u32x2*)(lds(xr) + 3072 + 8) = (u32x2){vw[1][8], vw[1][9]};
        SCHED();
        mf(1, 7);
        mf(1, 9);
        kp[2] = pk8(acc[2][2], acc[2][3]);
        mf(1, 13);
        qp[3] = pk8(acc[3][0], acc[3][1]);
        mf(1, 2);
        f32x4 s0[4], s1[4];
        u32x4 pk;
        auto sc = [&](int mt, int h) {
            if (h) { s1[mt] = MFMA(fr8(kp[mt]), fr8(qp[mt]), bcur); asm volatile("" : "+v"(s1[mt])); }
            else { s0[mt] = MFMA(fr8(mt ? kp[mt - (mt > 0)] : pk), fr8(qp[mt]), mt == 0 ? bprev0 : bprev); asm volatile("" : "+v"(s0[mt])); }
            SCHED();
        };
        auto csm = [&](int mt) { cs[0][mt] = ks[0][mt] * rr[mt]; asm volatile("" : "+v"(cs[0][mt])); cs[1][mt] = ks[1][mt] * rr[mt]; asm volatile("" : "+v"(cs[1][mt])); };
        float u[2][4], mx[4], iv[4];
        auto sv = [](float x) { asm volatile("" : "+v"(x)); return x; };
        auto mx2 = [&](float a, float b) { return max3(ninf, a, b); };
        auto pick = [&](int mt, int h) { u[h][mt] = sv(max3(mx2(s0[mt][2 * h], s0[mt][2 * h + 1]), s1[mt][2 * h], s1[mt][2 * h + 1]) * cs[h][mt]); };
        auto vf = [&](int i) { const int mt = 2 - (i >> 2), nt = 4 + (i & 1), set = (i >> 1) & 1; SCHED(); acc[mt][nt] = MFMA(xa[set][mt], wb[set][nt], acc[mt][nt]); asm volatile("" : "+v"(acc[mt][nt])); SCHED(); };
        mf(1, 6);
        kp[1] = pk8(acc[1][2], acc[1][3]);
        mf(1, 8);
        WAIT_L0();
        QSTAMP(7);
        BAR();
        FENCE();
        SCHED();
        QSTAMP(8);
        mf(1, 12);
        {
            const u32x2 k0 = *(const LAS u32x2*)(lds(xr) + 8);
            FENCE(); SCHED();
            const u32x2 k1 = *(const LAS u32x2*)lds(xr);
            FENCE(); SCHED();
            const u32x2 a = *(const LAS u32x2*)(lds(xr) + 1024 + 8);
            FENCE(); SCHED();
            const u32x2 b = *(const LAS u32x2*)(lds(xr) + 1024);
            pk = (u32x4){k0[0], k0[1], k1[0], k1[1]};
            vw[0][0] = a[0]; vw[0][1] = a[1]; vw[1][0] = b[0]; vw[1][1] = b[1];
        }
        SCHED();
        auto sw16 = [](float& a, float& b) { auto r = __builtin_amdgcn_permlane16_swap(__float_as_uint(a), __float_as_uint(b), false, false); a = __uint_as_float(r[0]); b = __uint_as_float(r[1]); };
        auto sw32 = [](float& a, float& b) { auto r = __builtin_amdgcn_permlane32_swap(__float_as_uint(a), __float_as_uint(b), false, false); a = __uint_as_float(r[0]); b = __uint_as_float(r[1]); };
        auto ldr = [&](int i) { };
        auto top = [&](int mt) { mx[mt] = sv(mx2(u[0][mt], u[1][mt])); };
        auto pv1 = [&](int mt, int k) {
            SCHED();
            vw[k >> 1][2 * mt + 2 + (k & 1)] = cvt_pk_bf16(acc[mt][4 + (k >> 1)][2 * (k & 1)], acc[mt][4 + (k >> 1)][2 * (k & 1) + 1]);
            asm volatile("" : "+v"(vw[k >> 1]));
            SCHED();
        };
        auto uk = [&](int mt) { SCHED(); u[0][mt] = sv(u[0][mt] * ks[0][mt]); u[1][mt] = sv(u[1][mt] * ks[1][mt]); SCHED(); };
        mf(1, 1);
        qp[2] = pk8(acc[2][0], acc[2][1]);
        mf(1, 5);
        csm(3); SCHED();
        mf(1, 0);
        kp[0] = pk8(acc[0][2], acc[0][3]);
        mf(1, 4);
        csm(2); SCHED();
        sc(3, 1);
        qp[1] = pk8(acc[1][0], acc[1][1]);
        sc(3, 0);
        csm(1); ldr(0); SCHED();
        sc(2, 1);
        qp[0] = pk8(acc[0][0], acc[0][1]);
        sc(2, 0);
        csm(0); pick(3, 0); SCHED();
        sc(1, 1);
        pick(3, 1); ldr(1); SCHED();
        sc(1, 0);
        pick(2, 0); top(3); SCHED();
        sc(0, 1);
        pick(2, 1); ldr(2); SCHED();
        sc(0, 0);
        if (c.wid == 0) STAMP_AT(l * 8 + 1);
        pick(1, 0); top(2);
        vf(0);
        pick(1, 1); ldr(3);
        vf(1);
        sw16(mx[2], mx[3]);
        pick(0, 0);
        vf(2);
        pick(0, 1); top(1);
        vf(3);
        top(0); mx[2] = sv(mx2(mx[2], mx[3]));
        vf(4);
        sw16(mx[0], mx[1]); mx[0] = sv(mx2(mx[0], mx[1]));
        vf(5); pv1(2, 0);
        sw32(mx[0], mx[2]);
        { const float a = mx[0], b = mx[2]; mx[0] = sv(mx2(a, b)); mx[2] = sv(mx2(b, a)); }
        vf(6); pv1(2, 1);
        sw32(mx[0], mx[2]);
        mx[3] = sv(mx[2]); mx[1] = sv(mx[0]);
        vf(7);
        sw16(mx[2], mx[3]); sw16(mx[0], mx[1]);
        SCHED();
#pragma unroll
        for (int mm = 0; mm < 4; ++mm) { const int mt = 3 - mm; u[0][mt] = sv(u[0][mt] - mx[mt]); u[1][mt] = sv(u[1][mt] - mx[mt]); if (mm == 1) vf(8); }
        vf(9);
#pragma unroll
        for (int mm = 0; mm < 4; ++mm) {
            const int mt = 3 - mm;
            u[0][mt] = sv(__builtin_amdgcn_exp2f(u[0][mt])); u[1][mt] = sv(__builtin_amdgcn_exp2f(u[1][mt]));
            if (mm > 0) iv[mt + 1] = sv(u[0][mt + 1] + u[1][mt + 1]);
            if (mm < 2) vf(10 + mm);
        }
        SCHED();
        sw16(iv[2], iv[3]);
        iv[0] = sv(u[0][0] + u[1][0]);
        iv[2] = sv(iv[2] + iv[3]);
        pv1(2, 2);
        sw16(iv[0], iv[1]); iv[0] = sv(iv[0] + iv[1]);
        uk(3);
        sw32(iv[0], iv[2]);
        iv[0] = sv(iv[0] + iv[2]);
        pv1(2, 3);
        iv[0] = sv(__builtin_amdgcn_rcpf(iv[0]));
        pv1(1, 0);
        iv[2] = sv(iv[0]);
        uk(2);
        sw32(iv[0], iv[2]);
        iv[3] = sv(iv[2]); iv[1] = sv(iv[0]);
        pv1(1, 1);
        sw16(iv[2], iv[3]); sw16(iv[0], iv[1]);
        SCHED();
        f32x4 o[4][2];
        u32x4 pf[4];
        auto nrm = [&](int mt) { u[0][mt] = sv(u[0][mt] * iv[mt]); u[1][mt] = sv(u[1][mt] * iv[mt]); SCHED(); };
        auto prb = [&](int mt) {
            const unsigned p = cvt_pk_bf16(u[0][mt], u[1][mt]);
            pf[mt] = (u32x4){__builtin_amdgcn_perm(0u, p, ps[0]), __builtin_amdgcn_perm(0u, p, ps[1]), __builtin_amdgcn_perm(0u, p, ps[2]), __builtin_amdgcn_perm(0u, p, ps[3])};
            asm volatile("" : "+v"(pf[mt]));
            SCHED();
        };
        auto pvm = [&](int mt, int cc) {
            o[mt][cc] = MFMA(fr8((u32x4){vw[cc][2 * mt], vw[cc][2 * mt + 1], vw[cc][2 * mt + 2], vw[cc][2 * mt + 3]}), fr8(pf[mt]), ((f32x4){0.f, 0.f, 0.f, 0.f}));
            asm volatile("" : "+v"(o[mt][cc]));
            SCHED();
        };
        auto out = [&](int mt) {
            const u32x2 lo = pack4(o[mt][0]), hi = pack4(o[mt][1]);
            __builtin_amdgcn_raw_buffer_store_b128((u32x4){lo[0], lo[1], hi[0], hi[1]}, rso, vg + hp<HO>(mt), 0, 16);
            SCHED();
        };
        vg = (unsigned)hv<HO>(lane);
        asm volatile("" : "+v"(vg));
        nrm(3); nrm(2); prb(3);
        pv1(1, 2); pv1(1, 3);
        pvm(3, 0); prb(2); pvm(3, 1);
        uk(1); uk(0);
        pvm(2, 0); nrm(1); nrm(0); pvm(2, 1);
        prb(1); pv1(0, 0); pv1(0, 1);
        pvm(1, 0); pv1(0, 2); pv1(0, 3); prb(0);
        pvm(1, 1); out(3); pvm(0, 0); out(2); pvm(0, 1); out(1); out(0);
        SCHED();
        QSTAMP(9);
        if (c.wid == 0) STAMP_AT(l * 8 + 2);
        QSTORE();
    }
}

#define MFMA32(a, b, c) __builtin_amdgcn_mfma_f32_32x32x16_bf16(__builtin_bit_cast(bf16x8, a), __builtin_bit_cast(bf16x8, b), (c), 0, 0, 0)
DI float max16(const f32x16& s) {
    float m = max3(s[0], s[1], s[2]), n = max3(s[3], s[4], s[5]);
    m = max3(m, s[6], s[7]); n = max3(n, s[8], s[9]);
    m = max3(m, s[10], s[11]); n = max3(n, s[12], s[13]);
    m = max3(max3(m, s[14], s[15]), n, n);
    auto b = __builtin_amdgcn_permlane32_swap(__float_as_uint(m), __float_as_uint(m), false, false);
    return __builtin_amdgcn_fmed3f(__uint_as_float(b[0]), __uint_as_float(b[1]), INFINITY);
}
template <bool HI> DI void attn_a(const Ctx& c0, int l, const char* Wo, KP kp) {
    const Ctx c = fresh(c0);
    asm volatile("" : "+s"(Wo));
    LAS char* smem = c.smem;
    const int lane = c.lane, w = c.wid;
    constexpr int H = HI ? 0 : 1;
    int qt = HI ? w - 4 : 3 - w;
    asm volatile("" : "+s"(qt));
    const float NEG = -INFINITY;
    int zq = (lane & 31) - 4 * (lane >> 5);
    unsigned lb = (unsigned)(((lane >> 4) & 1) * 1024 + ((lane >> 5) * 16 + (lane & 15)) * 16);
    unsigned qa = (unsigned)(AT_Q + H * 8192 + qt * 2048) + lb, ta = (unsigned)(AT_OWN + H * 16384 - 3 * 2048 + qt * 2048) + lb;
    asm volatile("" : "+v"(lb), "+v"(qa), "+v"(ta));
    auto rd = [&](unsigned base, int off) { return *(const LAS u32x4*)(lds(base) + off); };
    f32x16 ce;
    u32x4 sel;
    unsigned tm = (unsigned)(AT_N + lane * 16);
    auto masks = [&]() {
        if (HI) {
#pragma unroll
            for (int r = 0; r < 16; ++r) ce[r] = 8 * (r >> 2) + (r & 3) <= zq ? 0.f : NEG;
            asm volatile("" : "+v"(ce));
        }
        const unsigned s1 = ((lane >> 4) ^ (lane >> 2)) & 1 ? 0u : 0x3f803f80u;
        sel = (u32x4){s1, s1, s1, s1};
        asm volatile("" : "+v"(sel));
    };
    u64 ep, pa_, kvv, nkv, kact;
    unsigned po, pk_, cnta = M_CNT, one = 1u;
    char* ob;
    const char *kvb, *s1_, *s2_;
    unsigned vk, dst, fo;
    auto setup = [&]() {
        if (HI) {
            const u32x2 e = *(const LAS u32x2*)(smem + M_EP);
            ep = ((u64)(unsigned)__builtin_amdgcn_readfirstlane(e[1]) << 32) | (unsigned)__builtin_amdgcn_readfirstlane(e[0]);
            po = o_post(c);
            pk_ = pick(lane < 2, (unsigned)(WS_KBOX + ((c.pq + 1 + lane) * 16 + c.jq) * 256 + lane * 8), (unsigned)(WS_FLAGQ + (c.pq * 16 + c.jq) * FQS));
            pa_ = c.pq < 14 ? 7ull : (c.pq < 15 ? 5ull : 4ull);
            fo = (unsigned)(WS_KBOX + (c.pq * 16 + c.jq) * 256 + lane * 8);
            asm volatile("" : "+v"(po), "+v"(pk_), "+v"(fo));
            asm volatile("" : "+s"(ep), "+s"(pa_));
        }
        ob = c.ws + WS_O + (size_t)l * (8u << 20) + (size_t)(unsigned)(((c.jq * 2 + H) * 16 + c.pq) * 8192 + qt * 2048);
        kvb = c.ws + WS_KV + (size_t)(unsigned)((l >> 1) * (16u << 20) + c.jq * 32768);
        asm volatile("" : "+s"(ob), "+s"(kvb));
    };
    auto setup2 = [&]() {
        const int p1 = c.pq >= 1 ? c.pq - 1 : c.pq, p2 = c.pq >= 2 ? c.pq - 2 : c.pq;
        s1_ = kvb + (size_t)(unsigned)(p1 * 524288);
        s2_ = kvb + (size_t)(unsigned)(p2 * 524288);
        vk = (unsigned)((w & 3) * 1024 + lane * 16); dst = (unsigned)(AT_N + (w & 3) * 1024);
        asm volatile("" : "+s"(s1_), "+s"(dst));
        if (!HI) asm volatile("" : "+s"(s2_));
        asm volatile("" : "+v"(vk));
    };
    auto arrive = [&](auto cc, unsigned msk, unsigned val) {
        unsigned old = 0;
        if (lane == 0) asm volatile("ds_add_rtn_u32 %0, %1, %2 offset:%3\n\ts_waitcnt lgkmcnt(0)" : "+v"(old) : "v"(cnta), "v"(one), "i"(decltype(cc)::value * 4) : "memory");
        if ((__builtin_amdgcn_readfirstlane(old) & msk) == msk) { if (lane < 16) post(c.ws, po, ep + (4 * l + val)); }
    };
    static_assert((MBOX & 2) && !HO && !HK, "layout");
    int tv = 0;
    auto kvs = [&]() {
        if (!HI) {
            SCHED();
            LSTAMP(2);
            asm volatile("s_mov_b64 exec, 1\n\ts_waitcnt vmcnt(0)\n\tds_add_u32 %0, %1\n\ts_mov_b64 exec, -1" :: "v"(cnta), "v"(one) : "memory");
            LSTAMP(3);
            SCHED();
        }
    };
    asm volatile("" : "+v"(cnta), "+v"(one), "+v"(tm));
    if (HI) {
        masks(); setup(); setup2();
        if (w == 5) {
#pragma unroll
            for (int i = 0; i < 4; ++i) *(LAS f32x4*)(lds(tm) + i * 1024) = (f32x4){ce[4 * i], ce[4 * i + 1], ce[4 * i + 2], ce[4 * i + 3]};
        }
        kvv = ep + (4 * l + 2); nkv = 0 - kvv;
        kact = __builtin_amdgcn_ballot_w64(w == KPW && lane < 2 && lane < c.pq);
        asm volatile("" : "+v"(kvv), "+v"(nkv));
        asm volatile("" : "+s"(kact));
        WAIT_L0();
        WAIT_V0();
        LSTAMP(0);
        if (w == 4) __builtin_amdgcn_s_setprio(HP4);
        BAR();
        FENCE();
    }
    auto kvp = [&]() {
        if (HI && w == 4) {
            __builtin_amdgcn_s_setprio(3);
#pragma unroll 1
            for (unsigned spins = 0; spins < (1u << 24); ++spins) {
                if (__builtin_amdgcn_readfirstlane(*(volatile LAS int*)lds(cnta)) == 8 * l + 4) break;
            }
            if (__builtin_amdgcn_inverse_ballot_w64(pa_)) post(c.ws, pk_, kvv);
            STAMP_AT(l * 8 + 2);
            __builtin_amdgcn_s_setprio(HP4);
        }
        SCHED();
    };
    if (KPP == -1) kvp();
    FENCE();
    SCHED();
    LSTAMP(1);
    u32x4 q0, q1, k0, k1, va, vp, pa, pb;
    f32x16 cm, s0, s1, o;
    f32x4 ls;
    auto stage = [&](f32x16& sc, f32x16& sn, const f32x16& cn, bool pend, bool zero, unsigned kb, int ko, unsigned vb, int vo1, unsigned nb, int vo0) {
        auto ex = [&](int i) { float x = __builtin_amdgcn_exp2f(sc[i]); asm volatile("" : "+v"(x)); sc[i] = x; SCHED(); };
        auto cv = [&](u32x4& p, int j, int i) { unsigned x = cvt_pk_bf16(sc[i], sc[i + 1]); asm volatile("" : "+v"(x)); p[j] = x; SCHED(); };
        sn = MFMA32(k0, q0, cn); SCHED();
        k0 = rd(kb, ko); SCHED();
        ex(0); ex(1); ex(2);
        if (pend) { o = MFMA32(vp, pb, o); SCHED(); }
        ex(3); ex(8); ex(9);
        sn = MFMA32(k1, q1, sn); SCHED();
        k1 = rd(kb, ko + 512); SCHED();
        ex(10); ex(11); cv(pa, 0, 0); cv(pa, 1, 2);
        if (pend) { ls = MFMA(__builtin_bit_cast(bf16x8, sel), __builtin_bit_cast(bf16x8, pb), ls); SCHED(); }
        vp = rd(vb, 8192 + vo1 + 512); SCHED();
        cv(pa, 2, 8); cv(pa, 3, 10); ex(4); ex(5);
        o = MFMA32(va, pa, zero ? (f32x16)(0.f) : o); SCHED();
        va = rd(nb, 8192 + vo0); SCHED();
        ex(6); ex(7); ex(12);
        ls = MFMA(__builtin_bit_cast(bf16x8, sel), __builtin_bit_cast(bf16x8, pa), zero ? (f32x4)(0.f) : ls); SCHED();
        ex(13); ex(14); ex(15); cv(pb, 0, 4); cv(pb, 1, 6); cv(pb, 2, 12); cv(pb, 3, 14);
    };
    auto flush = [&]() {
        ls = MFMA(__builtin_bit_cast(bf16x8, sel), __builtin_bit_cast(bf16x8, pb), ls); SCHED();
        o = MFMA32(vp, pb, o); SCHED();
    };
    float nm;
    {
        if (!HI) {
#pragma unroll
            for (int i = 0; i < 4; ++i) {
                const f32x4 t = *(const LAS f32x4*)(lds(tm) + i * 1024);
                ce[4 * i] = t[0]; ce[4 * i + 1] = t[1]; ce[4 * i + 2] = t[2]; ce[4 * i + 3] = t[3];
            }
        }
        q0 = rd(qa, 0);
        const u32x4 d0 = rd(ta, 6144);
        q1 = rd(qa, 512);
        const u32x4 d1 = rd(ta, 6144 + 512);
        k0 = rd(ta, 4096); k1 = rd(ta, 4096 + 512); va = rd(ta, 8192 + 6144);
        SCHED();
        if (KSP == -1) kvs();
        if (!HI) { masks(); setup(); SCHED(); }
        s0 = MFMA32(d0, q0, ce);
        s0 = MFMA32(d1, q1, s0);
        SCHED();
        if (!HI) { setup2(); SCHED(); }
        if (KSP == 0) kvs();
        if (KPP == 0) kvp();
        nm = -max16(s0);
        asm volatile("" : "+v"(nm));
#pragma unroll
        for (int r = 0; r < 16; ++r) { float t; asm("v_mov_b32 %0, %1" : "=v"(t) : "v"(nm), "i"(r)); cm[r] = t; }
        asm volatile("" : "+v"(cm));
        SCHED();
#pragma unroll
        for (int r = 0; r < 16; ++r) s0[r] = addf(s0[r], nm);
        asm volatile("" : "+v"(s0));
        SCHED();
        if (KSP == 1) kvs();
        if (KPP == 1) kvp();
        stage(s0, s1, cm, false, true, ta, 2048, ta, 6144, ta, 4096);
        if (KSP == 2) kvs();
        if (KPP == 2) kvp();
        if (qt > 0) {
            stage(s1, s0, cm, true, false, ta, 0, ta, 4096, ta, 2048);
            if (KSP == 3) kvs();
            if (qt > 1) {
                stage(s0, s1, cm, true, false, ta, 0, ta, 2048, ta, 0);
                if (qt > 2) stage(s1, s0, cm, true, false, ta, 0, ta, 0, ta, 0);
            }
        } else if (KSP == 3) kvs();
        flush();
    }
    LSTAMP(4);
    f32x16 c2;
    unsigned na, nb;
    auto rso = __builtin_amdgcn_make_buffer_rsrc(ob, 0, 2048, 0x00020000);
    auto starts = [&]() {
        const float n1 = c.pq >= 1 ? nm : NEG, n2 = c.pq >= 2 ? nm : NEG;
        asm volatile("" : "+v"(zq));
#pragma unroll
        for (int r = 0; r < 16; ++r) { cm[r] = n1; c2[r] = n2; ce[r] = 8 * (r >> 2) + (r & 3) <= zq ? NEG : n2; }
        na = (unsigned)(AT_N + H * 32768) + lb; nb = na + (unsigned)(16384 + qt * 2048);
        asm volatile("" : "+v"(cm), "+v"(c2), "+v"(ce), "+v"(na), "+v"(nb));
        asm volatile("" : "+s"(rso));
        SCHED();
    };
    {
        auto piece = [&](int b, int i) { dma16<true>(dst + b * 16384 + i * 4096, vk, ((b & 1) ? s2_ : s1_) + (b >> 1) * 16384 + i * 4096); };
        auto block = [&](int b) {
#pragma unroll
            for (int i = 0; i < 4; ++i) piece(b, i);
        };
        Ask t;
        if (HI) {
            poll_rmw<true>(t, c.ws, fo, __builtin_amdgcn_inverse_ballot_w64(kact), nkv, 62 - 4 * l);
            if (w == KPW) STAMP_AT(32 + l * 8 + 1);
        }
        WAIT_L0();
        LSTAMP(5);
        BAR();
        FENCE();
        LSTAMP(6);
        piece(0, H); piece(0, 2 + H);
        if (HI) {
            starts();
            if (APRIO) __builtin_amdgcn_s_setprio(APRIO);
            WAIT_V0();
            keep(t);
            LSTAMP(7);
            BAR();
            FENCE();
            if (w == 4) STAMP_AT(l * 8 + 3);
        } else {
            if (AB2 >= 0) block(1);
            if (AB2 > 0) block(2);
            starts();
            Ctx c2 = c;
            if (ELO) c2.wid = 0;
            unsigned vw = wo_w_off(c2);
            int qs = c.cw == 3 ? 1024 : 8 * D_ * 2;
            asm volatile("" : "+v"(vw));
            asm volatile("" : "+s"(qs));
            SCHED();
            wait_v<4 * AB2 + 4>();
            LSTAMP(7);
            BAR();
            if (AB2 < 0) block(1);
            if (AB2 <= 0) block(2);
            block(3);
            wait_v<8>();
            BAR();
            if (!ELO) {
                wo_w_issue<WA0, WA1>(c, Wo, vw);
                wait_v<WA1 - WA0>();
            } else WAIT_V0();
            LSTAMP(9);
            BAR();
            FENCE();
            if (!ELO) wo_w_issue<WA1, 26>(c, Wo, vw);
            else {
                const int s0 = w == 0 ? WA0 : (w == 1 ? WA0 + EN0 : WA0 + EN0 + EN1), s1 = w == 0 ? WA0 + EN0 : (w == 1 ? WA0 + EN0 + EN1 : (w == 2 ? 22 : 0));
#pragma unroll 1
                for (int sl = s0; sl < s1; ++sl) {
                    const char* b = Wo + (size_t)(unsigned)((2 * (sl & 15) + (sl >> 4)) * 65536);
#pragma unroll
                    for (int q = 0; q < 4; ++q) dma16<true>(sl * 4096 + q * 1024, vw, b + q * qs);
                }
            }
            if (LSL) { for (int i = 0; i < qt; ++i) __builtin_amdgcn_s_sleep(LSL); }
        }
    }
    SCHED();
    LSTAMP(8);
    {
        const u32x4 d0 = rd(na, 0), d1 = rd(na, 512);
        k0 = rd(na, 2048); k1 = rd(na, 2048 + 512); va = rd(na, 8192);
        SCHED();
        s0 = MFMA32(d0, q0, cm);
        s0 = MFMA32(d1, q1, s0);
        SCHED();
        stage(s0, s1, cm, false, false, na, 4096, na, 0, na, 2048);
        stage(s1, s0, cm, true, false, na, 6144, na, 2048, na, 4096);
        if (HI) { BAR(); FENCE(); SCHED(); }
        stage(s0, s1, cm, true, false, nb, 0, na, 4096, na, 6144);
        if (HI && CPOS == 3) { BAR(); SCHED(); }
        stage(s1, s0, ce, true, false, nb, 2048, na, 6144, nb, 0);
        if (HI && CPOS == 4) { WAIT_L0(); BAR(); SCHED(); }
        stage(s0, s1, c2, true, false, nb, 4096, nb, 0, nb, 2048);
        if (HI && CPOS == 5) { BAR(); SCHED(); }
        if (qt < 3) {
            stage(s1, s0, c2, true, false, nb, 6144, nb, 2048, nb, 4096);
            if (qt < 2) {
                stage(s0, s1, c2, true, false, nb, 0, nb, 4096, nb, 6144);
                if (qt < 1) stage(s1, s0, c2, true, false, nb, 0, nb, 6144, nb, 0);
            }
        }
        flush();
    }
    LSTAMP(11);
    auto out = [&]() {
        const float iv = __builtin_amdgcn_rcpf(ls[0]);
#pragma unroll
        for (int u = 0; u < 2; ++u) {
            u32x4 t;
#pragma unroll
            for (int i = 0; i < 4; ++i) { const int r = 4 * u + (i >> 1) * 8 + (i & 1) * 2; t[i] = cvt_pk_bf16(o[r] * iv, o[r + 1] * iv); }
            __builtin_amdgcn_raw_buffer_store_b128(t, rso, lb + u * 512, 0, 16);
        }
        SCHED();
    };
    out();
    if (__builtin_expect(__any(!(ls[0] < SAFE_SUM)), 0)) {
        auto rk = __builtin_amdgcn_make_buffer_rsrc((void*)(kvb + H * 16384), 0, 16u << 20, 0x00020000);
        const int g0 = c.pq * 4 + qt - 8;
        float m = NEG;
#pragma unroll 1
        for (int ps = 0; ps < 2; ++ps) {
            o = (f32x16)(0.f); ls = (f32x4)(0.f);
#pragma unroll 1
            for (int j = g0 < 0 ? -g0 : 0; j < 9; ++j) {
                const int so = ((g0 + j) >> 2) * 524288 + ((g0 + j) & 3) * 2048;
                u32x4 f[4];
#pragma unroll
                for (int i = 0; i < 4; ++i) f[i] = __builtin_amdgcn_raw_buffer_load_b128(rk, lb + (i & 1) * 512, so + (i >> 1) * 8192, 16);
                f32x16 s;
                const float sb = ps ? -m : 0.f;
                float sa = j == 0 ? NEG : sb, sz = j == 8 ? NEG : sb;
                asm volatile("" : "+v"(zq), "+v"(sa), "+v"(sz));
#pragma unroll
                for (int r = 0; r < 16; ++r) s[r] = 8 * (r >> 2) + (r & 3) <= zq ? sa : sz;
                s = MFMA32(f[0], q0, s);
                s = MFMA32(f[1], q1, s);
                if (ps == 0) m = fmaxf(m, max16(s));
                else {
                    u32x4 p[2];
#pragma unroll
                    for (int r = 0; r < 16; r += 2) p[(r >> 2) & 1][(r >> 3) * 2 + ((r >> 1) & 1)] = cvt_pk_bf16(__builtin_amdgcn_exp2f(s[r]), __builtin_amdgcn_exp2f(s[r + 1]));
#pragma unroll
                    for (int u = 0; u < 2; ++u) {
                        o = MFMA32(f[2 + u], p[u], o);
                        ls = MFMA(__builtin_bit_cast(bf16x8, sel), __builtin_bit_cast(bf16x8, p[u]), ls);
                    }
                }
            }
        }
        __builtin_amdgcn_s_waitcnt(0x0F70);
        WAIT_V0();
        out();
    }
    SCHED();
    if (w == 3 || w == 4) STAMP_AT(32 + l * 8 + 3 + w);
    LSTAMP(12);
    if (HI) {
        WAIT_V0();
        arrive(std::integral_constant<int, 1>{}, 3u, 3u);
        if (w == 4) STAMP_AT(32 + l * 8 + 4);
        LSTAMP(13);
        if (APRIO) __builtin_amdgcn_s_setprio(0);
        if (lane == 0) asm volatile("ds_add_u32 %0, %1 offset:12" :: "v"(cnta), "v"(one) : "memory");
        WAIT_V0();
        arrive(std::integral_constant<int, 2>{}, 7u, 4u);
        if (w == 5 || w == 6) {
            Ctx c2 = c;
            c2.wid = 2 * w - 10;
            unsigned vw = wo_w_off(c2), vw2 = vw + (c.cw == 3 ? 1024u : 8u * D_ * 2);
            asm volatile("" : "+v"(vw), "+v"(vw2));
#pragma unroll 1
            for (unsigned spins = 0; spins < (1u << 22); ++spins) {
                if (__hip_atomic_load((LAS unsigned*)(smem + M_CNT + 12), __ATOMIC_RELAXED, __HIP_MEMORY_SCOPE_WORKGROUP) == (unsigned)(2 * l + 4)) break;
                __builtin_amdgcn_s_sleep(1);
            }
            FENCE();
            wo_w_issue<ELO ? 22 : 26, 30>(c2, Wo, vw);
            c2.wid += 1;
            wo_w_issue<ELO ? 22 : 26, 30>(c2, Wo, vw2);
        }
    }
    LSTAMP(23);
    ASTORE();
}

DI void wo_load(const Ctx& c0, int l, const char* Wn, const char* Wo) {
    const Ctx c = fresh(c0);
    asm volatile("" : "+s"(Wn), "+s"(Wo));
    const u64 ep = lds_epoch(c.smem);
    const unsigned fo = o_find(c);
        if (l == 0 && c.wid == 6) {
            const int fr = c.lane & 15, od = fr & 1, j0 = 4 * (c.lane >> 4) + od;
            const bool p0 = j0 > fr, p1 = j0 + 2 > fr;
            const unsigned N = 0xff800000u, Z = 0x0c0c0c0cu, lo = od ? 0x01000c0cu : 0x0c0c0100u, hi = od ? 0x03020c0cu : 0x0c0c0302u;
            LAS u32x4* t = (LAS u32x4*)(c.smem + M_TAB + c.lane * 16);
            t[0] = (u32x4){p0 && !od ? 0u : N, p0 && od ? 0u : N, p1 && !od ? 0u : N, p1 && od ? 0u : N};
            t[64] = (u32x4){!p0 && !od ? 0u : N, !p0 && od ? 0u : N, !p1 && !od ? 0u : N, !p1 && od ? 0u : N};
            t[128] = (u32x4){p0 ? lo : Z, p1 ? hi : Z, p0 ? Z : lo, p1 ? Z : hi};
            t[192] = (u32x4){(unsigned)(M_RSTD + j0 * 4 + (p0 ? 0 : 64)), (unsigned)(M_RSTD + j0 * 4 + 8 + (p1 ? 0 : 64)), 0u, 0u};
        }
    if (CPY && l == 0 && c.wid == CKW) {
#pragma unroll 1
        for (int i = 0; i < 2; ++i) {
            const int j = i ? c.jw : c.jq, g = i ? 2 - 2 * (j >> 3) : 1 + 2 * (j >> 3);
            if ((j & 7) == 3) poll_flags(c.ws, ep, (unsigned)WS_FLAGQ + ((c.lane >> 2) * 16 + 4 * g + (c.lane & 3)) * FQS, true, 2u);
        }
    }
    Ask t, t2;
    if (!(l & 1)) {
        Ctx c2 = c;
        c2.wid = 2 * c.wid - 10;
        unsigned vw = wo_w_off(c2), vw2 = vw + (c.cw == 3 ? 1024u : 8u * D_ * 2);
        asm volatile("" : "+v"(vw), "+v"(vw2));
        poll_rmw(t, c.ws, fo, c.wid == 7 && c.lane < 16, ep + (4 * l + 3), 61 - 4 * l);
        if (c.wid == 7) STAMP_AT(32 + l * 8 + 2);
        BAR();
        if (c.wid == 5 || c.wid == 6) {
            __builtin_amdgcn_s_sleep(W30S);
            wo_w_issue<30, 32>(c2, Wo, vw);
            c2.wid += 1;
            wo_w_issue<30, 32>(c2, Wo, vw2);
        }
        if (c.wid != 7) WAIT_V0();
    } else {
        asm volatile("" : "=v"(t.v[0]), "=v"(t.v[1]), "=v"(t.v[2]), "=v"(t.v[3]));
        WAIT_V0();
    }
    poll_rmw(t2, c.ws, fo, c.wid == 7 && c.lane < 16, ep + (4 * l + 4), 60 - 4 * l);
    if (c.wid == 7) STAMP_AT(32 + l * 8 + 2 + !(l & 1));
    BAR();
    WAIT_V0();
    keep(t); keep(t2);
    if (l == 3 && c.bid == 0 && c.wid == 7) {
#pragma unroll 1
        for (int i = 0; i < 4; ++i) poll_flags(c.ws, ep, (unsigned)WS_FLAGQ + (i * 64 + c.lane) * FQS, true, 1u);
        if (c.lane == 0) __hip_atomic_store((GAS u64*)(c.ws + WS_EPOCH), ep + 64, __ATOMIC_RELAXED, __HIP_MEMORY_SCOPE_AGENT);
    }
    if (RS0 && l == 3) BAR();
}

DI void wo_consume(const bool B, const Ctx& c0, const bf16_t* xin, bf16_t* xout, int l) {
    u32x4 res[2][2];
    const Ctx c = fresh(c0);
    LAS char* smem = c.smem;
    const int lane = c.lane, w = c.wid, fr = lane & 15, fq = lane >> 4;
    const char* obase = c.ws + WS_O + (size_t)l * (8u << 20);
    int ovo = (c.pw * 8 + w * 2) * 1024 + lane * 16;
    asm volatile("" : "+v"(ovo));
    constexpr int NP = WNQ, R = 3;
    static_assert(!HO && !HX && !HS && !RLD && BPR == 1, "layout");
    auto ro = __builtin_amdgcn_make_buffer_rsrc((void*)obase, 0, 4u << 20, 0x00020000);
    asm volatile("" : "+s"(ro));
    u32x4 xa[NP][2];
    s16x4 wf[R][8];
    f32x4 acc[2][4];
    unsigned bw[8];
    int coff, rmo, spr;
    u32x4 on = {0x3f803f80u, 0x3f803f80u, 0x3f803f80u, 0x3f803f80u};
    unsigned sto, xpo, coff1, cnta = M_CNT, one = 1;
    u64 xv, b0, b1, am;
    auto rs = __builtin_amdgcn_make_buffer_rsrc(xout, 0, S_ * D_ * 2, 0x00020000);
    auto rst = __builtin_amdgcn_make_buffer_rsrc(c.ws + WS_STATS + (size_t)((l + 1) & 3) * 524288, 0, 524288, 0x00020000);
    auto prep2 = [&]() {
        sto = fq == (fr >> 2) && l < 3 ? (unsigned)((c.pw * 16 + c.jw) * 1024 + (w * 32 + fr) * 8) : 0x80000000u;
        xpo = x_post(c);
        coff = (int)pick(l == 3, (unsigned)rmo, (unsigned)coff);
        coff1 = coff + (l == 3 ? xrow<0>(0, 16) : 2048);
        spr = l == 3 ? 64 : 1024;
        asm volatile("" : "+s"(spr));
        b0 = __builtin_amdgcn_ballot_w64(fr & 1); b1 = __builtin_amdgcn_ballot_w64(fr & 2); am = __builtin_amdgcn_ballot_w64(x_act(c, !B));
        asm volatile("" : "+v"(on), "+v"(sto), "+v"(xpo), "+v"(coff), "+v"(coff1), "+v"(xv), "+v"(cnta), "+v"(one));
        asm volatile("" : "+s"(rs), "+s"(rst), "+s"(b0), "+s"(b1), "+s"(am));
    };
    auto prep = [&]() {
        int ln = lane;
        asm volatile("" : "+v"(ln));
        const int fr = ln & 15, fq = ln >> 4, q4 = fr >> 2, p4 = ln & 3;
        coff = (c.pw * 16 + c.jw) * 16384 + w * 4096 + (((fq & 1) * 2 + (fq >> 1)) * 16 + fr) * 16;
        rmo = xrow<0>(c.pw, w * 32 + fr) + xcol<0>(c.jw) + (fq & 1) * 32 + (fq >> 1) * 16;
        asm volatile("" : "+v"(coff), "+v"(rmo));
        const int fb = (q4 & 2) | ((fq & 1) << 2);
        const unsigned swp = (B && w >= 2) ? 2048u : 0u;
#pragma unroll
        for (int cc = 0; cc < 4; ++cc) {
            const unsigned b = (unsigned)((4 * fq + q4) * 128 + (((2 * cc + (p4 >> 1)) ^ fb) << 4) + (p4 & 1) * 8);
            bw[2 * cc] = b + swp; bw[2 * cc + 1] = b + (swp ^ 2048u);
            asm volatile("" : "+v"(bw[2 * cc]), "+v"(bw[2 * cc + 1]));
        }
    };
    auto start = [&](int mt, int pr) {
            {
                auto r0 = __builtin_amdgcn_permlane16_swap(res[mt][pr][0], res[mt][pr][2], false, false);
                auto r1 = __builtin_amdgcn_permlane16_swap(res[mt][pr][1], res[mt][pr][3], false, false);
#pragma unroll
                for (int h = 0; h < 2; ++h) {
                    acc[mt][2 * pr + h] = (f32x4){bf_lo(r0[h]), bf_hi(r0[h]), bf_lo(r1[h]), bf_hi(r1[h])};
                    asm volatile("" : "+v"(acc[mt][2 * pr + h]));
                }
            }
    };
    auto rdw = [&](auto ic) {
        constexpr int i = decltype(ic)::value, h = i >> 3, k = i & 7;
        wf[h % R][k] = tr_read(lds(bw[k]) + (h & 15) * 4096);
        if constexpr (h == 15) { bw[k] += 65536; asm volatile("" : "+v"(bw[k])); }
    };
    {
        auto resid = [&]() {
            const unsigned roff = pick(l != 0, (unsigned)coff, (unsigned)rmo);
            const char* xp = (const char*)xin;
#pragma unroll
            for (int i = 0; i < 4; ++i) aload4c(res[i >> 1][i & 1], roff, xp + (i >> 1) * (l ? 2048 : xrow<0>(0, 16)) + (i & 1) * (l ? 1024 : 64));
            SCHED();
        };
        unsigned opo = o_post(c), oca = B ? M_CNT : M_CNT + 8, old = 0, msk = B ? 3u : 7u;
        xv = lds_epoch(smem) + (unsigned)(l + 2);
        u64 ov = xv + (unsigned)(3 * l + 2);
        prep();
        asm volatile("" : "+v"(opo), "+v"(oca), "+v"(ov), "+v"(old), "+v"(one));
        asm volatile("" : "+s"(msk));
        SCHED();
        if (RBP) resid();
        static_for<0, 16>([&](auto ic) { rdw(ic); SCHED(); });
        if (!P2A) prep2();
        SCHED();
        wait_v<RBP ? 4 : 0>();
        if (lane == 0) asm volatile("ds_add_rtn_u32 %0, %1, %2\n\ts_waitcnt lgkmcnt(0)" : "+v"(old) : "v"(oca), "v"(one) : "memory");
        if ((__builtin_amdgcn_readfirstlane(old) & msk) == msk) { if (lane < 16) post(c.ws, opo, ov); }
        if (w == 0) STAMP_AT(l * 8 + 4);
        SCHED();
        if (!RBP) resid();
        if (P2A == 1) prep2();
        SCHED();
        if (w == 3) STAMP_AT(32 + l * 8 + 5);
        BAR();
    }
    FENCE();
    SCHED();
    if (w == 0) STAMP_AT(l * 8 + 5);
    int so = 0;
    auto next = [&](int j) { so += j == 15 ? 131072 - 15 * 262144 : 262144; asm volatile("" : "+s"(so)); };
    auto ldo = [&](int j, int mt) { xa[j % NP][mt] = __builtin_amdgcn_raw_buffer_load_b128(ro, ovo + mt * 1024, so, 16); };
#pragma unroll
    for (int h = 0; h < NP; ++h) {
        ldo(h, 0);
        SCHED();
        if (h > SH0 && h <= SH0 + 2) { start(h - SH0 - 1, 0); SCHED(); }
        ldo(h, 1);
        if (!WAL || h + 1 < NP) next(h);
        SCHED();
        if (P2A >= 2 && h == P2A - 2) { prep2(); SCHED(); }
        if (h == SH0) {
            wait_v<2 * SH0 + 2>();
            asm volatile("" : "+v"(res[0][0]), "+v"(res[0][1]), "+v"(res[1][0]), "+v"(res[1][1]));
        }
        if (h > SH0 && h <= SH0 + 2) start(h - SH0 - 1, 1);
        SCHED();
    }
    int d0 = 262144, d1 = 131072 - 15 * 262144;
    asm volatile("" : "+s"(d0), "+s"(d1));
    static_assert(WEP || !WAL, "last step");
    static_for<0, WEP ? 31 : 32>([&](auto jc) {
        constexpr int j = decltype(jc)::value;
        constexpr int c0 = j < 30 ? 16 + 8 * j : 256, c1 = j < 30 ? c0 + 8 : 256;
        if constexpr (j + NP == 16) {
            static_assert(c0 <= 8 * 20, "weights that landed before the attention");
            if (!B) BAR();
            FENCE();
            asm volatile("" : "+v"(ovo));
            SCHED();
        }
        if constexpr (j == 0 || j + NP == 16) ALIGN8();
        constexpr int nv = WAL == 1 || j == 0 || j + NP > 32 ? 2 * (NP - 1 < 31 - j ? NP - 1 : 31 - j) : 2 * NP - 3;
        __builtin_amdgcn_s_waitcnt(0x0870 | nv);
        const int sp = so;
        static_assert(!RS0 || (RS0 + NP >= 32 && RS0 < 31 && QW_SLOT <= (RS0 - 1) * 4096), "R0");
        if constexpr (RS0 && j == RS0) BAR();
        else asm volatile("s_add_i32 %0, %1, %2" : "=&s"(so) : "s"(sp), "s"(j + NP == 16 ? d1 : d0) : "scc");
        SCHED();
        static_for<0, 8>([&](auto mc) {
            constexpr int m = decltype(mc)::value, mt = m >> 2, nt = mt ? 3 - (m & 3) : m & 3;
            const bf16x8 wv = __builtin_shufflevector(wf[j % R][2 * nt], wf[j % R][2 * nt + 1], 0, 1, 2, 3, 4, 5, 6, 7);
            if constexpr (j < 31) asm volatile("v_mfma_f32_16x16x32_bf16 %0, %1, %2, %0" : "+v"(acc[mt][nt]) : "v"(wv), "v"(xa[j % NP][mt]));
            else acc[mt][nt] = MFMA(wv, __builtin_bit_cast(bf16x8, xa[j % NP][mt]), acc[mt][nt]);
            SCHED();
            if constexpr (c0 < c1 && m < 7) { rdw(std::integral_constant<int, c0 + m>{}); SCHED(); }
            if constexpr (c0 < c1 && m == 6) rdw(std::integral_constant<int, c0 + 7>{});
            if constexpr (m == WL1 && j > 0 && j - 1 + NP < 32) xa[(j + NP - 1) % NP][1] = __builtin_amdgcn_raw_buffer_load_b128(ro, ovo + 1024, sp, 16);
            if constexpr (m == WL1 + 4 && j + NP < 32) ldo(j, 0);
            SCHED();
        });
    });
    const bf16x8 ones = __builtin_bit_cast(bf16x8, on);
    u32x4 o[2][2];
    f32x4 sm[2], g[2];
    const f32x4 zero = {0.f, 0.f, 0.f, 0.f};
    auto sel = [&](float a, float b, u64 m) { return __builtin_amdgcn_inverse_ballot_w64(m) ? b : a; };
    auto stats = [&](int mt) {
        float qa = sel(g[mt][0], g[mt][1], b0), qb = sel(g[mt][2], g[mt][3], b0);
        asm volatile("" : "+v"(qa), "+v"(qb));
        SCHED();
        if (WEP) asm volatile("" :: "v"(sm[1][1]), "v"(sm[1][2]), "v"(sm[1][3]), "v"(o[1][0]), "v"(o[1][1]), "v"(on));
        __builtin_amdgcn_raw_buffer_store_b64((u32x2){__float_as_uint(sm[mt][0]), __float_as_uint(sel(qa, qb, b1))}, rst, sto + mt * 128, 0, 16);
        SCHED();
    };
    u32x2 pk[2];
    auto last = [&](int mt, int nt) {
        acc[mt][nt] = MFMA(__builtin_shufflevector(wf[31 % R][2 * nt], wf[31 % R][2 * nt + 1], 0, 1, 2, 3, 4, 5, 6, 7), __builtin_bit_cast(bf16x8, xa[31 % NP][mt]), acc[mt][nt]);
        SCHED();
    };
    auto cv = [&](int mt, int pr) {
        pk[0] = pack4(acc[mt][2 * pr]); pk[1] = pack4(acc[mt][2 * pr + 1]);
        asm volatile("" : "+v"(pk[0]), "+v"(pk[1]));
        SCHED();
    };
    auto st = [&](int mt, int pr) {
        o[mt][pr] = widen(pk[0], pk[1]);
        __builtin_amdgcn_raw_buffer_store_b128(o[mt][pr], rs, mt ? coff1 : coff, pr ? spr : 0, 16);
        SCHED();
    };
    auto gq = [&](int mt, int pr, bool f) { const bf16x8 of = __builtin_bit_cast(bf16x8, o[mt][pr]); g[mt] = MFMA(of, of, f ? zero : g[mt]); SCHED(); };
    auto gs = [&](int mt, int pr, bool f) { sm[mt] = MFMA(ones, __builtin_bit_cast(bf16x8, o[mt][pr]), f ? zero : sm[mt]); SCHED(); };
    WAIT_L0();
    last(0, 0); last(0, 1); last(0, 2); last(0, 3);
    last(1, 3); cv(0, 0);
    last(1, 2); st(0, 0);
    last(1, 1); cv(0, 1);
    last(1, 0); st(0, 1);
    if (w == 0) STAMP_AT(l * 8 + 6);
    gq(0, 0, true); cv(1, 1);
    gs(0, 0, true); st(1, 1);
    gq(0, 1, false); cv(1, 0);
    gs(0, 1, false); st(1, 0);
    gq(1, 1, true);
    gs(1, 1, true);
    gq(1, 0, false);
    gs(1, 0, false);
    stats(0);
    stats(1);
    if (l < 3) {
        unsigned old = 0;
        if (!RS0 || RN1 > RN0) BAR();
        wait_v<XE2>();
        if (lane == 0) asm volatile("ds_add_rtn_u32 %0, %1, %2\n\ts_waitcnt lgkmcnt(0)" : "+v"(old) : "v"(cnta), "v"(one) : "memory");
        if ((__builtin_amdgcn_readfirstlane(old) & 3) == 3) {
            if (__builtin_amdgcn_inverse_ballot_w64(am)) post(c.ws, xpo, xv);
        }
    }
    if (w == 0) STAMP_AT(l * 8 + 7);
}

__global__ __launch_bounds__(512, 2) __attribute__((aligned(4096))) void k_main(Params P) {
    extern __shared__ __attribute__((aligned(16))) char smem_[];
    KP kp = (KP)__builtin_amdgcn_kernarg_segment_ptr();
    const char *x0, *w0, *wo;
    Ctx c;
    c.smem = (LAS char*)smem_;
    c.lane = threadIdx.x & 63;
    c.wid = __builtin_amdgcn_readfirstlane(threadIdx.x >> 6);
    c.bid = blockIdx.x;
    const bool loader = c.wid >= 4;
    unsigned va, vw;
    auto enter = [&](const bool LD) {
        if (LD) asm volatile("s_load_dwordx2 %0, %4, 0x0\n\ts_load_dwordx2 %1, %4, 0x8\n\ts_load_dwordx2 %2, %4, 0x10\n\ts_load_dwordx2 %3, %4, 0x20 ; loader" : "=&s"(x0), "=&s"(w0), "=&s"(c.ws), "=&s"(wo) : "s"(kp));
        else asm volatile("s_load_dwordx2 %0, %4, 0x0\n\ts_load_dwordx2 %1, %4, 0x8\n\ts_load_dwordx2 %2, %4, 0x10\n\ts_load_dwordx2 %3, %4, 0x20" : "=&s"(x0), "=&s"(w0), "=&s"(c.ws), "=&s"(wo) : "s"(kp));
        if (LD) asm volatile("; loader" : "+s"(c.bid), "+s"(c.wid)); else asm volatile("; consumer" : "+s"(c.bid), "+s"(c.wid));
        if (LD) asm volatile("; loader" : "+v"(c.lane)); else asm volatile("; consumer" : "+v"(c.lane));
        static_assert(QXM == 8 && WXM == 4 && ALT == 1, "map");
        c.pq = (((c.bid & 1) * 8) ^ PQX) + ((c.bid >> 3) & 7); c.jq = (c.bid & 6) * 2 + (c.bid >> 6);
        const int w = c.wid, lr = c.lane >> 3, lc = c.lane & 7;
        va = (unsigned)((c.pq * 128 + 8 * (w & 3) + lr) * (D_ * 2) + ((lc ^ lr) << 4));
        vw = (unsigned)((8 * (w & 3) + lr) * 6144 + c.jq * 128 + ((lc ^ ((lr & 2) | ((w & 1) << 2))) << 4));
        asm volatile("" : "+v"(va), "+v"(vw));
        c.pw = ((c.pq & 8) ^ 8) + ((c.bid >> 1) & 1) * 4 + ((c.bid >> 3) & 3); c.jw = (c.bid & 4) * 2 + (c.bid >> 5); c.cw = 0;
        if (LD) {
            asm volatile("" : "+s"(c.pw), "+s"(c.jw));
            asm volatile("s_waitcnt lgkmcnt(0)" : "+s"(x0), "+s"(w0), "+s"(c.ws), "+s"(wo));
            if (ESL) __builtin_amdgcn_s_sleep(ESL);
        } else {
            int t0 = first_tile(first_order(c.pq, c.jq), 0);
            asm volatile("" : "+s"(t0));
            asm volatile("s_waitcnt lgkmcnt(0) ; c" : "+s"(x0), "+s"(w0), "+s"(c.ws), "+s"(wo));
            if (ST0 && c.wid == 3) STAMP_AT(41);
            const char* xb = x0 + (unsigned)(t0 * 128);
            const char* wb = w0 + (unsigned)(t0 * (64 * 6144));
#pragma unroll
            for (int h = 0; h < 2; ++h) {
#pragma unroll
                for (int j = 0; j < 2; ++j) dma16<false>(QA_OFF + (j * 8 + h * 4 + w) * 1024, va, xb + (j * 64 + h * 32) * (D_ * 2));
#pragma unroll
                for (int j = 0; j < 3; ++j) dma16<false>((j * 8 + h * 4 + w) * 1024, vw, wb + h * (32 * 6144) + j * 2048);
            }
            if (ST0 && c.wid == 1) STAMP_AT(43);
            if (w == 0 && c.lane < 2) touch(M_EP, (unsigned)(WS_EPOCH + c.lane * 4), c.ws);
            if (w == 1 && c.lane < 5) *(LAS unsigned*)(c.smem + (c.lane < 4 ? M_CNT + c.lane * 4 : M_GO)) = 0u;
        }
        asm volatile("" : "+s"(kp), "+s"(c.bid), "+s"(c.ws));
    };
    if (c.wid == CPW) {
        const char* pc; int len;
        asm volatile("s_getpc_b64 %0\n.Lcpf_a:\n\ts_mov_b32 %1, .Lcpf_end-.Lcpf_a-4" : "=s"(pc), "=s"(len));
        const int o = min((int)(((c.bid >> 3) & 7) * 64 + c.lane) * 128, len);
        asm volatile("s_mov_b32 m0, %0\n\ts_nop 3\n\tglobal_load_lds_dword %1, %2" ::"s"(M_TCH), "v"(o), "s"(pc) : "memory");
    }
    const char* xi;
    auto step = [&](int l) {
        asm volatile("" : "+s"(kp), "+s"(c.ws), "+s"(c.jw), "+s"(c.bid));
        c.pq ^= 8; c.pw ^= 8; c.cw = CPY ? c.jw & 7 : 0;
        xi = c.ws + WS_X + (unsigned)(l * (8u << 20));
        wo = (const char*)((l & 1) ? kp->wo0 : kp->wo1);
        if (c.cw == 3) wo = c.ws + WS_CO + (((l & 1) ? 2 : 0) + (c.jw >> 3)) * 4096;
    };
    if (loader) {
        enter(true);
        xi = x0;
            qkv_load<false, true>(c, xi, w0, wo, 0, kp, va, vw);
#pragma unroll 1
        for (int l = 0;;) {
            asm volatile("" : "+s"(l));
            if (!(l & 1)) attn_a<true>(c, l, wo, kp);
            wo_load(c, l, nullptr, wo);
            if (l == 3) break;
            step(l);
            ++l;
            if (l == 2) { qkv_load<false, false>(c, xi, (const char*)kp->wqkv0, wo, l); }
            else { qkv_load<true, false>(c, xi, (const char*)kp->wqkv1, wo, l); }
        }
    } else {
        enter(false);
        xi = x0;
        bf16_t* xo = (bf16_t*)(c.ws + WS_X);
#pragma unroll 1
        for (int l = 0; l < 4; ++l) {
            if (l & 1) { qkv_consume<true>(c, l, kp); }
            else {
                qkv_consume<false>(c, l, kp, va, vw);
                attn_a<false>(c, l, wo, kp);
            }
            wo_consume(l & 1, c, (const bf16_t*)xi, xo, l);
            step(l);
            xo = l == 2 ? kp->out : (bf16_t*)(c.ws + WS_X + (unsigned)((l + 1) * (8u << 20)));
            asm volatile("" : "=v"(va), "=v"(vw));
        }
    }
    asm volatile(".Lcpf_end:");
}

extern "C" void kernel_launch(void* const* d_in, const int* in_sizes, int n_in, void* d_out, int out_size, void* d_ws, size_t ws_size,
                              hipStream_t stream) {
    Params P;
    P.x0 = (const bf16_t*)d_in[0];
    P.wqkv0 = (const bf16_t*)d_in[1];
    P.wo0 = (const bf16_t*)d_in[2];
    P.wqkv1 = (const bf16_t*)d_in[4];
    P.wo1 = (const bf16_t*)d_in[5];
    P.out = (bf16_t*)d_out;
    P.ws = (char*)d_ws;
    (void)hipFuncSetAttribute((const void*)k_main, hipFuncAttributeMaxDynamicSharedMemorySize, LDS_TOTAL);
    k_main<<<256, 512, LDS_TOTAL, stream>>>(P);
}
```

```cpp
#include <hip/hip_runtime.h>
#include <stdint.h>
#include <type_traits>

typedef unsigned short bf16_t;
typedef short bf16x8 __attribute__((ext_vector_type(8)));
typedef short s16x4 __attribute__((ext_vector_type(4)));
typedef float f32x4 __attribute__((ext_vector_type(4)));
typedef float f32x2 __attribute__((ext_vector_type(2)));
typedef float f32x16 __attribute__((ext_vector_type(16)));
typedef unsigned u32x4 __attribute__((ext_vector_type(4)));
typedef unsigned u32x2 __attribute__((ext_vector_type(2)));
#define DI __device__ __forceinline__
#define LAS __attribute__((address_space(3)))
#define GAS __attribute__((address_space(1)))
#define STAMP 0

constexpr int S_ = 2048, D_ = 1024, KT = 16;
constexpr float LN_EPS = 1e-5f;
constexpr float QSCALE = 0.17677669529663687f * 1.4426950408889634f;

#define FQO 0
#define XBO 524288
#define HX 0
#define HO 0
#define HK 0
#define HS 0
#define HM 1
constexpr size_t WS_FLAGQ = FQO;
constexpr size_t WS_FLAGW = 98304;
constexpr size_t WS_EPOCH = 65536;
constexpr size_t WS_CSUM = 131072;
constexpr size_t WS_XBOX = XBO;
constexpr size_t WS_OBOX = 655360;
constexpr size_t WS_KBOX = 720896;
constexpr size_t WS_STATS = 1u << 20;
constexpr size_t WS_X = 4u << 20;
constexpr size_t WS_O = 96u << 20;
constexpr size_t WS_KV = 28u << 20;
constexpr size_t WS_CQ = 36u << 20;
constexpr size_t WS_CO = 42u << 20;
constexpr size_t WS_STAMP = 64u << 20;

template <int H> DI constexpr int hp(int c) { return !H ? c * 1024 : (c >> 2) * 8192 + (HM == 1 ? (c & 3) * 256 : (c & 1) * 128 + (c & 2) * 256); }
template <int H> DI constexpr int hl(int u) { return !H ? u : (HM == 1 ? u + (u & ~127) : u + (u & ~255)); }
template <int H> DI constexpr int hv(int lane) { return !H ? lane * 16 : (lane >> 3) * 1024 + (lane & 7) * 16; }
template <int H> DI constexpr int xrow(int p, int r) { return H == 0 ? (p * 128 + r) * 2048 : (H == 1 ? (p * 128 + r) * 4096 : (H == 2 ? p * 524288 + r * 256 : p * 262144 + r * 128)); }
template <int H> DI constexpr int xcol(int t) { return H == 0 ? t * 128 : (H == 1 ? hl<1>(t * 128) : (H == 2 ? t * 32768 : t * 16384)); }
#define WTC 2
#define WT0 1
#define SPR 1
#define TOPJ 0x2000
#define WV0 1
#define FQS 256
#define SNK 1
#define WBE 1
#define WAE 1
#define KSP 0
#define K0E 1
#define TOPS 4
#define BPR 1
#define RLD 0
#define CPF 2
#define WB14 0
#define WB15 5
#define WT13 2
#define ENT 1
#define MBOX 3
#define OBS 256
#define PQX 8
#define ALT 1
#define ALN 1
#define NQA_ 3
#define NQW_ 4
#define LPRIO 3
#define TOUCH 1
#define TSTEP 13
#define XS_ 9
#define ROT0 0
#define WPRE 3
#define PSLEEP 1
#define TW0 5
#define APRIO 1
#define QXM 8
#define WNQ 5
#define WRG 2
#define WBS 1
#define WEP 1
#define WAL 2
#define WL1 0
#define RS0 27
#define RN0 6
#define RN1 6
#define WXM 4
#define CPOS 5
#define LSL 0
#define SAFE_SUM 0x1p80f
#define AB2 0
#define W30S 8
#define CPY 1
#define HT0 0
#define XST 2
#define CKW 4
#define XSP 1
#define EPOLL 1
#define RSLP 1
#define APOST 1
#define ASLP 2
#define APIPE 4
#define RBP 0
#define P2A 1
#define SH0 2
#define KPW 4
#define LWA 7
#define WA0 12
#define WA1 22
#define OSL 14
#define OPL 1
#define E30 1
#define KPP 1
#define HP4 1
#define TWQ 1
#define ERL 1
#define ESL 0
#define DM 0
#define DS 0
#define ELO 0
#define EN0 3
#define EN1 2
#define ST0 0
#define NC 1
#define NCM 1
#define NCB 4
#define AD1 3
#define AD2 0
#define AD3 4
#define TWO 1
#define XSB 10
#define TL0 0
#define TL1 12
#define FDM 1
#define TSP 0
#define CPW 4
#define EXS 1
#define NCH 2
#define XE2 0
constexpr int QA_SLOT = 18432, NQA = NQA_, QW_SLOT = 24576, NQW = NQW_, QA_OFF = 3 * QW_SLOT, QW_TOP = 131072;
DI constexpr int qw_slot(int s) { return s == 3 ? QW_TOP : s * QW_SLOT; }
constexpr int AT_N = 90112, AT_OWN = AT_N + 32768, AT_Q = 73728;
constexpr int MISC = 155648, M_MEAN = MISC, M_RSTD = MISC + 640, M_CS = MISC + 1280, M_CNT = MISC + 2816, M_EP = MISC + 2832, M_GO = MISC + 2848, M_TCH = MISC + 3072, M_TAB = MISC + 4096, M_XCH = 131072;
constexpr int LDS_TOTAL = 163840;
static_assert(QA_OFF + NQA * QA_SLOT <= QW_TOP && QW_TOP + QW_SLOT <= MISC && AT_N + 65536 <= MISC && AT_Q + 16384 <= AT_N, "lds");

DI unsigned cvt_pk_bf16(float lo, float hi) {
    typedef __bf16 bf2 __attribute__((ext_vector_type(2)));
    bf2 r; r[0] = (__bf16)lo; r[1] = (__bf16)hi;
    return __builtin_bit_cast(unsigned, r);
}
DI u32x2 pack4(const f32x4& v) { return (u32x2){cvt_pk_bf16(v[0], v[1]), cvt_pk_bf16(v[2], v[3])}; }
DI float bf_lo(unsigned u) { return __uint_as_float(u << 16); }
DI float bf_hi(unsigned u) { return __uint_as_float(u & 0xffff0000u); }
DI u32x4 widen(const u32x2& a, const u32x2& b) {
    auto r0 = __builtin_amdgcn_permlane16_swap(a[0], b[0], false, false);
    auto r1 = __builtin_amdgcn_permlane16_swap(a[1], b[1], false, false);
    return (u32x4){r0[0], r1[0], r0[1], r1[1]};
}
#define MFMA(a, b, c) __builtin_amdgcn_mfma_f32_16x16x32_bf16((a), (b), (c), 0, 0, 0)
#define WAIT_L0() __builtin_amdgcn_s_waitcnt(0xC07F)
#define WAIT_V0() asm volatile("s_waitcnt vmcnt(0)" ::: "memory")
#define BAR() __builtin_amdgcn_s_barrier()
#define SCHED() __builtin_amdgcn_sched_barrier(0)
template <int N> DI void wait_v() { asm volatile("s_waitcnt vmcnt(%0)" ::"i"(N) : "memory"); }
template <int I, int N, class F> DI void static_for(F&& f) { if constexpr (I < N) { f(std::integral_constant<int, I>{}); static_for<I + 1, N>(f); } }

template <bool SC1> DI void dma16(unsigned lds, unsigned voff, const char* sbase) {
    if (SC1) asm volatile("s_mov_b32 m0, %0\n\ts_nop 3\n\tglobal_load_lds_dwordx4 %1, %2 sc1" ::"s"(lds), "v"(voff), "s"(sbase) : "memory");
    else asm volatile("s_mov_b32 m0, %0\n\ts_nop 3\n\tglobal_load_lds_dwordx4 %1, %2" ::"s"(lds), "v"(voff), "s"(sbase) : "memory");
}
DI void touch(unsigned lds, unsigned voff, const char* sbase) {
    asm volatile("s_mov_b32 m0, %0\n\ts_nop 3\n\tglobal_load_lds_dword %1, %2 sc1" ::"s"(lds), "v"(voff), "s"(sbase) : "memory");
}
DI s16x4 tr_read(const LAS char* p) {
    typedef short v4i16_t __attribute__((ext_vector_type(4)));
    return __builtin_bit_cast(s16x4, __builtin_amdgcn_ds_read_tr16_b64_v4i16((LAS v4i16_t*)p));
}
DI LAS char* lds(unsigned off) { return (LAS char*)(uintptr_t)off; }
DI unsigned long long rstamp() { unsigned long long t; asm volatile("s_memrealtime %0\n\ts_waitcnt lgkmcnt(0)" : "=s"(t)::"memory"); return t; }

template <int NAI, int NWI, bool FIRST> struct Sched {
    int w[KT];
    constexpr Sched() : w{} {
        int endA[KT] = {}, endW[KT] = {};
        int n = 0;
        if (FIRST) {
            if (ENT == 0) { n += 2; endA[0] = n; n += 3; endW[0] = n; }
            n += NAI; endA[1] = n; n += NWI; endW[1] = n;
            if (ERL >= 5) { n += NAI; endA[2] = n; }
            if (ERL == 2 || ERL == 3 || ERL == 5) { n += NWI; endW[2] = n; }
        } else {
            for (int s = 0; s < WPRE; ++s) { n += NWI; endW[s] = n; }
            for (int s = 0; s < 2; ++s) { n += NAI; endA[s] = n; }
        }
        for (int kt = 0; kt < KT; ++kt) {
            const int need = endA[kt] > endW[kt] ? endA[kt] : endW[kt];
            w[kt] = (n - need) > 63 ? 63 : (n - need);
            if (kt + 2 < KT && !(FIRST && ERL >= 5 && kt == 0)) { n += NAI; endA[kt + 2] = n; }
            if (FIRST && (ERL == 4 || ERL == 6) && kt == 0) { n += NWI; endW[2] = n; }
            if (FIRST && ERL < 2 && kt < 5) {
                if (kt < 4) { n += NWI; endW[kt + 2] = n; }
                if (kt >= 3) n += NWI / 2;
                if (kt == 4) { endW[6] = n; n += NWI; endW[7] = n; }
                continue;
            }
            if (!FIRST && WPRE == 2 && kt < 2) { n += NWI; endW[kt + 2] = n; }
            if ((FIRST || WPRE == 3) && kt == 0) { n += NWI; endW[3] = n; }
            if (kt >= 1 && kt + 3 < KT) { n += NWI; endW[kt + 3] = n; }
            if (kt == 13) n += WT13;
            if (kt == 14) n += (NAI == 5 || WA0 > 12 ? 12 : WA0) - WT13 + (NAI == 5 ? WB14 : 0);
        }
    }
};

struct Params {
    const bf16_t* x0;
    const bf16_t* wqkv0;
    char* ws;
    const bf16_t* wqkv1;
    const bf16_t* wo0;
    const bf16_t* wo1;
    bf16_t* out;
};
typedef const __attribute__((address_space(4))) Params* KP;
DI int rot_first(int pq, int jq) {
    const int pr = 2 * (pq & 7) + (pq >> 3);
    if (ROT0 == 6) return 0;
    if (ROT0 == 7) return (pq & 1) * 8;
    if (ROT0 == 8) return (pq & 3) * 4;
    if (ROT0 == 9) return pq;
    if (ROT0 == 4) return (pq + (jq ^ 3)) & 15;
    if (ROT0 == 5) return (pr + (jq ^ 3)) & 15;
    return (ROT0 == 0 ? pq + jq : (ROT0 == 1 ? pr + jq : (ROT0 == 2 ? pr + 4 * (jq & 3) + (jq >> 2) : pq + 4 * (jq & 3) + (jq >> 2)))) & 15;
}

struct Order { int base, r, re; };
template <int N> DI Order make_order(int base, int rot) { Order o; o.base = base; o.r = (rot - base) & 15; o.re = o.r > N ? o.r : N; return o; }
DI Order first_order(int pq, int jq) { return make_order<NC>((pq & NCM) * (16 / (NCM + 1)) + NCB, rot_first(pq, jq)); }
template <int N = NC> DI int first_tile(const Order& o, int s) {
    const int v = o.re + s - N;
    if (s < N) { int a = N + o.base, b = s + o.base; if (N - s == 1) asm volatile("" : "+s"(a)); return (o.r == s ? a : b) & 15; }
    return ((s == N ? o.r : (v > 15 ? v - (16 - N) : v)) + o.base) & 15;
}

typedef unsigned long long u64;
DI u64 lds_epoch(LAS char* smem) { return *(const LAS u64*)(smem + M_EP); }
DI bool reached(u64 v, u64 ep, unsigned target) { return (v - ep - target) < (u64)(64u - target); }
DI void poll_flags(const char* ws, u64 ep, unsigned off, bool active, unsigned target) {
    const GAS u64* f = (const GAS u64*)(ws + off);
#pragma unroll 1
    for (unsigned spins = 0; spins < (1u << 22); ++spins) {
        const u64 v = __hip_atomic_load(f, __ATOMIC_RELAXED, __HIP_MEMORY_SCOPE_AGENT);
        if (__all(!active || reached(v, ep, target))) break;
        __builtin_amdgcn_s_sleep(PSLEEP);
    }
    asm volatile("" ::: "memory");
}
struct Ask { u64 v[4]; };
DI void keep(Ask& t) { asm volatile("" : "+v"(t.v[0]), "+v"(t.v[1]), "+v"(t.v[2]), "+v"(t.v[3])); }
#define ASK(r) "global_atomic_or_x2 " r ", %[off], %[z], %[ws] sc0 sc1\n\t"
#define CHK(r) "s_waitcnt vmcnt(%[n])\n\tv_lshl_add_u64 %[d], " r ", 0, %[nb]\n\tv_cmp_gt_u64 vcc, %[span], %[d]\n\ts_cmp_eq_u64 vcc, exec\n\ts_cbranch_scc1 .Lpe%=\n\ts_sleep %[sl]\n\t"
template <bool NEG = false> DI void poll_rmw(Ask& t, const char* ws, unsigned off, bool act, u64 base, unsigned span) {
    u64 z, d, nb = NEG ? base : 0 - base, sp = span;
    unsigned cnt;
    asm volatile("" : "=v"(t.v[0]), "=v"(t.v[1]), "=v"(t.v[2]), "=v"(t.v[3]));
    if (act) {
        if (APIPE == 4)
            asm volatile("v_mov_b64 %[z], 0\n\ts_mov_b32 %[cnt], 0x80000\n\ts_nop 3\n\t" ASK("%[a]") "s_sleep %[sl]\n\t" ASK("%[b]") "s_sleep %[sl]\n\t" ASK("%[c]") "s_sleep %[sl]\n"
                         ".Lpl%=:\n\t" ASK("%[e]") CHK("%[a]") ASK("%[a]") CHK("%[b]") ASK("%[b]") CHK("%[c]") ASK("%[c]") CHK("%[e]")
                         "s_sub_u32 %[cnt], %[cnt], 1\n\ts_cbranch_scc0 .Lpl%=\n.Lpe%=:"
                         : [a] "+v"(t.v[0]), [b] "+v"(t.v[1]), [c] "+v"(t.v[2]), [e] "+v"(t.v[3]), [d] "=&v"(d), [z] "=&v"(z), [cnt] "=&s"(cnt)
                         : [off] "v"(off), [ws] "s"(ws), [nb] "v"(nb), [span] "s"(sp), [sl] "i"(ASLP), [n] "i"(3) : "vcc", "scc", "memory");
        else if (APIPE == 2)
            asm volatile("v_mov_b64 %[z], 0\n\ts_mov_b32 %[cnt], 0x100000\n\ts_nop 3\n\t" ASK("%[a]") "s_sleep %[sl]\n"
                         ".Lpl%=:\n\t" ASK("%[b]") CHK("%[a]") ASK("%[a]") CHK("%[b]")
                         "s_sub_u32 %[cnt], %[cnt], 1\n\ts_cbranch_scc0 .Lpl%=\n.Lpe%=:"
                         : [a] "+v"(t.v[0]), [b] "+v"(t.v[1]), [d] "=&v"(d), [z] "=&v"(z), [cnt] "=&s"(cnt)
                         : [off] "v"(off), [ws] "s"(ws), [nb] "v"(nb), [span] "s"(sp), [sl] "i"(ASLP), [n] "i"(1) : "vcc", "scc", "memory");
        else
            asm volatile("v_mov_b64 %[z], 0\n\ts_mov_b32 %[cnt], 0x200000\n\ts_nop 3\n"
                         ".Lpl%=:\n\t" ASK("%[a]") CHK("%[a]")
                         "s_sub_u32 %[cnt], %[cnt], 1\n\ts_cbranch_scc0 .Lpl%=\n.Lpe%=:"
                         : [a] "+v"(t.v[0]), [d] "=&v"(d), [z] "=&v"(z), [cnt] "=&s"(cnt)
                         : [off] "v"(off), [ws] "s"(ws), [nb] "v"(nb), [span] "s"(sp), [sl] "i"(ASLP), [n] "i"(0) : "vcc", "scc", "memory");
    }
}
DI void post(char* ws, unsigned off, u64 v) {
    if (APOST) asm volatile("s_nop 4\n\tglobal_atomic_swap_x2 %0, %1, %2 sc1" :: "v"(off), "v"(v), "s"(ws) : "memory");
    else __hip_atomic_store((GAS u64*)(ws + off), v, __ATOMIC_RELAXED, __HIP_MEMORY_SCOPE_AGENT);
}
struct Ctx {
    LAS char* smem;
    char* ws;
    int lane, wid;
    int pq, jq;
    int pw, jw;
    int bid;
    int cw;
};
DI Ctx fresh(const Ctx& c0) {
    Ctx c = c0;
    asm volatile("" : "+v"(c.lane));
    asm volatile("" : "+s"(c.wid), "+s"(c.pq), "+s"(c.jq), "+s"(c.pw), "+s"(c.jw), "+s"(c.ws), "+s"(c.cw));
    return c;
}
DI unsigned o_post(const Ctx& c) { return (MBOX & 2) ? (unsigned)WS_OBOX + (c.pq * 16 + (c.lane & 15)) * OBS + c.jq * 8 : (unsigned)WS_FLAGQ + (c.pq * 16 + c.jq) * FQS; }
DI unsigned o_find(const Ctx& c) { return (MBOX & 2) ? (unsigned)WS_OBOX + (c.pw * 16 + c.jw) * OBS + (c.lane & 15) * 8 : (unsigned)WS_FLAGQ + (c.pw * 16 + (c.lane & 15)) * FQS; }
DI unsigned x_post(const Ctx& c) { return (MBOX & 1) ? (unsigned)WS_XBOX + (c.pw * 16 + (c.lane & 31)) * (2 * FQS) + (c.lane & 16) * (FQS / 16) + c.jw * 8 : (unsigned)WS_FLAGW + (c.pw * 16 + c.jw) * 128; }
DI bool x_act(const Ctx& c, bool halo) { return c.lane < ((MBOX & 1) ? (halo && c.pw < 15 ? 32 : 16) : 1); }
#define STAMP_AT(slot) do { } while (0)

#define LSTAMP(i) do { } while (0)
#define LSTORE(ph) do { } while (0)
#define LMARK() do { } while (0)
#define LPUT(i) do { } while (0)
#define ASTORE() do { } while (0)
#define QSTAMP(i) do { } while (0)
#define QSTORE() do { } while (0)
#define FENCE() asm volatile("" ::: "memory")
#define ALIGN8() do { if (ALN) { asm volatile(".p2align 3"); SCHED(); } } while (0)
DI float addf(float a, float b) { float r; asm("v_add_f32 %0, %1, %2" : "=v"(r) : "v"(a), "v"(b)); return r; }
DI float fold16(float a, float b) {
    auto r = __builtin_amdgcn_permlane16_swap(__float_as_uint(a), __float_as_uint(b), false, false);
    float x = __uint_as_float(r[0]) + __uint_as_float(r[1]);
    asm volatile("" : "+v"(x));
    return x;
}
DI float fold32(float a, float b) {
    auto r = __builtin_amdgcn_permlane32_swap(__float_as_uint(a), __float_as_uint(b), false, false);
    float x = __uint_as_float(r[0]) + __uint_as_float(r[1]);
    asm volatile("" : "+v"(x));
    return x;
}
DI void aload4c(u32x4& d, unsigned voff, const char* sb) { asm volatile("s_nop 4\n\tglobal_load_dwordx4 %0, %1, %2 sc1" : "=v"(d) : "v"(voff), "s"(sb) : "memory"); }
DI void aload2c(u32x2& d, unsigned voff, const char* sb) { asm volatile("s_nop 4\n\tglobal_load_dwordx2 %0, %1, %2 sc1" : "=v"(d) : "v"(voff), "s"(sb) : "memory"); }
DI unsigned wo_src(int q, int lane, int jw) { return (unsigned)((8 * q + (lane >> 3)) * (D_ * 2) + jw * 128 + (((lane & 7) ^ ((lane >> 3) & 6)) << 4)); }
DI unsigned pick(bool f, unsigned a, unsigned b) { asm volatile("" : "+v"(a), "+v"(b)); return f ? a : b; }
DI unsigned wo_w_off(const Ctx& c) { return pick(c.cw == 3, (unsigned)((c.wid & 3) * 1024 + c.lane * 16), wo_src(c.wid & 3, c.lane, c.jw)); }
DI unsigned wq_src(int ib, int lane, int jq) {
    const int seg = ib >> 3, ii = ib & 7, lr = lane >> 3;
    return (unsigned)((8 * ii + lr) * 6144 + seg * 2048 + jq * 128 + (((lane & 7) ^ ((lr & 2) | ((ii & 1) << 2))) << 4));
}
template <int S0, int S1> DI void wo_w_issue(const Ctx& c, const char* W, unsigned vw) {
    const int lw = c.wid & 3;
#pragma unroll
    for (int s = S0; s < S1; ++s) dma16<CPY != 0>(s * 4096 + lw * 1024, vw, W + (size_t)((2 * (s & 15) + (s >> 4)) * 32) * (D_ * 2));
}

template <bool HALO, bool FIRST> DI void qkv_load(const Ctx& c0, const char* xin, const char* W, const char* Wo, int l, KP kp = nullptr, unsigned va0 = 0, unsigned vw0 = 0) {
    const Ctx c = fresh(c0);
    asm volatile("" : "+s"(xin), "+s"(W), "+s"(Wo));
    LAS char* smem = c.smem;
    constexpr int NAI = HALO ? 5 : 4, NWI = 6, XS = FIRST ? XS_ : XSB, NR = HALO ? 144 : 128;
    constexpr Sched<NAI, NWI, FIRST> SC{};
    static_assert(NQA == 3 && NQW == 4, "schedule");
    const int lw = c.wid - 4, lane = c.lane, lr = lane >> 3, lc = lane & 7, fr = lane & 15, fq = lane >> 4;
    const int row0 = c.pq * 128;
    const bool cq_ = CPY && !FIRST && (c.jq & 7) == 3;
    if (cq_) W = c.ws + WS_CQ + ((HALO ? 0 : 2) + (c.jq >> 3)) * 24576;
    asm volatile("" : "+s"(W));
    constexpr int SP = FIRST ? NC : (HALO ? NCH : 0);
    static_assert(FIRST || SP + DS <= TL0 + 2 + TSP, "one flag for both kinds of sums");
    const int rot = FIRST ? rot_first(c.pq, c.jq) : (l == 1 ? c.pq : SP);
    Order od = FIRST ? first_order(c.pq, c.jq) : make_order<SP>(0, rot);
    if (FIRST) asm volatile("" : "+s"(od.base));
    if (FIRST || HALO) asm volatile("" : "+s"(od.r), "+s"(od.re));
    auto tile = [&](int s) { return FIRST || HALO ? first_tile<SP>(od, s) : s; };
    unsigned va[5], vw[NWI];
#pragma unroll
    for (int j = 0; j < 4; ++j) va[j] = (unsigned)(FIRST ? va0 + j * 32 * (D_ * 2) : c.pq * 262144 + (j * 4 + lw) * 1024 + lane * 16);
    if (HALO) va[4] = (unsigned)((c.pq > 0 ? (c.pq - 1) * 262144 + 14336 : 0) + lw * 512 + lane * 16);
#pragma unroll
    for (int j = 0; j < NWI; ++j) vw[j] = FIRST ? vw0 + (j & 1) * (32 * 6144) + (j >> 1) * 2048 : pick(cq_, (unsigned)((j * 4 + lw) * 1024 + lane * 16), wq_src(j * 4 + lw, lane, c.jq));
    auto issueA = [&](int s, int slot) {
        const char* b = xin + (unsigned)(tile(s) * (FIRST ? 128 : 16384));
#pragma unroll
        for (int j = 0; j < 4; ++j) dma16<true>(QA_OFF + slot * QA_SLOT + (j * 4 + lw) * 1024, va[j], b);
        if (HALO) { if (lane < 32) dma16<true>(QA_OFF + slot * QA_SLOT + 16384 + lw * 512, va[4], b); }
    };
    auto issueW = [&](int s, int slot, int j0 = 0, int j1 = NWI) {
        const char* b = W + (unsigned)(tile(s) * (64 * 6144));
#pragma unroll
        for (int j = j0; j < j1; ++j) dma16<CPY && !FIRST>(qw_slot(slot) + (j * 4 + lw) * 1024, vw[j], b);
    };
    auto ahead = [&]() {
        const int r3 = (lane * 43) >> 7;
        const unsigned o = (unsigned)((((rot - (c.pq & 7) + 10) * 64 + (c.pq & 7) * 48 + lw * 12 + r3) & 1023) * 6144 + (lane - 3 * r3) * 2048 + c.jq * 128);
        if (lane < 36 && (WTC != 2 || (c.jq & 7) == 3)) asm volatile("s_mov_b32 m0, %0\n\ts_nop 3\n\tglobal_load_lds_dword %1, %2" ::"s"(M_TCH), "v"(o), "s"(W) : "memory");
    };
    if (ST0 && FIRST && lw == 1) STAMP_AT(47);
    if (FIRST && ERL) {
        SCHED();
        issueA(1, 1); issueW(1, 1);
        SCHED();
    }
    const bool docs = FIRST || (HALO && l == 1);
    const int t = lw * 64 + lane;
    static_assert(MBOX & 1, "residual flag");
    unsigned foff, ftg, so, ho, fo_, co, l3, l4, vwo, wt[3], cso, xt, xt1, xso, tp, tl;
    u32x4 on;
    auto addr1 = [&]() {
        int lane = c.lane;
        asm volatile("" : "+v"(lane));
        const int fr = lane & 15, fq = lane >> 4;
        const int ph = c.pq > 0 ? c.pq - 1 : 0;
        foff = pick(lane != 32, (unsigned)WS_FLAGQ + pick(lane < 16, c.pq * 16 + lane, pick(lane < 32, fr * 16 + c.jq, ph * 16 + 14 + (lane & 1))) * FQS, (unsigned)WS_XBOX + (c.pw * 16 + c.jw) * (2 * FQS) + c.jw * 8);
        ftg = lane != 32 ? (unsigned)(4 * l + 1) : (unsigned)(l + 1);
        so = FIRST ? (unsigned)((c.pq * 16 + fq * 4) * 1024 + (lw * 32 + fr * 2) * 8) : (unsigned)((c.pq * 16 + (lane >> 2)) * 1024 + (c.jq * 8 + lw * 2 + (lane & 1)) * 8);
        ho = FDM ? (unsigned)(262144 + ((lw * 36 + lane < 16 ? ph * 128 + 128 : c.pq * 128) + lw * 36 + lane - 16) * 8)
                 : (unsigned)(262144 + pick(lane < 32, c.pq * 128 + lw * 32 + lane, ph * 128 + 112 + lw * 4 + (lane & 3)) * 8);
        fo_ = pick(lane < 2, (unsigned)(262144 + (c.pq * 128 + c.jq * 8 + lw * 2 + lane) * 8), 0x80000000u);
        const int cq = lw * 12 + (fr < 12 ? fr : 11);
        co = (unsigned)((fq * 4 * 3072 + (cq >> 4) * 1024 + c.jq * 64 + (cq & 15) * 4) * 4);
        l3 = (unsigned)(M_CS + (HALO ? 768 : 0) + (cq * 4 + fq) * 4);
        l4 = (unsigned)(M_MEAN + pick(lane < 32, 16 + lw * 32 + (FIRST ? fr * 2 + fq : lane), lw * 4 + (lane & 3)) * 4);
        asm volatile("" : "+v"(foff), "+v"(ftg), "+v"(so), "+v"(co), "+v"(l3), "+v"(l4));
        if (!FIRST) asm volatile("" : "+v"(ho), "+v"(fo_));
        SCHED();
    };
    auto addr2 = [&]() {
        int lane = c.lane;
        asm volatile("" : "+v"(lane));
        const int fr = lane & 15, fq = lane >> 4;
        const int q4 = fr >> 2, p4 = lane & 3, fb = (q4 & 2) | ((fq & 1) << 2);
#pragma unroll
        for (int i = 0; i < 3; ++i) {
            const int T = 3 * lw + i;
            wt[i] = (unsigned)(qw_slot(SP % NQW) + (T >> 2) * 8192 + (8 * fq + q4) * 128 + (((2 * (T & 3) + (p4 >> 1)) ^ fb) << 4) + (p4 & 1) * 8);
        }
        const int T = 3 * lw + fq;
        cso = (unsigned)((((HALO ? 16 : 0) + rot) * 3072 + (T >> 2) * 1024 + c.jq * 64 + (T & 3) * 16 + fr) * 4);
        xt = (unsigned)(QA_OFF + (SP % NQA) * QA_SLOT + (lw * 32 + fr) * 128 + ((fq ^ (fr & 7)) << 4));
        xt1 = xt ^ 64;
        xso = (unsigned)hl<HS>((c.pq * 16 + rot) * 1024 + (lw * 32 + fr) * 8);
        on = (u32x4){0x3f803f80u, 0x3f803f80u, 0x3f803f80u, 0x3f803f80u};
        if (FIRST || HALO) asm volatile("" : "+v"(wt[0]), "+v"(wt[1]), "+v"(wt[2]), "+v"(cso), "+v"(on));
        if (FIRST) asm volatile("" : "+v"(xt), "+v"(xt1), "+v"(xso));
        SCHED();
    };
    auto addr3 = [&]() {
        Ctx c2 = c;
        asm volatile("" : "+v"(c2.lane));
        asm volatile("" : "+s"(c2.bid));
        vwo = wo_w_off(c2);
        tp = (unsigned)(pick(c2.lane < 30, (c2.bid >> 3) * 30 + c2.lane, (c2.bid >> 3) * 16 + c2.lane - 30 + (int)(WS_O >> 16)) * 65536 + 65536 - 128);
        tl = (unsigned)(lw == 1 ? ((c2.bid >> 3) * 2 + c2.lane) * 65536 : c2.bid * 8192 + (TWO && !(EXS && (c.jq & 7) == 3) ? c2.lane * 128 : 0));
        asm volatile("" : "+v"(vwo));
        if (FIRST) asm volatile("" : "+v"(tp), "+v"(tl));
        SCHED();
    };
    if (!FIRST || AD1 < 0) addr1();
    if (FIRST && ERL >= 5) { issueA(2, 2); SCHED(); }
    if (FIRST && (ERL == 2 || ERL == 5)) { issueW(2, 2); SCHED(); }
    if (!FIRST || AD2 < 0) addr2();
    if (!FIRST || AD3 < 0) addr3();
#pragma unroll
    for (int j = 0; j < NAI; ++j) asm volatile("" : "+v"(va[j]));
#pragma unroll
    for (int j = 0; j < NWI; ++j) asm volatile("" : "+v"(vw[j]));
    SCHED();
    int tv = 0; unsigned long long lt = 0;
    if (FIRST) {
        LSTAMP(55);
    } else {
        static_assert(CPY == 1 && RS0 > 0 && RN0 == NWI && WPRE >= 3 && (FIRST || SC.w[0] == NAI), "order");
        unsigned vx[2], xfo = (unsigned)WS_XBOX + (c.pq * 16 + c.jq) * (2 * FQS) + (HALO && c.pq > 0 ? (lane & 16) * (FQS / 16) : 0) + fr * 8, go = M_GO;
        u64 xv = lds_epoch(smem) + (unsigned)(l + 1);
#pragma unroll
        for (int i = 0; i < 2; ++i) vx[i] = pick(cq_, (unsigned)(((2 * lw + i) * 4 + 3) * 1024 + lane * 16), wq_src((2 * lw + i) * 4 + 3, lane, c.jq));
        asm volatile("" : "+v"(vx[0]), "+v"(vx[1]), "+v"(xfo), "+v"(xv), "+v"(go));
        BAR();
        issueW(0, 0);
        auto word = [&](int off) { return __builtin_amdgcn_readfirstlane(*(volatile LAS int*)(lds(go) + off)); };
        auto until = [&](int off, int v) {
#pragma unroll 1
            for (unsigned spins = 0; spins < (1u << 24); ++spins) { if (word(off) == v) break; }
            FENCE();
        };
        static_assert(WPRE == 3, "stages");
        int t1 = tile(1), t2 = tile(2);
        asm volatile("" : "+s"(t1), "+s"(t2));
        auto stage = [&](int s) {
            const char* b = W + (unsigned)((s == 1 ? t1 : t2) * (64 * 6144));
            unsigned d = (unsigned)((s == 3 ? QW_TOP : s * QW_SLOT) + lw * 1024), dx = d + (unsigned)(lw * 7168 + 3072);
            asm volatile("" : "+s"(d), "+s"(dx), "+s"(b));
#pragma unroll
            for (int j = 0; j < NWI; ++j) dma16<true>(d + j * 4096, vw[j], b);
#pragma unroll
            for (int i = 0; i < 2; ++i) dma16<true>(dx + i * 4096, vx[i], b);
        };
        auto first2 = [&]() { __builtin_amdgcn_s_setprio(LPRIO); issueA(0, 0); issueA(1, 1); };
        if (lw == 3) {
            if (!EPOLL) until(M_CNT - M_GO, 8 * l);
            Ask t;
            poll_rmw(t, c.ws, xfo, lane < (HALO && c.pq > 0 ? 32 : 16), xv, 63 - l);
            *(volatile LAS int*)lds(go) = l;
            until(M_CNT - M_GO, 8 * l);
            first2();
            STAMP_AT(32 + l * 8 + 0);
            wait_v<NAI>();
            keep(t);
        } else {
            until(M_CNT - M_GO, 8 * l);
            if (RSLP) __builtin_amdgcn_s_sleep(RSLP);
            int ns = 1;
#pragma unroll 1
            for (; ns < WPRE; ++ns) {
                if (word(0) == l) break;
                stage(ns);
            }
            until(0, l);
            first2();
            const int left = WPRE - ns;
#pragma unroll 1
            for (; ns < WPRE; ++ns) stage(ns);
            if (left == 0) wait_v<NAI>();
            else if (left == 1) wait_v<NAI + 8>();
            else if (left == 2) wait_v<NAI + 16>();
            else wait_v<NAI + 24>();
        }
        FENCE();
    }
    if (FIRST && ERL == 3) issueW(2, 2);
    if (ST0 && FIRST && lw == 2) STAMP_AT(57);
    if (FIRST && !ERL) BAR();
    __builtin_amdgcn_s_setprio(LPRIO);
    if (FIRST && !ERL) { LSTAMP(57); issueA(1, 1); issueW(1, 1); LSTAMP(58); }
    u32x4 st[4], cs[4]; u32x2 sh, fl; u64 fv = 0; float rs_, rq_, hs;
    f32x4 d[3], sm[2], g[2];
    bf16x8 wf[3][2], xf[2][2];
    const bool fact = FIRST ? lane < 32 : ((lane < 16 || lane >= (docs ? 16 : 32)) && lane <= (HALO ? 34 : 32));
    static_assert(XS + 3 <= (FIRST ? 12 : 13) && SPR == 1, "aux schedule");
    auto side = [&](auto ktc) {
        constexpr int kt = decltype(ktc)::value;
        {
            constexpr int C0 = FIRST ? 12 : XS + 2, C1 = FIRST ? 13 : XS + 3, C2 = 13;
            auto acc4 = [](u32x4& a, const u32x4& b) {
#pragma unroll
                for (int i = 0; i < 4; ++i) a[i] = __float_as_uint(addf(__uint_as_float(a[i]), __uint_as_float(b[i])));
                asm volatile("" : "+v"(a));
            };
            auto fin = [&]() {
                rs_ *= -1.f / 1024.f;
                asm volatile("" : "+v"(rs_));
                rq_ = __builtin_amdgcn_rsqf(fmaxf(rq_ * (1.f / 1024.f) - rs_ * rs_, 0.f) + LN_EPS);
                asm volatile("" : "+v"(rq_));
            };
            if constexpr (FIRST && kt == XS + 2) {
                asm volatile("" : "+v"(st[0]), "+v"(st[1]), "+v"(st[2]));
                acc4(st[0], st[1]); acc4(st[0], st[2]);
            }
            if constexpr (FIRST && kt == XS + 3) { asm volatile("" : "+v"(st[3])); acc4(st[0], st[3]); }
            if constexpr (kt == C0) {
                if (docs) { asm volatile("" : "+v"(cs[0]), "+v"(cs[1])); acc4(cs[0], cs[1]); }
            }
            if constexpr (FIRST && kt == 12) {
                asm volatile("s_nop 1" : "+v"(st[0]));
                rs_ = fold16(__uint_as_float(st[0][0]), __uint_as_float(st[0][2])); rq_ = fold16(__uint_as_float(st[0][1]), __uint_as_float(st[0][3]));
                rs_ = fold32(rs_, rs_); rq_ = fold32(rq_, rq_);
            }
            if constexpr (FIRST && kt == 13) fin();
            if constexpr (!FIRST && kt == TL0 + 2) {
                asm volatile("" : "+v"(sh));
                hs = fold16(__uint_as_float(sh[0]), __uint_as_float(sh[1]));
                hs = fold32(hs, hs);
                hs += __uint_as_float(__builtin_amdgcn_update_dpp(0u, __float_as_uint(hs), 0x124, 0xf, 0xf, false));
                hs += __uint_as_float(__builtin_amdgcn_update_dpp(0u, __float_as_uint(hs), 0x128, 0xf, 0xf, false));
                asm volatile("" : "+v"(hs));
            }
            if constexpr (!FIRST && kt == TL0 + 2 + TSP) {
                auto hh = __builtin_amdgcn_permlane16_swap(__float_as_uint(hs), __float_as_uint(hs), false, false);
                rs_ = __uint_as_float(hh[0]); rq_ = __uint_as_float(hh[1]);
                asm volatile("" : "+v"(rs_), "+v"(rq_));
                fin();
                auto rf = __builtin_amdgcn_make_buffer_rsrc(c.ws + WS_STATS + (size_t)l * 524288, 0, 524288, 0x00020000);
                __builtin_amdgcn_raw_buffer_store_b64((u32x2){__float_as_uint(rs_), __float_as_uint(rq_)}, rf, fo_, 0, 16);
            }
            if constexpr (kt == C1) {
                if (docs) { asm volatile("" : "+v"(cs[2]), "+v"(cs[3])); acc4(cs[2], cs[3]); acc4(cs[0], cs[2]); }
            }
            if constexpr (kt == C2) {
                if (docs) {
                    asm volatile("s_nop 1" : "+v"(cs[0]));
                    hs = fold32(fold16(__uint_as_float(cs[0][0]), __uint_as_float(cs[0][1])), fold16(__uint_as_float(cs[0][2]), __uint_as_float(cs[0][3])));
                }
            }
            if constexpr (kt == 13) {
                static_assert(WV0 == 1, "wait");
                if (FIRST || !FDM || docs) WAIT_V0();
                if (!FIRST && !FDM) { asm volatile("" : "+v"(sh)); rs_ = __uint_as_float(sh[0]); rq_ = __uint_as_float(sh[1]); }
                if ((FIRST || !FDM) && lane < (HALO ? 36 : 32)) {
                    *(LAS float*)lds(l4) = rs_;
                    *(LAS float*)(lds(l4) + (M_RSTD - M_MEAN)) = rq_;
                }
                if (docs) *(LAS float*)lds(l3) = hs;
                FENCE();
            }
        }
    };
    static_for<0, KT>([&](auto ktc) {
        constexpr int kt = decltype(ktc)::value;
        LSTAMP(3 * kt);
        if constexpr (FIRST || kt > 0) wait_v<SC.w[kt]>();
        LSTAMP(3 * kt + 1);
        if constexpr (kt == XS) {
            if (lw == 0) {
                asm volatile("" : "+v"(fl));
                const u64 ep = lds_epoch(smem);
                if (!__all(!fact || reached(__builtin_bit_cast(u64, fl), ep, ftg))) poll_flags(c.ws, ep, foff, fact, ftg);
            }
        }
        if constexpr (kt == 14) asm volatile("s_waitcnt lgkmcnt(0)" ::: "memory");
        BAR();
        FENCE();
        LMARK();
        LSTAMP(3 * kt + 2);
        if constexpr (TOPJ & (1 << kt)) side(ktc);
        if constexpr (kt == (FIRST ? SP + DS : TL0 + 2 + TSP) + 3) {
            if (lw == 0 && lane == 0) __hip_atomic_store((GAS u64*)(c.ws + WS_FLAGQ + (c.pq * 16 + c.jq) * FQS), fv, __ATOMIC_RELAXED, __HIP_MEMORY_SCOPE_AGENT);
        }
        if constexpr (kt == XS + 1 - NQA) {
            if (lw == 0) { if (fact) aload2c(fl, foff, c.ws); }
        }
        {
            const char* sb = c.ws + WS_STATS + (size_t)l * 524288;
            const char* cb = c.ws + WS_CSUM + (HALO ? 16 * 3072 * 4 : 0);
            constexpr int sa = 3 * (kt - XS), ca = FIRST ? 2 * (kt - XS - 1) : 2 * (kt - XS), cn = 2;
            if constexpr (FIRST) {
#pragma unroll
                for (int i = sa; i < sa + 3; ++i) if (i >= 0 && i < 4) aload4c(st[i], so, sb + i * 1024);
            }
            if constexpr (!FIRST && kt == TL0) aload2c(sh, so, sb);
            if constexpr (!FIRST && kt == TL1) {
                static_assert(TL1 <= 12, "covered by the wait of step 14");
                if (FDM) { if (lane < 36) { touch(M_MEAN + lw * 144, ho, sb); touch(M_RSTD + lw * 144, ho, sb + 4); } }
                else aload2c(sh, ho, sb);
            }
            if constexpr (ca >= 0 && ca < 4) {
                if (docs) {
#pragma unroll
                    for (int i = ca; i < ca + cn; ++i) aload4c(cs[i], co, cb + i * 12288);
                }
            }
        }
        if constexpr (kt == SP) {
            if (docs) {
#pragma unroll
                for (int i = 0; i < 3; ++i)
#pragma unroll
                    for (int ks = 0; ks < 2; ++ks) {
                        const s16x4 lo = tr_read(lds(wt[i]) + ks * 4096), hi = tr_read(lds(wt[i]) + ks * 4096 + 512);
                        wf[i][ks] = __builtin_shufflevector(lo, hi, 0, 1, 2, 3, 4, 5, 6, 7);
                    }
                if (FIRST) {
#pragma unroll
                    for (int mt = 0; mt < 2; ++mt)
#pragma unroll
                        for (int ks = 0; ks < 2; ++ks) xf[mt][ks] = *(const LAS bf16x8*)(lds(ks ? xt1 : xt) + mt * 2048);
                }
            }
            SCHED();
        }
        if constexpr (kt + 2 < KT && !(FIRST && ERL >= 5 && kt == 0)) issueA(kt + 2, (kt + 2) % NQA);
        LPUT(3 * kt + 2);
        if constexpr (kt == SP + DM) {
            SCHED();
            if (docs) {
                const bf16x8 ones = __builtin_bit_cast(bf16x8, on);
                const f32x4 zero = {0.f, 0.f, 0.f, 0.f};
#pragma unroll
                for (int ks = 0; ks < 2; ++ks) {
#pragma unroll
                    for (int i = 0; i < 3; ++i) d[i] = MFMA(ones, wf[i][ks], ks ? d[i] : zero);
                    if (FIRST) {
#pragma unroll
                        for (int mt = 0; mt < 2; ++mt) { sm[mt] = MFMA(ones, xf[mt][ks], ks ? sm[mt] : zero); g[mt] = MFMA(xf[mt][ks], xf[mt][ks], ks ? g[mt] : zero); }
                    }
                }
            }
            SCHED();
        }
        if constexpr (FIRST && ERL < 2 && kt < 5) {
            if constexpr (kt < 4) issueW(kt + 2, (kt + 2) % NQW);
            if constexpr (kt == 3) issueW(6, 2, 0, NWI / 2);
            if constexpr (kt == 4) { issueW(6, 2, NWI / 2, NWI); issueW(7, 3); }
        } else {
            if constexpr (WPRE == 2 && kt < 2) issueW(kt + 2, kt + 2);
            if constexpr (FIRST && (ERL == 4 || ERL == 6) && kt == 0) issueW(2, 2);
            if constexpr ((FIRST || WPRE == 3) && kt == 0) issueW(3, 3);
            if constexpr (kt >= 1 && kt + 3 < KT) issueW(kt + 3, (kt + 3) % NQW);
        }
        if constexpr (FIRST && kt == WT0) ahead();
        if constexpr (FIRST && kt == TSTEP) {
            if (lw == 1 && lane < 46) touch(M_TCH, tp, c.ws);
        }
        if constexpr (FIRST && kt == TW0) {
            if (lw == 0) touch(M_TCH, tl, Wo);
            else if (lw == 1) { asm volatile("" : "+s"(kp)); const char* outp = (const char*)kp->out; if (lane < 2) touch(M_TCH, tl, outp); }
        }
        if constexpr (kt == SP && DM > 0) {
            SCHED();
            if (docs) {
                asm volatile("" : "+v"(wf[0][0]), "+v"(wf[0][1]), "+v"(wf[1][0]), "+v"(wf[1][1]), "+v"(wf[2][0]), "+v"(wf[2][1]));
                if (FIRST) asm volatile("" : "+v"(xf[0][0]), "+v"(xf[0][1]), "+v"(xf[1][0]), "+v"(xf[1][1]));
            }
        }
        if constexpr (kt == SP + DS) {
            SCHED();
            if (docs) {
                if (FIRST) {
                    auto rst = __builtin_amdgcn_make_buffer_rsrc(c.ws + WS_STATS, 0, 524288, 0x00020000);
                    const bool b0 = fr & 1, b1 = fr & 2;
                    u32x2 sv[2];
#pragma unroll
                    for (int mt = 0; mt < 2; ++mt) {
                        float qa = b0 ? g[mt][1] : g[mt][0], qb = b0 ? g[mt][3] : g[mt][2];
                        asm volatile("" : "+v"(qa), "+v"(qb));
                        sv[mt] = (u32x2){__float_as_uint(sm[mt][0]), __float_as_uint(b1 ? qb : qa)};
                    }
                    if (fq == (fr >> 2)) {
                        __builtin_amdgcn_raw_buffer_store_b64(sv[0], rst, xso, 0, 16);
                        __builtin_amdgcn_raw_buffer_store_b64(sv[1], rst, xso + hl<HS>(128), 0, 16);
                    }
                }
                float d0 = d[0][0], d1 = d[1][0], d2 = d[2][0];
                asm volatile("" : "+v"(d0), "+v"(d1), "+v"(d2));
                const float v = fq == 0 ? d0 : (fq == 1 ? d1 : d2);
                auto rc = __builtin_amdgcn_make_buffer_rsrc(c.ws + WS_CSUM, 0, 2 * 16 * 3072 * 4, 0x00020000);
                if (lane < 48) __builtin_amdgcn_raw_buffer_store_b32(__float_as_uint(v), rc, cso, 0, 16);
                asm volatile("s_waitcnt lgkmcnt(0)" ::: "memory");
            }
        }
        if constexpr (kt == 1) {
            if (lw == 0) {
                const u32x2 e = *(const LAS u32x2*)(smem + M_EP);
                const u64 es = ((u64)(unsigned)__builtin_amdgcn_readfirstlane(e[1]) << 32) | (unsigned)__builtin_amdgcn_readfirstlane(e[0]);
                fv = es + (unsigned)(4 * l + 1);
                asm volatile("" : "+s"(fv));
            }
        }
        if constexpr (FIRST && kt == AD1) addr1();
        if constexpr (FIRST && kt == AD2) addr2();
        if constexpr (FIRST && kt == AD3) addr3();
        if constexpr (!(TOPJ & (1 << kt))) side(ktc);
        if constexpr (kt == 13) wo_w_issue<0, WT13>(c, Wo, vwo);
        if constexpr (kt == 14) { wo_w_issue<WT13, (HALO || WA0 > 12) ? 12 : WA0>(c, Wo, vwo); if (HALO) wo_w_issue<23, 23 + WB14>(c, Wo, vwo); }
        if constexpr (kt == 15) {
            if (HALO ? WBE < 2 : WA0 > 12) wo_w_issue<12, HALO ? 18 : WA0>(c, Wo, vwo);
            if (HALO) wo_w_issue<23 + WB14, 23 + WB14 + WB15>(c, Wo, vwo);
        }
    });
    __builtin_amdgcn_s_setprio(0);
    BAR();
    LMARK(); LPUT(48);
    LSTAMP(48);
    LSTORE(0);
    if (HALO ? WBE < 1 : WAE < 1) wo_w_issue<18, HALO ? 23 : 20>(c, Wo, vwo);
    if (HALO) {
        static_assert(!WBS || (WBE == 1 && WB14 == 0 && WRG <= 12), "Wo weights of the first twelve steps are older than the last 6 + WB15");
        if (WBS) wait_v<6 + WB15>();
        BAR();
        if (WBE) wo_w_issue<WBE < 2 ? 18 : 12, 23>(c, Wo, vwo);
        wo_w_issue<23 + WB14 + WB15, 32>(c, Wo, vwo);
    }
}

DI float max3(float a, float b, float c) { return fmaxf(fmaxf(a, b), c); }
#define SGB(m, n) __builtin_amdgcn_sched_group_barrier(m, n, 0)
template <bool HALO> struct Tail {
    int f[26], n[4], t[4][26], d[4][27], v[4][27], h3[32], s3[32], o3[33], q3[112];
    constexpr Tail() : f{}, n{}, t{}, d{}, v{}, h3{}, s3{}, o3{}, q3{} {
        const int FB[26] = {0, 1, 2, 3, 4, 5, 6, 7, 8, 9, 10, 11, 12, 13, 14, 15, 24, 25, 19, 23, 18, 22, 17, 21, 16, 20};
        const int FA[24] = {0, 1, 2, 3, 4, 5, 6, 7, 8, 9, 10, 11, 12, 13, 14, 15, 16, 17, 20, 21, 18, 19, 22, 23};
        const int TA[4][26] = {{0, 1, 2, 3, 7, 6, 5, 4, 8, 9, 10, 11, 15, 14, 13, 12, 16, 17, 18, 19, 23, 22, 21, 20, 0, 0}, {4, 8, 9, 10, 11, 15, 14, 13, 12, 16, 17, 18, 19, 23, 22, 21, 20, 0, 1, 2, 3, 7, 6, 5, 0, 0}, {16, 17, 18, 19, 23, 22, 21, 20, 0, 1, 2, 3, 7, 6, 5, 4, 8, 9, 10, 11, 15, 14, 13, 12, 0, 0}, {17, 18, 19, 23, 22, 21, 20, 8, 9, 10, 11, 15, 14, 13, 12, 16, 0, 0, 0, 0, 0, 0, 0, 0, 0, 0}};
        const int DA[4][26] = {{1, 1, 1, 1, 1, 1, 1, 1, 1, 1, 1, 1, 1, 1, 1, 1, 1, 1, 1, 1, 1, 1, 0, 0, 0, 0}, {1, 1, 1, 1, 1, 1, 1, 1, 1, 1, 1, 1, 1, 1, 1, 1, 1, 1, 1, 1, 1, 1, 0, 0, 0, 0}, {1, 1, 1, 1, 1, 1, 1, 1, 1, 1, 1, 1, 1, 1, 1, 1, 0, 0, 0, 0, 0, 0, 0, 0, 0, 0}, {2, 2, 2, 2, 1, 1, 1, 1, 1, 1, 1, 1, 0, 0, 0, 0, 0, 0, 0, 0, 0, 0, 0, 0, 0, 0}};
        const int VA[4][26] = {{1, 1, 0, 0, 0, 0, 1, 1, 1, 1, 1, 1, 1, 1, 1, 1, 1, 1, 1, 1, 1, 1, 2, 2, 0, 0}, {1, 1, 1, 1, 1, 1, 1, 1, 1, 1, 1, 1, 1, 1, 1, 1, 1, 1, 1, 1, 1, 1, 2, 2, 0, 0}, {1, 1, 1, 1, 1, 1, 1, 1, 1, 1, 1, 1, 1, 1, 1, 1, 2, 2, 2, 2, 2, 3, 3, 2, 0, 0}, {0, 0, 0, 0, 1, 1, 1, 1, 1, 1, 1, 1, 2, 2, 2, 2, 0, 0, 0, 0, 0, 0, 0, 0, 0, 0}};
        const int TB[4][26] = {{0, 1, 2, 3, 7, 6, 5, 4, 24, 8, 9, 10, 11, 15, 14, 13, 12, 25, 16, 17, 18, 19, 23, 22, 21, 20}, {24, 8, 9, 10, 11, 15, 14, 13, 12, 25, 16, 17, 18, 19, 23, 22, 21, 20, 0, 1, 2, 3, 7, 6, 5, 4}, {25, 16, 17, 18, 19, 23, 22, 21, 20, 0, 1, 2, 3, 7, 6, 5, 4, 24, 8, 9, 10, 11, 15, 14, 13, 12}, {0, 1, 2, 3, 7, 6, 5, 4, 24, 8, 9, 10, 11, 15, 14, 13, 12, 25, 19, 23, 0, 0, 0, 0, 0, 0}};
        const int DB[4][26] = {{1, 1, 1, 1, 1, 1, 1, 1, 1, 1, 1, 1, 1, 1, 1, 1, 1, 1, 1, 1, 1, 1, 1, 0, 0, 0}, {1, 1, 1, 1, 1, 1, 1, 1, 1, 1, 1, 1, 1, 1, 1, 1, 1, 1, 1, 1, 1, 1, 0, 0, 0, 0}, {1, 1, 1, 1, 1, 1, 1, 1, 1, 1, 1, 1, 1, 1, 1, 1, 1, 1, 1, 1, 1, 1, 1, 0, 0, 0}, {2, 2, 1, 1, 1, 1, 1, 1, 1, 1, 1, 1, 1, 1, 1, 1, 1, 1, 1, 0, 0, 0, 0, 0, 0, 0}};
        const int VB[4][26] = {{1, 1, 1, 1, 1, 1, 1, 1, 1, 1, 1, 1, 1, 1, 1, 1, 1, 1, 1, 1, 1, 1, 1, 2, 2, 2}, {1, 1, 1, 1, 1, 1, 1, 1, 1, 1, 1, 1, 1, 1, 1, 1, 1, 1, 1, 1, 1, 1, 3, 3, 3, 2}, {1, 1, 1, 1, 1, 1, 1, 1, 1, 1, 1, 1, 1, 1, 1, 1, 1, 1, 1, 1, 1, 1, 1, 3, 3, 2}, {0, 0, 1, 1, 1, 1, 1, 1, 1, 1, 1, 1, 1, 1, 1, 1, 1, 1, 1, 2, 0, 0, 0, 0, 0, 0}};
        const int G[32] = {8, 12, 9, 13, 10, 14, 11, 15, 16, 17, 20, 21, 18, 19, 22, 23, 0, 4, 1, 5, 2, 6, 3, 7, 0, 4, 1, 5, 2, 6, 3, 7};
        const int NM = HALO ? 26 : 24;
        n[0] = n[1] = n[2] = NM; n[3] = HALO ? 20 : 16;
        for (int i = 0; i < NM; ++i) f[i] = HALO ? FB[i] : FA[i];
        int vv = 0;
        for (int h = 0; h < 4; ++h) {
            v[h][0] = vv;
            for (int i = 0; i < n[h]; ++i) {
                t[h][i] = HALO ? TB[h][i] : TA[h][i];
                d[h][i + 1] = d[h][i] + (HALO ? DB[h][i] : DA[h][i]);
                vv += HALO ? VB[h][i] : VA[h][i];
                v[h][i + 1] = vv;
            }
        }
        for (int i = 0; i < 32; ++i) { h3[i] = G[i]; s3[i] = i >= 16 && i < 24 ? 0 : 1; }
        for (int i = 3; i < 32; ++i) o3[i + 1] = o3[i] + TOPS;
        int nv = 0, ns = 0;
        for (int k = 0; k < 8 * 14; ++k) {
            const bool top = k % TOPS < 2 || nv == 96;
            if (top && ns < 16 && nv >= 12 * (ns / 2 + 1) && (nv == 96 || k % TOPS == (ns & 1))) { q3[k] = (ns / 2) * 14 + 13 - (ns & 1); ++ns; }
            else { q3[k] = (nv / 12) * 14 + nv % 12; ++nv; }
        }
    }
};
template <bool HALO> DI void qkv_consume(const Ctx& c0, int l, KP kp, unsigned va0 = 0, unsigned vw0 = 0) {
    const Ctx c = fresh(c0);
    if (ST0 && l == 0 && c.wid == 2) STAMP_AT(59);
    LAS char* smem = c.smem;
    const int lane = c.lane, wr = c.wid >> 1, wc = c.wid & 1, fr = lane & 15, fq = lane >> 4;
    const int sw = HALO ? wr : 0;
    f32x4 acc[4][6], ah[2];
#pragma unroll
    for (int mt = 0; mt < 4; ++mt)
#pragma unroll
        for (int nt = 0; nt < 6; ++nt) { acc[mt][nt] = (f32x4){0.f, 0.f, 0.f, 0.f}; asm volatile("" : "+v"(acc[mt][nt])); }
    ah[0] = ah[1] = (f32x4){0.f, 0.f, 0.f, 0.f};
    if (HALO) asm volatile("" : "+v"(ah[0]), "+v"(ah[1]));
    const unsigned aoff0 = (unsigned)(QA_OFF + wr * 8192 + (HALO || l ? lane * 16 : fr * 128 + ((fq ^ (fr & 7)) << 4)));
    const unsigned hoff0 = (unsigned)(QA_OFF + 16384 + lane * 16);
    unsigned xm = HALO || l ? 1024u : 64u;
    asm volatile("" : "+s"(xm));
    static_assert(((QA_OFF | QA_SLOT) & 2047) == 0, "halves");
    const int q4 = fr >> 2, p4 = lane & 3;
    const int fb = (q4 & 2) | ((fq & 1) << 2);
    unsigned boff[2];
#pragma unroll
    for (int cc = 0; cc < 2; ++cc)
        boff[cc] = (unsigned)((8 * fq + q4) * 128 + (((2 * (2 * wc + (cc ^ sw)) + (p4 >> 1)) ^ fb) << 4) + (p4 & 1) * 8);
    bf16x8 xa[2][4], xh[2], wb[2][6];
    auto ldfrag = [&](int set, unsigned pa, unsigned ph, unsigned pb0, unsigned pb1, int ks) {
#pragma unroll
        for (int mt = 0; mt < 4; ++mt) xa[set][mt] = *(const LAS bf16x8*)(lds(pa) + mt * 2048);
        if (HALO) xh[set] = *(const LAS bf16x8*)lds(ph);
#pragma unroll
        for (int nt = 0; nt < 6; ++nt) {
            const LAS char* pb = lds((nt & 1) ? pb1 : pb0) + (nt >> 1) * 8192 + ks * 4096;
            const s16x4 lo = tr_read(pb), hi = tr_read(pb + 512);
            wb[set][nt] = __builtin_shufflevector(lo, hi, 0, 1, 2, 3, 4, 5, 6, 7);
        }
    };
    constexpr int NM = HALO ? 26 : 24, NH = HALO ? 17 : 16;
    auto mm = [&](int set, int i0, int i1) {
#pragma unroll
        for (int ii = i0; ii < i1; ++ii) {
            const int i = !HALO || ii < 8 ? ii : (!SNK ? (ii < 10 ? ii + 16 : ii - 2) : (ii == 8 ? 24 : (ii < 17 ? ii - 1 : (ii == 17 ? 25 : ii - 2))));
            const int nt = i >> 2, mt = SNK && (nt & 1) ? 3 - (i & 3) : i & 3;
            if (i < 16) acc[mt][nt] = MFMA(wb[set][nt], xa[set][mt], acc[mt][nt]);
            else if (i < 24) acc[mt][nt] = MFMA(xa[set][mt], wb[set][nt], acc[mt][nt]);
            else if (i == 24) ah[0] = MFMA(wb[set][2], xh[set], ah[0]);
            else ah[1] = MFMA(xh[set], wb[set][4], ah[1]);
        }
    };
    auto interleave = [&]() {
#pragma unroll
        for (int i = 0; i < NH; ++i) { SGB(0x008, 1); SGB(0x100, 1); }
        SCHED();
    };
    unsigned sa = 0, sb = 0;
    unsigned pa0 = aoff0, pa1 = aoff0 ^ xm, ph0 = hoff0, ph1 = hoff0 ^ xm, pb0 = boff[0], pb1 = boff[1];
    asm volatile("" : "+v"(pa0), "+v"(pa1), "+v"(pb0), "+v"(pb1));
    if (HALO) asm volatile("" : "+v"(ph0), "+v"(ph1));
    constexpr int M_CSH = M_CS + (HALO ? 768 : 0);
    unsigned sa0 = (unsigned)(M_MEAN + 64 + (wr * 64 + fr) * 4), sa1 = (unsigned)(M_MEAN + 64 + wr * 256 + fq * 16);
    unsigned sa2 = (unsigned)(M_CSH + wc * 128 + sw * 64 + fq * 16), sa6 = (unsigned)(M_CSH + 512 + wc * 128 + sw * 64 + fr * 4);
    unsigned sa3 = sa2 ^ 64, sa7 = sa6 ^ 64, sa4 = (unsigned)(M_MEAN + fr * 4), sa5 = (unsigned)(M_MEAN + fq * 16);
    asm volatile("" : "+v"(sa0), "+v"(sa1), "+v"(sa2), "+v"(sa6), "+v"(sa3), "+v"(sa7));
    if (HALO) asm volatile("" : "+v"(sa4), "+v"(sa5));
    const int head = c.jq * 2 + wc;
    auto rso = __builtin_amdgcn_make_buffer_rsrc(HALO ? c.ws + WS_O + (size_t)l * (8u << 20) + (size_t)((head * 16 + c.pq) * 8 + wr * 4) * (1024 << HO)
                                                      : c.ws + WS_KV + (size_t)(l >> 1) * (16u << 20) + (size_t)((c.pq * 32 + head) * 4 + wr) * (4096 << HK), 0, HALO ? 4096 << HO : 12288 << HK, 0x00020000);
    unsigned xrs = (unsigned)(M_XCH + wc * 6144 + wr * 2048), xhs = (unsigned)(M_XCH + wc * 6144 + 8 - wr * 8);
    unsigned b0m = (c.pq == 0 && wr == 0) ? 15u * 16u : 0u;
    asm volatile("" : "+s"(rso));
    if (HALO) asm volatile("" : "+s"(xrs), "+s"(xhs), "+s"(b0m));
    u32x4 cpy[3];
    unsigned cv, vt, cdst;
    const char *cs_, *tw, *cd_;
    int more = !HALO && l == 0 ? KT - 3 - XST - (c.pq & 7) : 0, three = c.jq & 4;
    if (!HALO) {
        static_assert(CPY + TOUCH == 2 && XSP == 1, "extra");
        asm volatile("" : "+s"(kp));
        const int g = c.jq >> 2, n = c.pq * 4 + (c.jq & 3), m = n >> 5, kt = (n >> 1) & 15;
        const char* src = *(const char* const __attribute__((address_space(4)))*)((const __attribute__((address_space(4))) char*)kp + (three ? (m ? 8 : 24) : (m ? 32 : 40)));
        const char* tsr = *(const char* const __attribute__((address_space(4)))*)((const __attribute__((address_space(4))) char*)kp + (c.wid < 3 ? 24 : 40));
        int o1 = kt * (64 * 6144) + (n & 1) * (32 * 6144) + (3 + 4 * (g & 2) - c.jq) * 128 + 2048, o2 = (n & 31) * 65536 + (11 - 4 * g) * 128 - c.pq * 128 * (D_ * 2) + 2048;
        int d1 = (int)WS_CQ + kt * (64 * 6144) + (m * 2 + (g >> 1)) * 24576 + (n & 1) * 4096, d2 = (int)WS_CO + (n & 31) * 65536 + (m * 2 + 1 - (g >> 1)) * 4096;
        asm volatile("" : "+s"(o1), "+s"(o2), "+s"(d1), "+s"(d2));
        cs_ = src + (three ? o1 : o2);
        tw = tsr + (unsigned)((c.wid < 3 ? c.wid : 0) * (2u << 20) + c.bid * 8192);
        cd_ = c.ws + (unsigned)(three ? d1 : d2);
        cv = pick(three, vw0, va0 ^ ((lane & 8) << 1));
        cdst = (unsigned)(c.wid * 1024 + lane * 16);
        vt = TWQ ? (unsigned)(((lane & 7) == 3 ? lane - 1 : lane) * 128) : 0u;
        if (EXS) vt &= (c.jq & 7) == 3 ? 0u : ~0u;
        asm volatile("" : "+s"(more), "+s"(three), "+s"(cs_), "+s"(tw), "+s"(cd_));
        asm volatile("" : "+v"(cv), "+v"(cdst), "+v"(vt));
        asm volatile("" : "=v"(cpy[0]), "=v"(cpy[1]), "=v"(cpy[2]));
    }
    unsigned hto = HT0 == 2 ? 0u : 0x80000000u;
    const char* W0 = (const char*)kp->wqkv0;
    if (!HALO && HT0) {
        const int n = ((c.pq & 7) * 3 + (c.jq & 3)) * 64 + c.wid * 16 + fr, r3 = (n * 43691) >> 17;
        const int t0 = rot_first(c.pq & 8, c.jq | 3) + 8;
        if (l == 0 && !(c.jq & 4) && (c.jq & 3) != 3 && lane < 16) hto = (unsigned)(((t0 * 64 + r3) & 1023) * 6144 + (n - 3 * r3) * 2048 + (c.jq | 3) * 128);
        asm volatile("" : "+v"(hto));
        asm volatile("" : "+s"(W0));
    }
    if (!HALO && HT0 == 2) { asm volatile("s_mov_b32 m0, %0\n\ts_nop 3\n\tglobal_load_lds_dword %1, %2" ::"s"(M_TCH), "v"(hto), "s"(W0) : "memory"); }
    if (!HALO && !ERL && l == 0) BAR();
    if (ST0 && l == 0 && c.wid == 1) STAMP_AT(44);
    if (!HALO) { if (HT0 == 2) wait_v<1>(); else WAIT_V0(); }
    if (ST0 && l == 0 && c.wid == 1) STAMP_AT(46);
    BAR();
    if (!HALO && HT0 == 1) { if (hto < 0x80000000u) asm volatile("s_mov_b32 m0, %0\n\ts_nop 3\n\tglobal_load_lds_dword %1, %2" ::"s"(M_TCH), "v"(hto), "s"(W0) : "memory"); }
    if (c.wid == 0) STAMP_AT(l * 8 + 0);
    ldfrag(0, pa0, ph0, pb0, pb1, 0);
    SCHED();
    if (ALN) WAIT_L0();
    SCHED();
    int cnt = KT - 3 - more, tv = 0;
    QSTAMP(0);
    auto half0 = [&]() {
        ldfrag(1, pa1, ph1, pb0, pb1, 1);
        mm(0, 0, NH);
        interleave();
        mm(0, NH, NH + 1); SCHED();
        asm volatile("s_addk_i32 %0, %2\n\ts_addk_i32 %1, %3" : "+s"(sa), "+s"(sb) : "i"(QA_SLOT), "i"(QW_SLOT) : "scc"); SCHED();
        mm(0, NH + 1, NH + 2); SCHED();
        asm volatile("s_cmpk_lg_u32 %0, %2\n\ts_cselect_b32 %0, %0, 0\n\ts_cmp_lg_u32 %1, %3\n\ts_cselect_b32 %1, %1, %4" : "+s"(sa), "+s"(sb) : "i"(NQA * QA_SLOT), "i"(3 * QW_SLOT), "i"(QW_TOP) : "scc"); SCHED();
        mm(0, NH + 2, NH + 3); SCHED();
        asm volatile("s_cmp_lg_u32 %0, %2\n\ts_cselect_b32 %0, %0, 0\n\ts_addk_i32 %1, -1" : "+s"(sb), "+s"(cnt) : "i"(QW_TOP + QW_SLOT) : "scc"); SCHED();
        mm(0, NH + 3, NH + 4); SCHED();
        pa0 = aoff0 + sa; pb0 = boff[0] + sb;
        asm volatile("" : "+v"(pa0), "+v"(pb0)); SCHED();
        mm(0, NH + 4, NH + 5); SCHED();
        pb1 = boff[1] + sb; pa1 = pa0 ^ xm;
        asm volatile("" : "+v"(pa1), "+v"(pb1)); SCHED();
        mm(0, NH + 5, NH + 6); SCHED();
        if (HALO) { ph0 = hoff0 + sa; ph1 = ph0 ^ xm; asm volatile("" : "+v"(ph0), "+v"(ph1)); SCHED(); }
        mm(0, NH + 6, NM);
        SCHED();
        WAIT_L0();
        BAR();
        SCHED();
    };
    asm volatile("" : "+s"(sa), "+s"(sb), "+s"(cnt));
    do {
    ALIGN8();
    do {
        half0();
        ldfrag(0, pa0, ph0, pb0, pb1, 0);
        mm(1, 0, NH);
        interleave();
        mm(1, NH, NM - 1);
        SCHED();
        WAIT_L0();
        asm volatile("s_nop 0");
        SCHED();
        mm(1, NM - 1, NM);
        SCHED();
    } while (cnt != 0);
    if (!HALO) {
        asm volatile("s_cmp_eq_u32 %[more], 0\n\ts_cbranch_scc1 .Lex%=\n\ts_mov_b32 m0, %[tch]\n\ts_nop 3\n\tglobal_load_lds_dword %[vt], %[tw] sc1\n\t"
                     "global_load_dwordx4 %[c0], %[v0], %[cs] offset:-2048\n\ts_cmp_eq_u32 %[three], 0\n\ts_cbranch_scc1 .Lex%=\n\t"
                     "global_load_dwordx4 %[c1], %[v0], %[cs]\n\tglobal_load_dwordx4 %[c2], %[v0], %[cs] offset:2048\n.Lex%=:"
                     : [c0] "+v"(cpy[0]), [c1] "+v"(cpy[1]), [c2] "+v"(cpy[2])
                     : [more] "s"(more), [tch] "i"(M_TCH), [vt] "v"(vt), [tw] "s"(tw), [v0] "v"(cv), [cs] "s"(cs_), [three] "s"(three) : "scc", "memory");
        cnt = more; more = 0;
        asm volatile("" : "+s"(cnt), "+s"(more));
        SCHED();
    }
    } while (cnt != 0);
    if (!HALO) {
        asm volatile("s_cmp_lg_u32 %[l], 0\n\ts_cbranch_scc1 .Lex%=\n\ts_waitcnt vmcnt(0)\n\ts_nop 3\n\tglobal_store_dwordx4 %[d], %[c0], %[ws] sc1\n\ts_cmp_eq_u32 %[three], 0\n\ts_cbranch_scc1 .Lex%=\n\t"
                     "v_add_u32 %[d], 0x2000, %[d]\n\tglobal_store_dwordx4 %[d], %[c1], %[ws] sc1\n\tv_add_u32 %[d], 0x2000, %[d]\n\tglobal_store_dwordx4 %[d], %[c2], %[ws] sc1\n.Lex%=:\n\ts_nop 1"
                     : [d] "+v"(cdst) : [c0] "v"(cpy[0]), [c1] "v"(cpy[1]), [c2] "v"(cpy[2]), [l] "s"(l), [ws] "s"(cd_), [three] "s"(three) : "scc", "memory");
    }
    QSTAMP(1);
    FENCE();
    SCHED();

    f32x4 csq[4], m4[4], mh4; float csv[2], mr[4], rr[4], mh;
    auto r1 = [&](unsigned b, int off) { return *(const LAS float*)(lds(b) + off); };
    auto r4_ = [&](unsigned b, int off) { return *(const LAS f32x4*)(lds(b) + off); };
    auto mf = [&](int set, int i) {
        const int nt = i >> 2, mt = i & 3;
        if (i < 16) acc[mt][nt] = MFMA(wb[set][nt], xa[set][mt], acc[mt][nt]);
        else if (i < 24) acc[mt][nt] = MFMA(xa[set][mt], wb[set][nt], acc[mt][nt]);
        else if (i == 24) ah[0] = MFMA(wb[set][2], xh[set], ah[0]);
        else ah[1] = MFMA(xh[set], wb[set][4], ah[1]);
        SCHED();
    };
    auto fold = [&](int i, int e) {
        const int nt = i >> 2, mt = i & 3;
        float t;
        if (i < 16) t = __builtin_fmaf(mr[mt], csq[nt][e], acc[mt][nt][e]);
        else if (i < 24) t = __builtin_fmaf(m4[mt][e], csv[nt - 4], acc[mt][nt][e]);
        else if (i == 24) t = __builtin_fmaf(mh, csq[2][e], ah[0][e]);
        else t = __builtin_fmaf(mh4[e], csv[0], ah[1][e]);
        asm volatile("" : "+v"(t));
        if (i < 24) acc[mt][nt][e] = t; else ah[i - 24][e] = t;
    };
    constexpr Tail<HALO> TL{};
    constexpr int NP = HALO ? 6 : 2, FN = 4 * NM;
    static_assert(TL.v[3][TL.n[3]] == NP + FN + (HALO ? 2 : 0) && KT % NQA == 1 && KT % NQW == 0, "tail");
    s16x4 wl[6], wh[6];
    auto frd = [&](int set, int k, int ao) {
        if (k < 4) xa[set][k] = *(const LAS bf16x8*)(lds(set ? pa1 : pa0) + ao + k * 2048);
        else if (k == 16) xh[set] = *(const LAS bf16x8*)(lds(set ? ph1 : ph0) + ao);
        else {
            const int nt = (k - 4) >> 1;
            const LAS char* pb = lds((nt & 1) ? pb1 : pb0) + (nt >> 1) * 8192 + set * 4096;
            if (k & 1) { wh[nt] = tr_read(pb + 512); wb[set][nt] = __builtin_shufflevector(wl[nt], wh[nt], 0, 1, 2, 3, 4, 5, 6, 7); }
            else wl[nt] = tr_read(pb);
        }
    };
    constexpr int RS = M_RSTD - M_MEAN;
    unsigned vl, vg, tb, xr, xh_, b0a, qb0, qb1;
    f32x4 bprev, bcur, bprev0; u32x4 ps; u32x2 ko; float ninf;
    auto rdop = [&](int h, int k) {
        const int j = k - NH;
        if (h == 0) {
            if (k < 2) { mr[2 * k] = r1(sa0, k * 128); mr[2 * k + 1] = r1(sa0, k * 128 + 64); }
            else if (k < 4) csq[k - 2] = r4_((k & 1) ? sa3 : sa2, 0);
            else if (j < 4) frd(0, k - 4, 2 * QA_SLOT);
            else csq[j - 2] = r4_((j & 1) ? sa3 : sa2, 256);
        } else if (j < 0) frd(h & 1, k, h == 1 ? 2 * QA_SLOT : 0);
        else if (h == 1) {
            if (j < 2) csv[j] = r1(j ? sa7 : sa6, 0);
            else if (HALO && j == 2) mh = r1(sa4, 0);
            else if (HALO && j == 3) mh4 = r4_(sa5, 0);
            else { const int mt = HALO ? 7 - j : j - 2; m4[mt] = r4_(sa1, mt * 64); }
        } else if (h == 2) {
            if (j == 0) m4[2] = r4_(sa1, 128);
            if (j == 1) ko = *(const LAS u32x2*)(lds(tb) + 3072);
            if (j == 2) { rr[0] = r1(sa0, RS); rr[1] = r1(sa0, 64 + RS); }
            if (j == 3) { rr[2] = r1(sa0, 128 + RS); rr[3] = r1(sa0, 192 + RS); }
            if (j >= 4) m4[5 - j] = r4_(sa1, (5 - j) * 64);
        } else {
            if (j == 0) bprev = r4_(tb, 0);
            if (j == 1) bcur = r4_(tb, 1024);
            if (j == 2) ps = *(const LAS u32x4*)(lds(tb) + 2048);
            if (j == 3) bprev0 = r4_(b0a, 0);
        }
    };
    auto vop = [&](int k) {
        if (k == 0) { asm volatile("" : "+v"(boff[0])); qb0 = boff[0] + QW_TOP; asm volatile("" : "+v"(qb0)); }
        else if (k == 1) { asm volatile("" : "+v"(boff[1])); qb1 = boff[1] + QW_TOP; asm volatile("" : "+v"(qb1)); }
        else if (k < NP) {
            if (k == 2) { vl = (unsigned)(lane * 16); asm volatile("" : "+v"(vl)); }
            if (k == 3) { tb = vl + M_TAB; asm volatile("" : "+v"(tb)); }
            if (k == 4) { b0a = tb | b0m; asm volatile("" : "+v"(b0a)); }
            if (k == 5) { xr = xrs + vl; asm volatile("" : "+v"(xr)); }
        } else if (k < NP + FN) fold(TL.f[(k - NP) >> 2], (k - NP) & 3);
        else {
            if (k == NP + FN) { xh_ = xhs + vl; asm volatile("" : "+v"(xh_)); }
            if (k == NP + FN + 1) { ninf = -INFINITY; asm volatile("" : "+v"(ninf)); }
        }
    };
    unsigned wa, wq;
    int wr2 = wr, wc2 = wc;
#pragma unroll
    for (int i = 0; i < NM; ++i) {
        mf(0, TL.t[0][i]);
        if (TL.t[0][i] < 24) asm volatile("" : "+v"(acc[TL.t[0][i] & 3][TL.t[0][i] >> 2])); else asm volatile("" : "+v"(ah[TL.t[0][i] & 1]));
        FENCE();
        if (i < NH) frd(1, i, 0);
        if (!HALO && i == NH + 3) { asm volatile("" : "+s"(wr2), "+s"(wc2)); vl = (unsigned)(lane * 16); wq = (unsigned)(AT_Q + (wc2 * 8 + wr2 * 4) * 1024) + vl; asm volatile("" : "+v"(vl), "+v"(wq)); }
        if (!HALO && i == NH + 4) { asm volatile("" : "+s"(wr2), "+s"(wc2)); wa = (unsigned)(AT_OWN + wc2 * 16384 + wr2 * 4096) + vl; asm volatile("" : "+v"(wa)); }
        if (i == NH + 1) { asm volatile("" : "+v"(boff[0]), "+v"(boff[1])); pb0 = boff[0] + 2 * QW_SLOT; pb1 = boff[1] + 2 * QW_SLOT; asm volatile("" : "+v"(pb0), "+v"(pb1)); }
        if (i == NH + 2) { pa0 = aoff0; pa1 = aoff0 ^ xm; asm volatile("" : "+v"(pa1)); if (HALO) { ph0 = hoff0; ph1 = hoff0 ^ xm; asm volatile("" : "+v"(ph1)); } }
        SCHED();
    }
    WAIT_L0();
    BAR();
    FENCE();
    SCHED();
    auto frags = [&](int h) {
        static_assert(TL.d[0][NM] == NH + 6 && TL.d[1][NM] == NH + (HALO ? 5 : 6) && TL.d[2][NM] == NH + (HALO ? 6 : 0) && TL.d[3][TL.n[3]] == NH + (HALO ? 4 : 0), "reads");
        if (h == 0) __builtin_amdgcn_s_waitcnt(0xC27F);
        if (h == 1) __builtin_amdgcn_s_waitcnt(HALO ? 0xC57F : 0xC67F);
        if (h == 2) __builtin_amdgcn_s_waitcnt(HALO ? 0xC67F : 0xC07F);
        if (h == 3) __builtin_amdgcn_s_waitcnt(HALO ? 0xC47F : 0xC07F);
    };
#pragma unroll
    for (int h = 0; h < 4; ++h) {
        if (h == 2) { pb0 = qb0; pb1 = qb1; }
#pragma unroll
        for (int i = 0; i < TL.n[h]; ++i) {
            if (!(h & 1) && i == TL.n[h] - 1) { frags(h); SCHED(); if (h) QSTAMP(4); }
            mf(~h & 1, TL.t[h][i]);
#pragma unroll
            for (int k = TL.d[h][i]; k < TL.d[h][i + 1]; ++k) { FENCE(); rdop(h, k); }
            SCHED();
#pragma unroll
            for (int k = TL.v[h][i]; k < TL.v[h][i + 1]; ++k) vop(k);
            SCHED();
        }
        if (h & 1) {
            frags(h);
            if (h == 1) QSTAMP(2); else QSTAMP(5);
            BAR();
            FENCE();
            SCHED();
            if (h == 1) QSTAMP(3); else QSTAMP(6);
        }
    }
    if (!HALO) {
        f32x4 r4[4];
        mf(1, 8);
#pragma unroll
        for (int mt = 0; mt < 4; ++mt) rr[mt] = r1(sa0, mt * 64 + RS);
        SCHED();
        mf(1, 12);
#pragma unroll
        for (int mt = 0; mt < 4; ++mt) r4[mt] = r4_(sa1, mt * 64 + RS);
        SCHED();
        mf(1, 9);
        vg = HK ? (unsigned)hv<HK>(lane) : vl;
        if (HK) asm volatile("" : "+v"(vg));
        SCHED();
        u32x4 pk[8];
        auto op = [&](int g, int k) {
            const int ta = g < 4 ? g : (g - 4) & 2, tb = g < 4 ? g : ta + 1, na = g < 4 ? 2 : 4 + (g & 1), nb = g < 4 ? 3 : na;
            f32x4& A = acc[ta][na]; f32x4& Bv = acc[tb][nb];
            if (k < 12) {
                const int h = k / 6, kk = k % 6;
                f32x4& X = h ? Bv : A;
                const int tx = h ? tb : ta;
                if (kk < 4) { float t = X[kk] * (g < 4 ? rr[tx] : r4[tx][kk]); asm volatile("" : "+v"(t)); X[kk] = t; }
                else { unsigned t = cvt_pk_bf16(X[2 * (kk - 4)], X[2 * (kk - 4) + 1]); asm volatile("" : "+v"(t)); pk[g][2 * h + kk - 4] = t; }
            } else if (k == 12) __builtin_amdgcn_raw_buffer_store_b128(pk[g], rso, vg + hp<HK>(g & 3), (g >> 2) * (8192 << HK), 16);
            else *(LAS u32x4*)(lds(wa) + (g >> 2) * 8192 + (g & 3) * 1024) = pk[g];
            SCHED();
        };
        auto pm = [&](int i) {
            if (i < 16) mf(1, TL.h3[i]);
            else if (i < 32) mf(((i - 16) >> 1) & 1, ((i - 16) >> 2) + 4 * (i & 1));
        };
        pm(3);
#pragma unroll
        for (int g = 0; g < 8; ++g) {
#pragma unroll
            for (int k = 0; k < 6; ++k) op(g, k);
            pm(2 * g + 4);
            if (g > 0) op(g - 1, 12);
#pragma unroll
            for (int k = 6; k < 12; ++k) op(g, k);
            pm(2 * g + 5);
            if (g > 0 && g <= LWA) op(g - 1, 13);
        }
        op(7, 12);
        QSTAMP(7);
        u32x4 qk;
        auto qop = [&](int mt, int k) {
            if (k == 0) { rr[mt] *= QSCALE; asm volatile("" : "+v"(rr[mt])); }
            else if (k < 13) {
                const int h = (k - 1) / 6, kk = (k - 1) % 6;
                f32x4& X = acc[mt][h];
                if (kk < 4) { float t = X[kk] * rr[mt]; asm volatile("" : "+v"(t)); X[kk] = t; }
                else { unsigned t = cvt_pk_bf16(X[2 * (kk - 4)], X[2 * (kk - 4) + 1]); asm volatile("" : "+v"(t)); qk[2 * h + kk - 4] = t; }
            } else *(LAS u32x4*)(lds(wq) + mt * 1024) = qk;
            SCHED();
        };
#pragma unroll
        for (int mt = 0; mt < 3; ++mt) {
            pm(20 + 4 * mt); if (3 * mt + LWA < 8) op(3 * mt + LWA, 13); qop(mt, 0); qop(mt, 1); qop(mt, 2);
            pm(21 + 4 * mt); if (3 * mt + 1 + LWA < 8) op(3 * mt + 1 + LWA, 13); qop(mt, 3); qop(mt, 4); qop(mt, 5);
            pm(22 + 4 * mt); if (3 * mt + 2 + LWA < 8) op(3 * mt + 2 + LWA, 13); qop(mt, 6); qop(mt, 7); qop(mt, 8);
            pm(23 + 4 * mt); qop(mt, 9); qop(mt, 10); qop(mt, 11); qop(mt, 12); qop(mt, 13);
        }
        if (c.wid == 0) STAMP_AT(l * 8 + 1);
        QSTAMP(8);
        {
            const u32x2 q0 = pack4(acc[3][0] * (rr[3] * QSCALE)), q1 = pack4(acc[3][1] * (rr[3] * QSCALE));
            *(LAS u32x4*)(lds(wq) + 3 * 1024) = (u32x4){q0[0], q0[1], q1[0], q1[1]};
        }
        QSTAMP(9);
        QSTORE();
        if (K0E) {
            SCHED();
            WAIT_L0();
            BAR();
            FENCE();
            SCHED();
        }
    } else {
        typedef unsigned u32x10 __attribute__((ext_vector_type(10)));
        u32x4 kp[4], qp[4];
        u32x10 vw[2];
        float ks[2][4], cs[2][4];
        auto pk8 = [&](const f32x4& x, const f32x4& y) { const u32x2 a = pack4(x), b = pack4(y); u32x4 r = {a[0], a[1], b[0], b[1]}; asm volatile("" : "+v"(r)); SCHED(); return r; };
        auto pkv = [&](int mt, int cc) { const u32x2 a = pack4(acc[mt][4 + cc]); vw[cc][2 * mt + 2] = a[0]; vw[cc][2 * mt + 3] = a[1]; };
        auto fr8 = [](const u32x4& x) { return __builtin_bit_cast(bf16x8, x); };
        mf(1, 24);
        unsigned ka0 = ko[0] + wr * 256, ka1 = ko[1] + wr * 256;
        asm volatile("" : "+v"(ka0), "+v"(ka1));
        SCHED();
        mf(1, 25);
#pragma unroll
        for (int mt = 0; mt < 4; ++mt) ks[0][mt] = r1(ka0, mt * 64);
        rr[0] *= QSCALE; asm volatile("" : "+v"(rr[0]));
        SCHED();
        mf(1, 11);
#pragma unroll
        for (int mt = 0; mt < 4; ++mt) ks[1][mt] = r1(ka1, mt * 64);
        rr[1] *= QSCALE; asm volatile("" : "+v"(rr[1]));
        SCHED();
        mf(1, 15);
        rr[2] *= QSCALE; asm volatile("" : "+v"(rr[2]));
        rr[3] *= QSCALE; asm volatile("" : "+v"(rr[3]));
        SCHED();
        mf(1, 19);
        const u32x4 hh = pk8(ah[0], ah[1]);
        mf(1, 23);
        *(LAS u32x2*)lds(xh_) = (u32x2){hh[0], hh[1]}; *(LAS u32x2*)(lds(xh_) + 1024) = (u32x2){hh[2], hh[3]};
        SCHED();
        mf(1, 10);
        kp[3] = pk8(acc[3][2], acc[3][3]);
        mf(1, 14);
        *(LAS u32x4*)(lds(xr) + 2048) = kp[3];
        pkv(3, 0); asm volatile("" : "+v"(vw[0]));
        *(LAS u32x2*)(lds(xr) + 3072) = (u32x2){vw[0][8], vw[0][9]};
        SCHED();
        mf(1, 3);
        pkv(3, 1); asm volatile("" : "+v"(vw[1]));
        *(LAS u32x2*)(lds(xr) + 3072 + 8) = (u32x2){vw[1][8], vw[1][9]};
        SCHED();
        mf(1, 7);
        mf(1, 9);
        kp[2] = pk8(acc[2][2], acc[2][3]);
        mf(1, 13);
        qp[3] = pk8(acc[3][0], acc[3][1]);
        mf(1, 2);
        f32x4 s0[4], s1[4];
        u32x4 pk;
        auto sc = [&](int mt, int h) {
            if (h) { s1[mt] = MFMA(fr8(kp[mt]), fr8(qp[mt]), bcur); asm volatile("" : "+v"(s1[mt])); }
            else { s0[mt] = MFMA(fr8(mt ? kp[mt - (mt > 0)] : pk), fr8(qp[mt]), mt == 0 ? bprev0 : bprev); asm volatile("" : "+v"(s0[mt])); }
            SCHED();
        };
        auto csm = [&](int mt) { cs[0][mt] = ks[0][mt] * rr[mt]; asm volatile("" : "+v"(cs[0][mt])); cs[1][mt] = ks[1][mt] * rr[mt]; asm volatile("" : "+v"(cs[1][mt])); };
        float u[2][4], mx[4], iv[4];
        auto sv = [](float x) { asm volatile("" : "+v"(x)); return x; };
        auto mx2 = [&](float a, float b) { return max3(ninf, a, b); };
        auto pick = [&](int mt, int h) { u[h][mt] = sv(max3(mx2(s0[mt][2 * h], s0[mt][2 * h + 1]), s1[mt][2 * h], s1[mt][2 * h + 1]) * cs[h][mt]); };
        auto vf = [&](int i) { const int mt = 2 - (i >> 2), nt = 4 + (i & 1), set = (i >> 1) & 1; SCHED(); acc[mt][nt] = MFMA(xa[set][mt], wb[set][nt], acc[mt][nt]); asm volatile("" : "+v"(acc[mt][nt])); SCHED(); };
        mf(1, 6);
        kp[1] = pk8(acc[1][2], acc[1][3]);
        mf(1, 8);
        WAIT_L0();
        QSTAMP(7);
        BAR();
        FENCE();
        SCHED();
        QSTAMP(8);
        mf(1, 12);
        {
            const u32x2 k0 = *(const LAS u32x2*)(lds(xr) + 8);
            FENCE(); SCHED();
            const u32x2 k1 = *(const LAS u32x2*)lds(xr);
            FENCE(); SCHED();
            const u32x2 a = *(const LAS u32x2*)(lds(xr) + 1024 + 8);
            FENCE(); SCHED();
            const u32x2 b = *(const LAS u32x2*)(lds(xr) + 1024);
            pk = (u32x4){k0[0], k0[1], k1[0], k1[1]};
            vw[0][0] = a[0]; vw[0][1] = a[1]; vw[1][0] = b[0]; vw[1][1] = b[1];
        }
        SCHED();
        auto sw16 = [](float& a, float& b) { auto r = __builtin_amdgcn_permlane16_swap(__float_as_uint(a), __float_as_uint(b), false, false); a = __uint_as_float(r[0]); b = __uint_as_float(r[1]); };
        auto sw32 = [](float& a, float& b) { auto r = __builtin_amdgcn_permlane32_swap(__float_as_uint(a), __float_as_uint(b), false, false); a = __uint_as_float(r[0]); b = __uint_as_float(r[1]); };
        auto ldr = [&](int i) { };
        auto top = [&](int mt) { mx[mt] = sv(mx2(u[0][mt], u[1][mt])); };
        auto pv1 = [&](int mt, int k) {
            SCHED();
            vw[k >> 1][2 * mt + 2 + (k & 1)] = cvt_pk_bf16(acc[mt][4 + (k >> 1)][2 * (k & 1)], acc[mt][4 + (k >> 1)][2 * (k & 1) + 1]);
            asm volatile("" : "+v"(vw[k >> 1]));
            SCHED();
        };
        auto uk = [&](int mt) { SCHED(); u[0][mt] = sv(u[0][mt] * ks[0][mt]); u[1][mt] = sv(u[1][mt] * ks[1][mt]); SCHED(); };
        mf(1, 1);
        qp[2] = pk8(acc[2][0], acc[2][1]);
        mf(1, 5);
        csm(3); SCHED();
        mf(1, 0);
        kp[0] = pk8(acc[0][2], acc[0][3]);
        mf(1, 4);
        csm(2); SCHED();
        sc(3, 1);
        qp[1] = pk8(acc[1][0], acc[1][1]);
        sc(3, 0);
        csm(1); ldr(0); SCHED();
        sc(2, 1);
        qp[0] = pk8(acc[0][0], acc[0][1]);
        sc(2, 0);
        csm(0); pick(3, 0); SCHED();
        sc(1, 1);
        pick(3, 1); ldr(1); SCHED();
        sc(1, 0);
        pick(2, 0); top(3); SCHED();
        sc(0, 1);
        pick(2, 1); ldr(2); SCHED();
        sc(0, 0);
        if (c.wid == 0) STAMP_AT(l * 8 + 1);
        pick(1, 0); top(2);
        vf(0);
        pick(1, 1); ldr(3);
        vf(1);
        sw16(mx[2], mx[3]);
        pick(0, 0);
        vf(2);
        pick(0, 1); top(1);
        vf(3);
        top(0); mx[2] = sv(mx2(mx[2], mx[3]));
        vf(4);
        sw16(mx[0], mx[1]); mx[0] = sv(mx2(mx[0], mx[1]));
        vf(5); pv1(2, 0);
        sw32(mx[0], mx[2]);
        { const float a = mx[0], b = mx[2]; mx[0] = sv(mx2(a, b)); mx[2] = sv(mx2(b, a)); }
        vf(6); pv1(2, 1);
        sw32(mx[0], mx[2]);
        mx[3] = sv(mx[2]); mx[1] = sv(mx[0]);
        vf(7);
        sw16(mx[2], mx[3]); sw16(mx[0], mx[1]);
        SCHED();
#pragma unroll
        for (int mm = 0; mm < 4; ++mm) { const int mt = 3 - mm; u[0][mt] = sv(u[0][mt] - mx[mt]); u[1][mt] = sv(u[1][mt] - mx[mt]); if (mm == 1) vf(8); }
        vf(9);
#pragma unroll
        for (int mm = 0; mm < 4; ++mm) {
            const int mt = 3 - mm;
            u[0][mt] = sv(__builtin_amdgcn_exp2f(u[0][mt])); u[1][mt] = sv(__builtin_amdgcn_exp2f(u[1][mt]));
            if (mm > 0) iv[mt + 1] = sv(u[0][mt + 1] + u[1][mt + 1]);
            if (mm < 2) vf(10 + mm);
        }
        SCHED();
        sw16(iv[2], iv[3]);
        iv[0] = sv(u[0][0] + u[1][0]);
        iv[2] = sv(iv[2] + iv[3]);
        pv1(2, 2);
        sw16(iv[0], iv[1]); iv[0] = sv(iv[0] + iv[1]);
        uk(3);
        sw32(iv[0], iv[2]);
        iv[0] = sv(iv[0] + iv[2]);
        pv1(2, 3);
        iv[0] = sv(__builtin_amdgcn_rcpf(iv[0]));
        pv1(1, 0);
        iv[2] = sv(iv[0]);
        uk(2);
        sw32(iv[0], iv[2]);
        iv[3] = sv(iv[2]); iv[1] = sv(iv[0]);
        pv1(1, 1);
        sw16(iv[2], iv[3]); sw16(iv[0], iv[1]);
        SCHED();
        f32x4 o[4][2];
        u32x4 pf[4];
        auto nrm = [&](int mt) { u[0][mt] = sv(u[0][mt] * iv[mt]); u[1][mt] = sv(u[1][mt] * iv[mt]); SCHED(); };
        auto prb = [&](int mt) {
            const unsigned p = cvt_pk_bf16(u[0][mt], u[1][mt]);
            pf[mt] = (u32x4){__builtin_amdgcn_perm(0u, p, ps[0]), __builtin_amdgcn_perm(0u, p, ps[1]), __builtin_amdgcn_perm(0u, p, ps[2]), __builtin_amdgcn_perm(0u, p, ps[3])};
            asm volatile("" : "+v"(pf[mt]));
            SCHED();
        };
        auto pvm = [&](int mt, int cc) {
            o[mt][cc] = MFMA(fr8((u32x4){vw[cc][2 * mt], vw[cc][2 * mt + 1], vw[cc][2 * mt + 2], vw[cc][2 * mt + 3]}), fr8(pf[mt]), ((f32x4){0.f, 0.f, 0.f, 0.f}));
            asm volatile("" : "+v"(o[mt][cc]));
            SCHED();
        };
        auto out = [&](int mt) {
            const u32x2 lo = pack4(o[mt][0]), hi = pack4(o[mt][1]);
            __builtin_amdgcn_raw_buffer_store_b128((u32x4){lo[0], lo[1], hi[0], hi[1]}, rso, vg + hp<HO>(mt), 0, 16);
            SCHED();
        };
        vg = (unsigned)hv<HO>(lane);
        asm volatile("" : "+v"(vg));
        nrm(3); nrm(2); prb(3);
        pv1(1, 2); pv1(1, 3);
        pvm(3, 0); prb(2); pvm(3, 1);
        uk(1); uk(0);
        pvm(2, 0); nrm(1); nrm(0); pvm(2, 1);
        prb(1); pv1(0, 0); pv1(0, 1);
        pvm(1, 0); pv1(0, 2); pv1(0, 3); prb(0);
        pvm(1, 1); out(3); pvm(0, 0); out(2); pvm(0, 1); out(1); out(0);
        SCHED();
        QSTAMP(9);
        if (c.wid == 0) STAMP_AT(l * 8 + 2);
        QSTORE();
    }
}

#define MFMA32(a, b, c) __builtin_amdgcn_mfma_f32_32x32x16_bf16(__builtin_bit_cast(bf16x8, a), __builtin_bit_cast(bf16x8, b), (c), 0, 0, 0)
DI float max16(const f32x16& s) {
    float m = max3(s[0], s[1], s[2]), n = max3(s[3], s[4], s[5]);
    m = max3(m, s[6], s[7]); n = max3(n, s[8], s[9]);
    m = max3(m, s[10], s[11]); n = max3(n, s[12], s[13]);
    m = max3(max3(m, s[14], s[15]), n, n);
    auto b = __builtin_amdgcn_permlane32_swap(__float_as_uint(m), __float_as_uint(m), false, false);
    return __builtin_amdgcn_fmed3f(__uint_as_float(b[0]), __uint_as_float(b[1]), INFINITY);
}
template <bool HI> DI void attn_a(const Ctx& c0, int l, const char* Wo, KP kp) {
    const Ctx c = fresh(c0);
    asm volatile("" : "+s"(Wo));
    LAS char* smem = c.smem;
    const int lane = c.lane, w = c.wid;
    constexpr int H = HI ? 0 : 1;
    int qt = HI ? w - 4 : 3 - w;
    asm volatile("" : "+s"(qt));
    const float NEG = -INFINITY;
    int zq = (lane & 31) - 4 * (lane >> 5);
    unsigned lb = (unsigned)(((lane >> 4) & 1) * 1024 + ((lane >> 5) * 16 + (lane & 15)) * 16);
    unsigned qa = (unsigned)(AT_Q + H * 8192 + qt * 2048) + lb, ta = (unsigned)(AT_OWN + H * 16384 - 3 * 2048 + qt * 2048) + lb;
    asm volatile("" : "+v"(lb), "+v"(qa), "+v"(ta));
    auto rd = [&](unsigned base, int off) { return *(const LAS u32x4*)(lds(base) + off); };
    f32x16 ce;
    u32x4 sel;
    unsigned tm = (unsigned)(AT_N + lane * 16);
    auto masks = [&]() {
        if (HI) {
#pragma unroll
            for (int r = 0; r < 16; ++r) ce[r] = 8 * (r >> 2) + (r & 3) <= zq ? 0.f : NEG;
            asm volatile("" : "+v"(ce));
        }
        const unsigned s1 = ((lane >> 4) ^ (lane >> 2)) & 1 ? 0u : 0x3f803f80u;
        sel = (u32x4){s1, s1, s1, s1};
        asm volatile("" : "+v"(sel));
    };
    u64 ep, pa_, kvv, nkv, kact;
    unsigned po, pk_, cnta = M_CNT, one = 1u;
    char* ob;
    const char *kvb, *s1_, *s2_;
    unsigned vk, dst, fo;
    auto setup = [&]() {
        if (HI) {
            const u32x2 e = *(const LAS u32x2*)(smem + M_EP);
            ep = ((u64)(unsigned)__builtin_amdgcn_readfirstlane(e[1]) << 32) | (unsigned)__builtin_amdgcn_readfirstlane(e[0]);
            po = o_post(c);
            pk_ = pick(lane < 2, (unsigned)(WS_KBOX + ((c.pq + 1 + lane) * 16 + c.jq) * 256 + lane * 8), (unsigned)(WS_FLAGQ + (c.pq * 16 + c.jq) * FQS));
            pa_ = c.pq < 14 ? 7ull : (c.pq < 15 ? 5ull : 4ull);
            fo = (unsigned)(WS_KBOX + (c.pq * 16 + c.jq) * 256 + lane * 8);
            asm volatile("" : "+v"(po), "+v"(pk_), "+v"(fo));
            asm volatile("" : "+s"(ep), "+s"(pa_));
        }
        ob = c.ws + WS_O + (size_t)l * (8u << 20) + (size_t)(unsigned)(((c.jq * 2 + H) * 16 + c.pq) * 8192 + qt * 2048);
        kvb = c.ws + WS_KV + (size_t)(unsigned)((l >> 1) * (16u << 20) + c.jq * 32768);
        asm volatile("" : "+s"(ob), "+s"(kvb));
    };
    auto setup2 = [&]() {
        const int p1 = c.pq >= 1 ? c.pq - 1 : c.pq, p2 = c.pq >= 2 ? c.pq - 2 : c.pq;
        s1_ = kvb + (size_t)(unsigned)(p1 * 524288);
        s2_ = kvb + (size_t)(unsigned)(p2 * 524288);
        vk = (unsigned)((w & 3) * 1024 + lane * 16); dst = (unsigned)(AT_N + (w & 3) * 1024);
        asm volatile("" : "+s"(s1_), "+s"(dst));
        if (!HI) asm volatile("" : "+s"(s2_));
        asm volatile("" : "+v"(vk));
    };
    auto arrive = [&](auto cc, unsigned msk, unsigned val) {
        unsigned old = 0;
        if (lane == 0) asm volatile("ds_add_rtn_u32 %0, %1, %2 offset:%3\n\ts_waitcnt lgkmcnt(0)" : "+v"(old) : "v"(cnta), "v"(one), "i"(decltype(cc)::value * 4) : "memory");
        if ((__builtin_amdgcn_readfirstlane(old) & msk) == msk) { if (lane < 16) post(c.ws, po, ep + (4 * l + val)); }
    };
    static_assert((MBOX & 2) && !HO && !HK, "layout");
    int tv = 0;
    auto kvs = [&]() {
        if (!HI) {
            SCHED();
            LSTAMP(2);
            asm volatile("s_mov_b64 exec, 1\n\ts_waitcnt vmcnt(0)\n\tds_add_u32 %0, %1\n\ts_mov_b64 exec, -1" :: "v"(cnta), "v"(one) : "memory");
            LSTAMP(3);
            SCHED();
        }
    };
    asm volatile("" : "+v"(cnta), "+v"(one), "+v"(tm));
    if (HI) {
        masks(); setup(); setup2();
        if (w == 5) {
#pragma unroll
            for (int i = 0; i < 4; ++i) *(LAS f32x4*)(lds(tm) + i * 1024) = (f32x4){ce[4 * i], ce[4 * i + 1], ce[4 * i + 2], ce[4 * i + 3]};
        }
        kvv = ep + (4 * l + 2); nkv = 0 - kvv;
        kact = __builtin_amdgcn_ballot_w64(w == KPW && lane < 2 && lane < c.pq);
        asm volatile("" : "+v"(kvv), "+v"(nkv));
        asm volatile("" : "+s"(kact));
        WAIT_L0();
        WAIT_V0();
        LSTAMP(0);
        if (w == 4) __builtin_amdgcn_s_setprio(HP4);
        BAR();
        FENCE();
    }
    auto kvp = [&]() {
        if (HI && w == 4) {
            __builtin_amdgcn_s_setprio(3);
#pragma unroll 1
            for (unsigned spins = 0; spins < (1u << 24); ++spins) {
                if (__builtin_amdgcn_readfirstlane(*(volatile LAS int*)lds(cnta)) == 8 * l + 4) break;
            }
            if (__builtin_amdgcn_inverse_ballot_w64(pa_)) post(c.ws, pk_, kvv);
            STAMP_AT(l * 8 + 2);
            __builtin_amdgcn_s_setprio(HP4);
        }
        SCHED();
    };
    if (KPP == -1) kvp();
    FENCE();
    SCHED();
    LSTAMP(1);
    u32x4 q0, q1, k0, k1, va, vp, pa, pb;
    f32x16 cm, s0, s1, o;
    f32x4 ls;
    auto stage = [&](f32x16& sc, f32x16& sn, const f32x16& cn, bool pend, bool zero, unsigned kb, int ko, unsigned vb, int vo1, unsigned nb, int vo0) {
        auto ex = [&](int i) { float x = __builtin_amdgcn_exp2f(sc[i]); asm volatile("" : "+v"(x)); sc[i] = x; SCHED(); };
        auto cv = [&](u32x4& p, int j, int i) { unsigned x = cvt_pk_bf16(sc[i], sc[i + 1]); asm volatile("" : "+v"(x)); p[j] = x; SCHED(); };
        sn = MFMA32(k0, q0, cn); SCHED();
        k0 = rd(kb, ko); SCHED();
        ex(0); ex(1); ex(2);
        if (pend) { o = MFMA32(vp, pb, o); SCHED(); }
        ex(3); ex(8); ex(9);
        sn = MFMA32(k1, q1, sn); SCHED();
        k1 = rd(kb, ko + 512); SCHED();
        ex(10); ex(11); cv(pa, 0, 0); cv(pa, 1, 2);
        if (pend) { ls = MFMA(__builtin_bit_cast(bf16x8, sel), __builtin_bit_cast(bf16x8, pb), ls); SCHED(); }
        vp = rd(vb, 8192 + vo1 + 512); SCHED();
        cv(pa, 2, 8); cv(pa, 3, 10); ex(4); ex(5);
        o = MFMA32(va, pa, zero ? (f32x16)(0.f) : o); SCHED();
        va = rd(nb, 8192 + vo0); SCHED();
        ex(6); ex(7); ex(12);
        ls = MFMA(__builtin_bit_cast(bf16x8, sel), __builtin_bit_cast(bf16x8, pa), zero ? (f32x4)(0.f) : ls); SCHED();
        ex(13); ex(14); ex(15); cv(pb, 0, 4); cv(pb, 1, 6); cv(pb, 2, 12); cv(pb, 3, 14);
    };
    auto flush = [&]() {
        ls = MFMA(__builtin_bit_cast(bf16x8, sel), __builtin_bit_cast(bf16x8, pb), ls); SCHED();
        o = MFMA32(vp, pb, o); SCHED();
    };
    float nm;
    {
        if (!HI) {
#pragma unroll
            for (int i = 0; i < 4; ++i) {
                const f32x4 t = *(const LAS f32x4*)(lds(tm) + i * 1024);
                ce[4 * i] = t[0]; ce[4 * i + 1] = t[1]; ce[4 * i + 2] = t[2]; ce[4 * i + 3] = t[3];
            }
        }
        q0 = rd(qa, 0);
        const u32x4 d0 = rd(ta, 6144);
        q1 = rd(qa, 512);
        const u32x4 d1 = rd(ta, 6144 + 512);
        k0 = rd(ta, 4096); k1 = rd(ta, 4096 + 512); va = rd(ta, 8192 + 6144);
        SCHED();
        if (KSP == -1) kvs();
        if (!HI) { masks(); setup(); SCHED(); }
        s0 = MFMA32(d0, q0, ce);
        s0 = MFMA32(d1, q1, s0);
        SCHED();
        if (!HI) { setup2(); SCHED(); }
        if (KSP == 0) kvs();
        if (KPP == 0) kvp();
        nm = -max16(s0);
        asm volatile("" : "+v"(nm));
#pragma unroll
        for (int r = 0; r < 16; ++r) { float t; asm("v_mov_b32 %0, %1" : "=v"(t) : "v"(nm), "i"(r)); cm[r] = t; }
        asm volatile("" : "+v"(cm));
        SCHED();
#pragma unroll
        for (int r = 0; r < 16; ++r) s0[r] = addf(s0[r], nm);
        asm volatile("" : "+v"(s0));
        SCHED();
        if (KSP == 1) kvs();
        if (KPP == 1) kvp();
        stage(s0, s1, cm, false, true, ta, 2048, ta, 6144, ta, 4096);
        if (KSP == 2) kvs();
        if (KPP == 2) kvp();
        if (qt > 0) {
            stage(s1, s0, cm, true, false, ta, 0, ta, 4096, ta, 2048);
            if (KSP == 3) kvs();
            if (qt > 1) {
                stage(s0, s1, cm, true, false, ta, 0, ta, 2048, ta, 0);
                if (qt > 2) stage(s1, s0, cm, true, false, ta, 0, ta, 0, ta, 0);
            }
        } else if (KSP == 3) kvs();
        flush();
    }
    LSTAMP(4);
    f32x16 c2;
    unsigned na, nb;
    auto rso = __builtin_amdgcn_make_buffer_rsrc(ob, 0, 2048, 0x00020000);
    auto starts = [&]() {
        const float n1 = c.pq >= 1 ? nm : NEG, n2 = c.pq >= 2 ? nm : NEG;
        asm volatile("" : "+v"(zq));
#pragma unroll
        for (int r = 0; r < 16; ++r) { cm[r] = n1; c2[r] = n2; ce[r] = 8 * (r >> 2) + (r & 3) <= zq ? NEG : n2; }
        na = (unsigned)(AT_N + H * 32768) + lb; nb = na + (unsigned)(16384 + qt * 2048);
        asm volatile("" : "+v"(cm), "+v"(c2), "+v"(ce), "+v"(na), "+v"(nb));
        asm volatile("" : "+s"(rso));
        SCHED();
    };
    {
        auto piece = [&](int b, int i) { dma16<true>(dst + b * 16384 + i * 4096, vk, ((b & 1) ? s2_ : s1_) + (b >> 1) * 16384 + i * 4096); };
        auto block = [&](int b) {
#pragma unroll
            for (int i = 0; i < 4; ++i) piece(b, i);
        };
        Ask t;
        if (HI) {
            poll_rmw<true>(t, c.ws, fo, __builtin_amdgcn_inverse_ballot_w64(kact), nkv, 62 - 4 * l);
            if (w == KPW) STAMP_AT(32 + l * 8 + 1);
        }
        WAIT_L0();
        LSTAMP(5);
        BAR();
        FENCE();
        LSTAMP(6);
        piece(0, H); piece(0, 2 + H);
        if (HI) {
            starts();
            if (APRIO) __builtin_amdgcn_s_setprio(APRIO);
            WAIT_V0();
            keep(t);
            LSTAMP(7);
            BAR();
            FENCE();
            if (w == 4) STAMP_AT(l * 8 + 3);
        } else {
            if (AB2 >= 0) block(1);
            if (AB2 > 0) block(2);
            starts();
            Ctx c2 = c;
            if (ELO) c2.wid = 0;
            unsigned vw = wo_w_off(c2);
            int qs = c.cw == 3 ? 1024 : 8 * D_ * 2;
            asm volatile("" : "+v"(vw));
            asm volatile("" : "+s"(qs));
            SCHED();
            wait_v<4 * AB2 + 4>();
            LSTAMP(7);
            BAR();
            if (AB2 < 0) block(1);
            if (AB2 <= 0) block(2);
            block(3);
            wait_v<8>();
            BAR();
            if (!ELO) {
                wo_w_issue<WA0, WA1>(c, Wo, vw);
                wait_v<WA1 - WA0>();
            } else WAIT_V0();
            LSTAMP(9);
            BAR();
            FENCE();
            if (!ELO) wo_w_issue<WA1, 26>(c, Wo, vw);
            else {
                const int s0 = w == 0 ? WA0 : (w == 1 ? WA0 + EN0 : WA0 + EN0 + EN1), s1 = w == 0 ? WA0 + EN0 : (w == 1 ? WA0 + EN0 + EN1 : (w == 2 ? 22 : 0));
#pragma unroll 1
                for (int sl = s0; sl < s1; ++sl) {
                    const char* b = Wo + (size_t)(unsigned)((2 * (sl & 15) + (sl >> 4)) * 65536);
#pragma unroll
                    for (int q = 0; q < 4; ++q) dma16<true>(sl * 4096 + q * 1024, vw, b + q * qs);
                }
            }
            if (LSL) { for (int i = 0; i < qt; ++i) __builtin_amdgcn_s_sleep(LSL); }
        }
    }
    SCHED();
    LSTAMP(8);
    {
        const u32x4 d0 = rd(na, 0), d1 = rd(na, 512);
        k0 = rd(na, 2048); k1 = rd(na, 2048 + 512); va = rd(na, 8192);
        SCHED();
        s0 = MFMA32(d0, q0, cm);
        s0 = MFMA32(d1, q1, s0);
        SCHED();
        stage(s0, s1, cm, false, false, na, 4096, na, 0, na, 2048);
        stage(s1, s0, cm, true, false, na, 6144, na, 2048, na, 4096);
        if (HI) { BAR(); FENCE(); SCHED(); }
        stage(s0, s1, cm, true, false, nb, 0, na, 4096, na, 6144);
        if (HI && CPOS == 3) { BAR(); SCHED(); }
        if (!HI && E30) {
            asm volatile("s_mov_b64 exec, 1\n\tds_add_u32 %0, %1 offset:12\n\ts_mov_b64 exec, -1" :: "v"(cnta), "v"(one) : "memory");
            SCHED();
        }
        stage(s1, s0, ce, true, false, nb, 2048, na, 6144, nb, 0);
        if (HI && CPOS == 4) { WAIT_L0(); BAR(); SCHED(); }
        stage(s0, s1, c2, true, false, nb, 4096, nb, 0, nb, 2048);
        if (HI && CPOS == 5) { BAR(); SCHED(); }
        if (qt < 3) {
            stage(s1, s0, c2, true, false, nb, 6144, nb, 2048, nb, 4096);
            if (qt < 2) {
                stage(s0, s1, c2, true, false, nb, 0, nb, 4096, nb, 6144);
                if (qt < 1) stage(s1, s0, c2, true, false, nb, 0, nb, 6144, nb, 0);
            }
        }
        flush();
    }
    LSTAMP(11);
    auto out = [&]() {
        const float iv = __builtin_amdgcn_rcpf(ls[0]);
#pragma unroll
        for (int u = 0; u < 2; ++u) {
            u32x4 t;
#pragma unroll
            for (int i = 0; i < 4; ++i) { const int r = 4 * u + (i >> 1) * 8 + (i & 1) * 2; t[i] = cvt_pk_bf16(o[r] * iv, o[r + 1] * iv); }
            __builtin_amdgcn_raw_buffer_store_b128(t, rso, lb + u * 512, 0, 16);
        }
        SCHED();
    };
    out();
    if (__builtin_expect(__any(!(ls[0] < SAFE_SUM)), 0)) {
        auto rk = __builtin_amdgcn_make_buffer_rsrc((void*)(kvb + H * 16384), 0, 16u << 20, 0x00020000);
        const int g0 = c.pq * 4 + qt - 8;
        float m = NEG;
#pragma unroll 1
        for (int ps = 0; ps < 2; ++ps) {
            o = (f32x16)(0.f); ls = (f32x4)(0.f);
#pragma unroll 1
            for (int j = g0 < 0 ? -g0 : 0; j < 9; ++j) {
                const int so = ((g0 + j) >> 2) * 524288 + ((g0 + j) & 3) * 2048;
                u32x4 f[4];
#pragma unroll
                for (int i = 0; i < 4; ++i) f[i] = __builtin_amdgcn_raw_buffer_load_b128(rk, lb + (i & 1) * 512, so + (i >> 1) * 8192, 16);
                f32x16 s;
                const float sb = ps ? -m : 0.f;
                float sa = j == 0 ? NEG : sb, sz = j == 8 ? NEG : sb;
                asm volatile("" : "+v"(zq), "+v"(sa), "+v"(sz));
#pragma unroll
                for (int r = 0; r < 16; ++r) s[r] = 8 * (r >> 2) + (r & 3) <= zq ? sa : sz;
                s = MFMA32(f[0], q0, s);
                s = MFMA32(f[1], q1, s);
                if (ps == 0) m = fmaxf(m, max16(s));
                else {
                    u32x4 p[2];
#pragma unroll
                    for (int r = 0; r < 16; r += 2) p[(r >> 2) & 1][(r >> 3) * 2 + ((r >> 1) & 1)] = cvt_pk_bf16(__builtin_amdgcn_exp2f(s[r]), __builtin_amdgcn_exp2f(s[r + 1]));
#pragma unroll
                    for (int u = 0; u < 2; ++u) {
                        o = MFMA32(f[2 + u], p[u], o);
                        ls = MFMA(__builtin_bit_cast(bf16x8, sel), __builtin_bit_cast(bf16x8, p[u]), ls);
                    }
                }
            }
        }
        __builtin_amdgcn_s_waitcnt(0x0F70);
        WAIT_V0();
        out();
    }
    SCHED();
    if (w == 3 || w == 4) STAMP_AT(32 + l * 8 + 3 + w);
    LSTAMP(12);
    if (HI) {
        WAIT_V0();
        arrive(std::integral_constant<int, 1>{}, 3u, 3u);
        if (w == 4) STAMP_AT(32 + l * 8 + 4);
        LSTAMP(13);
        if (APRIO) __builtin_amdgcn_s_setprio(0);
        if (lane == 0) asm volatile("ds_add_u32 %0, %1 offset:12" :: "v"(cnta), "v"(one) : "memory");
        WAIT_V0();
        arrive(std::integral_constant<int, 2>{}, 7u, 4u);
        if (w == 5 || w == 6) {
            Ctx c2 = c;
            c2.wid = 2 * w - 10;
            unsigned vw = wo_w_off(c2), vw2 = vw + (c.cw == 3 ? 1024u : 8u * D_ * 2);
            asm volatile("" : "+v"(vw), "+v"(vw2));
#pragma unroll 1
            for (unsigned spins = 0; spins < (1u << 22); ++spins) {
                if (__hip_atomic_load((LAS unsigned*)(smem + M_CNT + 12), __ATOMIC_RELAXED, __HIP_MEMORY_SCOPE_WORKGROUP) == (unsigned)(E30 ? 4 * l + 8 : 2 * l + 4)) break;
                __builtin_amdgcn_s_sleep(1);
            }
            FENCE();
            wo_w_issue<ELO ? 22 : 26, E30 ? 32 : 30>(c2, Wo, vw);
            c2.wid += 1;
            wo_w_issue<ELO ? 22 : 26, E30 ? 32 : 30>(c2, Wo, vw2);
        }
    }
    LSTAMP(23);
    ASTORE();
}

DI void wo_load(const Ctx& c0, int l, const char* Wn, const char* Wo) {
    const Ctx c = fresh(c0);
    asm volatile("" : "+s"(Wn), "+s"(Wo));
    const u64 ep = lds_epoch(c.smem);
    const unsigned fo = o_find(c);
        if (l == 0 && c.wid == 6) {
            const int fr = c.lane & 15, od = fr & 1, j0 = 4 * (c.lane >> 4) + od;
            const bool p0 = j0 > fr, p1 = j0 + 2 > fr;
            const unsigned N = 0xff800000u, Z = 0x0c0c0c0cu, lo = od ? 0x01000c0cu : 0x0c0c0100u, hi = od ? 0x03020c0cu : 0x0c0c0302u;
            LAS u32x4* t = (LAS u32x4*)(c.smem + M_TAB + c.lane * 16);
            t[0] = (u32x4){p0 && !od ? 0u : N, p0 && od ? 0u : N, p1 && !od ? 0u : N, p1 && od ? 0u : N};
            t[64] = (u32x4){!p0 && !od ? 0u : N, !p0 && od ? 0u : N, !p1 && !od ? 0u : N, !p1 && od ? 0u : N};
            t[128] = (u32x4){p0 ? lo : Z, p1 ? hi : Z, p0 ? Z : lo, p1 ? Z : hi};
            t[192] = (u32x4){(unsigned)(M_RSTD + j0 * 4 + (p0 ? 0 : 64)), (unsigned)(M_RSTD + j0 * 4 + 8 + (p1 ? 0 : 64)), 0u, 0u};
        }
    if (CPY && l == 0 && c.wid == CKW) {
#pragma unroll 1
        for (int i = 0; i < 2; ++i) {
            const int j = i ? c.jw : c.jq, g = i ? 2 - 2 * (j >> 3) : 1 + 2 * (j >> 3);
            if ((j & 7) == 3) poll_flags(c.ws, ep, (unsigned)WS_FLAGQ + ((c.lane >> 2) * 16 + 4 * g + (c.lane & 3)) * FQS, true, 2u);
        }
    }
    Ask t, t2;
    if (!(l & 1)) {
        Ctx c2 = c;
        c2.wid = 2 * c.wid - 10;
        unsigned vw = wo_w_off(c2), vw2 = vw + (c.cw == 3 ? 1024u : 8u * D_ * 2);
        asm volatile("" : "+v"(vw), "+v"(vw2));
        poll_rmw(t, c.ws, fo, c.wid == 7 && c.lane < 16, ep + (4 * l + 3), 61 - 4 * l);
        if (c.wid == 7) STAMP_AT(32 + l * 8 + 2);
        BAR();
        if (!E30 && (c.wid == 5 || c.wid == 6)) {
            __builtin_amdgcn_s_sleep(W30S);
            wo_w_issue<30, 32>(c2, Wo, vw);
            c2.wid += 1;
            wo_w_issue<30, 32>(c2, Wo, vw2);
        }
        if (c.wid != 7) WAIT_V0();
        else {
            __builtin_amdgcn_s_sleep(OSL);
            if (OPL) poll_flags(c.ws, ep, fo, c.lane < 16, (unsigned)(4 * l + 4));
        }
    } else {
        asm volatile("" : "=v"(t.v[0]), "=v"(t.v[1]), "=v"(t.v[2]), "=v"(t.v[3]));
        WAIT_V0();
    }
    poll_rmw(t2, c.ws, fo, c.wid == 7 && c.lane < 16 && (!OPL || (l & 1)), ep + (4 * l + 4), 60 - 4 * l);
    if (c.wid == 7) STAMP_AT(32 + l * 8 + 2 + !(l & 1));
    BAR();
    WAIT_V0();
    keep(t); keep(t2);
    if (l == 3 && c.bid == 0 && c.wid == 7) {
#pragma unroll 1
        for (int i = 0; i < 4; ++i) poll_flags(c.ws, ep, (unsigned)WS_FLAGQ + (i * 64 + c.lane) * FQS, true, 1u);
        if (c.lane == 0) __hip_atomic_store((GAS u64*)(c.ws + WS_EPOCH), ep + 64, __ATOMIC_RELAXED, __HIP_MEMORY_SCOPE_AGENT);
    }
    if (RS0 && l == 3) BAR();
}

DI void wo_consume(const bool B, const Ctx& c0, const bf16_t* xin, bf16_t* xout, int l) {
    u32x4 res[2][2];
    const Ctx c = fresh(c0);
    LAS char* smem = c.smem;
    const int lane = c.lane, w = c.wid, fr = lane & 15, fq = lane >> 4;
    const char* obase = c.ws + WS_O + (size_t)l * (8u << 20);
    int ovo = (c.pw * 8 + w * 2) * 1024 + lane * 16;
    asm volatile("" : "+v"(ovo));
    constexpr int NP = WNQ, R = 3;
    static_assert(!HO && !HX && !HS && !RLD && BPR == 1, "layout");
    auto ro = __builtin_amdgcn_make_buffer_rsrc((void*)obase, 0, 4u << 20, 0x00020000);
    asm volatile("" : "+s"(ro));
    u32x4 xa[NP][2];
    s16x4 wf[R][8];
    f32x4 acc[2][4];
    unsigned bw[8];
    int coff, rmo, spr;
    u32x4 on = {0x3f803f80u, 0x3f803f80u, 0x3f803f80u, 0x3f803f80u};
    unsigned sto, xpo, coff1, cnta = M_CNT, one = 1;
    u64 xv, b0, b1, am;
    auto rs = __builtin_amdgcn_make_buffer_rsrc(xout, 0, S_ * D_ * 2, 0x00020000);
    auto rst = __builtin_amdgcn_make_buffer_rsrc(c.ws + WS_STATS + (size_t)((l + 1) & 3) * 524288, 0, 524288, 0x00020000);
    auto prep2 = [&]() {
        sto = fq == (fr >> 2) && l < 3 ? (unsigned)((c.pw * 16 + c.jw) * 1024 + (w * 32 + fr) * 8) : 0x80000000u;
        xpo = x_post(c);
        coff = (int)pick(l == 3, (unsigned)rmo, (unsigned)coff);
        coff1 = coff + (l == 3 ? xrow<0>(0, 16) : 2048);
        spr = l == 3 ? 64 : 1024;
        asm volatile("" : "+s"(spr));
        b0 = __builtin_amdgcn_ballot_w64(fr & 1); b1 = __builtin_amdgcn_ballot_w64(fr & 2); am = __builtin_amdgcn_ballot_w64(x_act(c, !B));
        asm volatile("" : "+v"(on), "+v"(sto), "+v"(xpo), "+v"(coff), "+v"(coff1), "+v"(xv), "+v"(cnta), "+v"(one));
        asm volatile("" : "+s"(rs), "+s"(rst), "+s"(b0), "+s"(b1), "+s"(am));
    };
    auto prep = [&]() {
        int ln = lane;
        asm volatile("" : "+v"(ln));
        const int fr = ln & 15, fq = ln >> 4, q4 = fr >> 2, p4 = ln & 3;
        coff = (c.pw * 16 + c.jw) * 16384 + w * 4096 + (((fq & 1) * 2 + (fq >> 1)) * 16 + fr) * 16;
        rmo = xrow<0>(c.pw, w * 32 + fr) + xcol<0>(c.jw) + (fq & 1) * 32 + (fq >> 1) * 16;
        asm volatile("" : "+v"(coff), "+v"(rmo));
        const int fb = (q4 & 2) | ((fq & 1) << 2);
        const unsigned swp = (B && w >= 2) ? 2048u : 0u;
#pragma unroll
        for (int cc = 0; cc < 4; ++cc) {
            const unsigned b = (unsigned)((4 * fq + q4) * 128 + (((2 * cc + (p4 >> 1)) ^ fb) << 4) + (p4 & 1) * 8);
            bw[2 * cc] = b + swp; bw[2 * cc + 1] = b + (swp ^ 2048u);
            asm volatile("" : "+v"(bw[2 * cc]), "+v"(bw[2 * cc + 1]));
        }
    };
    auto start = [&](int mt, int pr) {
            {
                auto r0 = __builtin_amdgcn_permlane16_swap(res[mt][pr][0], res[mt][pr][2], false, false);
                auto r1 = __builtin_amdgcn_permlane16_swap(res[mt][pr][1], res[mt][pr][3], false, false);
#pragma unroll
                for (int h = 0; h < 2; ++h) {
                    acc[mt][2 * pr + h] = (f32x4){bf_lo(r0[h]), bf_hi(r0[h]), bf_lo(r1[h]), bf_hi(r1[h])};
                    asm volatile("" : "+v"(acc[mt][2 * pr + h]));
                }
            }
    };
    auto rdw = [&](auto ic) {
        constexpr int i = decltype(ic)::value, h = i >> 3, k = i & 7;
        wf[h % R][k] = tr_read(lds(bw[k]) + (h & 15) * 4096);
        if constexpr (h == 15) { bw[k] += 65536; asm volatile("" : "+v"(bw[k])); }
    };
    {
        auto resid = [&]() {
            const unsigned roff = pick(l != 0, (unsigned)coff, (unsigned)rmo);
            const char* xp = (const char*)xin;
#pragma unroll
            for (int i = 0; i < 4; ++i) aload4c(res[i >> 1][i & 1], roff, xp + (i >> 1) * (l ? 2048 : xrow<0>(0, 16)) + (i & 1) * (l ? 1024 : 64));
            SCHED();
        };
        unsigned opo = o_post(c), oca = B ? M_CNT : M_CNT + 8, old = 0, msk = B ? 3u : 7u;
        xv = lds_epoch(smem) + (unsigned)(l + 2);
        u64 ov = xv + (unsigned)(3 * l + 2);
        prep();
        asm volatile("" : "+v"(opo), "+v"(oca), "+v"(ov), "+v"(old), "+v"(one));
        asm volatile("" : "+s"(msk));
        SCHED();
        if (RBP) resid();
        static_for<0, 16>([&](auto ic) { rdw(ic); SCHED(); });
        if (!P2A) prep2();
        SCHED();
        wait_v<RBP ? 4 : 0>();
        if (lane == 0) asm volatile("ds_add_rtn_u32 %0, %1, %2\n\ts_waitcnt lgkmcnt(0)" : "+v"(old) : "v"(oca), "v"(one) : "memory");
        if ((__builtin_amdgcn_readfirstlane(old) & msk) == msk) { if (lane < 16) post(c.ws, opo, ov); }
        if (w == 0) STAMP_AT(l * 8 + 4);
        SCHED();
        if (!RBP) resid();
        if (P2A == 1) prep2();
        SCHED();
        if (w == 3) STAMP_AT(32 + l * 8 + 5);
        BAR();
    }
    FENCE();
    SCHED();
    if (w == 0) STAMP_AT(l * 8 + 5);
    int so = 0;
    auto next = [&](int j) { so += j == 15 ? 131072 - 15 * 262144 : 262144; asm volatile("" : "+s"(so)); };
    auto ldo = [&](int j, int mt) { xa[j % NP][mt] = __builtin_amdgcn_raw_buffer_load_b128(ro, ovo + mt * 1024, so, 16); };
#pragma unroll
    for (int h = 0; h < NP; ++h) {
        ldo(h, 0);
        SCHED();
        if (h > SH0 && h <= SH0 + 2) { start(h - SH0 - 1, 0); SCHED(); }
        ldo(h, 1);
        if (!WAL || h + 1 < NP) next(h);
        SCHED();
        if (P2A >= 2 && h == P2A - 2) { prep2(); SCHED(); }
        if (h == SH0) {
            wait_v<2 * SH0 + 2>();
            asm volatile("" : "+v"(res[0][0]), "+v"(res[0][1]), "+v"(res[1][0]), "+v"(res[1][1]));
        }
        if (h > SH0 && h <= SH0 + 2) start(h - SH0 - 1, 1);
        SCHED();
    }
    int d0 = 262144, d1 = 131072 - 15 * 262144;
    asm volatile("" : "+s"(d0), "+s"(d1));
    static_assert(WEP || !WAL, "last step");
    static_for<0, WEP ? 31 : 32>([&](auto jc) {
        constexpr int j = decltype(jc)::value;
        constexpr int c0 = j < 30 ? 16 + 8 * j : 256, c1 = j < 30 ? c0 + 8 : 256;
        if constexpr (j + NP == 16) {
            static_assert(c0 <= 8 * 20, "weights that landed before the attention");
            if (!B) BAR();
            FENCE();
            asm volatile("" : "+v"(ovo));
            SCHED();
        }
        if constexpr (j == 0 || j + NP == 16) ALIGN8();
        constexpr int nv = WAL == 1 || j == 0 || j + NP > 32 ? 2 * (NP - 1 < 31 - j ? NP - 1 : 31 - j) : 2 * NP - 3;
        __builtin_amdgcn_s_waitcnt(0x0870 | nv);
        const int sp = so;
        static_assert(!RS0 || (RS0 + NP >= 32 && RS0 < 31 && QW_SLOT <= (RS0 - 1) * 4096), "R0");
        if constexpr (RS0 && j == RS0) BAR();
        else asm volatile("s_add_i32 %0, %1, %2" : "=&s"(so) : "s"(sp), "s"(j + NP == 16 ? d1 : d0) : "scc");
        SCHED();
        static_for<0, 8>([&](auto mc) {
            constexpr int m = decltype(mc)::value, mt = m >> 2, nt = mt ? 3 - (m & 3) : m & 3;
            const bf16x8 wv = __builtin_shufflevector(wf[j % R][2 * nt], wf[j % R][2 * nt + 1], 0, 1, 2, 3, 4, 5, 6, 7);
            if constexpr (j < 31) asm volatile("v_mfma_f32_16x16x32_bf16 %0, %1, %2, %0" : "+v"(acc[mt][nt]) : "v"(wv), "v"(xa[j % NP][mt]));
            else acc[mt][nt] = MFMA(wv, __builtin_bit_cast(bf16x8, xa[j % NP][mt]), acc[mt][nt]);
            SCHED();
            if constexpr (c0 < c1 && m < 7) { rdw(std::integral_constant<int, c0 + m>{}); SCHED(); }
            if constexpr (c0 < c1 && m == 6) rdw(std::integral_constant<int, c0 + 7>{});
            if constexpr (m == WL1 && j > 0 && j - 1 + NP < 32) xa[(j + NP - 1) % NP][1] = __builtin_amdgcn_raw_buffer_load_b128(ro, ovo + 1024, sp, 16);
            if constexpr (m == WL1 + 4 && j + NP < 32) ldo(j, 0);
            SCHED();
        });
    });
    const bf16x8 ones = __builtin_bit_cast(bf16x8, on);
    u32x4 o[2][2];
    f32x4 sm[2], g[2];
    const f32x4 zero = {0.f, 0.f, 0.f, 0.f};
    auto sel = [&](float a, float b, u64 m) { return __builtin_amdgcn_inverse_ballot_w64(m) ? b : a; };
    auto stats = [&](int mt) {
        float qa = sel(g[mt][0], g[mt][1], b0), qb = sel(g[mt][2], g[mt][3], b0);
        asm volatile("" : "+v"(qa), "+v"(qb));
        SCHED();
        if (WEP) asm volatile("" :: "v"(sm[1][1]), "v"(sm[1][2]), "v"(sm[1][3]), "v"(o[1][0]), "v"(o[1][1]), "v"(on));
        __builtin_amdgcn_raw_buffer_store_b64((u32x2){__float_as_uint(sm[mt][0]), __float_as_uint(sel(qa, qb, b1))}, rst, sto + mt * 128, 0, 16);
        SCHED();
    };
    u32x2 pk[2];
    auto last = [&](int mt, int nt) {
        acc[mt][nt] = MFMA(__builtin_shufflevector(wf[31 % R][2 * nt], wf[31 % R][2 * nt + 1], 0, 1, 2, 3, 4, 5, 6, 7), __builtin_bit_cast(bf16x8, xa[31 % NP][mt]), acc[mt][nt]);
        SCHED();
    };
    auto cv = [&](int mt, int pr) {
        pk[0] = pack4(acc[mt][2 * pr]); pk[1] = pack4(acc[mt][2 * pr + 1]);
        asm volatile("" : "+v"(pk[0]), "+v"(pk[1]));
        SCHED();
    };
    auto st = [&](int mt, int pr) {
        o[mt][pr] = widen(pk[0], pk[1]);
        __builtin_amdgcn_raw_buffer_store_b128(o[mt][pr], rs, mt ? coff1 : coff, pr ? spr : 0, 16);
        SCHED();
    };
    auto gq = [&](int mt, int pr, bool f) { const bf16x8 of = __builtin_bit_cast(bf16x8, o[mt][pr]); g[mt] = MFMA(of, of, f ? zero : g[mt]); SCHED(); };
    auto gs = [&](int mt, int pr, bool f) { sm[mt] = MFMA(ones, __builtin_bit_cast(bf16x8, o[mt][pr]), f ? zero : sm[mt]); SCHED(); };
    WAIT_L0();
    last(0, 0); last(0, 1); last(0, 2); last(0, 3);
    last(1, 3); cv(0, 0);
    last(1, 2); st(0, 0);
    last(1, 1); cv(0, 1);
    last(1, 0); st(0, 1);
    if (w == 0) STAMP_AT(l * 8 + 6);
    gq(0, 0, true); cv(1, 1);
    gs(0, 0, true); st(1, 1);
    gq(0, 1, false); cv(1, 0);
    gs(0, 1, false); st(1, 0);
    gq(1, 1, true);
    gs(1, 1, true);
    gq(1, 0, false);
    gs(1, 0, false);
    stats(0);
    stats(1);
    if (l < 3) {
        unsigned old = 0;
        if (!RS0 || RN1 > RN0) BAR();
        wait_v<XE2>();
        if (lane == 0) asm volatile("ds_add_rtn_u32 %0, %1, %2\n\ts_waitcnt lgkmcnt(0)" : "+v"(old) : "v"(cnta), "v"(one) : "memory");
        if ((__builtin_amdgcn_readfirstlane(old) & 3) == 3) {
            if (__builtin_amdgcn_inverse_ballot_w64(am)) post(c.ws, xpo, xv);
        }
    }
    if (w == 0) STAMP_AT(l * 8 + 7);
}

__global__ __launch_bounds__(512, 2) __attribute__((aligned(4096))) void k_main(Params P) {
    extern __shared__ __attribute__((aligned(16))) char smem_[];
    KP kp = (KP)__builtin_amdgcn_kernarg_segment_ptr();
    const char *x0, *w0, *wo;
    Ctx c;
    c.smem = (LAS char*)smem_;
    c.lane = threadIdx.x & 63;
    c.wid = __builtin_amdgcn_readfirstlane(threadIdx.x >> 6);
    c.bid = blockIdx.x;
    const bool loader = c.wid >= 4;
    unsigned va, vw;
    auto enter = [&](const bool LD) {
        if (LD) asm volatile("s_load_dwordx2 %0, %4, 0x0\n\ts_load_dwordx2 %1, %4, 0x8\n\ts_load_dwordx2 %2, %4, 0x10\n\ts_load_dwordx2 %3, %4, 0x20 ; loader" : "=&s"(x0), "=&s"(w0), "=&s"(c.ws), "=&s"(wo) : "s"(kp));
        else asm volatile("s_load_dwordx2 %0, %4, 0x0\n\ts_load_dwordx2 %1, %4, 0x8\n\ts_load_dwordx2 %2, %4, 0x10\n\ts_load_dwordx2 %3, %4, 0x20" : "=&s"(x0), "=&s"(w0), "=&s"(c.ws), "=&s"(wo) : "s"(kp));
        if (LD) asm volatile("; loader" : "+s"(c.bid), "+s"(c.wid)); else asm volatile("; consumer" : "+s"(c.bid), "+s"(c.wid));
        if (LD) asm volatile("; loader" : "+v"(c.lane)); else asm volatile("; consumer" : "+v"(c.lane));
        static_assert(QXM == 8 && WXM == 4 && ALT == 1, "map");
        c.pq = (((c.bid & 1) * 8) ^ PQX) + ((c.bid >> 3) & 7); c.jq = (c.bid & 6) * 2 + (c.bid >> 6);
        const int w = c.wid, lr = c.lane >> 3, lc = c.lane & 7;
        va = (unsigned)((c.pq * 128 + 8 * (w & 3) + lr) * (D_ * 2) + ((lc ^ lr) << 4));
        vw = (unsigned)((8 * (w & 3) + lr) * 6144 + c.jq * 128 + ((lc ^ ((lr & 2) | ((w & 1) << 2))) << 4));
        asm volatile("" : "+v"(va), "+v"(vw));
        c.pw = ((c.pq & 8) ^ 8) + ((c.bid >> 1) & 1) * 4 + ((c.bid >> 3) & 3); c.jw = (c.bid & 4) * 2 + (c.bid >> 5); c.cw = 0;
        if (LD) {
            asm volatile("" : "+s"(c.pw), "+s"(c.jw));
            asm volatile("s_waitcnt lgkmcnt(0)" : "+s"(x0), "+s"(w0), "+s"(c.ws), "+s"(wo));
            if (ESL) __builtin_amdgcn_s_sleep(ESL);
        } else {
            int t0 = first_tile(first_order(c.pq, c.jq), 0);
            asm volatile("" : "+s"(t0));
            asm volatile("s_waitcnt lgkmcnt(0) ; c" : "+s"(x0), "+s"(w0), "+s"(c.ws), "+s"(wo));
            if (ST0 && c.wid == 3) STAMP_AT(41);
            const char* xb = x0 + (unsigned)(t0 * 128);
            const char* wb = w0 + (unsigned)(t0 * (64 * 6144));
#pragma unroll
            for (int h = 0; h < 2; ++h) {
#pragma unroll
                for (int j = 0; j < 2; ++j) dma16<false>(QA_OFF + (j * 8 + h * 4 + w) * 1024, va, xb + (j * 64 + h * 32) * (D_ * 2));
#pragma unroll
                for (int j = 0; j < 3; ++j) dma16<false>((j * 8 + h * 4 + w) * 1024, vw, wb + h * (32 * 6144) + j * 2048);
            }
            if (ST0 && c.wid == 1) STAMP_AT(43);
            if (w == 0 && c.lane < 2) touch(M_EP, (unsigned)(WS_EPOCH + c.lane * 4), c.ws);
            if (w == 1 && c.lane < 5) *(LAS unsigned*)(c.smem + (c.lane < 4 ? M_CNT + c.lane * 4 : M_GO)) = 0u;
        }
        asm volatile("" : "+s"(kp), "+s"(c.bid), "+s"(c.ws));
    };
    if (c.wid == CPW) {
        const char* pc; int len;
        asm volatile("s_getpc_b64 %0\n.Lcpf_a:\n\ts_mov_b32 %1, .Lcpf_end-.Lcpf_a-4" : "=s"(pc), "=s"(len));
        const int o = min((int)(((c.bid >> 3) & 7) * 64 + c.lane) * 128, len);
        asm volatile("s_mov_b32 m0, %0\n\ts_nop 3\n\tglobal_load_lds_dword %1, %2" ::"s"(M_TCH), "v"(o), "s"(pc) : "memory");
    }
    const char* xi;
    auto step = [&](int l) {
        asm volatile("" : "+s"(kp), "+s"(c.ws), "+s"(c.jw), "+s"(c.bid));
        c.pq ^= 8; c.pw ^= 8; c.cw = CPY ? c.jw & 7 : 0;
        xi = c.ws + WS_X + (unsigned)(l * (8u << 20));
        wo = (const char*)((l & 1) ? kp->wo0 : kp->wo1);
        if (c.cw == 3) wo = c.ws + WS_CO + (((l & 1) ? 2 : 0) + (c.jw >> 3)) * 4096;
    };
    if (loader) {
        enter(true);
        xi = x0;
            qkv_load<false, true>(c, xi, w0, wo, 0, kp, va, vw);
#pragma unroll 1
        for (int l = 0;;) {
            asm volatile("" : "+s"(l));
            if (!(l & 1)) attn_a<true>(c, l, wo, kp);
            wo_load(c, l, nullptr, wo);
            if (l == 3) break;
            step(l);
            ++l;
            if (l == 2) { qkv_load<false, false>(c, xi, (const char*)kp->wqkv0, wo, l); }
            else { qkv_load<true, false>(c, xi, (const char*)kp->wqkv1, wo, l); }
        }
    } else {
        enter(false);
        xi = x0;
        bf16_t* xo = (bf16_t*)(c.ws + WS_X);
#pragma unroll 1
        for (int l = 0; l < 4; ++l) {
            if (l & 1) { qkv_consume<true>(c, l, kp); }
            else {
                qkv_consume<false>(c, l, kp, va, vw);
                attn_a<false>(c, l, wo, kp);
            }
            wo_consume(l & 1, c, (const bf16_t*)xi, xo, l);
            step(l);
            xo = l == 2 ? kp->out : (bf16_t*)(c.ws + WS_X + (unsigned)((l + 1) * (8u << 20)));
            asm volatile("" : "=v"(va), "=v"(vw));
        }
    }
    asm volatile(".Lcpf_end:");
}

extern "C" void kernel_launch(void* const* d_in, const int* in_sizes, int n_in, void* d_out, int out_size, void* d_ws, size_t ws_size,
                              hipStream_t stream) {
    Params P;
    P.x0 = (const bf16_t*)d_in[0];
    P.wqkv0 = (const bf16_t*)d_in[1];
    P.wo0 = (const bf16_t*)d_in[2];
    P.wqkv1 = (const bf16_t*)d_in[4];
    P.wo1 = (const bf16_t*)d_in[5];
    P.out = (bf16_t*)d_out;
    P.ws = (char*)d_ws;
    (void)hipFuncSetAttribute((const void*)k_main, hipFuncAttributeMaxDynamicSharedMemorySize, LDS_TOTAL);
    k_main<<<256, 512, LDS_TOTAL, stream>>>(P);
}
```

```cpp
#include <hip/hip_runtime.h>
#include <stdint.h>
#include <type_traits>

typedef unsigned short bf16_t;
typedef short bf16x8 __attribute__((ext_vector_type(8)));
typedef short s16x4 __attribute__((ext_vector_type(4)));
typedef float f32x4 __attribute__((ext_vector_type(4)));
typedef float f32x2 __attribute__((ext_vector_type(2)));
typedef float f32x16 __attribute__((ext_vector_type(16)));
typedef unsigned u32x4 __attribute__((ext_vector_type(4)));
typedef unsigned u32x2 __attribute__((ext_vector_type(2)));
#define DI __device__ __forceinline__
#define LAS __attribute__((address_space(3)))
#define GAS __attribute__((address_space(1)))
#define STAMP 0

constexpr int S_ = 2048, D_ = 1024, KT = 16;
constexpr float LN_EPS = 1e-5f;
constexpr float QSCALE = 0.17677669529663687f * 1.4426950408889634f;

#define FQO 0
#define XBO 524288
#define HX 0
#define HO 0
#define HK 0
#define HS 0
#define HM 1
constexpr size_t WS_FLAGQ = FQO;
constexpr size_t WS_FLAGW = 98304;
constexpr size_t WS_EPOCH = 65536;
constexpr size_t WS_CSUM = 131072;
constexpr size_t WS_XBOX = XBO;
constexpr size_t WS_OBOX = 655360;
constexpr size_t WS_KBOX = 720896;
constexpr size_t WS_STATS = 1u << 20;
constexpr size_t WS_X = 4u << 20;
constexpr size_t WS_O = 96u << 20;
constexpr size_t WS_KV = 28u << 20;
constexpr size_t WS_CQ = 36u << 20;
constexpr size_t WS_CO = 42u << 20;
constexpr size_t WS_STAMP = 64u << 20;

template <int H> DI constexpr int hp(int c) { return !H ? c * 1024 : (c >> 2) * 8192 + (HM == 1 ? (c & 3) * 256 : (c & 1) * 128 + (c & 2) * 256); }
template <int H> DI constexpr int hl(int u) { return !H ? u : (HM == 1 ? u + (u & ~127) : u + (u & ~255)); }
template <int H> DI constexpr int hv(int lane) { return !H ? lane * 16 : (lane >> 3) * 1024 + (lane & 7) * 16; }
template <int H> DI constexpr int xrow(int p, int r) { return H == 0 ? (p * 128 + r) * 2048 : (H == 1 ? (p * 128 + r) * 4096 : (H == 2 ? p * 524288 + r * 256 : p * 262144 + r * 128)); }
template <int H> DI constexpr int xcol(int t) { return H == 0 ? t * 128 : (H == 1 ? hl<1>(t * 128) : (H == 2 ? t * 32768 : t * 16384)); }
#define WTC 2
#define WT0 1
#define SPR 1
#define TOPJ 0x2000
#define WV0 1
#define FQS 256
#define SNK 1
#define WBE 1
#define WAE 1
#define KSP 0
#define K0E 1
#define TOPS 4
#define BPR 1
#define RLD 0
#define CPF 2
#define WB14 0
#define WB15 5
#define WT13 2
#define ENT 1
#define MBOX 3
#define OBS 256
#define PQX 8
#define ALT 1
#define ALN 1
#define NQA_ 3
#define NQW_ 4
#define LPRIO 3
#define TOUCH 1
#define TSTEP 13
#define XS_ 9
#define ROT0 0
#define WPRE 3
#define PSLEEP 1
#define TW0 5
#define APRIO 1
#define QXM 8
#define WNQ 5
#define WRG 2
#define WBS 1
#define WEP 1
#define WAL 2
#define WL1 0
#define RS0 27
#define RN0 6
#define RN1 6
#define WXM 4
#define CPOS 5
#define LSL 0
#define SAFE_SUM 0x1p80f
#define AB2 0
#define W30S 8
#define CPY 1
#define HT0 0
#define XST 2
#define CKW 4
#define XSP 1
#define EPOLL 1
#define RSLP 1
#define APOST 1
#define ASLP 2
#define APIPE 4
#define RBP 0
#define P2A 1
#define SH0 2
#define KPW 4
#define LWA 7
#define WA0 12
#define WA1 22
#define OSL 14
#define OPL 1
#define E30 1
#define HYB 1
#define MW 0x808
#define MB1 0
#define MB3 0
#define MA2 0
#define OWN 1
#define CUP 1
#define TL0B 0
#define CK2 1
#define XH 0
#define OWA 1
#define UNI 0
#define KPP 1
#define HP4 1
#define TWQ 1
#define ERL 1
#define ESL 0
#define DM 0
#define DS 0
#define ELO 0
#define EN0 3
#define EN1 2
#define ST0 0
#define NC 1
#define NCM 1
#define NCB 4
#define AD1 3
#define AD2 0
#define AD3 4
#define TWO 1
#define XSB 10
#define TL0 0
#define TL1 12
#define FDM 1
#define TSP 0
#define CPW 4
#define EXS 1
#define NCH 2
#define XE2 0
constexpr int QA_SLOT = 18432, NQA = NQA_, QW_SLOT = 24576, NQW = NQW_, QA_OFF = 3 * QW_SLOT, QW_TOP = 131072;
DI constexpr int qw_slot(int s) { return s == 3 ? QW_TOP : s * QW_SLOT; }
constexpr int AT_N = 90112, AT_OWN = AT_N + 32768, AT_Q = 73728;
constexpr int MISC = 155648, M_MEAN = MISC, M_RSTD = MISC + 640, M_CS = MISC + 1280, M_CNT = MISC + 2816, M_EP = MISC + 2832, M_GO = MISC + 2848, M_W0 = MISC + 2852, M_H0 = QA_OFF + NQA * QA_SLOT, M_TCH = MISC + 3072, M_TAB = MISC + 4096, M_XCH = 131072;
constexpr int LDS_TOTAL = 163840;
static_assert(M_H0 + 2048 <= QW_TOP && QW_TOP + QW_SLOT <= MISC && AT_N + 65536 <= MISC && AT_Q + 16384 <= AT_N, "lds");

DI unsigned cvt_pk_bf16(float lo, float hi) {
    typedef __bf16 bf2 __attribute__((ext_vector_type(2)));
    bf2 r; r[0] = (__bf16)lo; r[1] = (__bf16)hi;
    return __builtin_bit_cast(unsigned, r);
}
DI u32x2 pack4(const f32x4& v) { return (u32x2){cvt_pk_bf16(v[0], v[1]), cvt_pk_bf16(v[2], v[3])}; }
DI float bf_lo(unsigned u) { return __uint_as_float(u << 16); }
DI float bf_hi(unsigned u) { return __uint_as_float(u & 0xffff0000u); }
DI u32x4 widen(const u32x2& a, const u32x2& b) {
    auto r0 = __builtin_amdgcn_permlane16_swap(a[0], b[0], false, false);
    auto r1 = __builtin_amdgcn_permlane16_swap(a[1], b[1], false, false);
    return (u32x4){r0[0], r1[0], r0[1], r1[1]};
}
#define MFMA(a, b, c) __builtin_amdgcn_mfma_f32_16x16x32_bf16((a), (b), (c), 0, 0, 0)
#define WAIT_L0() __builtin_amdgcn_s_waitcnt(0xC07F)
#define WAIT_V0() asm volatile("s_waitcnt vmcnt(0)" ::: "memory")
#define BAR() __builtin_amdgcn_s_barrier()
#define SCHED() __builtin_amdgcn_sched_barrier(0)
template <int N> DI void wait_v() { asm volatile("s_waitcnt vmcnt(%0)" ::"i"(N) : "memory"); }
template <int I, int N, class F> DI void static_for(F&& f) { if constexpr (I < N) { f(std::integral_constant<int, I>{}); static_for<I + 1, N>(f); } }

template <bool SC1> DI void dma16(unsigned lds, unsigned voff, const char* sbase) {
    if (SC1) asm volatile("s_mov_b32 m0, %0\n\ts_nop 3\n\tglobal_load_lds_dwordx4 %1, %2 sc1" ::"s"(lds), "v"(voff), "s"(sbase) : "memory");
    else asm volatile("s_mov_b32 m0, %0\n\ts_nop 3\n\tglobal_load_lds_dwordx4 %1, %2" ::"s"(lds), "v"(voff), "s"(sbase) : "memory");
}
DI void touch(unsigned lds, unsigned voff, const char* sbase) {
    asm volatile("s_mov_b32 m0, %0\n\ts_nop 3\n\tglobal_load_lds_dword %1, %2 sc1" ::"s"(lds), "v"(voff), "s"(sbase) : "memory");
}
DI s16x4 tr_read(const LAS char* p) {
    typedef short v4i16_t __attribute__((ext_vector_type(4)));
    return __builtin_bit_cast(s16x4, __builtin_amdgcn_ds_read_tr16_b64_v4i16((LAS v4i16_t*)p));
}
DI LAS char* lds(unsigned off) { return (LAS char*)(uintptr_t)off; }
DI unsigned long long rstamp() { unsigned long long t; asm volatile("s_memrealtime %0\n\ts_waitcnt lgkmcnt(0)" : "=s"(t)::"memory"); return t; }

constexpr int ow_w(int kt) { return CUP == 3 ? (kt < 1 ? 6 : 6 * kt) : kt < 2 ? 0 : (CUP == 0 ? (kt < 5 ? 8 * (kt - 1) : 30) : (CUP == 1 ? 8 * kt - 10 : (kt == 2 ? 6 : (kt == 3 ? 13 : (kt == 4 ? 21 : 30))))); }
template <int NAI, int NWI, bool FIRST> struct Sched {
    int w[KT];
    constexpr Sched() : w{} {
        int endA[KT] = {}, endW[KT] = {};
        int n = 0;
        if (FIRST) {
            if (ENT == 0) { n += 2; endA[0] = n; n += 3; endW[0] = n; }
            n += NAI; endA[1] = n; n += NWI; endW[1] = n;
            if (ERL >= 5) { n += NAI; endA[2] = n; }
            if (ERL == 2 || ERL == 3 || ERL == 5) { n += NWI; endW[2] = n; }
        } else {
            for (int s = 0; s < WPRE; ++s) { n += NWI; endW[s] = n; }
            for (int s = 0; s < 2; ++s) { n += NAI; endA[s] = n; }
        }
        for (int kt = 0; kt < KT; ++kt) {
            const int need = endA[kt] > endW[kt] ? endA[kt] : endW[kt];
            w[kt] = (n - need) > 63 ? 63 : (n - need);
            if (kt + 2 < KT && !(FIRST && ERL >= 5 && kt == 0)) { n += NAI; endA[kt + 2] = n; }
            if (FIRST && (ERL == 4 || ERL == 6) && kt == 0) { n += NWI; endW[2] = n; }
            if (OWN && !FIRST && (NAI == 5 || OWA) && kt < 5) {
                for (int p = ow_w(kt); p < ow_w(kt + 1); ++p) { ++n; endW[3 + p / 6] = n; }
                continue;
            }
            if (FIRST && ERL < 2 && kt < 5) {
                if (kt < 4) { n += NWI; endW[kt + 2] = n; }
                if (kt >= 3) n += NWI / 2;
                if (kt == 4) { endW[6] = n; n += NWI; endW[7] = n; }
                continue;
            }
            if (!FIRST && WPRE == 2 && kt < 2) { n += NWI; endW[kt + 2] = n; }
            if ((FIRST || WPRE == 3) && kt == 0) { n += NWI; endW[3] = n; }
            if (kt >= 1 && kt + 3 < KT) { n += NWI; endW[kt + 3] = n; }
            if (kt == 13) n += WT13;
            if (kt == 14) n += (NAI == 5 || WA0 > 12 ? 12 : WA0) - WT13 + (NAI == 5 ? WB14 : 0);
        }
    }
};

struct Params {
    const bf16_t* x0;
    const bf16_t* wqkv0;
    char* ws;
    const bf16_t* wqkv1;
    const bf16_t* wo0;
    const bf16_t* wo1;
    bf16_t* out;
};
typedef const __attribute__((address_space(4))) Params* KP;
DI int rot_first(int pq, int jq) {
    const int pr = 2 * (pq & 7) + (pq >> 3);
    if (ROT0 == 6) return 0;
    if (ROT0 == 7) return (pq & 1) * 8;
    if (ROT0 == 8) return (pq & 3) * 4;
    if (ROT0 == 9) return pq;
    if (ROT0 == 4) return (pq + (jq ^ 3)) & 15;
    if (ROT0 == 5) return (pr + (jq ^ 3)) & 15;
    return (ROT0 == 0 ? pq + jq : (ROT0 == 1 ? pr + jq : (ROT0 == 2 ? pr + 4 * (jq & 3) + (jq >> 2) : pq + 4 * (jq & 3) + (jq >> 2)))) & 15;
}

struct Order { int base, r, re; };
template <int N> DI Order make_order(int base, int rot) { Order o; o.base = base; o.r = (rot - base) & 15; o.re = o.r > N ? o.r : N; return o; }
DI Order first_order(int pq, int jq) { return make_order<NC>((pq & NCM) * (16 / (NCM + 1)) + NCB, rot_first(pq, jq)); }
template <int N = NC> DI int first_tile(const Order& o, int s) {
    const int v = o.re + s - N;
    if (s < N) { int a = N + o.base, b = s + o.base; if (N - s == 1) asm volatile("" : "+s"(a)); return (o.r == s ? a : b) & 15; }
    return ((s == N ? o.r : (v > 15 ? v - (16 - N) : v)) + o.base) & 15;
}

typedef unsigned long long u64;
DI u64 lds_epoch(LAS char* smem) { return *(const LAS u64*)(smem + M_EP); }
DI bool reached(u64 v, u64 ep, unsigned target) { return (v - ep - target) < (u64)(64u - target); }
DI void poll_flags(const char* ws, u64 ep, unsigned off, bool active, unsigned target) {
    const GAS u64* f = (const GAS u64*)(ws + off);
#pragma unroll 1
    for (unsigned spins = 0; spins < (1u << 22); ++spins) {
        const u64 v = __hip_atomic_load(f, __ATOMIC_RELAXED, __HIP_MEMORY_SCOPE_AGENT);
        if (__all(!active || reached(v, ep, target))) break;
        __builtin_amdgcn_s_sleep(PSLEEP);
    }
    asm volatile("" ::: "memory");
}
struct Ask { u64 v[4]; };
DI void keep(Ask& t) { asm volatile("" : "+v"(t.v[0]), "+v"(t.v[1]), "+v"(t.v[2]), "+v"(t.v[3])); }
#define ASK(r) "global_atomic_or_x2 " r ", %[off], %[z], %[ws] sc0 sc1\n\t"
#define CHK(r) "s_waitcnt vmcnt(%[n])\n\tv_lshl_add_u64 %[d], " r ", 0, %[nb]\n\tv_cmp_gt_u64 vcc, %[span], %[d]\n\ts_cmp_eq_u64 vcc, exec\n\ts_cbranch_scc1 .Lpe%=\n\ts_sleep %[sl]\n\t"
template <bool NEG = false> DI void poll_rmw(Ask& t, const char* ws, unsigned off, bool act, u64 base, unsigned span) {
    u64 z, d, nb = NEG ? base : 0 - base, sp = span;
    unsigned cnt;
    asm volatile("" : "=v"(t.v[0]), "=v"(t.v[1]), "=v"(t.v[2]), "=v"(t.v[3]));
    if (act) {
        if (APIPE == 4)
            asm volatile("v_mov_b64 %[z], 0\n\ts_mov_b32 %[cnt], 0x80000\n\ts_nop 3\n\t" ASK("%[a]") "s_sleep %[sl]\n\t" ASK("%[b]") "s_sleep %[sl]\n\t" ASK("%[c]") "s_sleep %[sl]\n"
                         ".Lpl%=:\n\t" ASK("%[e]") CHK("%[a]") ASK("%[a]") CHK("%[b]") ASK("%[b]") CHK("%[c]") ASK("%[c]") CHK("%[e]")
                         "s_sub_u32 %[cnt], %[cnt], 1\n\ts_cbranch_scc0 .Lpl%=\n.Lpe%=:"
                         : [a] "+v"(t.v[0]), [b] "+v"(t.v[1]), [c] "+v"(t.v[2]), [e] "+v"(t.v[3]), [d] "=&v"(d), [z] "=&v"(z), [cnt] "=&s"(cnt)
                         : [off] "v"(off), [ws] "s"(ws), [nb] "v"(nb), [span] "s"(sp), [sl] "i"(ASLP), [n] "i"(3) : "vcc", "scc", "memory");
        else if (APIPE == 2)
            asm volatile("v_mov_b64 %[z], 0\n\ts_mov_b32 %[cnt], 0x100000\n\ts_nop 3\n\t" ASK("%[a]") "s_sleep %[sl]\n"
                         ".Lpl%=:\n\t" ASK("%[b]") CHK("%[a]") ASK("%[a]") CHK("%[b]")
                         "s_sub_u32 %[cnt], %[cnt], 1\n\ts_cbranch_scc0 .Lpl%=\n.Lpe%=:"
                         : [a] "+v"(t.v[0]), [b] "+v"(t.v[1]), [d] "=&v"(d), [z] "=&v"(z), [cnt] "=&s"(cnt)
                         : [off] "v"(off), [ws] "s"(ws), [nb] "v"(nb), [span] "s"(sp), [sl] "i"(ASLP), [n] "i"(1) : "vcc", "scc", "memory");
        else
            asm volatile("v_mov_b64 %[z], 0\n\ts_mov_b32 %[cnt], 0x200000\n\ts_nop 3\n"
                         ".Lpl%=:\n\t" ASK("%[a]") CHK("%[a]")
                         "s_sub_u32 %[cnt], %[cnt], 1\n\ts_cbranch_scc0 .Lpl%=\n.Lpe%=:"
                         : [a] "+v"(t.v[0]), [d] "=&v"(d), [z] "=&v"(z), [cnt] "=&s"(cnt)
                         : [off] "v"(off), [ws] "s"(ws), [nb] "v"(nb), [span] "s"(sp), [sl] "i"(ASLP), [n] "i"(0) : "vcc", "scc", "memory");
    }
}
DI void post(char* ws, unsigned off, u64 v) {
    if (APOST) asm volatile("s_nop 4\n\tglobal_atomic_swap_x2 %0, %1, %2 sc1" :: "v"(off), "v"(v), "s"(ws) : "memory");
    else __hip_atomic_store((GAS u64*)(ws + off), v, __ATOMIC_RELAXED, __HIP_MEMORY_SCOPE_AGENT);
}
struct Ctx {
    LAS char* smem;
    char* ws;
    int lane, wid;
    int pq, jq;
    int pw, jw;
    int bid;
    int cw;
};
DI Ctx fresh(const Ctx& c0) {
    Ctx c = c0;
    asm volatile("" : "+v"(c.lane));
    asm volatile("" : "+s"(c.wid), "+s"(c.pq), "+s"(c.jq), "+s"(c.pw), "+s"(c.jw), "+s"(c.ws), "+s"(c.cw));
    return c;
}
DI unsigned o_post(const Ctx& c) { return (MBOX & 2) ? (unsigned)WS_OBOX + (c.pq * 16 + (c.lane & 15)) * OBS + c.jq * 8 : (unsigned)WS_FLAGQ + (c.pq * 16 + c.jq) * FQS; }
DI unsigned o_find(const Ctx& c) { return (MBOX & 2) ? (unsigned)WS_OBOX + (c.pw * 16 + c.jw) * OBS + (c.lane & 15) * 8 : (unsigned)WS_FLAGQ + (c.pw * 16 + (c.lane & 15)) * FQS; }
DI unsigned x_post(const Ctx& c) { return (MBOX & 1) ? (unsigned)WS_XBOX + (c.pw * 16 + (c.lane & 31)) * (2 * FQS) + (c.lane & 16) * (FQS / 16) + c.jw * 8 : (unsigned)WS_FLAGW + (c.pw * 16 + c.jw) * 128; }
DI bool x_act(const Ctx& c, bool halo) { return c.lane < ((MBOX & 1) ? (halo && c.pw < 15 ? 32 : 16) : 1); }
#define STAMP_AT(slot) do { } while (0)

#define LSTAMP(i) do { } while (0)
#define LSTORE(ph) do { } while (0)
#define LMARK() do { } while (0)
#define LPUT(i) do { } while (0)
#define ASTORE() do { } while (0)
#define QSTAMP(i) do { } while (0)
#define QSTORE() do { } while (0)
#define FENCE() asm volatile("" ::: "memory")
#define ALIGN8() do { if (ALN) { asm volatile(".p2align 3"); SCHED(); } } while (0)
DI float addf(float a, float b) { float r; asm("v_add_f32 %0, %1, %2" : "=v"(r) : "v"(a), "v"(b)); return r; }
DI float fold16(float a, float b) {
    auto r = __builtin_amdgcn_permlane16_swap(__float_as_uint(a), __float_as_uint(b), false, false);
    float x = __uint_as_float(r[0]) + __uint_as_float(r[1]);
    asm volatile("" : "+v"(x));
    return x;
}
DI float fold32(float a, float b) {
    auto r = __builtin_amdgcn_permlane32_swap(__float_as_uint(a), __float_as_uint(b), false, false);
    float x = __uint_as_float(r[0]) + __uint_as_float(r[1]);
    asm volatile("" : "+v"(x));
    return x;
}
DI void aload4c(u32x4& d, unsigned voff, const char* sb) { asm volatile("s_nop 4\n\tglobal_load_dwordx4 %0, %1, %2 sc1" : "=v"(d) : "v"(voff), "s"(sb) : "memory"); }
DI void aload2c(u32x2& d, unsigned voff, const char* sb) { asm volatile("s_nop 4\n\tglobal_load_dwordx2 %0, %1, %2 sc1" : "=v"(d) : "v"(voff), "s"(sb) : "memory"); }
DI unsigned wo_src(int q, int lane, int jw) { return (unsigned)((8 * q + (lane >> 3)) * (D_ * 2) + jw * 128 + (((lane & 7) ^ ((lane >> 3) & 6)) << 4)); }
DI unsigned pick(bool f, unsigned a, unsigned b) { asm volatile("" : "+v"(a), "+v"(b)); return f ? a : b; }
DI unsigned wo_w_off(const Ctx& c) { return pick(c.cw == 3, (unsigned)((c.wid & 3) * 1024 + c.lane * 16), wo_src(c.wid & 3, c.lane, c.jw)); }
DI unsigned wq_src(int ib, int lane, int jq) {
    const int seg = ib >> 3, ii = ib & 7, lr = lane >> 3;
    return (unsigned)((8 * ii + lr) * 6144 + seg * 2048 + jq * 128 + (((lane & 7) ^ ((lr & 2) | ((ii & 1) << 2))) << 4));
}
template <int S0, int S1> DI void wo_w_issue(const Ctx& c, const char* W, unsigned vw) {
    const int lw = c.wid & 3;
#pragma unroll
    for (int s = S0; s < S1; ++s) dma16<CPY != 0>(s * 4096 + lw * 1024, vw, W + (size_t)((2 * (s & 15) + (s >> 4)) * 32) * (D_ * 2));
}

template <bool HALO, bool FIRST> DI void qkv_load(const Ctx& c0, const char* xin, const char* W, const char* Wo, int l, KP kp = nullptr, unsigned va0 = 0, unsigned vw0 = 0) {
    const Ctx c = fresh(c0);
    asm volatile("" : "+s"(xin), "+s"(W), "+s"(Wo));
    LAS char* smem = c.smem;
    constexpr int NAI = HALO ? 5 : 4, NWI = 6, XS = FIRST ? XS_ : XSB, NR = HALO ? 144 : 128;
    constexpr Sched<NAI, NWI, FIRST> SC{};
    static_assert(NQA == 3 && NQW == 4, "schedule");
    const int lw = c.wid - 4, lane = c.lane, lr = lane >> 3, lc = lane & 7, fr = lane & 15, fq = lane >> 4;
    const int row0 = c.pq * 128;
    const bool cq_ = CPY && !FIRST && (c.jq & 7) == 3;
    if (cq_) W = c.ws + WS_CQ + ((HALO ? 0 : 2) + (c.jq >> 3)) * 24576;
    asm volatile("" : "+s"(W));
    constexpr int SP = FIRST ? NC : (HALO ? NCH : 0), T0 = OWN && HALO ? TL0B : TL0;
    static_assert(FIRST || SP + DS <= T0 + 2 + TSP, "one flag for both kinds of sums");
    constexpr bool OW = OWN && !FIRST && (HALO || OWA);
    const int rot = FIRST ? rot_first(c.pq, c.jq) : (l == 1 ? c.pq : SP);
    const bool diag = OW && rot == c.jq;
    Order od = FIRST ? first_order(c.pq, c.jq) : make_order<SP>(0, rot);
    if (FIRST) asm volatile("" : "+s"(od.base));
    if (FIRST || (HALO && !OW)) asm volatile("" : "+s"(od.r), "+s"(od.re));
    u64 tab = 0;
    if (OW) {
        static_assert(!OW || ((!HALO || SP == 2) && HYB == 1 && MB1 == 0 && MB3 == 0 && MW == 0x808), "order");
        auto drop = [](u64 x, int p) { const u64 m = (1ull << (4 * p)) - 1; return (x & m) | ((x >> 4) & ~m); };
        tab = drop(0xfedcba9876543210ull, c.jq);
        if (l == 1 && !diag) {
            const int cc = __builtin_ctz(~((1u << c.jq) | (1u << rot))), k = 4 * (rot - (rot > c.jq ? 1 : 0) - (rot > cc ? 1 : 0));
            tab = drop(tab, cc - (cc > c.jq ? 1 : 0));
            tab = (((tab >> k) | (tab << (56 - k))) << 4) | (u64)cc;
        }
        tab = (tab << 4) | (u64)c.jq;
        asm volatile("" : "+s"(tab));
    }
    auto tile = [&](int s) { return OW ? (int)((tab >> (4 * s)) & 15) : (FIRST || HALO ? first_tile<SP>(od, s) : s); };
    unsigned va[5], vw[NWI];
#pragma unroll
    for (int j = 0; j < 4; ++j) va[j] = (unsigned)(FIRST ? va0 + j * 32 * (D_ * 2) : c.pq * 262144 + (j * 4 + lw) * 1024 + lane * 16);
    if (HALO) va[4] = (unsigned)((c.pq > 0 ? (c.pq - 1) * 262144 + 14336 : 0) + lw * 512 + lane * 16);
#pragma unroll
    for (int j = 0; j < NWI; ++j) vw[j] = FIRST ? vw0 + (j & 1) * (32 * 6144) + (j >> 1) * 2048 : pick(cq_, (unsigned)((j * 4 + lw) * 1024 + lane * 16), wq_src(j * 4 + lw, lane, c.jq));
    auto issueA = [&](int s, int slot) {
        const char* b = xin + (unsigned)(tile(s) * (FIRST ? 128 : 16384));
#pragma unroll
        for (int j = 0; j < 4; ++j) dma16<true>(QA_OFF + slot * QA_SLOT + (j * 4 + lw) * 1024, va[j], b);
        if (HALO) { if (lane < 32) dma16<true>(QA_OFF + slot * QA_SLOT + 16384 + lw * 512, va[4], b); }
    };
    auto issueW = [&](int s, int slot, int j0 = 0, int j1 = NWI) {
        const char* b = W + (unsigned)(tile(s) * (64 * 6144));
#pragma unroll
        for (int j = j0; j < j1; ++j) dma16<CPY && !FIRST>(qw_slot(slot) + (j * 4 + lw) * 1024, vw[j], b);
    };
    auto ahead = [&]() {
        const int r3 = (lane * 43) >> 7;
        const unsigned o = (unsigned)((((rot - (c.pq & 7) + 10) * 64 + (c.pq & 7) * 48 + lw * 12 + r3) & 1023) * 6144 + (lane - 3 * r3) * 2048 + c.jq * 128);
        if (lane < 36 && (WTC != 2 || (c.jq & 7) == 3)) asm volatile("s_mov_b32 m0, %0\n\ts_nop 3\n\tglobal_load_lds_dword %1, %2" ::"s"(M_TCH), "v"(o), "s"(W) : "memory");
    };
    if (ST0 && FIRST && lw == 1) STAMP_AT(47);
    if (FIRST && ERL) {
        SCHED();
        issueA(1, 1); issueW(1, 1);
        SCHED();
    }
    const bool docs = FIRST || (HALO && l == 1);
    const int t = lw * 64 + lane;
    static_assert(MBOX & 1, "residual flag");
    unsigned foff, ftg, so, ho, fo_, co, l3, l4, vwo, wt[3], cso, xt, xt1, xso, tp, tl;
    u32x4 on;
    auto addr1 = [&]() {
        int lane = c.lane;
        asm volatile("" : "+v"(lane));
        const int fr = lane & 15, fq = lane >> 4;
        const int ph = c.pq > 0 ? c.pq - 1 : 0;
        foff = pick(lane != 32, (unsigned)WS_FLAGQ + pick(lane < 16, c.pq * 16 + lane, pick(lane < 32, fr * 16 + c.jq, ph * 16 + 14 + (lane & 1))) * FQS, (unsigned)WS_XBOX + (c.pw * 16 + c.jw) * (2 * FQS) + c.jw * 8);
        ftg = lane != 32 ? (unsigned)(4 * l + 1) : (unsigned)(l + 1);
        so = FIRST ? (unsigned)((c.pq * 16 + fq * 4) * 1024 + (lw * 32 + fr * 2) * 8) : (unsigned)((c.pq * 16 + (lane >> 2)) * 1024 + (c.jq * 8 + lw * 2 + (lane & 1)) * 8);
        ho = FDM ? (unsigned)(262144 + ((lw * 36 + lane < 16 ? ph * 128 + 128 : c.pq * 128) + lw * 36 + lane - 16) * 8)
                 : (unsigned)(262144 + pick(lane < 32, c.pq * 128 + lw * 32 + lane, ph * 128 + 112 + lw * 4 + (lane & 3)) * 8);
        fo_ = pick(lane < 2, (unsigned)(262144 + (c.pq * 128 + c.jq * 8 + lw * 2 + lane) * 8), 0x80000000u);
        const int cq = lw * 12 + (fr < 12 ? fr : 11);
        co = (unsigned)((fq * 4 * 3072 + (cq >> 4) * 1024 + c.jq * 64 + (cq & 15) * 4) * 4);
        l3 = (unsigned)(M_CS + (HALO ? 768 : 0) + (cq * 4 + fq) * 4);
        l4 = (unsigned)(M_MEAN + pick(lane < 32, 16 + lw * 32 + (FIRST ? fr * 2 + fq : lane), lw * 4 + (lane & 3)) * 4);
        asm volatile("" : "+v"(foff), "+v"(ftg), "+v"(so), "+v"(co), "+v"(l3), "+v"(l4));
        if (!FIRST) asm volatile("" : "+v"(ho), "+v"(fo_));
        SCHED();
    };
    auto addr2 = [&]() {
        int lane = c.lane;
        asm volatile("" : "+v"(lane));
        const int fr = lane & 15, fq = lane >> 4;
        const int q4 = fr >> 2, p4 = lane & 3, fb = (q4 & 2) | ((fq & 1) << 2);
#pragma unroll
        for (int i = 0; i < 3; ++i) {
            const int T = 3 * lw + i;
            wt[i] = (unsigned)((OW && diag ? 0 : qw_slot(SP % NQW)) + (T >> 2) * 8192 + (8 * fq + q4) * 128 + (((2 * (T & 3) + (p4 >> 1)) ^ fb) << 4) + (p4 & 1) * 8);
        }
        const int T = 3 * lw + fq;
        cso = (unsigned)((((HALO ? 16 : 0) + rot) * 3072 + (T >> 2) * 1024 + c.jq * 64 + (T & 3) * 16 + fr) * 4);
        xt = (unsigned)(QA_OFF + (SP % NQA) * QA_SLOT + (lw * 32 + fr) * 128 + ((fq ^ (fr & 7)) << 4));
        xt1 = xt ^ 64;
        xso = (unsigned)hl<HS>((c.pq * 16 + rot) * 1024 + (lw * 32 + fr) * 8);
        on = (u32x4){0x3f803f80u, 0x3f803f80u, 0x3f803f80u, 0x3f803f80u};
        if (FIRST || HALO) asm volatile("" : "+v"(wt[0]), "+v"(wt[1]), "+v"(wt[2]), "+v"(cso), "+v"(on));
        if (FIRST) asm volatile("" : "+v"(xt), "+v"(xt1), "+v"(xso));
        SCHED();
    };
    auto addr3 = [&]() {
        Ctx c2 = c;
        asm volatile("" : "+v"(c2.lane));
        asm volatile("" : "+s"(c2.bid));
        vwo = wo_w_off(c2);
        tp = (unsigned)(pick(c2.lane < 30, (c2.bid >> 3) * 30 + c2.lane, (c2.bid >> 3) * 16 + c2.lane - 30 + (int)(WS_O >> 16)) * 65536 + 65536 - 128);
        tl = (unsigned)(lw == 1 ? ((c2.bid >> 3) * 2 + c2.lane) * 65536 : c2.bid * 8192 + (TWO && !(EXS && (c.jq & 7) == 3) ? c2.lane * 128 : 0));
        asm volatile("" : "+v"(vwo));
        if (FIRST) asm volatile("" : "+v"(tp), "+v"(tl));
        SCHED();
    };
    if (!FIRST || AD1 < 0) addr1();
    if (FIRST && ERL >= 5) { issueA(2, 2); SCHED(); }
    if (FIRST && (ERL == 2 || ERL == 5)) { issueW(2, 2); SCHED(); }
    if (!FIRST || AD2 < 0) addr2();
    if (!FIRST || AD3 < 0) addr3();
#pragma unroll
    for (int j = 0; j < NAI; ++j) asm volatile("" : "+v"(va[j]));
#pragma unroll
    for (int j = 0; j < NWI; ++j) asm volatile("" : "+v"(vw[j]));
    SCHED();
    int tv = 0; unsigned long long lt = 0;
    u64 fv = 0;
    if (FIRST) {
        LSTAMP(55);
    } else {
        static_assert(CPY == 1 && RS0 > 0 && RN0 == NWI && WPRE >= 3 && (FIRST || SC.w[0] == NAI), "order");
        unsigned vx[2], xfo = (unsigned)WS_XBOX + (c.pq * 16 + c.jq) * (2 * FQS) + (HALO && c.pq > 0 ? (lane & 16) * (FQS / 16) : 0) + fr * 8, go = M_GO;
        u64 xv = lds_epoch(smem) + (unsigned)(l + 1);
#pragma unroll
        for (int i = 0; i < 2; ++i) vx[i] = pick(cq_, (unsigned)(((2 * lw + i) * 4 + 3) * 1024 + lane * 16), wq_src((2 * lw + i) * 4 + 3, lane, c.jq));
        asm volatile("" : "+v"(vx[0]), "+v"(vx[1]), "+v"(xfo), "+v"(xv), "+v"(go));
        fv = (((u64)(unsigned)__builtin_amdgcn_readfirstlane((unsigned)(xv >> 32)) << 32) | (unsigned)__builtin_amdgcn_readfirstlane((unsigned)xv)) + (unsigned)(3 * l);
        asm volatile("" : "+s"(fv));
        BAR();
        issueW(0, 0);
        if (OW) asm volatile("s_mov_b64 exec, 1\n\ts_waitcnt vmcnt(0)\n\tds_add_u32 %0, %1 offset:4\n\ts_mov_b64 exec, -1" :: "v"(go), "v"(1u) : "memory");
        auto word = [&](int off) { return __builtin_amdgcn_readfirstlane(*(volatile LAS int*)(lds(go) + off)); };
        auto until = [&](int off, int v) {
#pragma unroll 1
            for (unsigned spins = 0; spins < (1u << 24); ++spins) { if (word(off) == v) break; }
            FENCE();
        };
        static_assert(WPRE == 3, "stages");
        constexpr int WP = OW && CUP == 3 ? 4 : 3;
        int t1 = tile(1), t2 = tile(2);
        if (!OW) asm volatile("" : "+s"(t1), "+s"(t2));
        auto stage = [&](int s) {
            const char* b = W + (unsigned)((OW ? tile(s) : (s == 1 ? t1 : t2)) * (64 * 6144));
            unsigned d = (unsigned)((s == 3 ? QW_TOP : s * QW_SLOT) + lw * 1024), dx = d + (unsigned)(lw * 7168 + 3072);
            asm volatile("" : "+s"(d), "+s"(dx), "+s"(b));
#pragma unroll
            for (int j = 0; j < NWI; ++j) dma16<true>(d + j * 4096, vw[j], b);
#pragma unroll
            for (int i = 0; i < 2; ++i) dma16<true>(dx + i * 4096, vx[i], b);
        };
        auto first2 = [&]() {
            __builtin_amdgcn_s_setprio(LPRIO);
            if (OW) { issueA(1, 1); if (HALO && lane < 32) dma16<true>(M_H0 + lw * 512, va[4], xin + (unsigned)(c.jq * 16384)); }
            else { issueA(0, 0); issueA(1, 1); }
        };
        if (OW && docs && diag) {
            until(M_W0 - M_GO, (OWA ? 4 * l : 2 * l + 2));
            bf16x8 wf[3][2];
            f32x4 d[3];
#pragma unroll
            for (int i = 0; i < 3; ++i)
#pragma unroll
                for (int ks = 0; ks < 2; ++ks) {
                    const s16x4 lo = tr_read(lds(wt[i]) + ks * 4096), hi = tr_read(lds(wt[i]) + ks * 4096 + 512);
                    wf[i][ks] = __builtin_shufflevector(lo, hi, 0, 1, 2, 3, 4, 5, 6, 7);
                }
#pragma unroll
            for (int i = 0; i < 3; ++i) d[i] = MFMA(__builtin_bit_cast(bf16x8, on), wf[i][1], MFMA(__builtin_bit_cast(bf16x8, on), wf[i][0], ((f32x4){0.f, 0.f, 0.f, 0.f})));
            float d0 = d[0][0], d1 = d[1][0], d2 = d[2][0];
            asm volatile("" : "+v"(d0), "+v"(d1), "+v"(d2));
            const float v = fq == 0 ? d0 : (fq == 1 ? d1 : d2);
            auto rc = __builtin_amdgcn_make_buffer_rsrc(c.ws + WS_CSUM, 0, 2 * 16 * 3072 * 4, 0x00020000);
            if (lane < 48) __builtin_amdgcn_raw_buffer_store_b32(__float_as_uint(v), rc, cso, 0, 16);
        }
        if (lw == 3) {
            if (!EPOLL) until(M_CNT - M_GO, 8 * l);
            Ask t;
            poll_rmw(t, c.ws, xfo, lane < (HALO && c.pq > 0 ? 32 : 16), xv, 63 - l);
            *(volatile LAS int*)lds(go) = l;
            until(M_CNT - M_GO, 8 * l);
            first2();
            STAMP_AT(32 + l * 8 + 0);
            wait_v<OW && HALO ? NAI + 1 : NAI>();
            keep(t);
        } else {
            until(M_CNT - M_GO, 8 * l);
            if (RSLP) __builtin_amdgcn_s_sleep(RSLP);
            int ns = 1;
#pragma unroll 1
            for (; ns < WP; ++ns) {
                if (word(0) == l) break;
                stage(ns);
            }
            until(0, l);
            first2();
            const int left = WPRE - ns;
#pragma unroll 1
            for (; ns < WP; ++ns) stage(ns);
            if (OW) { }
            else if (left == 0) wait_v<NAI>();
            else if (left == 1) wait_v<NAI + 8>();
            else if (left == 2) wait_v<NAI + 16>();
            else wait_v<NAI + 24>();
        }
        FENCE();
    }
    if (FIRST && ERL == 3) issueW(2, 2);
    if (ST0 && FIRST && lw == 2) STAMP_AT(57);
    if (FIRST && !ERL) BAR();
    __builtin_amdgcn_s_setprio(LPRIO);
    if (FIRST && !ERL) { LSTAMP(57); issueA(1, 1); issueW(1, 1); LSTAMP(58); }
    u32x4 st[4], cs[4]; u32x2 sh, fl; float rs_, rq_, hs;
    f32x4 d[3], sm[2], g[2];
    bf16x8 wf[3][2], xf[2][2];
    const bool fact = FIRST ? lane < 32 : ((lane < 16 || lane >= (docs ? 16 : 32)) && lane <= (HALO ? 34 : 32));
    static_assert(XS + 3 <= (FIRST ? 12 : 13) && SPR == 1, "aux schedule");
    auto side = [&](auto ktc) {
        constexpr int kt = decltype(ktc)::value;
        {
            constexpr int C0 = FIRST ? 12 : XS + 2, C1 = FIRST ? 13 : XS + 3, C2 = 13;
            auto acc4 = [](u32x4& a, const u32x4& b) {
#pragma unroll
                for (int i = 0; i < 4; ++i) a[i] = __float_as_uint(addf(__uint_as_float(a[i]), __uint_as_float(b[i])));
                asm volatile("" : "+v"(a));
            };
            auto fin = [&]() {
                rs_ *= -1.f / 1024.f;
                asm volatile("" : "+v"(rs_));
                rq_ = __builtin_amdgcn_rsqf(fmaxf(rq_ * (1.f / 1024.f) - rs_ * rs_, 0.f) + LN_EPS);
                asm volatile("" : "+v"(rq_));
            };
            if constexpr (FIRST && kt == XS + 2) {
                asm volatile("" : "+v"(st[0]), "+v"(st[1]), "+v"(st[2]));
                acc4(st[0], st[1]); acc4(st[0], st[2]);
            }
            if constexpr (FIRST && kt == XS + 3) { asm volatile("" : "+v"(st[3])); acc4(st[0], st[3]); }
            if constexpr (kt == C0) {
                if (docs) { asm volatile("" : "+v"(cs[0]), "+v"(cs[1])); acc4(cs[0], cs[1]); }
            }
            if constexpr (FIRST && kt == 12) {
                asm volatile("s_nop 1" : "+v"(st[0]));
                rs_ = fold16(__uint_as_float(st[0][0]), __uint_as_float(st[0][2])); rq_ = fold16(__uint_as_float(st[0][1]), __uint_as_float(st[0][3]));
                rs_ = fold32(rs_, rs_); rq_ = fold32(rq_, rq_);
            }
            if constexpr (FIRST && kt == 13) fin();
            if constexpr (!FIRST && kt == T0 + 2) {
                asm volatile("" : "+v"(sh));
                hs = fold16(__uint_as_float(sh[0]), __uint_as_float(sh[1]));
                hs = fold32(hs, hs);
                hs += __uint_as_float(__builtin_amdgcn_update_dpp(0u, __float_as_uint(hs), 0x124, 0xf, 0xf, false));
                hs += __uint_as_float(__builtin_amdgcn_update_dpp(0u, __float_as_uint(hs), 0x128, 0xf, 0xf, false));
                asm volatile("" : "+v"(hs));
            }
            if constexpr (!FIRST && kt == T0 + 2 + TSP) {
                auto hh = __builtin_amdgcn_permlane16_swap(__float_as_uint(hs), __float_as_uint(hs), false, false);
                rs_ = __uint_as_float(hh[0]); rq_ = __uint_as_float(hh[1]);
                asm volatile("" : "+v"(rs_), "+v"(rq_));
                fin();
                auto rf = __builtin_amdgcn_make_buffer_rsrc(c.ws + WS_STATS + (size_t)l * 524288, 0, 524288, 0x00020000);
                __builtin_amdgcn_raw_buffer_store_b64((u32x2){__float_as_uint(rs_), __float_as_uint(rq_)}, rf, fo_, 0, 16);
            }
            if constexpr (kt == C1) {
                if (docs) { asm volatile("" : "+v"(cs[2]), "+v"(cs[3])); acc4(cs[2], cs[3]); acc4(cs[0], cs[2]); }
            }
            if constexpr (kt == C2) {
                if (docs) {
                    asm volatile("s_nop 1" : "+v"(cs[0]));
                    hs = fold32(fold16(__uint_as_float(cs[0][0]), __uint_as_float(cs[0][1])), fold16(__uint_as_float(cs[0][2]), __uint_as_float(cs[0][3])));
                }
            }
            if constexpr (kt == 13) {
                static_assert(WV0 == 1, "wait");
                if (FIRST || !FDM || docs) WAIT_V0();
                if (!FIRST && !FDM) { asm volatile("" : "+v"(sh)); rs_ = __uint_as_float(sh[0]); rq_ = __uint_as_float(sh[1]); }
                if ((FIRST || !FDM) && lane < (HALO ? 36 : 32)) {
                    *(LAS float*)lds(l4) = rs_;
                    *(LAS float*)(lds(l4) + (M_RSTD - M_MEAN)) = rq_;
                }
                if (docs) *(LAS float*)lds(l3) = hs;
                FENCE();
            }
        }
    };
    static_for<0, KT>([&](auto ktc) {
        constexpr int kt = decltype(ktc)::value;
        LSTAMP(3 * kt);
        if constexpr (FIRST || kt > 0) wait_v<SC.w[kt] + (!FIRST && kt == T0 + 1)>();
        LSTAMP(3 * kt + 1);
        if constexpr (kt == XS) {
            if (lw == 0) {
                asm volatile("" : "+v"(fl));
                const u64 ep = fv - (unsigned)(4 * l + 1);
                if (!__all(!fact || reached(__builtin_bit_cast(u64, fl), ep, ftg))) poll_flags(c.ws, ep, foff, fact, ftg);
            }
        }
        if constexpr (kt == 14) asm volatile("s_waitcnt lgkmcnt(0)" ::: "memory");
        if constexpr (!(OW && kt == 0)) BAR();
        FENCE();
        LMARK();
        LSTAMP(3 * kt + 2);
        if constexpr (TOPJ & (1 << kt)) side(ktc);
        if constexpr (kt == (FIRST ? SP + DS : T0 + 2 + TSP) + 3) {
            if (lw == 0 && lane == 0) __hip_atomic_store((GAS u64*)(c.ws + WS_FLAGQ + (c.pq * 16 + c.jq) * FQS), fv, __ATOMIC_RELAXED, __HIP_MEMORY_SCOPE_AGENT);
        }
        if constexpr (kt == XS + 1 - NQA) {
            if (lw == 0) { if (fact) aload2c(fl, foff, c.ws); }
        }
        {
            const char* sb = c.ws + WS_STATS + (size_t)l * 524288;
            const char* cb = c.ws + WS_CSUM + (HALO ? 16 * 3072 * 4 : 0);
            constexpr int sa = 3 * (kt - XS), ca = FIRST ? 2 * (kt - XS - 1) : 2 * (kt - XS), cn = 2;
            if constexpr (FIRST) {
#pragma unroll
                for (int i = sa; i < sa + 3; ++i) if (i >= 0 && i < 4) aload4c(st[i], so, sb + i * 1024);
            }
            if constexpr (!FIRST && kt == T0) aload2c(sh, so, sb);
            if constexpr (!FIRST && kt == TL1) {
                static_assert(TL1 <= 12, "covered by the wait of step 14");
                if (FDM) { if (lane < 36) { touch(M_MEAN + lw * 144, ho, sb); touch(M_RSTD + lw * 144, ho, sb + 4); } }
                else aload2c(sh, ho, sb);
            }
            if constexpr (ca >= 0 && ca < 4) {
                if (docs) {
#pragma unroll
                    for (int i = ca; i < ca + cn; ++i) aload4c(cs[i], co, cb + i * 12288);
                }
            }
        }
        if constexpr (kt == SP) {
            if (docs && !diag) {
#pragma unroll
                for (int i = 0; i < 3; ++i)
#pragma unroll
                    for (int ks = 0; ks < 2; ++ks) {
                        const s16x4 lo = tr_read(lds(wt[i]) + ks * 4096), hi = tr_read(lds(wt[i]) + ks * 4096 + 512);
                        wf[i][ks] = __builtin_shufflevector(lo, hi, 0, 1, 2, 3, 4, 5, 6, 7);
                    }
                if (FIRST) {
#pragma unroll
                    for (int mt = 0; mt < 2; ++mt)
#pragma unroll
                        for (int ks = 0; ks < 2; ++ks) xf[mt][ks] = *(const LAS bf16x8*)(lds(ks ? xt1 : xt) + mt * 2048);
                }
            }
            SCHED();
        }
        if constexpr (kt + 2 < KT && !(FIRST && ERL >= 5 && kt == 0)) issueA(kt + 2, (kt + 2) % NQA);
        LPUT(3 * kt + 2);
        if constexpr (kt == SP + DM) {
            SCHED();
            if (docs && !diag) {
                const bf16x8 ones = __builtin_bit_cast(bf16x8, on);
                const f32x4 zero = {0.f, 0.f, 0.f, 0.f};
#pragma unroll
                for (int ks = 0; ks < 2; ++ks) {
#pragma unroll
                    for (int i = 0; i < 3; ++i) d[i] = MFMA(ones, wf[i][ks], ks ? d[i] : zero);
                    if (FIRST) {
#pragma unroll
                        for (int mt = 0; mt < 2; ++mt) { sm[mt] = MFMA(ones, xf[mt][ks], ks ? sm[mt] : zero); g[mt] = MFMA(xf[mt][ks], xf[mt][ks], ks ? g[mt] : zero); }
                    }
                }
            }
            SCHED();
        }
        if constexpr (OW && kt < 5) {
            static_assert(ow_w(kt + 1) <= 6 * (kt + 1) && ow_w(5) == 30, "free slots");
#pragma unroll
            for (int s = 3; s < 8; ++s) {
                const int j0 = ow_w(kt) - 6 * (s - 3), j1 = ow_w(kt + 1) - 6 * (s - 3);
                if (j1 > 0 && j0 < 6) issueW(s, s % NQW, j0 < 0 ? 0 : j0, j1 > 6 ? 6 : j1);
            }
        } else if constexpr (FIRST && ERL < 2 && kt < 5) {
            if constexpr (kt < 4) issueW(kt + 2, (kt + 2) % NQW);
            if constexpr (kt == 3) issueW(6, 2, 0, NWI / 2);
            if constexpr (kt == 4) { issueW(6, 2, NWI / 2, NWI); issueW(7, 3); }
        } else {
            if constexpr (WPRE == 2 && kt < 2) issueW(kt + 2, kt + 2);
            if constexpr (FIRST && (ERL == 4 || ERL == 6) && kt == 0) issueW(2, 2);
            if constexpr ((FIRST || WPRE == 3) && kt == 0) issueW(3, 3);
            if constexpr (kt >= 1 && kt + 3 < KT) issueW(kt + 3, (kt + 3) % NQW);
        }
        if constexpr (FIRST && kt == WT0) ahead();
        if constexpr (FIRST && kt == TSTEP) {
            if (lw == 1 && lane < 46) touch(M_TCH, tp, c.ws);
        }
        if constexpr (FIRST && kt == TW0) {
            if (lw == 0) touch(M_TCH, tl, Wo);
            else if (lw == 1) { asm volatile("" : "+s"(kp)); const char* outp = (const char*)kp->out; if (lane < 2) touch(M_TCH, tl, outp); }
        }
        if constexpr (kt == SP && DM > 0) {
            SCHED();
            if (docs) {
                asm volatile("" : "+v"(wf[0][0]), "+v"(wf[0][1]), "+v"(wf[1][0]), "+v"(wf[1][1]), "+v"(wf[2][0]), "+v"(wf[2][1]));
                if (FIRST) asm volatile("" : "+v"(xf[0][0]), "+v"(xf[0][1]), "+v"(xf[1][0]), "+v"(xf[1][1]));
            }
        }
        if constexpr (kt == SP + DS) {
            SCHED();
            if (docs && !diag) {
                if (FIRST) {
                    auto rst = __builtin_amdgcn_make_buffer_rsrc(c.ws + WS_STATS, 0, 524288, 0x00020000);
                    const bool b0 = fr & 1, b1 = fr & 2;
                    u32x2 sv[2];
#pragma unroll
                    for (int mt = 0; mt < 2; ++mt) {
                        float qa = b0 ? g[mt][1] : g[mt][0], qb = b0 ? g[mt][3] : g[mt][2];
                        asm volatile("" : "+v"(qa), "+v"(qb));
                        sv[mt] = (u32x2){__float_as_uint(sm[mt][0]), __float_as_uint(b1 ? qb : qa)};
                    }
                    if (fq == (fr >> 2)) {
                        __builtin_amdgcn_raw_buffer_store_b64(sv[0], rst, xso, 0, 16);
                        __builtin_amdgcn_raw_buffer_store_b64(sv[1], rst, xso + hl<HS>(128), 0, 16);
                    }
                }
                float d0 = d[0][0], d1 = d[1][0], d2 = d[2][0];
                asm volatile("" : "+v"(d0), "+v"(d1), "+v"(d2));
                const float v = fq == 0 ? d0 : (fq == 1 ? d1 : d2);
                auto rc = __builtin_amdgcn_make_buffer_rsrc(c.ws + WS_CSUM, 0, 2 * 16 * 3072 * 4, 0x00020000);
                if (lane < 48) __builtin_amdgcn_raw_buffer_store_b32(__float_as_uint(v), rc, cso, 0, 16);
                asm volatile("s_waitcnt lgkmcnt(0)" ::: "memory");
            }
        }
        if constexpr (FIRST && kt == 1) {
            if (lw == 0) {
                const u32x2 e = *(const LAS u32x2*)(smem + M_EP);
                const u64 es = ((u64)(unsigned)__builtin_amdgcn_readfirstlane(e[1]) << 32) | (unsigned)__builtin_amdgcn_readfirstlane(e[0]);
                fv = es + (unsigned)(4 * l + 1);
                asm volatile("" : "+s"(fv));
            }
        }
        if constexpr (FIRST && kt == AD1) addr1();
        if constexpr (FIRST && kt == AD2) addr2();
        if constexpr (FIRST && kt == AD3) addr3();
        if constexpr (!(TOPJ & (1 << kt))) side(ktc);
        if constexpr (kt == 13) wo_w_issue<0, WT13>(c, Wo, vwo);
        if constexpr (kt == 14) { wo_w_issue<WT13, (HALO || WA0 > 12) ? 12 : WA0>(c, Wo, vwo); if (HALO) wo_w_issue<23, 23 + WB14>(c, Wo, vwo); }
        if constexpr (kt == 15) {
            if (HALO ? WBE < 2 : WA0 > 12) wo_w_issue<12, HALO ? 18 : WA0>(c, Wo, vwo);
            if (HALO) wo_w_issue<23 + WB14, 23 + WB14 + WB15>(c, Wo, vwo);
        }
    });
    __builtin_amdgcn_s_setprio(0);
    BAR();
    LMARK(); LPUT(48);
    LSTAMP(48);
    LSTORE(0);
    if (HALO ? WBE < 1 : WAE < 1) wo_w_issue<18, HALO ? 23 : 20>(c, Wo, vwo);
    if (HALO) {
        static_assert(!WBS || (WBE == 1 && WB14 == 0 && WRG <= 12), "Wo weights of the first twelve steps are older than the last 6 + WB15");
        if (WBS) wait_v<6 + WB15>();
        BAR();
        if (WBE) wo_w_issue<WBE < 2 ? 18 : 12, 23>(c, Wo, vwo);
        wo_w_issue<23 + WB14 + WB15, 32>(c, Wo, vwo);
    }
}

DI float max3(float a, float b, float c) { return fmaxf(fmaxf(a, b), c); }
#define SGB(m, n) __builtin_amdgcn_sched_group_barrier(m, n, 0)
template <bool HALO> struct Tail {
    int f[26], n[4], t[4][26], d[4][27], v[4][27], h3[32], s3[32], o3[33], q3[112];
    constexpr Tail() : f{}, n{}, t{}, d{}, v{}, h3{}, s3{}, o3{}, q3{} {
        const int FB[26] = {0, 1, 2, 3, 4, 5, 6, 7, 8, 9, 10, 11, 12, 13, 14, 15, 24, 25, 19, 23, 18, 22, 17, 21, 16, 20};
        const int FA[24] = {0, 1, 2, 3, 4, 5, 6, 7, 8, 9, 10, 11, 12, 13, 14, 15, 16, 17, 20, 21, 18, 19, 22, 23};
        const int TA[4][26] = {{0, 1, 2, 3, 7, 6, 5, 4, 8, 9, 10, 11, 15, 14, 13, 12, 16, 17, 18, 19, 23, 22, 21, 20, 0, 0}, {4, 8, 9, 10, 11, 15, 14, 13, 12, 16, 17, 18, 19, 23, 22, 21, 20, 0, 1, 2, 3, 7, 6, 5, 0, 0}, {16, 17, 18, 19, 23, 22, 21, 20, 0, 1, 2, 3, 7, 6, 5, 4, 8, 9, 10, 11, 15, 14, 13, 12, 0, 0}, {17, 18, 19, 23, 22, 21, 20, 8, 9, 10, 11, 15, 14, 13, 12, 16, 0, 0, 0, 0, 0, 0, 0, 0, 0, 0}};
        const int DA[4][26] = {{1, 1, 1, 1, 1, 1, 1, 1, 1, 1, 1, 1, 1, 1, 1, 1, 1, 1, 1, 1, 1, 1, 0, 0, 0, 0}, {1, 1, 1, 1, 1, 1, 1, 1, 1, 1, 1, 1, 1, 1, 1, 1, 1, 1, 1, 1, 1, 1, 0, 0, 0, 0}, {1, 1, 1, 1, 1, 1, 1, 1, 1, 1, 1, 1, 1, 1, 1, 1, 0, 0, 0, 0, 0, 0, 0, 0, 0, 0}, {2, 2, 2, 2, 1, 1, 1, 1, 1, 1, 1, 1, 0, 0, 0, 0, 0, 0, 0, 0, 0, 0, 0, 0, 0, 0}};
        const int VA[4][26] = {{1, 1, 0, 0, 0, 0, 1, 1, 1, 1, 1, 1, 1, 1, 1, 1, 1, 1, 1, 1, 1, 1, 2, 2, 0, 0}, {1, 1, 1, 1, 1, 1, 1, 1, 1, 1, 1, 1, 1, 1, 1, 1, 1, 1, 1, 1, 1, 1, 2, 2, 0, 0}, {1, 1, 1, 1, 1, 1, 1, 1, 1, 1, 1, 1, 1, 1, 1, 1, 2, 2, 2, 2, 2, 3, 3, 2, 0, 0}, {0, 0, 0, 0, 1, 1, 1, 1, 1, 1, 1, 1, 2, 2, 2, 2, 0, 0, 0, 0, 0, 0, 0, 0, 0, 0}};
        const int TB[4][26] = {{0, 1, 2, 3, 7, 6, 5, 4, 24, 8, 9, 10, 11, 15, 14, 13, 12, 25, 16, 17, 18, 19, 23, 22, 21, 20}, {24, 8, 9, 10, 11, 15, 14, 13, 12, 25, 16, 17, 18, 19, 23, 22, 21, 20, 0, 1, 2, 3, 7, 6, 5, 4}, {25, 16, 17, 18, 19, 23, 22, 21, 20, 0, 1, 2, 3, 7, 6, 5, 4, 24, 8, 9, 10, 11, 15, 14, 13, 12}, {0, 1, 2, 3, 7, 6, 5, 4, 24, 8, 9, 10, 11, 15, 14, 13, 12, 25, 19, 23, 0, 0, 0, 0, 0, 0}};
        const int DB[4][26] = {{1, 1, 1, 1, 1, 1, 1, 1, 1, 1, 1, 1, 1, 1, 1, 1, 1, 1, 1, 1, 1, 1, 1, 0, 0, 0}, {1, 1, 1, 1, 1, 1, 1, 1, 1, 1, 1, 1, 1, 1, 1, 1, 1, 1, 1, 1, 1, 1, 0, 0, 0, 0}, {1, 1, 1, 1, 1, 1, 1, 1, 1, 1, 1, 1, 1, 1, 1, 1, 1, 1, 1, 1, 1, 1, 1, 0, 0, 0}, {2, 2, 1, 1, 1, 1, 1, 1, 1, 1, 1, 1, 1, 1, 1, 1, 1, 1, 1, 0, 0, 0, 0, 0, 0, 0}};
        const int VB[4][26] = {{1, 1, 1, 1, 1, 1, 1, 1, 1, 1, 1, 1, 1, 1, 1, 1, 1, 1, 1, 1, 1, 1, 1, 2, 2, 2}, {1, 1, 1, 1, 1, 1, 1, 1, 1, 1, 1, 1, 1, 1, 1, 1, 1, 1, 1, 1, 1, 1, 3, 3, 3, 2}, {1, 1, 1, 1, 1, 1, 1, 1, 1, 1, 1, 1, 1, 1, 1, 1, 1, 1, 1, 1, 1, 1, 1, 3, 3, 2}, {0, 0, 1, 1, 1, 1, 1, 1, 1, 1, 1, 1, 1, 1, 1, 1, 1, 1, 1, 2, 0, 0, 0, 0, 0, 0}};
        const int G[32] = {8, 12, 9, 13, 10, 14, 11, 15, 16, 17, 20, 21, 18, 19, 22, 23, 0, 4, 1, 5, 2, 6, 3, 7, 0, 4, 1, 5, 2, 6, 3, 7};
        const int NM = HALO ? 26 : 24;
        n[0] = n[1] = n[2] = NM; n[3] = HALO ? 20 : 16;
        for (int i = 0; i < NM; ++i) f[i] = HALO ? FB[i] : FA[i];
        int vv = 0;
        for (int h = 0; h < 4; ++h) {
            v[h][0] = vv;
            for (int i = 0; i < n[h]; ++i) {
                t[h][i] = HALO ? TB[h][i] : TA[h][i];
                d[h][i + 1] = d[h][i] + (HALO ? DB[h][i] : DA[h][i]);
                vv += HALO ? VB[h][i] : VA[h][i];
                v[h][i + 1] = vv;
            }
        }
        for (int i = 0; i < 32; ++i) { h3[i] = G[i]; s3[i] = i >= 16 && i < 24 ? 0 : 1; }
        for (int i = 3; i < 32; ++i) o3[i + 1] = o3[i] + TOPS;
        int nv = 0, ns = 0;
        for (int k = 0; k < 8 * 14; ++k) {
            const bool top = k % TOPS < 2 || nv == 96;
            if (top && ns < 16 && nv >= 12 * (ns / 2 + 1) && (nv == 96 || k % TOPS == (ns & 1))) { q3[k] = (ns / 2) * 14 + 13 - (ns & 1); ++ns; }
            else { q3[k] = (nv / 12) * 14 + nv % 12; ++nv; }
        }
    }
};
template <bool HALO> DI void qkv_consume(const Ctx& c0, int l, KP kp, unsigned va0 = 0, unsigned vw0 = 0) {
    const Ctx c = fresh(c0);
    if (ST0 && l == 0 && c.wid == 2) STAMP_AT(59);
    LAS char* smem = c.smem;
    const int lane = c.lane, wr = c.wid >> 1, wc = c.wid & 1, fr = lane & 15, fq = lane >> 4;
    const int sw = HALO ? wr : 0;
    f32x4 acc[4][6], ah[2];
#pragma unroll
    for (int mt = 0; mt < 4; ++mt)
#pragma unroll
        for (int nt = 0; nt < 6; ++nt) { acc[mt][nt] = (f32x4){0.f, 0.f, 0.f, 0.f}; if (!(OWN && HALO)) asm volatile("" : "+v"(acc[mt][nt])); }
    ah[0] = ah[1] = (f32x4){0.f, 0.f, 0.f, 0.f};
    if (HALO && !OWN) asm volatile("" : "+v"(ah[0]), "+v"(ah[1]));
    const unsigned aoff0 = (unsigned)(QA_OFF + wr * 8192 + (HALO || l ? lane * 16 : fr * 128 + ((fq ^ (fr & 7)) << 4)));
    const unsigned hoff0 = (unsigned)(QA_OFF + 16384 + lane * 16);
    unsigned xm = HALO || l ? 1024u : 64u;
    asm volatile("" : "+s"(xm));
    static_assert(((QA_OFF | QA_SLOT) & 2047) == 0, "halves");
    const int q4 = fr >> 2, p4 = lane & 3;
    const int fb = (q4 & 2) | ((fq & 1) << 2);
    unsigned boff[2];
#pragma unroll
    for (int cc = 0; cc < 2; ++cc)
        boff[cc] = (unsigned)((8 * fq + q4) * 128 + (((2 * (2 * wc + (cc ^ sw)) + (p4 >> 1)) ^ fb) << 4) + (p4 & 1) * 8);
    bf16x8 xa[2][4], xh[2], wb[2][6];
    auto ldfrag = [&](int set, unsigned pa, unsigned ph, unsigned pb0, unsigned pb1, int ks) {
#pragma unroll
        for (int mt = 0; mt < 4; ++mt) xa[set][mt] = *(const LAS bf16x8*)(lds(pa) + mt * 2048);
        if (HALO) xh[set] = *(const LAS bf16x8*)lds(ph);
#pragma unroll
        for (int nt = 0; nt < 6; ++nt) {
            const LAS char* pb = lds((nt & 1) ? pb1 : pb0) + (nt >> 1) * 8192 + ks * 4096;
            const s16x4 lo = tr_read(pb), hi = tr_read(pb + 512);
            wb[set][nt] = __builtin_shufflevector(lo, hi, 0, 1, 2, 3, 4, 5, 6, 7);
        }
    };
    constexpr int NM = HALO ? 26 : 24, NH = HALO ? 17 : 16;
    auto mm = [&](int set, int i0, int i1) {
#pragma unroll
        for (int ii = i0; ii < i1; ++ii) {
            const int i = !HALO || ii < 8 ? ii : (!SNK ? (ii < 10 ? ii + 16 : ii - 2) : (ii == 8 ? 24 : (ii < 17 ? ii - 1 : (ii == 17 ? 25 : ii - 2))));
            const int nt = i >> 2, mt = SNK && (nt & 1) ? 3 - (i & 3) : i & 3;
            if (i < 16) acc[mt][nt] = MFMA(wb[set][nt], xa[set][mt], acc[mt][nt]);
            else if (i < 24) acc[mt][nt] = MFMA(xa[set][mt], wb[set][nt], acc[mt][nt]);
            else if (i == 24) ah[0] = MFMA(wb[set][2], xh[set], ah[0]);
            else ah[1] = MFMA(xh[set], wb[set][4], ah[1]);
        }
    };
    auto interleave = [&]() {
#pragma unroll
        for (int i = 0; i < NH; ++i) { SGB(0x008, 1); SGB(0x100, 1); }
        SCHED();
    };
    unsigned sa = 0, sb = 0;
    unsigned pa0 = aoff0, pa1 = aoff0 ^ xm, ph0 = hoff0, ph1 = hoff0 ^ xm, pb0 = boff[0], pb1 = boff[1];
    asm volatile("" : "+v"(pa0), "+v"(pa1), "+v"(pb0), "+v"(pb1));
    if (HALO) asm volatile("" : "+v"(ph0), "+v"(ph1));
    constexpr int M_CSH = M_CS + (HALO ? 768 : 0);
    unsigned sa0 = (unsigned)(M_MEAN + 64 + (wr * 64 + fr) * 4), sa1 = (unsigned)(M_MEAN + 64 + wr * 256 + fq * 16);
    unsigned sa2 = (unsigned)(M_CSH + wc * 128 + sw * 64 + fq * 16), sa6 = (unsigned)(M_CSH + 512 + wc * 128 + sw * 64 + fr * 4);
    unsigned sa3 = sa2 ^ 64, sa7 = sa6 ^ 64, sa4 = (unsigned)(M_MEAN + fr * 4), sa5 = (unsigned)(M_MEAN + fq * 16);
    asm volatile("" : "+v"(sa0), "+v"(sa1), "+v"(sa2), "+v"(sa6), "+v"(sa3), "+v"(sa7));
    if (HALO) asm volatile("" : "+v"(sa4), "+v"(sa5));
    const int head = c.jq * 2 + wc;
    auto rso = __builtin_amdgcn_make_buffer_rsrc(HALO ? c.ws + WS_O + (size_t)l * (8u << 20) + (size_t)((head * 16 + c.pq) * 8 + wr * 4) * (1024 << HO)
                                                      : c.ws + WS_KV + (size_t)(l >> 1) * (16u << 20) + (size_t)((c.pq * 32 + head) * 4 + wr) * (4096 << HK), 0, HALO ? 4096 << HO : 12288 << HK, 0x00020000);
    unsigned xrs = (unsigned)(M_XCH + wc * 6144 + wr * 2048), xhs = (unsigned)(M_XCH + wc * 6144 + 8 - wr * 8);
    unsigned b0m = (c.pq == 0 && wr == 0) ? 15u * 16u : 0u;
    asm volatile("" : "+s"(rso));
    if (HALO) asm volatile("" : "+s"(xrs), "+s"(xhs), "+s"(b0m));
    u32x4 cpy[3];
    unsigned cv, vt, cdst;
    const char *cs_, *tw, *cd_;
    int more = !HALO && l == 0 ? KT - 3 - XST - (c.pq & 7) : 0, three = c.jq & 4;
    if (!HALO) {
        static_assert(CPY + TOUCH == 2 && XSP == 1, "extra");
        asm volatile("" : "+s"(kp));
        const int g = c.jq >> 2, n = c.pq * 4 + (c.jq & 3), m = n >> 5, kt = (n >> 1) & 15;
        const char* src = *(const char* const __attribute__((address_space(4)))*)((const __attribute__((address_space(4))) char*)kp + (three ? (m ? 8 : 24) : (m ? 32 : 40)));
        const char* tsr = *(const char* const __attribute__((address_space(4)))*)((const __attribute__((address_space(4))) char*)kp + (c.wid < 3 ? 24 : 40));
        int o1 = kt * (64 * 6144) + (n & 1) * (32 * 6144) + (3 + 4 * (g & 2) - c.jq) * 128 + 2048, o2 = (n & 31) * 65536 + (11 - 4 * g) * 128 - c.pq * 128 * (D_ * 2) + 2048;
        int d1 = (int)WS_CQ + kt * (64 * 6144) + (m * 2 + (g >> 1)) * 24576 + (n & 1) * 4096, d2 = (int)WS_CO + (n & 31) * 65536 + (m * 2 + 1 - (g >> 1)) * 4096;
        asm volatile("" : "+s"(o1), "+s"(o2), "+s"(d1), "+s"(d2));
        cs_ = src + (three ? o1 : o2);
        tw = tsr + (unsigned)((c.wid < 3 ? c.wid : 0) * (2u << 20) + c.bid * 8192);
        cd_ = c.ws + (unsigned)(three ? d1 : d2);
        cv = pick(three, vw0, va0 ^ ((lane & 8) << 1));
        cdst = (unsigned)(c.wid * 1024 + lane * 16);
        vt = TWQ ? (unsigned)(((lane & 7) == 3 ? lane - 1 : lane) * 128) : 0u;
        if (EXS) vt &= (c.jq & 7) == 3 ? 0u : ~0u;
        asm volatile("" : "+s"(more), "+s"(three), "+s"(cs_), "+s"(tw), "+s"(cd_));
        asm volatile("" : "+v"(cv), "+v"(cdst), "+v"(vt));
        asm volatile("" : "=v"(cpy[0]), "=v"(cpy[1]), "=v"(cpy[2]));
    }
    unsigned hto = HT0 == 2 ? 0u : 0x80000000u;
    const char* W0 = (const char*)kp->wqkv0;
    if (!HALO && HT0) {
        const int n = ((c.pq & 7) * 3 + (c.jq & 3)) * 64 + c.wid * 16 + fr, r3 = (n * 43691) >> 17;
        const int t0 = rot_first(c.pq & 8, c.jq | 3) + 8;
        if (l == 0 && !(c.jq & 4) && (c.jq & 3) != 3 && lane < 16) hto = (unsigned)(((t0 * 64 + r3) & 1023) * 6144 + (n - 3 * r3) * 2048 + (c.jq | 3) * 128);
        asm volatile("" : "+v"(hto));
        asm volatile("" : "+s"(W0));
    }
    if (!HALO && HT0 == 2) { asm volatile("s_mov_b32 m0, %0\n\ts_nop 3\n\tglobal_load_lds_dword %1, %2" ::"s"(M_TCH), "v"(hto), "s"(W0) : "memory"); }
    if (!HALO && !ERL && l == 0) BAR();
    if (ST0 && l == 0 && c.wid == 1) STAMP_AT(44);
    if (!HALO) { if (HT0 == 2) wait_v<1>(); else WAIT_V0(); }
    if (ST0 && l == 0 && c.wid == 1) STAMP_AT(46);
    bf16x8 hk[2], hv_[2];
    if (OWN && (HALO || (OWA && l > 0))) {
        unsigned cw = M_CNT;
        asm volatile("" : "+v"(cw));
#pragma unroll 1
        for (unsigned spins = 0; spins < (1u << 24); ++spins) {
            const int a = *(volatile LAS int*)lds(cw), b = *(volatile LAS int*)(lds(cw) + (M_W0 - M_CNT));
            if (__builtin_amdgcn_readfirstlane(a) == 8 * l && __builtin_amdgcn_readfirstlane(b) == (OWA ? 4 * l : 2 * l + 2)) break;
        }
        FENCE();
        SCHED();
        ldfrag(0, pa0, ph0, pb0, pb1, 0);
        ldfrag(1, pa1, ph1, pb0, pb1, 1);
#pragma unroll
        for (int set = 0; set < 2; ++set) {
            if (HALO) { mm(set, 0, 8); mm(set, 9, 17); mm(set, 18, NM); } else mm(set, 0, NM);
            hk[set] = wb[set][2]; hv_[set] = wb[set][4];
        }
        static_assert(SNK == 1, "places of the halo products");
        sa = QA_SLOT; sb = QW_SLOT;
        pa0 = aoff0 + QA_SLOT; pa1 = pa0 ^ xm; ph0 = hoff0 + QA_SLOT; ph1 = ph0 ^ xm; pb0 = boff[0] + QW_SLOT; pb1 = boff[1] + QW_SLOT;
        asm volatile("" : "+v"(pa0), "+v"(pa1), "+v"(pb0), "+v"(pb1));
        if (HALO) asm volatile("" : "+v"(ph0), "+v"(ph1));
#pragma unroll
        for (int mt = 0; mt < 4; ++mt)
#pragma unroll
            for (int nt = 0; nt < 6; ++nt) asm volatile("" : "+v"(acc[mt][nt]));
        SCHED();
    }
    BAR();
    FENCE();
    if (!HALO && HT0 == 1) { if (hto < 0x80000000u) asm volatile("s_mov_b32 m0, %0\n\ts_nop 3\n\tglobal_load_lds_dword %1, %2" ::"s"(M_TCH), "v"(hto), "s"(W0) : "memory"); }
    if (c.wid == 0) STAMP_AT(l * 8 + 0);
    bf16x8 h0[2];
    if (OWN && HALO) {
#pragma unroll
        for (int set = 0; set < 2; ++set) h0[set] = *(const LAS bf16x8*)(lds(ph0) + (M_H0 - (QA_OFF + QA_SLOT + 16384)) + set * 1024);
        SCHED();
    }
    ldfrag(0, pa0, ph0, pb0, pb1, 0);
    SCHED();
    if (OWN && HALO) {
#pragma unroll
        for (int set = 0; set < (XH ? 0 : 2); ++set) { ah[0] = MFMA(hk[set], h0[set], ah[0]); ah[1] = MFMA(h0[set], hv_[set], ah[1]); }
        asm volatile("" : "+v"(ah[0]), "+v"(ah[1]));
        SCHED();
        __builtin_amdgcn_s_setprio(0); __builtin_amdgcn_s_setprio(0);
        SCHED();
    }
    if (ALN) WAIT_L0();
    SCHED();
    int cnt = KT - 3 - more - (OWN == 0 ? 0 : (HALO ? 1 : (OWA ? l >> 1 : 0))), tv = 0;
    QSTAMP(0);
    auto half0 = [&]() {
        ldfrag(1, pa1, ph1, pb0, pb1, 1);
        mm(0, 0, NH);
        interleave();
        mm(0, NH, NH + 1); SCHED();
        asm volatile("s_addk_i32 %0, %2\n\ts_addk_i32 %1, %3" : "+s"(sa), "+s"(sb) : "i"(QA_SLOT), "i"(QW_SLOT) : "scc"); SCHED();
        mm(0, NH + 1, NH + 2); SCHED();
        asm volatile("s_cmpk_lg_u32 %0, %2\n\ts_cselect_b32 %0, %0, 0\n\ts_cmp_lg_u32 %1, %3\n\ts_cselect_b32 %1, %1, %4" : "+s"(sa), "+s"(sb) : "i"(NQA * QA_SLOT), "i"(3 * QW_SLOT), "i"(QW_TOP) : "scc"); SCHED();
        mm(0, NH + 2, NH + 3); SCHED();
        asm volatile("s_cmp_lg_u32 %0, %2\n\ts_cselect_b32 %0, %0, 0\n\ts_addk_i32 %1, -1" : "+s"(sb), "+s"(cnt) : "i"(QW_TOP + QW_SLOT) : "scc"); SCHED();
        mm(0, NH + 3, NH + 4); SCHED();
        pa0 = aoff0 + sa; pb0 = boff[0] + sb;
        asm volatile("" : "+v"(pa0), "+v"(pb0)); SCHED();
        mm(0, NH + 4, NH + 5); SCHED();
        pb1 = boff[1] + sb; pa1 = pa0 ^ xm;
        asm volatile("" : "+v"(pa1), "+v"(pb1)); SCHED();
        mm(0, NH + 5, NH + 6); SCHED();
        if (HALO) { ph0 = hoff0 + sa; ph1 = ph0 ^ xm; asm volatile("" : "+v"(ph0), "+v"(ph1)); SCHED(); }
        mm(0, NH + 6, NM);
        SCHED();
        WAIT_L0();
        BAR();
        SCHED();
    };
    asm volatile("" : "+s"(sa), "+s"(sb), "+s"(cnt));
    do {
    ALIGN8();
    do {
        half0();
        ldfrag(0, pa0, ph0, pb0, pb1, 0);
        mm(1, 0, NH);
        interleave();
        mm(1, NH, NM - 1);
        SCHED();
        WAIT_L0();
        asm volatile("s_nop 0");
        SCHED();
        mm(1, NM - 1, NM);
        SCHED();
    } while (cnt != 0);
    if (!HALO) {
        asm volatile("s_cmp_eq_u32 %[more], 0\n\ts_cbranch_scc1 .Lex%=\n\ts_mov_b32 m0, %[tch]\n\ts_nop 3\n\tglobal_load_lds_dword %[vt], %[tw] sc1\n\t"
                     "global_load_dwordx4 %[c0], %[v0], %[cs] offset:-2048\n\ts_cmp_eq_u32 %[three], 0\n\ts_cbranch_scc1 .Lex%=\n\t"
                     "global_load_dwordx4 %[c1], %[v0], %[cs]\n\tglobal_load_dwordx4 %[c2], %[v0], %[cs] offset:2048\n.Lex%=:"
                     : [c0] "+v"(cpy[0]), [c1] "+v"(cpy[1]), [c2] "+v"(cpy[2])
                     : [more] "s"(more), [tch] "i"(M_TCH), [vt] "v"(vt), [tw] "s"(tw), [v0] "v"(cv), [cs] "s"(cs_), [three] "s"(three) : "scc", "memory");
        cnt = more; more = 0;
        asm volatile("" : "+s"(cnt), "+s"(more));
        SCHED();
    }
    } while (cnt != 0);
    if (!HALO) {
        asm volatile("s_cmp_lg_u32 %[l], 0\n\ts_cbranch_scc1 .Lex%=\n\ts_waitcnt vmcnt(0)\n\ts_nop 3\n\tglobal_store_dwordx4 %[d], %[c0], %[ws] sc1\n\ts_cmp_eq_u32 %[three], 0\n\ts_cbranch_scc1 .Lex%=\n\t"
                     "v_add_u32 %[d], 0x2000, %[d]\n\tglobal_store_dwordx4 %[d], %[c1], %[ws] sc1\n\tv_add_u32 %[d], 0x2000, %[d]\n\tglobal_store_dwordx4 %[d], %[c2], %[ws] sc1\n.Lex%=:\n\ts_nop 1"
                     : [d] "+v"(cdst) : [c0] "v"(cpy[0]), [c1] "v"(cpy[1]), [c2] "v"(cpy[2]), [l] "s"(l), [ws] "s"(cd_), [three] "s"(three) : "scc", "memory");
    }
    QSTAMP(1);
    FENCE();
    SCHED();

    f32x4 csq[4], m4[4], mh4; float csv[2], mr[4], rr[4], mh;
    auto r1 = [&](unsigned b, int off) { return *(const LAS float*)(lds(b) + off); };
    auto r4_ = [&](unsigned b, int off) { return *(const LAS f32x4*)(lds(b) + off); };
    auto mf = [&](int set, int i) {
        const int nt = i >> 2, mt = i & 3;
        if (i < 16) acc[mt][nt] = MFMA(wb[set][nt], xa[set][mt], acc[mt][nt]);
        else if (i < 24) acc[mt][nt] = MFMA(xa[set][mt], wb[set][nt], acc[mt][nt]);
        else if (i == 24) ah[0] = MFMA(wb[set][2], xh[set], ah[0]);
        else ah[1] = MFMA(xh[set], wb[set][4], ah[1]);
        SCHED();
    };
    auto fold = [&](int i, int e) {
        const int nt = i >> 2, mt = i & 3;
        float t;
        if (i < 16) t = __builtin_fmaf(mr[mt], csq[nt][e], acc[mt][nt][e]);
        else if (i < 24) t = __builtin_fmaf(m4[mt][e], csv[nt - 4], acc[mt][nt][e]);
        else if (i == 24) t = __builtin_fmaf(mh, csq[2][e], ah[0][e]);
        else t = __builtin_fmaf(mh4[e], csv[0], ah[1][e]);
        asm volatile("" : "+v"(t));
        if (i < 24) acc[mt][nt][e] = t; else ah[i - 24][e] = t;
    };
    constexpr Tail<HALO> TL{};
    constexpr int NP = HALO ? 6 : 2, FN = 4 * NM;
    static_assert(TL.v[3][TL.n[3]] == NP + FN + (HALO ? 2 : 0) && KT % NQA == 1 && KT % NQW == 0, "tail");
    s16x4 wl[6], wh[6];
    auto frd = [&](int set, int k, int ao) {
        if (k < 4) xa[set][k] = *(const LAS bf16x8*)(lds(set ? pa1 : pa0) + ao + k * 2048);
        else if (k == 16) xh[set] = *(const LAS bf16x8*)(lds(set ? ph1 : ph0) + ao);
        else {
            const int nt = (k - 4) >> 1;
            const LAS char* pb = lds((nt & 1) ? pb1 : pb0) + (nt >> 1) * 8192 + set * 4096;
            if (k & 1) { wh[nt] = tr_read(pb + 512); wb[set][nt] = __builtin_shufflevector(wl[nt], wh[nt], 0, 1, 2, 3, 4, 5, 6, 7); }
            else wl[nt] = tr_read(pb);
        }
    };
    constexpr int RS = M_RSTD - M_MEAN;
    unsigned vl, vg, tb, xr, xh_, b0a, qb0, qb1;
    f32x4 bprev, bcur, bprev0; u32x4 ps; u32x2 ko; float ninf;
    auto rdop = [&](int h, int k) {
        const int j = k - NH;
        if (h == 0) {
            if (k < 2) { mr[2 * k] = r1(sa0, k * 128); mr[2 * k + 1] = r1(sa0, k * 128 + 64); }
            else if (k < 4) csq[k - 2] = r4_((k & 1) ? sa3 : sa2, 0);
            else if (j < 4) frd(0, k - 4, 2 * QA_SLOT);
            else csq[j - 2] = r4_((j & 1) ? sa3 : sa2, 256);
        } else if (j < 0) frd(h & 1, k, h == 1 ? 2 * QA_SLOT : 0);
        else if (h == 1) {
            if (j < 2) csv[j] = r1(j ? sa7 : sa6, 0);
            else if (HALO && j == 2) mh = r1(sa4, 0);
            else if (HALO && j == 3) mh4 = r4_(sa5, 0);
            else { const int mt = HALO ? 7 - j : j - 2; m4[mt] = r4_(sa1, mt * 64); }
        } else if (h == 2) {
            if (j == 0) m4[2] = r4_(sa1, 128);
            if (j == 1) ko = *(const LAS u32x2*)(lds(tb) + 3072);
            if (j == 2) { rr[0] = r1(sa0, RS); rr[1] = r1(sa0, 64 + RS); }
            if (j == 3) { rr[2] = r1(sa0, 128 + RS); rr[3] = r1(sa0, 192 + RS); }
            if (j >= 4) m4[5 - j] = r4_(sa1, (5 - j) * 64);
        } else {
            if (j == 0) bprev = r4_(tb, 0);
            if (j == 1) bcur = r4_(tb, 1024);
            if (j == 2) ps = *(const LAS u32x4*)(lds(tb) + 2048);
            if (j == 3) bprev0 = r4_(b0a, 0);
        }
    };
    auto vop = [&](int k) {
        if (k == 0) { asm volatile("" : "+v"(boff[0])); qb0 = boff[0] + QW_TOP; asm volatile("" : "+v"(qb0)); }
        else if (k == 1) { asm volatile("" : "+v"(boff[1])); qb1 = boff[1] + QW_TOP; asm volatile("" : "+v"(qb1)); }
        else if (k < NP) {
            if (k == 2) { vl = (unsigned)(lane * 16); asm volatile("" : "+v"(vl)); }
            if (k == 3) { tb = vl + M_TAB; asm volatile("" : "+v"(tb)); }
            if (k == 4) { b0a = tb | b0m; asm volatile("" : "+v"(b0a)); }
            if (k == 5) { xr = xrs + vl; asm volatile("" : "+v"(xr)); }
        } else if (k < NP + FN) fold(TL.f[(k - NP) >> 2], (k - NP) & 3);
        else {
            if (k == NP + FN) { xh_ = xhs + vl; asm volatile("" : "+v"(xh_)); }
            if (k == NP + FN + 1) { ninf = -INFINITY; asm volatile("" : "+v"(ninf)); }
        }
    };
    unsigned wa, wq;
    int wr2 = wr, wc2 = wc;
#pragma unroll
    for (int i = 0; i < NM; ++i) {
        mf(0, TL.t[0][i]);
        if (TL.t[0][i] < 24) asm volatile("" : "+v"(acc[TL.t[0][i] & 3][TL.t[0][i] >> 2])); else asm volatile("" : "+v"(ah[TL.t[0][i] & 1]));
        FENCE();
        if (i < NH) frd(1, i, 0);
        if (!HALO && i == NH + 3) { asm volatile("" : "+s"(wr2), "+s"(wc2)); vl = (unsigned)(lane * 16); wq = (unsigned)(AT_Q + (wc2 * 8 + wr2 * 4) * 1024) + vl; asm volatile("" : "+v"(vl), "+v"(wq)); }
        if (!HALO && i == NH + 4) { asm volatile("" : "+s"(wr2), "+s"(wc2)); wa = (unsigned)(AT_OWN + wc2 * 16384 + wr2 * 4096) + vl; asm volatile("" : "+v"(wa)); }
        if (i == NH + 1) { asm volatile("" : "+v"(boff[0]), "+v"(boff[1])); pb0 = boff[0] + 2 * QW_SLOT; pb1 = boff[1] + 2 * QW_SLOT; asm volatile("" : "+v"(pb0), "+v"(pb1)); }
        if (i == NH + 2) { pa0 = aoff0; pa1 = aoff0 ^ xm; asm volatile("" : "+v"(pa1)); if (HALO) { ph0 = hoff0; ph1 = hoff0 ^ xm; asm volatile("" : "+v"(ph1)); } }
        SCHED();
    }
    WAIT_L0();
    BAR();
    FENCE();
    SCHED();
    auto frags = [&](int h) {
        static_assert(TL.d[0][NM] == NH + 6 && TL.d[1][NM] == NH + (HALO ? 5 : 6) && TL.d[2][NM] == NH + (HALO ? 6 : 0) && TL.d[3][TL.n[3]] == NH + (HALO ? 4 : 0), "reads");
        if (h == 0) __builtin_amdgcn_s_waitcnt(0xC27F);
        if (h == 1) __builtin_amdgcn_s_waitcnt(HALO ? 0xC57F : 0xC67F);
        if (h == 2) __builtin_amdgcn_s_waitcnt(HALO ? 0xC67F : 0xC07F);
        if (h == 3) __builtin_amdgcn_s_waitcnt(HALO ? 0xC47F : 0xC07F);
    };
#pragma unroll
    for (int h = 0; h < 4; ++h) {
        if (h == 2) { pb0 = qb0; pb1 = qb1; }
#pragma unroll
        for (int i = 0; i < TL.n[h]; ++i) {
            if (!(h & 1) && i == TL.n[h] - 1) { frags(h); SCHED(); if (h) QSTAMP(4); }
            mf(~h & 1, TL.t[h][i]);
#pragma unroll
            for (int k = TL.d[h][i]; k < TL.d[h][i + 1]; ++k) { FENCE(); rdop(h, k); }
            SCHED();
#pragma unroll
            for (int k = TL.v[h][i]; k < TL.v[h][i + 1]; ++k) vop(k);
            SCHED();
        }
        if (h & 1) {
            frags(h);
            if (h == 1) QSTAMP(2); else QSTAMP(5);
            BAR();
            FENCE();
            SCHED();
            if (h == 1) QSTAMP(3); else QSTAMP(6);
        }
    }
    if (!HALO) {
        f32x4 r4[4];
        mf(1, 8);
#pragma unroll
        for (int mt = 0; mt < 4; ++mt) rr[mt] = r1(sa0, mt * 64 + RS);
        SCHED();
        mf(1, 12);
#pragma unroll
        for (int mt = 0; mt < 4; ++mt) r4[mt] = r4_(sa1, mt * 64 + RS);
        SCHED();
        mf(1, 9);
        vg = HK ? (unsigned)hv<HK>(lane) : vl;
        if (HK) asm volatile("" : "+v"(vg));
        SCHED();
        u32x4 pk[8];
        auto op = [&](int g, int k) {
            const int ta = g < 4 ? g : (g - 4) & 2, tb = g < 4 ? g : ta + 1, na = g < 4 ? 2 : 4 + (g & 1), nb = g < 4 ? 3 : na;
            f32x4& A = acc[ta][na]; f32x4& Bv = acc[tb][nb];
            if (k < 12) {
                const int h = k / 6, kk = k % 6;
                f32x4& X = h ? Bv : A;
                const int tx = h ? tb : ta;
                if (kk < 4) { float t = X[kk] * (g < 4 ? rr[tx] : r4[tx][kk]); asm volatile("" : "+v"(t)); X[kk] = t; }
                else { unsigned t = cvt_pk_bf16(X[2 * (kk - 4)], X[2 * (kk - 4) + 1]); asm volatile("" : "+v"(t)); pk[g][2 * h + kk - 4] = t; }
            } else if (k == 12) __builtin_amdgcn_raw_buffer_store_b128(pk[g], rso, vg + hp<HK>(g & 3), (g >> 2) * (8192 << HK), 16);
            else *(LAS u32x4*)(lds(wa) + (g >> 2) * 8192 + (g & 3) * 1024) = pk[g];
            SCHED();
        };
        auto pm = [&](int i) {
            if (i < 16) mf(1, TL.h3[i]);
            else if (i < 32) mf(((i - 16) >> 1) & 1, ((i - 16) >> 2) + 4 * (i & 1));
        };
        pm(3);
#pragma unroll
        for (int g = 0; g < 8; ++g) {
#pragma unroll
            for (int k = 0; k < 6; ++k) op(g, k);
            pm(2 * g + 4);
            if (g > 0) op(g - 1, 12);
#pragma unroll
            for (int k = 6; k < 12; ++k) op(g, k);
            pm(2 * g + 5);
            if (g > 0 && g <= LWA) op(g - 1, 13);
        }
        op(7, 12);
        QSTAMP(7);
        u32x4 qk;
        auto qop = [&](int mt, int k) {
            if (k == 0) { rr[mt] *= QSCALE; asm volatile("" : "+v"(rr[mt])); }
            else if (k < 13) {
                const int h = (k - 1) / 6, kk = (k - 1) % 6;
                f32x4& X = acc[mt][h];
                if (kk < 4) { float t = X[kk] * rr[mt]; asm volatile("" : "+v"(t)); X[kk] = t; }
                else { unsigned t = cvt_pk_bf16(X[2 * (kk - 4)], X[2 * (kk - 4) + 1]); asm volatile("" : "+v"(t)); qk[2 * h + kk - 4] = t; }
            } else *(LAS u32x4*)(lds(wq) + mt * 1024) = qk;
            SCHED();
        };
#pragma unroll
        for (int mt = 0; mt < 3; ++mt) {
            pm(20 + 4 * mt); if (3 * mt + LWA < 8) op(3 * mt + LWA, 13); qop(mt, 0); qop(mt, 1); qop(mt, 2);
            pm(21 + 4 * mt); if (3 * mt + 1 + LWA < 8) op(3 * mt + 1 + LWA, 13); qop(mt, 3); qop(mt, 4); qop(mt, 5);
            pm(22 + 4 * mt); if (3 * mt + 2 + LWA < 8) op(3 * mt + 2 + LWA, 13); qop(mt, 6); qop(mt, 7); qop(mt, 8);
            pm(23 + 4 * mt); qop(mt, 9); qop(mt, 10); qop(mt, 11); qop(mt, 12); qop(mt, 13);
        }
        if (c.wid == 0) STAMP_AT(l * 8 + 1);
        QSTAMP(8);
        {
            const u32x2 q0 = pack4(acc[3][0] * (rr[3] * QSCALE)), q1 = pack4(acc[3][1] * (rr[3] * QSCALE));
            *(LAS u32x4*)(lds(wq) + 3 * 1024) = (u32x4){q0[0], q0[1], q1[0], q1[1]};
        }
        QSTAMP(9);
        QSTORE();
        if (K0E) {
            SCHED();
            WAIT_L0();
            BAR();
            FENCE();
            SCHED();
        }
    } else {
        typedef unsigned u32x10 __attribute__((ext_vector_type(10)));
        u32x4 kp[4], qp[4];
        u32x10 vw[2];
        float ks[2][4], cs[2][4];
        auto pk8 = [&](const f32x4& x, const f32x4& y) { const u32x2 a = pack4(x), b = pack4(y); u32x4 r = {a[0], a[1], b[0], b[1]}; asm volatile("" : "+v"(r)); SCHED(); return r; };
        auto pkv = [&](int mt, int cc) { const u32x2 a = pack4(acc[mt][4 + cc]); vw[cc][2 * mt + 2] = a[0]; vw[cc][2 * mt + 3] = a[1]; };
        auto fr8 = [](const u32x4& x) { return __builtin_bit_cast(bf16x8, x); };
        mf(1, 24);
        unsigned ka0 = ko[0] + wr * 256, ka1 = ko[1] + wr * 256;
        asm volatile("" : "+v"(ka0), "+v"(ka1));
        SCHED();
        mf(1, 25);
#pragma unroll
        for (int mt = 0; mt < 4; ++mt) ks[0][mt] = r1(ka0, mt * 64);
        rr[0] *= QSCALE; asm volatile("" : "+v"(rr[0]));
        SCHED();
        mf(1, 11);
#pragma unroll
        for (int mt = 0; mt < 4; ++mt) ks[1][mt] = r1(ka1, mt * 64);
        rr[1] *= QSCALE; asm volatile("" : "+v"(rr[1]));
        SCHED();
        mf(1, 15);
        rr[2] *= QSCALE; asm volatile("" : "+v"(rr[2]));
        rr[3] *= QSCALE; asm volatile("" : "+v"(rr[3]));
        SCHED();
        mf(1, 19);
        const u32x4 hh = pk8(ah[0], ah[1]);
        mf(1, 23);
        *(LAS u32x2*)lds(xh_) = (u32x2){hh[0], hh[1]}; *(LAS u32x2*)(lds(xh_) + 1024) = (u32x2){hh[2], hh[3]};
        SCHED();
        mf(1, 10);
        kp[3] = pk8(acc[3][2], acc[3][3]);
        mf(1, 14);
        *(LAS u32x4*)(lds(xr) + 2048) = kp[3];
        pkv(3, 0); asm volatile("" : "+v"(vw[0]));
        *(LAS u32x2*)(lds(xr) + 3072) = (u32x2){vw[0][8], vw[0][9]};
        SCHED();
        mf(1, 3);
        pkv(3, 1); asm volatile("" : "+v"(vw[1]));
        *(LAS u32x2*)(lds(xr) + 3072 + 8) = (u32x2){vw[1][8], vw[1][9]};
        SCHED();
        mf(1, 7);
        mf(1, 9);
        kp[2] = pk8(acc[2][2], acc[2][3]);
        mf(1, 13);
        qp[3] = pk8(acc[3][0], acc[3][1]);
        mf(1, 2);
        f32x4 s0[4], s1[4];
        u32x4 pk;
        auto sc = [&](int mt, int h) {
            if (h) { s1[mt] = MFMA(fr8(kp[mt]), fr8(qp[mt]), bcur); asm volatile("" : "+v"(s1[mt])); }
            else { s0[mt] = MFMA(fr8(mt ? kp[mt - (mt > 0)] : pk), fr8(qp[mt]), mt == 0 ? bprev0 : bprev); asm volatile("" : "+v"(s0[mt])); }
            SCHED();
        };
        auto csm = [&](int mt) { cs[0][mt] = ks[0][mt] * rr[mt]; asm volatile("" : "+v"(cs[0][mt])); cs[1][mt] = ks[1][mt] * rr[mt]; asm volatile("" : "+v"(cs[1][mt])); };
        float u[2][4], mx[4], iv[4];
        auto sv = [](float x) { asm volatile("" : "+v"(x)); return x; };
        auto mx2 = [&](float a, float b) { return max3(ninf, a, b); };
        auto pick = [&](int mt, int h) { u[h][mt] = sv(max3(mx2(s0[mt][2 * h], s0[mt][2 * h + 1]), s1[mt][2 * h], s1[mt][2 * h + 1]) * cs[h][mt]); };
        auto vf = [&](int i) { const int mt = 2 - (i >> 2), nt = 4 + (i & 1), set = (i >> 1) & 1; SCHED(); acc[mt][nt] = MFMA(xa[set][mt], wb[set][nt], acc[mt][nt]); asm volatile("" : "+v"(acc[mt][nt])); SCHED(); };
        mf(1, 6);
        kp[1] = pk8(acc[1][2], acc[1][3]);
        mf(1, 8);
        WAIT_L0();
        QSTAMP(7);
        BAR();
        FENCE();
        SCHED();
        QSTAMP(8);
        mf(1, 12);
        {
            const u32x2 k0 = *(const LAS u32x2*)(lds(xr) + 8);
            FENCE(); SCHED();
            const u32x2 k1 = *(const LAS u32x2*)lds(xr);
            FENCE(); SCHED();
            const u32x2 a = *(const LAS u32x2*)(lds(xr) + 1024 + 8);
            FENCE(); SCHED();
            const u32x2 b = *(const LAS u32x2*)(lds(xr) + 1024);
            pk = (u32x4){k0[0], k0[1], k1[0], k1[1]};
            vw[0][0] = a[0]; vw[0][1] = a[1]; vw[1][0] = b[0]; vw[1][1] = b[1];
        }
        SCHED();
        auto sw16 = [](float& a, float& b) { auto r = __builtin_amdgcn_permlane16_swap(__float_as_uint(a), __float_as_uint(b), false, false); a = __uint_as_float(r[0]); b = __uint_as_float(r[1]); };
        auto sw32 = [](float& a, float& b) { auto r = __builtin_amdgcn_permlane32_swap(__float_as_uint(a), __float_as_uint(b), false, false); a = __uint_as_float(r[0]); b = __uint_as_float(r[1]); };
        auto ldr = [&](int i) { };
        auto top = [&](int mt) { mx[mt] = sv(mx2(u[0][mt], u[1][mt])); };
        auto pv1 = [&](int mt, int k) {
            SCHED();
            vw[k >> 1][2 * mt + 2 + (k & 1)] = cvt_pk_bf16(acc[mt][4 + (k >> 1)][2 * (k & 1)], acc[mt][4 + (k >> 1)][2 * (k & 1) + 1]);
            asm volatile("" : "+v"(vw[k >> 1]));
            SCHED();
        };
        auto uk = [&](int mt) { SCHED(); u[0][mt] = sv(u[0][mt] * ks[0][mt]); u[1][mt] = sv(u[1][mt] * ks[1][mt]); SCHED(); };
        mf(1, 1);
        qp[2] = pk8(acc[2][0], acc[2][1]);
        mf(1, 5);
        csm(3); SCHED();
        mf(1, 0);
        kp[0] = pk8(acc[0][2], acc[0][3]);
        mf(1, 4);
        csm(2); SCHED();
        sc(3, 1);
        qp[1] = pk8(acc[1][0], acc[1][1]);
        sc(3, 0);
        csm(1); ldr(0); SCHED();
        sc(2, 1);
        qp[0] = pk8(acc[0][0], acc[0][1]);
        sc(2, 0);
        csm(0); pick(3, 0); SCHED();
        sc(1, 1);
        pick(3, 1); ldr(1); SCHED();
        sc(1, 0);
        pick(2, 0); top(3); SCHED();
        sc(0, 1);
        pick(2, 1); ldr(2); SCHED();
        sc(0, 0);
        if (c.wid == 0) STAMP_AT(l * 8 + 1);
        pick(1, 0); top(2);
        vf(0);
        pick(1, 1); ldr(3);
        vf(1);
        sw16(mx[2], mx[3]);
        pick(0, 0);
        vf(2);
        pick(0, 1); top(1);
        vf(3);
        top(0); mx[2] = sv(mx2(mx[2], mx[3]));
        vf(4);
        sw16(mx[0], mx[1]); mx[0] = sv(mx2(mx[0], mx[1]));
        vf(5); pv1(2, 0);
        sw32(mx[0], mx[2]);
        { const float a = mx[0], b = mx[2]; mx[0] = sv(mx2(a, b)); mx[2] = sv(mx2(b, a)); }
        vf(6); pv1(2, 1);
        sw32(mx[0], mx[2]);
        mx[3] = sv(mx[2]); mx[1] = sv(mx[0]);
        vf(7);
        sw16(mx[2], mx[3]); sw16(mx[0], mx[1]);
        SCHED();
#pragma unroll
        for (int mm = 0; mm < 4; ++mm) { const int mt = 3 - mm; u[0][mt] = sv(u[0][mt] - mx[mt]); u[1][mt] = sv(u[1][mt] - mx[mt]); if (mm == 1) vf(8); }
        vf(9);
#pragma unroll
        for (int mm = 0; mm < 4; ++mm) {
            const int mt = 3 - mm;
            u[0][mt] = sv(__builtin_amdgcn_exp2f(u[0][mt])); u[1][mt] = sv(__builtin_amdgcn_exp2f(u[1][mt]));
            if (mm > 0) iv[mt + 1] = sv(u[0][mt + 1] + u[1][mt + 1]);
            if (mm < 2) vf(10 + mm);
        }
        SCHED();
        sw16(iv[2], iv[3]);
        iv[0] = sv(u[0][0] + u[1][0]);
        iv[2] = sv(iv[2] + iv[3]);
        pv1(2, 2);
        sw16(iv[0], iv[1]); iv[0] = sv(iv[0] + iv[1]);
        uk(3);
        sw32(iv[0], iv[2]);
        iv[0] = sv(iv[0] + iv[2]);
        pv1(2, 3);
        iv[0] = sv(__builtin_amdgcn_rcpf(iv[0]));
        pv1(1, 0);
        iv[2] = sv(iv[0]);
        uk(2);
        sw32(iv[0], iv[2]);
        iv[3] = sv(iv[2]); iv[1] = sv(iv[0]);
        pv1(1, 1);
        sw16(iv[2], iv[3]); sw16(iv[0], iv[1]);
        SCHED();
        f32x4 o[4][2];
        u32x4 pf[4];
        auto nrm = [&](int mt) { u[0][mt] = sv(u[0][mt] * iv[mt]); u[1][mt] = sv(u[1][mt] * iv[mt]); SCHED(); };
        auto prb = [&](int mt) {
            const unsigned p = cvt_pk_bf16(u[0][mt], u[1][mt]);
            pf[mt] = (u32x4){__builtin_amdgcn_perm(0u, p, ps[0]), __builtin_amdgcn_perm(0u, p, ps[1]), __builtin_amdgcn_perm(0u, p, ps[2]), __builtin_amdgcn_perm(0u, p, ps[3])};
            asm volatile("" : "+v"(pf[mt]));
            SCHED();
        };
        auto pvm = [&](int mt, int cc) {
            o[mt][cc] = MFMA(fr8((u32x4){vw[cc][2 * mt], vw[cc][2 * mt + 1], vw[cc][2 * mt + 2], vw[cc][2 * mt + 3]}), fr8(pf[mt]), ((f32x4){0.f, 0.f, 0.f, 0.f}));
            asm volatile("" : "+v"(o[mt][cc]));
            SCHED();
        };
        auto out = [&](int mt) {
            const u32x2 lo = pack4(o[mt][0]), hi = pack4(o[mt][1]);
            __builtin_amdgcn_raw_buffer_store_b128((u32x4){lo[0], lo[1], hi[0], hi[1]}, rso, vg + hp<HO>(mt), 0, 16);
            SCHED();
        };
        vg = (unsigned)hv<HO>(lane);
        asm volatile("" : "+v"(vg));
        nrm(3); nrm(2); prb(3);
        pv1(1, 2); pv1(1, 3);
        pvm(3, 0); prb(2); pvm(3, 1);
        uk(1); uk(0);
        pvm(2, 0); nrm(1); nrm(0); pvm(2, 1);
        prb(1); pv1(0, 0); pv1(0, 1);
        pvm(1, 0); pv1(0, 2); pv1(0, 3); prb(0);
        pvm(1, 1); out(3); pvm(0, 0); out(2); pvm(0, 1); out(1); out(0);
        SCHED();
        QSTAMP(9);
        if (c.wid == 0) STAMP_AT(l * 8 + 2);
        QSTORE();
    }
}

#define MFMA32(a, b, c) __builtin_amdgcn_mfma_f32_32x32x16_bf16(__builtin_bit_cast(bf16x8, a), __builtin_bit_cast(bf16x8, b), (c), 0, 0, 0)
DI float max16(const f32x16& s) {
    float m = max3(s[0], s[1], s[2]), n = max3(s[3], s[4], s[5]);
    m = max3(m, s[6], s[7]); n = max3(n, s[8], s[9]);
    m = max3(m, s[10], s[11]); n = max3(n, s[12], s[13]);
    m = max3(max3(m, s[14], s[15]), n, n);
    auto b = __builtin_amdgcn_permlane32_swap(__float_as_uint(m), __float_as_uint(m), false, false);
    return __builtin_amdgcn_fmed3f(__uint_as_float(b[0]), __uint_as_float(b[1]), INFINITY);
}
template <bool HI> DI void attn_a(const Ctx& c0, int l, const char* Wo, KP kp) {
    const Ctx c = fresh(c0);
    asm volatile("" : "+s"(Wo));
    LAS char* smem = c.smem;
    const int lane = c.lane, w = c.wid;
    constexpr int H = HI ? 0 : 1;
    int qt = HI ? w - 4 : 3 - w;
    asm volatile("" : "+s"(qt));
    const float NEG = -INFINITY;
    int zq = (lane & 31) - 4 * (lane >> 5);
    unsigned lb = (unsigned)(((lane >> 4) & 1) * 1024 + ((lane >> 5) * 16 + (lane & 15)) * 16);
    unsigned qa = (unsigned)(AT_Q + H * 8192 + qt * 2048) + lb, ta = (unsigned)(AT_OWN + H * 16384 - 3 * 2048 + qt * 2048) + lb;
    asm volatile("" : "+v"(lb), "+v"(qa), "+v"(ta));
    auto rd = [&](unsigned base, int off) { return *(const LAS u32x4*)(lds(base) + off); };
    f32x16 ce;
    u32x4 sel;
    unsigned tm = (unsigned)(AT_N + lane * 16);
    auto masks = [&]() {
        if (HI) {
#pragma unroll
            for (int r = 0; r < 16; ++r) ce[r] = 8 * (r >> 2) + (r & 3) <= zq ? 0.f : NEG;
            asm volatile("" : "+v"(ce));
        }
        const unsigned s1 = ((lane >> 4) ^ (lane >> 2)) & 1 ? 0u : 0x3f803f80u;
        sel = (u32x4){s1, s1, s1, s1};
        asm volatile("" : "+v"(sel));
    };
    u64 ep, pa_, kvv, nkv, kact;
    unsigned po, pk_, cnta = M_CNT, one = 1u;
    char* ob;
    const char *kvb, *s1_, *s2_;
    unsigned vk, dst, fo;
    auto setup = [&]() {
        if (HI) {
            const u32x2 e = *(const LAS u32x2*)(smem + M_EP);
            ep = ((u64)(unsigned)__builtin_amdgcn_readfirstlane(e[1]) << 32) | (unsigned)__builtin_amdgcn_readfirstlane(e[0]);
            po = o_post(c);
            pk_ = pick(lane < 2, (unsigned)(WS_KBOX + ((c.pq + 1 + lane) * 16 + c.jq) * 256 + lane * 8), (unsigned)(WS_FLAGQ + (c.pq * 16 + c.jq) * FQS));
            pa_ = c.pq < 14 ? 7ull : (c.pq < 15 ? 5ull : 4ull);
            fo = (unsigned)(WS_KBOX + (c.pq * 16 + c.jq) * 256 + lane * 8);
            asm volatile("" : "+v"(po), "+v"(pk_), "+v"(fo));
            asm volatile("" : "+s"(ep), "+s"(pa_));
        }
        ob = c.ws + WS_O + (size_t)l * (8u << 20) + (size_t)(unsigned)(((c.jq * 2 + H) * 16 + c.pq) * 8192 + qt * 2048);
        kvb = c.ws + WS_KV + (size_t)(unsigned)((l >> 1) * (16u << 20) + c.jq * 32768);
        asm volatile("" : "+s"(ob), "+s"(kvb));
    };
    auto setup2 = [&]() {
        const int p1 = c.pq >= 1 ? c.pq - 1 : c.pq, p2 = c.pq >= 2 ? c.pq - 2 : c.pq;
        s1_ = kvb + (size_t)(unsigned)(p1 * 524288);
        s2_ = kvb + (size_t)(unsigned)(p2 * 524288);
        vk = (unsigned)((w & 3) * 1024 + lane * 16); dst = (unsigned)(AT_N + (w & 3) * 1024);
        asm volatile("" : "+s"(s1_), "+s"(dst));
        if (!HI) asm volatile("" : "+s"(s2_));
        asm volatile("" : "+v"(vk));
    };
    auto arrive = [&](auto cc, unsigned msk, unsigned val) {
        unsigned old = 0;
        if (lane == 0) asm volatile("ds_add_rtn_u32 %0, %1, %2 offset:%3\n\ts_waitcnt lgkmcnt(0)" : "+v"(old) : "v"(cnta), "v"(one), "i"(decltype(cc)::value * 4) : "memory");
        if ((__builtin_amdgcn_readfirstlane(old) & msk) == msk) { if (lane < 16) post(c.ws, po, ep + (4 * l + val)); }
    };
    static_assert((MBOX & 2) && !HO && !HK, "layout");
    int tv = 0;
    auto kvs = [&]() {
        if (!HI) {
            SCHED();
            LSTAMP(2);
            asm volatile("s_mov_b64 exec, 1\n\ts_waitcnt vmcnt(0)\n\tds_add_u32 %0, %1\n\ts_mov_b64 exec, -1" :: "v"(cnta), "v"(one) : "memory");
            LSTAMP(3);
            SCHED();
        }
    };
    asm volatile("" : "+v"(cnta), "+v"(one), "+v"(tm));
    if (HI) {
        masks(); setup(); setup2();
        if (w == 5) {
#pragma unroll
            for (int i = 0; i < 4; ++i) *(LAS f32x4*)(lds(tm) + i * 1024) = (f32x4){ce[4 * i], ce[4 * i + 1], ce[4 * i + 2], ce[4 * i + 3]};
        }
        kvv = ep + (4 * l + 2); nkv = 0 - kvv;
        kact = __builtin_amdgcn_ballot_w64(w == KPW && lane < 2 && lane < c.pq);
        asm volatile("" : "+v"(kvv), "+v"(nkv));
        asm volatile("" : "+s"(kact));
        WAIT_L0();
        WAIT_V0();
        LSTAMP(0);
        if (w == 4) __builtin_amdgcn_s_setprio(HP4);
        BAR();
        FENCE();
    }
    auto kvp = [&]() {
        if (HI && w == 4) {
            __builtin_amdgcn_s_setprio(3);
#pragma unroll 1
            for (unsigned spins = 0; spins < (1u << 24); ++spins) {
                if (__builtin_amdgcn_readfirstlane(*(volatile LAS int*)lds(cnta)) == 8 * l + 4) break;
            }
            if (__builtin_amdgcn_inverse_ballot_w64(pa_)) post(c.ws, pk_, kvv);
            STAMP_AT(l * 8 + 2);
            __builtin_amdgcn_s_setprio(HP4);
        }
        SCHED();
    };
    if (KPP == -1) kvp();
    FENCE();
    SCHED();
    LSTAMP(1);
    u32x4 q0, q1, k0, k1, va, vp, pa, pb;
    f32x16 cm, s0, s1, o;
    f32x4 ls;
    auto stage = [&](f32x16& sc, f32x16& sn, const f32x16& cn, bool pend, bool zero, unsigned kb, int ko, unsigned vb, int vo1, unsigned nb, int vo0) {
        auto ex = [&](int i) { float x = __builtin_amdgcn_exp2f(sc[i]); asm volatile("" : "+v"(x)); sc[i] = x; SCHED(); };
        auto cv = [&](u32x4& p, int j, int i) { unsigned x = cvt_pk_bf16(sc[i], sc[i + 1]); asm volatile("" : "+v"(x)); p[j] = x; SCHED(); };
        sn = MFMA32(k0, q0, cn); SCHED();
        k0 = rd(kb, ko); SCHED();
        ex(0); ex(1); ex(2);
        if (pend) { o = MFMA32(vp, pb, o); SCHED(); }
        ex(3); ex(8); ex(9);
        sn = MFMA32(k1, q1, sn); SCHED();
        k1 = rd(kb, ko + 512); SCHED();
        ex(10); ex(11); cv(pa, 0, 0); cv(pa, 1, 2);
        if (pend) { ls = MFMA(__builtin_bit_cast(bf16x8, sel), __builtin_bit_cast(bf16x8, pb), ls); SCHED(); }
        vp = rd(vb, 8192 + vo1 + 512); SCHED();
        cv(pa, 2, 8); cv(pa, 3, 10); ex(4); ex(5);
        o = MFMA32(va, pa, zero ? (f32x16)(0.f) : o); SCHED();
        va = rd(nb, 8192 + vo0); SCHED();
        ex(6); ex(7); ex(12);
        ls = MFMA(__builtin_bit_cast(bf16x8, sel), __builtin_bit_cast(bf16x8, pa), zero ? (f32x4)(0.f) : ls); SCHED();
        ex(13); ex(14); ex(15); cv(pb, 0, 4); cv(pb, 1, 6); cv(pb, 2, 12); cv(pb, 3, 14);
    };
    auto flush = [&]() {
        ls = MFMA(__builtin_bit_cast(bf16x8, sel), __builtin_bit_cast(bf16x8, pb), ls); SCHED();
        o = MFMA32(vp, pb, o); SCHED();
    };
    float nm;
    {
        if (!HI) {
#pragma unroll
            for (int i = 0; i < 4; ++i) {
                const f32x4 t = *(const LAS f32x4*)(lds(tm) + i * 1024);
                ce[4 * i] = t[0]; ce[4 * i + 1] = t[1]; ce[4 * i + 2] = t[2]; ce[4 * i + 3] = t[3];
            }
        }
        q0 = rd(qa, 0);
        const u32x4 d0 = rd(ta, 6144);
        q1 = rd(qa, 512);
        const u32x4 d1 = rd(ta, 6144 + 512);
        k0 = rd(ta, 4096); k1 = rd(ta, 4096 + 512); va = rd(ta, 8192 + 6144);
        SCHED();
        if (KSP == -1) kvs();
        if (!HI) { masks(); setup(); SCHED(); }
        s0 = MFMA32(d0, q0, ce);
        s0 = MFMA32(d1, q1, s0);
        SCHED();
        if (!HI) { setup2(); SCHED(); }
        if (KSP == 0) kvs();
        if (KPP == 0) kvp();
        nm = -max16(s0);
        asm volatile("" : "+v"(nm));
#pragma unroll
        for (int r = 0; r < 16; ++r) { float t; asm("v_mov_b32 %0, %1" : "=v"(t) : "v"(nm), "i"(r)); cm[r] = t; }
        asm volatile("" : "+v"(cm));
        SCHED();
#pragma unroll
        for (int r = 0; r < 16; ++r) s0[r] = addf(s0[r], nm);
        asm volatile("" : "+v"(s0));
        SCHED();
        if (KSP == 1) kvs();
        if (KPP == 1) kvp();
        stage(s0, s1, cm, false, true, ta, 2048, ta, 6144, ta, 4096);
        if (KSP == 2) kvs();
        if (KPP == 2) kvp();
        if (qt > 0) {
            stage(s1, s0, cm, true, false, ta, 0, ta, 4096, ta, 2048);
            if (KSP == 3) kvs();
            if (qt > 1) {
                stage(s0, s1, cm, true, false, ta, 0, ta, 2048, ta, 0);
                if (qt > 2) stage(s1, s0, cm, true, false, ta, 0, ta, 0, ta, 0);
            }
        } else if (KSP == 3) kvs();
        flush();
    }
    LSTAMP(4);
    f32x16 c2;
    unsigned na, nb;
    auto rso = __builtin_amdgcn_make_buffer_rsrc(ob, 0, 2048, 0x00020000);
    auto starts = [&]() {
        const float n1 = c.pq >= 1 ? nm : NEG, n2 = c.pq >= 2 ? nm : NEG;
        asm volatile("" : "+v"(zq));
#pragma unroll
        for (int r = 0; r < 16; ++r) { cm[r] = n1; c2[r] = n2; ce[r] = 8 * (r >> 2) + (r & 3) <= zq ? NEG : n2; }
        na = (unsigned)(AT_N + H * 32768) + lb; nb = na + (unsigned)(16384 + qt * 2048);
        asm volatile("" : "+v"(cm), "+v"(c2), "+v"(ce), "+v"(na), "+v"(nb));
        asm volatile("" : "+s"(rso));
        SCHED();
    };
    {
        auto piece = [&](int b, int i) { dma16<true>(dst + b * 16384 + i * 4096, vk, ((b & 1) ? s2_ : s1_) + (b >> 1) * 16384 + i * 4096); };
        auto block = [&](int b) {
#pragma unroll
            for (int i = 0; i < 4; ++i) piece(b, i);
        };
        Ask t;
        if (HI) {
            poll_rmw<true>(t, c.ws, fo, __builtin_amdgcn_inverse_ballot_w64(kact), nkv, 62 - 4 * l);
            if (w == KPW) STAMP_AT(32 + l * 8 + 1);
        }
        WAIT_L0();
        LSTAMP(5);
        BAR();
        FENCE();
        LSTAMP(6);
        piece(0, H); piece(0, 2 + H);
        if (HI) {
            starts();
            if (APRIO) __builtin_amdgcn_s_setprio(APRIO);
            WAIT_V0();
            keep(t);
            LSTAMP(7);
            BAR();
            FENCE();
            if (w == 4) STAMP_AT(l * 8 + 3);
        } else {
            if (AB2 >= 0) block(1);
            if (AB2 > 0) block(2);
            starts();
            Ctx c2 = c;
            if (ELO) c2.wid = 0;
            unsigned vw = wo_w_off(c2);
            int qs = c.cw == 3 ? 1024 : 8 * D_ * 2;
            asm volatile("" : "+v"(vw));
            asm volatile("" : "+s"(qs));
            SCHED();
            wait_v<4 * AB2 + 4>();
            LSTAMP(7);
            BAR();
            if (AB2 < 0) block(1);
            if (AB2 <= 0) block(2);
            block(3);
            wait_v<8>();
            BAR();
            if (!ELO) {
                wo_w_issue<WA0, WA1>(c, Wo, vw);
                wait_v<WA1 - WA0>();
            } else WAIT_V0();
            LSTAMP(9);
            BAR();
            FENCE();
            if (!ELO) wo_w_issue<WA1, 26>(c, Wo, vw);
            else {
                const int s0 = w == 0 ? WA0 : (w == 1 ? WA0 + EN0 : WA0 + EN0 + EN1), s1 = w == 0 ? WA0 + EN0 : (w == 1 ? WA0 + EN0 + EN1 : (w == 2 ? 22 : 0));
#pragma unroll 1
                for (int sl = s0; sl < s1; ++sl) {
                    const char* b = Wo + (size_t)(unsigned)((2 * (sl & 15) + (sl >> 4)) * 65536);
#pragma unroll
                    for (int q = 0; q < 4; ++q) dma16<true>(sl * 4096 + q * 1024, vw, b + q * qs);
                }
            }
            if (LSL) { for (int i = 0; i < qt; ++i) __builtin_amdgcn_s_sleep(LSL); }
        }
    }
    SCHED();
    LSTAMP(8);
    {
        const u32x4 d0 = rd(na, 0), d1 = rd(na, 512);
        k0 = rd(na, 2048); k1 = rd(na, 2048 + 512); va = rd(na, 8192);
        SCHED();
        s0 = MFMA32(d0, q0, cm);
        s0 = MFMA32(d1, q1, s0);
        SCHED();
        stage(s0, s1, cm, false, false, na, 4096, na, 0, na, 2048);
        stage(s1, s0, cm, true, false, na, 6144, na, 2048, na, 4096);
        if (HI) { BAR(); FENCE(); SCHED(); }
        stage(s0, s1, cm, true, false, nb, 0, na, 4096, na, 6144);
        if (HI && CPOS == 3) { BAR(); SCHED(); }
        if (!HI && E30) {
            asm volatile("s_mov_b64 exec, 1\n\tds_add_u32 %0, %1 offset:12\n\ts_mov_b64 exec, -1" :: "v"(cnta), "v"(one) : "memory");
            SCHED();
        }
        stage(s1, s0, ce, true, false, nb, 2048, na, 6144, nb, 0);
        if (HI && CPOS == 4) { WAIT_L0(); BAR(); SCHED(); }
        stage(s0, s1, c2, true, false, nb, 4096, nb, 0, nb, 2048);
        if (HI && CPOS == 5) { BAR(); SCHED(); }
        if (qt < 3) {
            stage(s1, s0, c2, true, false, nb, 6144, nb, 2048, nb, 4096);
            if (qt < 2) {
                stage(s0, s1, c2, true, false, nb, 0, nb, 4096, nb, 6144);
                if (qt < 1) stage(s1, s0, c2, true, false, nb, 0, nb, 6144, nb, 0);
            }
        }
        flush();
    }
    LSTAMP(11);
    auto out = [&]() {
        const float iv = __builtin_amdgcn_rcpf(ls[0]);
#pragma unroll
        for (int u = 0; u < 2; ++u) {
            u32x4 t;
#pragma unroll
            for (int i = 0; i < 4; ++i) { const int r = 4 * u + (i >> 1) * 8 + (i & 1) * 2; t[i] = cvt_pk_bf16(o[r] * iv, o[r + 1] * iv); }
            __builtin_amdgcn_raw_buffer_store_b128(t, rso, lb + u * 512, 0, 16);
        }
        SCHED();
    };
    out();
    if (__builtin_expect(__any(!(ls[0] < SAFE_SUM)), 0)) {
        auto rk = __builtin_amdgcn_make_buffer_rsrc((void*)(kvb + H * 16384), 0, 16u << 20, 0x00020000);
        const int g0 = c.pq * 4 + qt - 8;
        float m = NEG;
#pragma unroll 1
        for (int ps = 0; ps < 2; ++ps) {
            o = (f32x16)(0.f); ls = (f32x4)(0.f);
#pragma unroll 1
            for (int j = g0 < 0 ? -g0 : 0; j < 9; ++j) {
                const int so = ((g0 + j) >> 2) * 524288 + ((g0 + j) & 3) * 2048;
                u32x4 f[4];
#pragma unroll
                for (int i = 0; i < 4; ++i) f[i] = __builtin_amdgcn_raw_buffer_load_b128(rk, lb + (i & 1) * 512, so + (i >> 1) * 8192, 16);
                f32x16 s;
                const float sb = ps ? -m : 0.f;
                float sa = j == 0 ? NEG : sb, sz = j == 8 ? NEG : sb;
                asm volatile("" : "+v"(zq), "+v"(sa), "+v"(sz));
#pragma unroll
                for (int r = 0; r < 16; ++r) s[r] = 8 * (r >> 2) + (r & 3) <= zq ? sa : sz;
                s = MFMA32(f[0], q0, s);
                s = MFMA32(f[1], q1, s);
                if (ps == 0) m = fmaxf(m, max16(s));
                else {
                    u32x4 p[2];
#pragma unroll
                    for (int r = 0; r < 16; r += 2) p[(r >> 2) & 1][(r >> 3) * 2 + ((r >> 1) & 1)] = cvt_pk_bf16(__builtin_amdgcn_exp2f(s[r]), __builtin_amdgcn_exp2f(s[r + 1]));
#pragma unroll
                    for (int u = 0; u < 2; ++u) {
                        o = MFMA32(f[2 + u], p[u], o);
                        ls = MFMA(__builtin_bit_cast(bf16x8, sel), __builtin_bit_cast(bf16x8, p[u]), ls);
                    }
                }
            }
        }
        __builtin_amdgcn_s_waitcnt(0x0F70);
        WAIT_V0();
        out();
    }
    SCHED();
    if (w == 3 || w == 4) STAMP_AT(32 + l * 8 + 3 + w);
    LSTAMP(12);
    if (HI) {
        WAIT_V0();
        arrive(std::integral_constant<int, 1>{}, 3u, 3u);
        if (w == 4) STAMP_AT(32 + l * 8 + 4);
        LSTAMP(13);
        if (APRIO) __builtin_amdgcn_s_setprio(0);
        if (lane == 0) asm volatile("ds_add_u32 %0, %1 offset:12" :: "v"(cnta), "v"(one) : "memory");
        WAIT_V0();
        arrive(std::integral_constant<int, 2>{}, 7u, 4u);
        if (w == 5 || w == 6) {
            Ctx c2 = c;
            c2.wid = 2 * w - 10;
            unsigned vw = wo_w_off(c2), vw2 = vw + (c.cw == 3 ? 1024u : 8u * D_ * 2);
            asm volatile("" : "+v"(vw), "+v"(vw2));
#pragma unroll 1
            for (unsigned spins = 0; spins < (1u << 22); ++spins) {
                if (__hip_atomic_load((LAS unsigned*)(smem + M_CNT + 12), __ATOMIC_RELAXED, __HIP_MEMORY_SCOPE_WORKGROUP) == (unsigned)(E30 ? 4 * l + 8 : 2 * l + 4)) break;
                __builtin_amdgcn_s_sleep(1);
            }
            FENCE();
            wo_w_issue<ELO ? 22 : 26, E30 ? 32 : 30>(c2, Wo, vw);
            c2.wid += 1;
            wo_w_issue<ELO ? 22 : 26, E30 ? 32 : 30>(c2, Wo, vw2);
        }
    }
    LSTAMP(23);
    ASTORE();
}

DI void wo_load(const Ctx& c0, int l, const char* Wn, const char* Wo) {
    const Ctx c = fresh(c0);
    asm volatile("" : "+s"(Wn), "+s"(Wo));
    const u64 ep = lds_epoch(c.smem);
    const unsigned fo = o_find(c);
        if (l == 0 && c.wid == 6) {
            const int fr = c.lane & 15, od = fr & 1, j0 = 4 * (c.lane >> 4) + od;
            const bool p0 = j0 > fr, p1 = j0 + 2 > fr;
            const unsigned N = 0xff800000u, Z = 0x0c0c0c0cu, lo = od ? 0x01000c0cu : 0x0c0c0100u, hi = od ? 0x03020c0cu : 0x0c0c0302u;
            LAS u32x4* t = (LAS u32x4*)(c.smem + M_TAB + c.lane * 16);
            t[0] = (u32x4){p0 && !od ? 0u : N, p0 && od ? 0u : N, p1 && !od ? 0u : N, p1 && od ? 0u : N};
            t[64] = (u32x4){!p0 && !od ? 0u : N, !p0 && od ? 0u : N, !p1 && !od ? 0u : N, !p1 && od ? 0u : N};
            t[128] = (u32x4){p0 ? lo : Z, p1 ? hi : Z, p0 ? Z : lo, p1 ? Z : hi};
            t[192] = (u32x4){(unsigned)(M_RSTD + j0 * 4 + (p0 ? 0 : 64)), (unsigned)(M_RSTD + j0 * 4 + 8 + (p1 ? 0 : 64)), 0u, 0u};
        }
    if (CPY && l == 0 && c.wid < 6) {
#pragma unroll 1
        for (int i = 0; i < 2; ++i) {
            const int j = i ? (c.bid & 4) * 2 + (c.bid >> 5) : c.jq, g = c.wid == 5 ? 2 - 2 * (j >> 3) : 1 + 2 * (j >> 3);
            if ((j & 7) == 3) poll_flags(c.ws, ep, (unsigned)WS_FLAGQ + ((c.lane >> 2) * 16 + 4 * g + (c.lane & 3)) * FQS, true, 2u);
        }
    }
    Ask t, t2;
    if (!(l & 1)) {
        Ctx c2 = c;
        c2.wid = 2 * c.wid - 10;
        unsigned vw = wo_w_off(c2), vw2 = vw + (c.cw == 3 ? 1024u : 8u * D_ * 2);
        asm volatile("" : "+v"(vw), "+v"(vw2));
        poll_rmw(t, c.ws, fo, c.wid == 7 && c.lane < 16, ep + (4 * l + 3), 61 - 4 * l);
        if (c.wid == 7) STAMP_AT(32 + l * 8 + 2);
        BAR();
        if (!E30 && (c.wid == 5 || c.wid == 6)) {
            __builtin_amdgcn_s_sleep(W30S);
            wo_w_issue<30, 32>(c2, Wo, vw);
            c2.wid += 1;
            wo_w_issue<30, 32>(c2, Wo, vw2);
        }
        if (c.wid != 7) WAIT_V0();
        else {
            __builtin_amdgcn_s_sleep(OSL);
            if (OPL) poll_flags(c.ws, ep, fo, c.lane < 16, (unsigned)(4 * l + 4));
        }
    } else {
        asm volatile("" : "=v"(t.v[0]), "=v"(t.v[1]), "=v"(t.v[2]), "=v"(t.v[3]));
        WAIT_V0();
    }
    poll_rmw(t2, c.ws, fo, c.wid == 7 && c.lane < 16 && (!OPL || (l & 1)), ep + (4 * l + 4), 60 - 4 * l);
    if (c.wid == 7) STAMP_AT(32 + l * 8 + 2 + !(l & 1));
    BAR();
    WAIT_V0();
    keep(t); keep(t2);
    if (l == 3 && c.bid == 0 && c.wid == 7) {
#pragma unroll 1
        for (int i = 0; i < 4; ++i) poll_flags(c.ws, ep, (unsigned)WS_FLAGQ + (i * 64 + c.lane) * FQS, true, 1u);
        if (c.lane == 0) __hip_atomic_store((GAS u64*)(c.ws + WS_EPOCH), ep + 64, __ATOMIC_RELAXED, __HIP_MEMORY_SCOPE_AGENT);
    }
    if (RS0 && l == 3) BAR();
}

DI void wo_consume(const bool B, const Ctx& c0, const bf16_t* xin, bf16_t* xout, int l) {
    u32x4 res[2][2];
    const Ctx c = fresh(c0);
    LAS char* smem = c.smem;
    const int lane = c.lane, w = c.wid, fr = lane & 15, fq = lane >> 4;
    const char* obase = c.ws + WS_O + (size_t)l * (8u << 20);
    int ovo = (c.pw * 8 + w * 2) * 1024 + lane * 16;
    asm volatile("" : "+v"(ovo));
    constexpr int NP = WNQ, R = 3;
    static_assert(!HO && !HX && !HS && !RLD && BPR == 1, "layout");
    auto ro = __builtin_amdgcn_make_buffer_rsrc((void*)obase, 0, 4u << 20, 0x00020000);
    asm volatile("" : "+s"(ro));
    u32x4 xa[NP][2];
    s16x4 wf[R][8];
    f32x4 acc[2][4];
    unsigned bw[8];
    int coff, rmo, spr;
    u32x4 on = {0x3f803f80u, 0x3f803f80u, 0x3f803f80u, 0x3f803f80u};
    unsigned sto, xpo, coff1, cnta = M_CNT, one = 1;
    u64 xv, b0, b1, am;
    auto rs = __builtin_amdgcn_make_buffer_rsrc(xout, 0, S_ * D_ * 2, 0x00020000);
    auto rst = __builtin_amdgcn_make_buffer_rsrc(c.ws + WS_STATS + (size_t)((l + 1) & 3) * 524288, 0, 524288, 0x00020000);
    auto prep2 = [&]() {
        sto = fq == (fr >> 2) && l < 3 ? (unsigned)((c.pw * 16 + c.jw) * 1024 + (w * 32 + fr) * 8) : 0x80000000u;
        xpo = x_post(c);
        coff = (int)pick(l == 3, (unsigned)rmo, (unsigned)coff);
        coff1 = coff + (l == 3 ? xrow<0>(0, 16) : 2048);
        spr = l == 3 ? 64 : 1024;
        asm volatile("" : "+s"(spr));
        b0 = __builtin_amdgcn_ballot_w64(fr & 1); b1 = __builtin_amdgcn_ballot_w64(fr & 2); am = __builtin_amdgcn_ballot_w64(x_act(c, !B));
        asm volatile("" : "+v"(on), "+v"(sto), "+v"(xpo), "+v"(coff), "+v"(coff1), "+v"(xv), "+v"(cnta), "+v"(one));
        asm volatile("" : "+s"(rs), "+s"(rst), "+s"(b0), "+s"(b1), "+s"(am));
    };
    auto prep = [&]() {
        int ln = lane;
        asm volatile("" : "+v"(ln));
        const int fr = ln & 15, fq = ln >> 4, q4 = fr >> 2, p4 = ln & 3;
        coff = (c.pw * 16 + c.jw) * 16384 + w * 4096 + (((fq & 1) * 2 + (fq >> 1)) * 16 + fr) * 16;
        rmo = xrow<0>(c.pw, w * 32 + fr) + xcol<0>(c.jw) + (fq & 1) * 32 + (fq >> 1) * 16;
        asm volatile("" : "+v"(coff), "+v"(rmo));
        const int fb = (q4 & 2) | ((fq & 1) << 2);
        const unsigned swp = (B && w >= 2) ? 2048u : 0u;
#pragma unroll
        for (int cc = 0; cc < 4; ++cc) {
            const unsigned b = (unsigned)((4 * fq + q4) * 128 + (((2 * cc + (p4 >> 1)) ^ fb) << 4) + (p4 & 1) * 8);
            bw[2 * cc] = b + swp; bw[2 * cc + 1] = b + (swp ^ 2048u);
            asm volatile("" : "+v"(bw[2 * cc]), "+v"(bw[2 * cc + 1]));
        }
    };
    auto start = [&](int mt, int pr) {
            {
                auto r0 = __builtin_amdgcn_permlane16_swap(res[mt][pr][0], res[mt][pr][2], false, false);
                auto r1 = __builtin_amdgcn_permlane16_swap(res[mt][pr][1], res[mt][pr][3], false, false);
#pragma unroll
                for (int h = 0; h < 2; ++h) {
                    acc[mt][2 * pr + h] = (f32x4){bf_lo(r0[h]), bf_hi(r0[h]), bf_lo(r1[h]), bf_hi(r1[h])};
                    asm volatile("" : "+v"(acc[mt][2 * pr + h]));
                }
            }
    };
    auto rdw = [&](auto ic) {
        constexpr int i = decltype(ic)::value, h = i >> 3, k = i & 7;
        wf[h % R][k] = tr_read(lds(bw[k]) + (h & 15) * 4096);
        if constexpr (h == 15) { bw[k] += 65536; asm volatile("" : "+v"(bw[k])); }
    };
    {
        auto resid = [&]() {
            const unsigned roff = pick(l != 0, (unsigned)coff, (unsigned)rmo);
            const char* xp = (const char*)xin;
#pragma unroll
            for (int i = 0; i < 4; ++i) aload4c(res[i >> 1][i & 1], roff, xp + (i >> 1) * (l ? 2048 : xrow<0>(0, 16)) + (i & 1) * (l ? 1024 : 64));
            SCHED();
        };
        unsigned opo = o_post(c), oca = B ? M_CNT : M_CNT + 8, old = 0, msk = B ? 3u : 7u;
        xv = lds_epoch(smem) + (unsigned)(l + 2);
        u64 ov = xv + (unsigned)(3 * l + 2);
        prep();
        asm volatile("" : "+v"(opo), "+v"(oca), "+v"(ov), "+v"(old), "+v"(one));
        asm volatile("" : "+s"(msk));
        SCHED();
        if (RBP) resid();
        static_for<0, 16>([&](auto ic) { rdw(ic); SCHED(); });
        if (!P2A) prep2();
        SCHED();
        wait_v<RBP ? 4 : 0>();
        if (lane == 0) asm volatile("ds_add_rtn_u32 %0, %1, %2\n\ts_waitcnt lgkmcnt(0)" : "+v"(old) : "v"(oca), "v"(one) : "memory");
        if ((__builtin_amdgcn_readfirstlane(old) & msk) == msk) { if (lane < 16) post(c.ws, opo, ov); }
        if (w == 0) STAMP_AT(l * 8 + 4);
        SCHED();
        if (!RBP) resid();
        if (P2A == 1) prep2();
        SCHED();
        if (w == 3) STAMP_AT(32 + l * 8 + 5);
        BAR();
    }
    FENCE();
    SCHED();
    if (w == 0) STAMP_AT(l * 8 + 5);
    int so = 0;
    auto next = [&](int j) { so += j == 15 ? 131072 - 15 * 262144 : 262144; asm volatile("" : "+s"(so)); };
    auto ldo = [&](int j, int mt) { xa[j % NP][mt] = __builtin_amdgcn_raw_buffer_load_b128(ro, ovo + mt * 1024, so, 16); };
#pragma unroll
    for (int h = 0; h < NP; ++h) {
        ldo(h, 0);
        SCHED();
        if (h > SH0 && h <= SH0 + 2) { start(h - SH0 - 1, 0); SCHED(); }
        ldo(h, 1);
        if (!WAL || h + 1 < NP) next(h);
        SCHED();
        if (P2A >= 2 && h == P2A - 2) { prep2(); SCHED(); }
        if (h == SH0) {
            wait_v<2 * SH0 + 2>();
            asm volatile("" : "+v"(res[0][0]), "+v"(res[0][1]), "+v"(res[1][0]), "+v"(res[1][1]));
        }
        if (h > SH0 && h <= SH0 + 2) start(h - SH0 - 1, 1);
        SCHED();
    }
    int d0 = 262144, d1 = 131072 - 15 * 262144;
    asm volatile("" : "+s"(d0), "+s"(d1));
    static_assert(WEP || !WAL, "last step");
    static_for<0, WEP ? 31 : 32>([&](auto jc) {
        constexpr int j = decltype(jc)::value;
        constexpr int c0 = j < 30 ? 16 + 8 * j : 256, c1 = j < 30 ? c0 + 8 : 256;
        if constexpr (j + NP == 16) {
            static_assert(c0 <= 8 * 20, "weights that landed before the attention");
            if (!B) BAR();
            FENCE();
            asm volatile("" : "+v"(ovo));
            SCHED();
        }
        if constexpr (j == 0 || j + NP == 16) ALIGN8();
        constexpr int nv = WAL == 1 || j == 0 || j + NP > 32 ? 2 * (NP - 1 < 31 - j ? NP - 1 : 31 - j) : 2 * NP - 3;
        __builtin_amdgcn_s_waitcnt(0x0870 | nv);
        const int sp = so;
        static_assert(!RS0 || (RS0 + NP >= 32 && RS0 < 31 && QW_SLOT <= (RS0 - 1) * 4096), "R0");
        if constexpr (RS0 && j == RS0) BAR();
        else asm volatile("s_add_i32 %0, %1, %2" : "=&s"(so) : "s"(sp), "s"(j + NP == 16 ? d1 : d0) : "scc");
        SCHED();
        static_for<0, 8>([&](auto mc) {
            constexpr int m = decltype(mc)::value, mt = m >> 2, nt = mt ? 3 - (m & 3) : m & 3;
            const bf16x8 wv = __builtin_shufflevector(wf[j % R][2 * nt], wf[j % R][2 * nt + 1], 0, 1, 2, 3, 4, 5, 6, 7);
            if constexpr (j < 31) asm volatile("v_mfma_f32_16x16x32_bf16 %0, %1, %2, %0" : "+v"(acc[mt][nt]) : "v"(wv), "v"(xa[j % NP][mt]));
            else acc[mt][nt] = MFMA(wv, __builtin_bit_cast(bf16x8, xa[j % NP][mt]), acc[mt][nt]);
            SCHED();
            if constexpr (c0 < c1 && m < 7) { rdw(std::integral_constant<int, c0 + m>{}); SCHED(); }
            if constexpr (c0 < c1 && m == 6) rdw(std::integral_constant<int, c0 + 7>{});
            if constexpr (m == WL1 && j > 0 && j - 1 + NP < 32) xa[(j + NP - 1) % NP][1] = __builtin_amdgcn_raw_buffer_load_b128(ro, ovo + 1024, sp, 16);
            if constexpr (m == WL1 + 4 && j + NP < 32) ldo(j, 0);
            SCHED();
        });
    });
    const bf16x8 ones = __builtin_bit_cast(bf16x8, on);
    u32x4 o[2][2];
    f32x4 sm[2], g[2];
    const f32x4 zero = {0.f, 0.f, 0.f, 0.f};
    auto sel = [&](float a, float b, u64 m) { return __builtin_amdgcn_inverse_ballot_w64(m) ? b : a; };
    auto stats = [&](int mt) {
        float qa = sel(g[mt][0], g[mt][1], b0), qb = sel(g[mt][2], g[mt][3], b0);
        asm volatile("" : "+v"(qa), "+v"(qb));
        SCHED();
        if (WEP) asm volatile("" :: "v"(sm[1][1]), "v"(sm[1][2]), "v"(sm[1][3]), "v"(o[1][0]), "v"(o[1][1]), "v"(on));
        __builtin_amdgcn_raw_buffer_store_b64((u32x2){__float_as_uint(sm[mt][0]), __float_as_uint(sel(qa, qb, b1))}, rst, sto + mt * 128, 0, 16);
        SCHED();
    };
    u32x2 pk[2];
    auto last = [&](int mt, int nt) {
        acc[mt][nt] = MFMA(__builtin_shufflevector(wf[31 % R][2 * nt], wf[31 % R][2 * nt + 1], 0, 1, 2, 3, 4, 5, 6, 7), __builtin_bit_cast(bf16x8, xa[31 % NP][mt]), acc[mt][nt]);
        SCHED();
    };
    auto cv = [&](int mt, int pr) {
        pk[0] = pack4(acc[mt][2 * pr]); pk[1] = pack4(acc[mt][2 * pr + 1]);
        asm volatile("" : "+v"(pk[0]), "+v"(pk[1]));
        SCHED();
    };
    auto st = [&](int mt, int pr) {
        o[mt][pr] = widen(pk[0], pk[1]);
        __builtin_amdgcn_raw_buffer_store_b128(o[mt][pr], rs, mt ? coff1 : coff, pr ? spr : 0, 16);
        SCHED();
    };
    auto gq = [&](int mt, int pr, bool f) { const bf16x8 of = __builtin_bit_cast(bf16x8, o[mt][pr]); g[mt] = MFMA(of, of, f ? zero : g[mt]); SCHED(); };
    auto gs = [&](int mt, int pr, bool f) { sm[mt] = MFMA(ones, __builtin_bit_cast(bf16x8, o[mt][pr]), f ? zero : sm[mt]); SCHED(); };
    WAIT_L0();
    last(0, 0); last(0, 1); last(0, 2); last(0, 3);
    last(1, 3); cv(0, 0);
    last(1, 2); st(0, 0);
    last(1, 1); cv(0, 1);
    last(1, 0); st(0, 1);
    if (w == 0) STAMP_AT(l * 8 + 6);
    gq(0, 0, true); cv(1, 1);
    gs(0, 0, true); st(1, 1);
    gq(0, 1, false); cv(1, 0);
    gs(0, 1, false); st(1, 0);
    gq(1, 1, true);
    gs(1, 1, true);
    gq(1, 0, false);
    gs(1, 0, false);
    stats(0);
    stats(1);
    if (OWN && (OWA ? l < 3 : !B)) {
        const unsigned la = QA_OFF + ((unsigned)coff & 16383u);
#pragma unroll
        for (int i = 0; i < 4; ++i) *(LAS u32x4*)(lds(la) + i * 1024) = o[i >> 1][i & 1];
        FENCE();
    }
    if (l < 3) {
        unsigned old = 0;
        if (!RS0 || RN1 > RN0) BAR();
        wait_v<XE2>();
        if (lane == 0) asm volatile("ds_add_rtn_u32 %0, %1, %2\n\ts_waitcnt lgkmcnt(0)" : "+v"(old) : "v"(cnta), "v"(one) : "memory");
        if ((__builtin_amdgcn_readfirstlane(old) & 3) == 3) {
            if (__builtin_amdgcn_inverse_ballot_w64(am)) post(c.ws, xpo, xv);
        }
    }
    if (w == 0) STAMP_AT(l * 8 + 7);
}

__global__ __launch_bounds__(512, 2) __attribute__((aligned(4096))) void k_main(Params P) {
    extern __shared__ __attribute__((aligned(16))) char smem_[];
    KP kp = (KP)__builtin_amdgcn_kernarg_segment_ptr();
    const char *x0, *w0, *wo;
    Ctx c;
    c.smem = (LAS char*)smem_;
    c.lane = threadIdx.x & 63;
    c.wid = __builtin_amdgcn_readfirstlane(threadIdx.x >> 6);
    c.bid = blockIdx.x;
    const bool loader = c.wid >= 4;
    unsigned va, vw;
    auto enter = [&](const bool LD) {
        if (LD) asm volatile("s_load_dwordx2 %0, %4, 0x0\n\ts_load_dwordx2 %1, %4, 0x8\n\ts_load_dwordx2 %2, %4, 0x10\n\ts_load_dwordx2 %3, %4, 0x20 ; loader" : "=&s"(x0), "=&s"(w0), "=&s"(c.ws), "=&s"(wo) : "s"(kp));
        else asm volatile("s_load_dwordx2 %0, %4, 0x0\n\ts_load_dwordx2 %1, %4, 0x8\n\ts_load_dwordx2 %2, %4, 0x10\n\ts_load_dwordx2 %3, %4, 0x20" : "=&s"(x0), "=&s"(w0), "=&s"(c.ws), "=&s"(wo) : "s"(kp));
        if (LD) asm volatile("; loader" : "+s"(c.bid), "+s"(c.wid)); else asm volatile("; consumer" : "+s"(c.bid), "+s"(c.wid));
        if (LD) asm volatile("; loader" : "+v"(c.lane)); else asm volatile("; consumer" : "+v"(c.lane));
        static_assert(QXM == 8 && WXM == 4 && ALT == 1, "map");
        c.pq = (((c.bid & 1) * 8) ^ PQX) + ((c.bid >> 3) & 7); c.jq = (c.bid & 6) * 2 + (c.bid >> 6);
        const int w = c.wid, lr = c.lane >> 3, lc = c.lane & 7;
        va = (unsigned)((c.pq * 128 + 8 * (w & 3) + lr) * (D_ * 2) + ((lc ^ lr) << 4));
        vw = (unsigned)((8 * (w & 3) + lr) * 6144 + c.jq * 128 + ((lc ^ ((lr & 2) | ((w & 1) << 2))) << 4));
        asm volatile("" : "+v"(va), "+v"(vw));
        c.pw = ((c.pq & 8) ^ 8) + ((c.bid >> 1) & 1) * 4 + ((c.bid >> 3) & 3); c.jw = (c.bid & 4) * 2 + (c.bid >> 5); c.cw = 0;
        if (UNI) { c.pw = c.pq ^ 8; c.jw = c.jq; }
        if (LD) {
            asm volatile("" : "+s"(c.pw), "+s"(c.jw));
            asm volatile("s_waitcnt lgkmcnt(0)" : "+s"(x0), "+s"(w0), "+s"(c.ws), "+s"(wo));
            if (ESL) __builtin_amdgcn_s_sleep(ESL);
        } else {
            int t0 = first_tile(first_order(c.pq, c.jq), 0);
            asm volatile("" : "+s"(t0));
            asm volatile("s_waitcnt lgkmcnt(0) ; c" : "+s"(x0), "+s"(w0), "+s"(c.ws), "+s"(wo));
            if (ST0 && c.wid == 3) STAMP_AT(41);
            const char* xb = x0 + (unsigned)(t0 * 128);
            const char* wb = w0 + (unsigned)(t0 * (64 * 6144));
#pragma unroll
            for (int h = 0; h < 2; ++h) {
#pragma unroll
                for (int j = 0; j < 2; ++j) dma16<false>(QA_OFF + (j * 8 + h * 4 + w) * 1024, va, xb + (j * 64 + h * 32) * (D_ * 2));
#pragma unroll
                for (int j = 0; j < 3; ++j) dma16<false>((j * 8 + h * 4 + w) * 1024, vw, wb + h * (32 * 6144) + j * 2048);
            }
            if (ST0 && c.wid == 1) STAMP_AT(43);
            if (w == 0 && c.lane < 2) touch(M_EP, (unsigned)(WS_EPOCH + c.lane * 4), c.ws);
            if (w == 1 && c.lane < 6) *(LAS unsigned*)(c.smem + (c.lane < 4 ? M_CNT + c.lane * 4 : M_GO - 16 + c.lane * 4)) = 0u;
        }
        asm volatile("" : "+s"(kp), "+s"(c.bid), "+s"(c.ws));
    };
    if (c.wid == CPW) {
        const char* pc; int len;
        asm volatile("s_getpc_b64 %0\n.Lcpf_a:\n\ts_mov_b32 %1, .Lcpf_end-.Lcpf_a-4" : "=s"(pc), "=s"(len));
        const int o = min((int)(((c.bid >> 3) & 7) * 64 + c.lane) * 128, len);
        asm volatile("s_mov_b32 m0, %0\n\ts_nop 3\n\tglobal_load_lds_dword %1, %2" ::"s"(M_TCH), "v"(o), "s"(pc) : "memory");
    }
    const char* xi;
    auto step = [&](int l) {
        asm volatile("" : "+s"(kp), "+s"(c.ws), "+s"(c.jw), "+s"(c.bid));
        if (HYB) {
            const int base = (c.bid & 1) * 8 + (c.bid & 2) * 2 + ((c.bid >> 3) & 3), jb = (c.bid & 4) * 2 + (c.bid >> 5);
            const int pa = ((((c.bid & 1) * 8) ^ PQX) + ((c.bid >> 3) & 7)) ^ MA2, ja = (c.bid & 6) * 2 + (c.bid >> 6);
            c.pw = OWA && l == 0 ? pa : base ^ ((MW >> (4 * l)) & 15);
            c.jw = OWA && l == 0 ? ja : jb;
            c.pq = (l & 1) ? pa : base ^ (l ? MB3 : MB1);
            c.jq = (l & 1) ? ja : jb;
            if (UNI) { c.pq = (l & 1) ? pa : pa ^ 8; c.jq = ja; if (l < 2 || UNI == 2) { c.pw = c.pq ^ 8; c.jw = ja; } }
        } else { c.pq ^= 8; c.pw ^= 8; } c.cw = CPY ? c.jw & 7 : 0;
        xi = c.ws + WS_X + (unsigned)(l * (8u << 20));
        wo = (const char*)((l & 1) ? kp->wo0 : kp->wo1);
        if (c.cw == 3) wo = c.ws + WS_CO + (((l & 1) ? 2 : 0) + (c.jw >> 3)) * 4096;
    };
    if (loader) {
        enter(true);
        xi = x0;
            qkv_load<false, true>(c, xi, w0, wo, 0, kp, va, vw);
#pragma unroll 1
        for (int l = 0;;) {
            asm volatile("" : "+s"(l));
            if (!(l & 1)) attn_a<true>(c, l, wo, kp);
            wo_load(c, l, nullptr, wo);
            if (l == 3) break;
            step(l);
            ++l;
            if (l == 2) { qkv_load<false, false>(c, xi, (const char*)kp->wqkv0, wo, l); }
            else { qkv_load<true, false>(c, xi, (const char*)kp->wqkv1, wo, l); }
        }
    } else {
        enter(false);
        xi = x0;
        bf16_t* xo = (bf16_t*)(c.ws + WS_X);
#pragma unroll 1
        for (int l = 0; l < 4; ++l) {
            if (l & 1) { qkv_consume<true>(c, l, kp); }
            else {
                qkv_consume<false>(c, l, kp, va, vw);
                attn_a<false>(c, l, wo, kp);
            }
            wo_consume(l & 1, c, (const bf16_t*)xi, xo, l);
            step(l);
            xo = l == 2 ? kp->out : (bf16_t*)(c.ws + WS_X + (unsigned)((l + 1) * (8u << 20)));
            asm volatile("" : "=v"(va), "=v"(vw));
        }
    }
    asm volatile(".Lcpf_end:");
}

extern "C" void kernel_launch(void* const* d_in, const int* in_sizes, int n_in, void* d_out, int out_size, void* d_ws, size_t ws_size,
                              hipStream_t stream) {
    Params P;
    P.x0 = (const bf16_t*)d_in[0];
    P.wqkv0 = (const bf16_t*)d_in[1];
    P.wo0 = (const bf16_t*)d_in[2];
    P.wqkv1 = (const bf16_t*)d_in[4];
    P.wo1 = (const bf16_t*)d_in[5];
    P.out = (bf16_t*)d_out;
    P.ws = (char*)d_ws;
    (void)hipFuncSetAttribute((const void*)k_main, hipFuncAttributeMaxDynamicSharedMemorySize, LDS_TOTAL);
    k_main<<<256, 512, LDS_TOTAL, stream>>>(P);
}
```

```cpp
#include <hip/hip_runtime.h>
#include <stdint.h>
#include <type_traits>

typedef unsigned short bf16_t;
typedef short bf16x8 __attribute__((ext_vector_type(8)));
typedef short s16x4 __attribute__((ext_vector_type(4)));
typedef float f32x4 __attribute__((ext_vector_type(4)));
typedef float f32x2 __attribute__((ext_vector_type(2)));
typedef float f32x16 __attribute__((ext_vector_type(16)));
typedef unsigned u32x4 __attribute__((ext_vector_type(4)));
typedef unsigned u32x2 __attribute__((ext_vector_type(2)));
#define DI __device__ __forceinline__
#define LAS __attribute__((address_space(3)))
#define GAS __attribute__((address_space(1)))
#define STAMP 0

constexpr int S_ = 2048, D_ = 1024, KT = 16;
constexpr float LN_EPS = 1e-5f;
constexpr float QSCALE = 0.17677669529663687f * 1.4426950408889634f;

#define FQO 0
#define XBO 524288
#define HX 0
#define HO 0
#define HK 0
#define HS 0
#define HM 1
constexpr size_t WS_FLAGQ = FQO;
constexpr size_t WS_FLAGW = 98304;
constexpr size_t WS_EPOCH = 65536;
constexpr size_t WS_CSUM = 131072;
constexpr size_t WS_XBOX = XBO;
constexpr size_t WS_OBOX = 655360;
constexpr size_t WS_KBOX = 720896;
constexpr size_t WS_STATS = 1u << 20;
constexpr size_t WS_X = 4u << 20;
constexpr size_t WS_O = 96u << 20;
constexpr size_t WS_KV = 28u << 20;
constexpr size_t WS_CQ = 36u << 20;
constexpr size_t WS_CO = 42u << 20;
constexpr size_t WS_STAMP = 64u << 20;

template <int H> DI constexpr int hp(int c) { return !H ? c * 1024 : (c >> 2) * 8192 + (HM == 1 ? (c & 3) * 256 : (c & 1) * 128 + (c & 2) * 256); }
template <int H> DI constexpr int hl(int u) { return !H ? u : (HM == 1 ? u + (u & ~127) : u + (u & ~255)); }
template <int H> DI constexpr int hv(int lane) { return !H ? lane * 16 : (lane >> 3) * 1024 + (lane & 7) * 16; }
template <int H> DI constexpr int xrow(int p, int r) { return H == 0 ? (p * 128 + r) * 2048 : (H == 1 ? (p * 128 + r) * 4096 : (H == 2 ? p * 524288 + r * 256 : p * 262144 + r * 128)); }
template <int H> DI constexpr int xcol(int t) { return H == 0 ? t * 128 : (H == 1 ? hl<1>(t * 128) : (H == 2 ? t * 32768 : t * 16384)); }
#define WTC 2
#define WT0 1
#define SPR 1
#define TOPJ 0x2000
#define WV0 1
#define FQS 256
#define SNK 1
#define WBE 1
#define WAE 1
#define KSP 0
#define K0E 1
#define TOPS 4
#define BPR 1
#define RLD 0
#define CPF 2
#define WB14 0
#define WB15 5
#define WT13 2
#define ENT 1
#define MBOX 3
#define OBS 256
#define PQX 8
#define ALT 1
#define ALN 1
#define NQA_ 3
#define NQW_ 4
#define LPRIO 3
#define TOUCH 1
#define TSTEP 13
#define XS_ 9
#define ROT0 0
#define WPRE 3
#define PSLEEP 1
#define TW0 5
#define APRIO 1
#define QXM 8
#define WNQ 5
#define WRG 2
#define WBS 1
#define WEP 1
#define WAL 2
#define WL1 0
#define RS0 27
#define RN0 6
#define RN1 6
#define WXM 4
#define CPOS 5
#define LSL 0
#define SAFE_SUM 0x1p80f
#define AB2 0
#define W30S 8
#define CPY 1
#define HT0 0
#define XST 2
#define CKW 4
#define XSP 1
#define EPOLL 1
#define RSLP 1
#define APOST 1
#define ASLP 2
#define APIPE 4
#define RBP 0
#define P2A 1
#define SH0 2
#define KPW 4
#define LWA 7
#define WA0 12
#define WA1 22
#define OSL 14
#define OPL 1
#define E30 1
#define HYB 1
#define MW 0x808
#define MB1 0
#define MB3 0
#define MA2 0
#define OWN 1
#define CUP 1
#define TL0B 0
#define CK2 1
#define XH 0
#define OWA 1
#define UNI 0
#define PRT 0
#define PRO 0
#define EXP0 0
#define KPP 1
#define HP4 1
#define TWQ 1
#define ERL 1
#define ESL 0
#define DM 0
#define DS 0
#define ELO 0
#define EN0 3
#define EN1 2
#define ST0 0
#define NC 1
#define NCM 1
#define NCB 4
#define AD1 3
#define AD2 0
#define AD3 4
#define TWO 1
#define XSB 10
#define TL0 0
#define TL1 12
#define FDM 1
#define TSP 0
#define CPW 4
#define EXS 1
#define NCH 2
#define XE2 0
constexpr int QA_SLOT = 18432, NQA = NQA_, QW_SLOT = 24576, NQW = NQW_, QA_OFF = 3 * QW_SLOT, QW_TOP = 131072;
DI constexpr int qw_slot(int s) { return s == 3 ? QW_TOP : s * QW_SLOT; }
constexpr int AT_N = 90112, AT_OWN = AT_N + 32768, AT_Q = 73728;
constexpr int MISC = 155648, M_MEAN = MISC, M_RSTD = MISC + 640, M_CS = MISC + 1280, M_CNT = MISC + 2816, M_EP = MISC + 2832, M_GO = MISC + 2848, M_W0 = MISC + 2852, M_H0 = QA_OFF + NQA * QA_SLOT, M_TCH = MISC + 3072, M_TAB = MISC + 4096, M_XCH = 131072;
constexpr int LDS_TOTAL = 163840;
static_assert(M_H0 + 2048 <= QW_TOP && QW_TOP + QW_SLOT <= MISC && AT_N + 65536 <= MISC && AT_Q + 16384 <= AT_N, "lds");

DI unsigned cvt_pk_bf16(float lo, float hi) {
    typedef __bf16 bf2 __attribute__((ext_vector_type(2)));
    bf2 r; r[0] = (__bf16)lo; r[1] = (__bf16)hi;
    return __builtin_bit_cast(unsigned, r);
}
DI u32x2 pack4(const f32x4& v) { return (u32x2){cvt_pk_bf16(v[0], v[1]), cvt_pk_bf16(v[2], v[3])}; }
DI float bf_lo(unsigned u) { return __uint_as_float(u << 16); }
DI float bf_hi(unsigned u) { return __uint_as_float(u & 0xffff0000u); }
DI u32x4 widen(const u32x2& a, const u32x2& b) {
    auto r0 = __builtin_amdgcn_permlane16_swap(a[0], b[0], false, false);
    auto r1 = __builtin_amdgcn_permlane16_swap(a[1], b[1], false, false);
    return (u32x4){r0[0], r1[0], r0[1], r1[1]};
}
#define MFMA(a, b, c) __builtin_amdgcn_mfma_f32_16x16x32_bf16((a), (b), (c), 0, 0, 0)
#define WAIT_L0() __builtin_amdgcn_s_waitcnt(0xC07F)
#define WAIT_V0() asm volatile("s_waitcnt vmcnt(0)" ::: "memory")
#define BAR() __builtin_amdgcn_s_barrier()
#define SCHED() __builtin_amdgcn_sched_barrier(0)
template <int N> DI void wait_v() { asm volatile("s_waitcnt vmcnt(%0)" ::"i"(N) : "memory"); }
template <int I, int N, class F> DI void static_for(F&& f) { if constexpr (I < N) { f(std::integral_constant<int, I>{}); static_for<I + 1, N>(f); } }

template <bool SC1> DI void dma16(unsigned lds, unsigned voff, const char* sbase) {
    if (SC1) asm volatile("s_mov_b32 m0, %0\n\ts_nop 3\n\tglobal_load_lds_dwordx4 %1, %2 sc1" ::"s"(lds), "v"(voff), "s"(sbase) : "memory");
    else asm volatile("s_mov_b32 m0, %0\n\ts_nop 3\n\tglobal_load_lds_dwordx4 %1, %2" ::"s"(lds), "v"(voff), "s"(sbase) : "memory");
}
DI void touch(unsigned lds, unsigned voff, const char* sbase) {
    asm volatile("s_mov_b32 m0, %0\n\ts_nop 3\n\tglobal_load_lds_dword %1, %2 sc1" ::"s"(lds), "v"(voff), "s"(sbase) : "memory");
}
DI s16x4 tr_read(const LAS char* p) {
    typedef short v4i16_t __attribute__((ext_vector_type(4)));
    return __builtin_bit_cast(s16x4, __builtin_amdgcn_ds_read_tr16_b64_v4i16((LAS v4i16_t*)p));
}
DI LAS char* lds(unsigned off) { return (LAS char*)(uintptr_t)off; }
DI unsigned long long rstamp() { unsigned long long t; asm volatile("s_memrealtime %0\n\ts_waitcnt lgkmcnt(0)" : "=s"(t)::"memory"); return t; }

constexpr int ow_w(int kt) { return CUP == 3 ? (kt < 1 ? 6 : 6 * kt) : kt < 2 ? 0 : (CUP == 0 ? (kt < 5 ? 8 * (kt - 1) : 30) : (CUP == 1 ? 8 * kt - 10 : (kt == 2 ? 6 : (kt == 3 ? 13 : (kt == 4 ? 21 : 30))))); }
template <int NAI, int NWI, bool FIRST> struct Sched {
    int w[KT];
    constexpr Sched() : w{} {
        int endA[KT] = {}, endW[KT] = {};
        int n = 0;
        if (FIRST) {
            if (ENT == 0) { n += 2; endA[0] = n; n += 3; endW[0] = n; }
            n += NAI; endA[1] = n; n += NWI; endW[1] = n;
            if (ERL >= 5) { n += NAI; endA[2] = n; }
            if (ERL == 2 || ERL == 3 || ERL == 5) { n += NWI; endW[2] = n; }
        } else {
            for (int s = 0; s < WPRE; ++s) { n += NWI; endW[s] = n; }
            for (int s = 0; s < 2; ++s) { n += NAI; endA[s] = n; }
        }
        for (int kt = 0; kt < KT; ++kt) {
            const int need = endA[kt] > endW[kt] ? endA[kt] : endW[kt];
            w[kt] = (n - need) > 63 ? 63 : (n - need);
            if (kt + 2 < KT && !(FIRST && ERL >= 5 && kt == 0)) { n += NAI; endA[kt + 2] = n; }
            if (FIRST && (ERL == 4 || ERL == 6) && kt == 0) { n += NWI; endW[2] = n; }
            if (OWN && !FIRST && (NAI == 5 || OWA) && kt < 5) {
                for (int p = ow_w(kt); p < ow_w(kt + 1); ++p) { ++n; endW[3 + p / 6] = n; }
                continue;
            }
            if (FIRST && ERL < 2 && kt < 5) {
                if (kt < 4) { n += NWI; endW[kt + 2] = n; }
                if (kt >= 3) n += NWI / 2;
                if (kt == 4) { endW[6] = n; n += NWI; endW[7] = n; }
                continue;
            }
            if (!FIRST && WPRE == 2 && kt < 2) { n += NWI; endW[kt + 2] = n; }
            if ((FIRST || WPRE == 3) && kt == 0) { n += NWI; endW[3] = n; }
            if (kt >= 1 && kt + 3 < KT) { n += NWI; endW[kt + 3] = n; }
            if (kt == 13) n += WT13;
            if (kt == 14) n += (NAI == 5 || WA0 > 12 ? 12 : WA0) - WT13 + (NAI == 5 ? WB14 : 0);
        }
    }
};

struct Params {
    const bf16_t* x0;
    const bf16_t* wqkv0;
    char* ws;
    const bf16_t* wqkv1;
    const bf16_t* wo0;
    const bf16_t* wo1;
    bf16_t* out;
};
typedef const __attribute__((address_space(4))) Params* KP;
DI int rot_first(int pq, int jq) {
    const int pr = 2 * (pq & 7) + (pq >> 3);
    if (ROT0 == 6) return 0;
    if (ROT0 == 7) return (pq & 1) * 8;
    if (ROT0 == 8) return (pq & 3) * 4;
    if (ROT0 == 9) return pq;
    if (ROT0 == 4) return (pq + (jq ^ 3)) & 15;
    if (ROT0 == 5) return (pr + (jq ^ 3)) & 15;
    return (ROT0 == 0 ? pq + jq : (ROT0 == 1 ? pr + jq : (ROT0 == 2 ? pr + 4 * (jq & 3) + (jq >> 2) : pq + 4 * (jq & 3) + (jq >> 2)))) & 15;
}

struct Order { int base, r, re; };
template <int N> DI Order make_order(int base, int rot) { Order o; o.base = base; o.r = (rot - base) & 15; o.re = o.r > N ? o.r : N; return o; }
DI Order first_order(int pq, int jq) { return make_order<NC>((pq & NCM) * (16 / (NCM + 1)) + NCB, rot_first(pq, jq)); }
template <int N = NC> DI int first_tile(const Order& o, int s) {
    const int v = o.re + s - N;
    if (s < N) { int a = N + o.base, b = s + o.base; if (N - s == 1) asm volatile("" : "+s"(a)); return (o.r == s ? a : b) & 15; }
    return ((s == N ? o.r : (v > 15 ? v - (16 - N) : v)) + o.base) & 15;
}

typedef unsigned long long u64;
DI u64 lds_epoch(LAS char* smem) { return *(const LAS u64*)(smem + M_EP); }
DI bool reached(u64 v, u64 ep, unsigned target) { return (v - ep - target) < (u64)(64u - target); }
DI void poll_flags(const char* ws, u64 ep, unsigned off, bool active, unsigned target) {
    const GAS u64* f = (const GAS u64*)(ws + off);
#pragma unroll 1
    for (unsigned spins = 0; spins < (1u << 22); ++spins) {
        const u64 v = __hip_atomic_load(f, __ATOMIC_RELAXED, __HIP_MEMORY_SCOPE_AGENT);
        if (__all(!active || reached(v, ep, target))) break;
        __builtin_amdgcn_s_sleep(PSLEEP);
    }
    asm volatile("" ::: "memory");
}
struct Ask { u64 v[4]; };
DI void keep(Ask& t) { asm volatile("" : "+v"(t.v[0]), "+v"(t.v[1]), "+v"(t.v[2]), "+v"(t.v[3])); }
#define ASK(r) "global_atomic_or_x2 " r ", %[off], %[z], %[ws] sc0 sc1\n\t"
#define CHK(r) "s_waitcnt vmcnt(%[n])\n\tv_lshl_add_u64 %[d], " r ", 0, %[nb]\n\tv_cmp_gt_u64 vcc, %[span], %[d]\n\ts_cmp_eq_u64 vcc, exec\n\ts_cbranch_scc1 .Lpe%=\n\ts_sleep %[sl]\n\t"
template <bool NEG = false> DI void poll_rmw(Ask& t, const char* ws, unsigned off, bool act, u64 base, unsigned span) {
    u64 z, d, nb = NEG ? base : 0 - base, sp = span;
    unsigned cnt;
    asm volatile("" : "=v"(t.v[0]), "=v"(t.v[1]), "=v"(t.v[2]), "=v"(t.v[3]));
    if (act) {
        if (APIPE == 4)
            asm volatile("v_mov_b64 %[z], 0\n\ts_mov_b32 %[cnt], 0x80000\n\ts_nop 3\n\t" ASK("%[a]") "s_sleep %[sl]\n\t" ASK("%[b]") "s_sleep %[sl]\n\t" ASK("%[c]") "s_sleep %[sl]\n"
                         ".Lpl%=:\n\t" ASK("%[e]") CHK("%[a]") ASK("%[a]") CHK("%[b]") ASK("%[b]") CHK("%[c]") ASK("%[c]") CHK("%[e]")
                         "s_sub_u32 %[cnt], %[cnt], 1\n\ts_cbranch_scc0 .Lpl%=\n.Lpe%=:"
                         : [a] "+v"(t.v[0]), [b] "+v"(t.v[1]), [c] "+v"(t.v[2]), [e] "+v"(t.v[3]), [d] "=&v"(d), [z] "=&v"(z), [cnt] "=&s"(cnt)
                         : [off] "v"(off), [ws] "s"(ws), [nb] "v"(nb), [span] "s"(sp), [sl] "i"(ASLP), [n] "i"(3) : "vcc", "scc", "memory");
        else if (APIPE == 2)
            asm volatile("v_mov_b64 %[z], 0\n\ts_mov_b32 %[cnt], 0x100000\n\ts_nop 3\n\t" ASK("%[a]") "s_sleep %[sl]\n"
                         ".Lpl%=:\n\t" ASK("%[b]") CHK("%[a]") ASK("%[a]") CHK("%[b]")
                         "s_sub_u32 %[cnt], %[cnt], 1\n\ts_cbranch_scc0 .Lpl%=\n.Lpe%=:"
                         : [a] "+v"(t.v[0]), [b] "+v"(t.v[1]), [d] "=&v"(d), [z] "=&v"(z), [cnt] "=&s"(cnt)
                         : [off] "v"(off), [ws] "s"(ws), [nb] "v"(nb), [span] "s"(sp), [sl] "i"(ASLP), [n] "i"(1) : "vcc", "scc", "memory");
        else
            asm volatile("v_mov_b64 %[z], 0\n\ts_mov_b32 %[cnt], 0x200000\n\ts_nop 3\n"
                         ".Lpl%=:\n\t" ASK("%[a]") CHK("%[a]")
                         "s_sub_u32 %[cnt], %[cnt], 1\n\ts_cbranch_scc0 .Lpl%=\n.Lpe%=:"
                         : [a] "+v"(t.v[0]), [d] "=&v"(d), [z] "=&v"(z), [cnt] "=&s"(cnt)
                         : [off] "v"(off), [ws] "s"(ws), [nb] "v"(nb), [span] "s"(sp), [sl] "i"(ASLP), [n] "i"(0) : "vcc", "scc", "memory");
    }
}
DI void post(char* ws, unsigned off, u64 v) {
    if (APOST) asm volatile("s_nop 4\n\tglobal_atomic_swap_x2 %0, %1, %2 sc1" :: "v"(off), "v"(v), "s"(ws) : "memory");
    else __hip_atomic_store((GAS u64*)(ws + off), v, __ATOMIC_RELAXED, __HIP_MEMORY_SCOPE_AGENT);
}
struct Ctx {
    LAS char* smem;
    char* ws;
    int lane, wid;
    int pq, jq;
    int pw, jw;
    int bid;
    int cw;
};
DI Ctx fresh(const Ctx& c0) {
    Ctx c = c0;
    asm volatile("" : "+v"(c.lane));
    asm volatile("" : "+s"(c.wid), "+s"(c.pq), "+s"(c.jq), "+s"(c.pw), "+s"(c.jw), "+s"(c.ws), "+s"(c.cw));
    return c;
}
DI unsigned o_post(const Ctx& c) { return (MBOX & 2) ? (unsigned)WS_OBOX + (c.pq * 16 + (c.lane & 15)) * OBS + c.jq * 8 : (unsigned)WS_FLAGQ + (c.pq * 16 + c.jq) * FQS; }
DI unsigned o_find(const Ctx& c) { return (MBOX & 2) ? (unsigned)WS_OBOX + (c.pw * 16 + c.jw) * OBS + (c.lane & 15) * 8 : (unsigned)WS_FLAGQ + (c.pw * 16 + (c.lane & 15)) * FQS; }
DI unsigned x_post(const Ctx& c) { return (MBOX & 1) ? (unsigned)WS_XBOX + (c.pw * 16 + (c.lane & 31)) * (2 * FQS) + (c.lane & 16) * (FQS / 16) + c.jw * 8 : (unsigned)WS_FLAGW + (c.pw * 16 + c.jw) * 128; }
DI bool x_act(const Ctx& c, bool halo) { return c.lane < ((MBOX & 1) ? (halo && c.pw < 15 ? 32 : 16) : 1); }
#define STAMP_AT(slot) do { } while (0)

#define LSTAMP(i) do { } while (0)
#define LSTORE(ph) do { } while (0)
#define LMARK() do { } while (0)
#define LPUT(i) do { } while (0)
#define ASTORE() do { } while (0)
#define QSTAMP(i) do { } while (0)
#define QSTORE() do { } while (0)
#define FENCE() asm volatile("" ::: "memory")
#define ALIGN8() do { if (ALN) { asm volatile(".p2align 3"); SCHED(); } } while (0)
DI float addf(float a, float b) { float r; asm("v_add_f32 %0, %1, %2" : "=v"(r) : "v"(a), "v"(b)); return r; }
DI float fold16(float a, float b) {
    auto r = __builtin_amdgcn_permlane16_swap(__float_as_uint(a), __float_as_uint(b), false, false);
    float x = __uint_as_float(r[0]) + __uint_as_float(r[1]);
    asm volatile("" : "+v"(x));
    return x;
}
DI float fold32(float a, float b) {
    auto r = __builtin_amdgcn_permlane32_swap(__float_as_uint(a), __float_as_uint(b), false, false);
    float x = __uint_as_float(r[0]) + __uint_as_float(r[1]);
    asm volatile("" : "+v"(x));
    return x;
}
DI void aload4c(u32x4& d, unsigned voff, const char* sb) { asm volatile("s_nop 4\n\tglobal_load_dwordx4 %0, %1, %2 sc1" : "=v"(d) : "v"(voff), "s"(sb) : "memory"); }
DI void aload2p(u32x2& d, unsigned voff, const char* sb) { asm volatile("s_nop 4\n\tglobal_load_dwordx2 %0, %1, %2 sc1" : "+v"(d) : "v"(voff), "s"(sb) : "memory"); }
DI void aload2c(u32x2& d, unsigned voff, const char* sb) { asm volatile("s_nop 4\n\tglobal_load_dwordx2 %0, %1, %2 sc1" : "=v"(d) : "v"(voff), "s"(sb) : "memory"); }
DI unsigned wo_src(int q, int lane, int jw) { return (unsigned)((8 * q + (lane >> 3)) * (D_ * 2) + jw * 128 + (((lane & 7) ^ ((lane >> 3) & 6)) << 4)); }
DI unsigned pick(bool f, unsigned a, unsigned b) { asm volatile("" : "+v"(a), "+v"(b)); return f ? a : b; }
DI unsigned wo_w_off(const Ctx& c) { return pick(c.cw == 3, (unsigned)((c.wid & 3) * 1024 + c.lane * 16), wo_src(c.wid & 3, c.lane, c.jw)); }
DI unsigned wq_src(int ib, int lane, int jq) {
    const int seg = ib >> 3, ii = ib & 7, lr = lane >> 3;
    return (unsigned)((8 * ii + lr) * 6144 + seg * 2048 + jq * 128 + (((lane & 7) ^ ((lr & 2) | ((ii & 1) << 2))) << 4));
}
template <int S0, int S1> DI void wo_w_issue(const Ctx& c, const char* W, unsigned vw) {
    const int lw = c.wid & 3;
#pragma unroll
    for (int s = S0; s < S1; ++s) dma16<CPY != 0>(s * 4096 + lw * 1024, vw, W + (size_t)((2 * (s & 15) + (s >> 4)) * 32) * (D_ * 2));
}

template <bool HALO, bool FIRST> DI void qkv_load(const Ctx& c0, const char* xin, const char* W, const char* Wo, int l, KP kp = nullptr, unsigned va0 = 0, unsigned vw0 = 0) {
    const Ctx c = fresh(c0);
    asm volatile("" : "+s"(xin), "+s"(W), "+s"(Wo));
    LAS char* smem = c.smem;
    constexpr int NAI = HALO ? 5 : 4, NWI = 6, XS = FIRST ? XS_ : XSB, NR = HALO ? 144 : 128;
    constexpr Sched<NAI, NWI, FIRST> SC{};
    static_assert(NQA == 3 && NQW == 4, "schedule");
    const int lw = c.wid - 4, lane = c.lane, lr = lane >> 3, lc = lane & 7, fr = lane & 15, fq = lane >> 4;
    const int row0 = c.pq * 128;
    const bool cq_ = CPY && !FIRST && (c.jq & 7) == 3;
    if (cq_) W = c.ws + WS_CQ + ((HALO ? 0 : 2) + (c.jq >> 3)) * 24576;
    asm volatile("" : "+s"(W));
    constexpr int SP = FIRST ? NC : (HALO ? NCH : 0), T0 = PRT ? 2 : (OWN && HALO ? TL0B : TL0);
    constexpr bool OW = OWN && !FIRST && (HALO || OWA);
    const int rot = FIRST ? rot_first(c.pq, c.jq) : (l == 1 ? c.pq : SP);
    const bool diag = OW && rot == c.jq;
    Order od = FIRST ? first_order(c.pq, c.jq) : make_order<SP>(0, rot);
    if (FIRST) asm volatile("" : "+s"(od.base));
    if (FIRST || (HALO && !OW)) asm volatile("" : "+s"(od.r), "+s"(od.re));
    u64 tab = 0;
    if (OW) {
        static_assert(!OW || ((!HALO || SP == 2) && HYB == 1 && MB1 == 0 && MB3 == 0 && MW == 0x808), "order");
        auto drop = [](u64 x, int p) { const u64 m = (1ull << (4 * p)) - 1; return (x & m) | ((x >> 4) & ~m); };
        const int g0 = PRT && l > 1 ? c.jq & (HALO ? 8 : 12) : 0;
        tab = drop((0xfedcba9876543210ull >> (4 * g0)) | (0xfedcba9876543210ull << 1 << (63 - 4 * g0)), c.jq - g0);
        if (l == 1 && !diag) {
            const int cc = __builtin_ctz(~((1u << c.jq) | (1u << rot))), k = 4 * (rot - (rot > c.jq ? 1 : 0) - (rot > cc ? 1 : 0));
            tab = drop(tab, cc - (cc > c.jq ? 1 : 0));
            tab = (((tab >> k) | (tab << (56 - k))) << 4) | (u64)cc;
        }
        tab = (tab << 4) | (u64)c.jq;
        asm volatile("" : "+s"(tab));
    }
    auto tile = [&](int s) { return OW ? (int)((tab >> (4 * s)) & 15) : (FIRST || HALO ? first_tile<SP>(od, s) : s); };
    unsigned va[5], vw[NWI];
#pragma unroll
    for (int j = 0; j < 4; ++j) va[j] = (unsigned)(FIRST ? va0 + j * 32 * (D_ * 2) : c.pq * 262144 + (j * 4 + lw) * 1024 + lane * 16);
    if (HALO) va[4] = (unsigned)((c.pq > 0 ? (c.pq - 1) * 262144 + 14336 : 0) + lw * 512 + lane * 16);
#pragma unroll
    for (int j = 0; j < NWI; ++j) vw[j] = FIRST ? vw0 + (j & 1) * (32 * 6144) + (j >> 1) * 2048 : pick(cq_, (unsigned)((j * 4 + lw) * 1024 + lane * 16), wq_src(j * 4 + lw, lane, c.jq));
    auto issueA = [&](int s, int slot) {
        const char* b = xin + (unsigned)(tile(s) * (FIRST ? 128 : 16384));
#pragma unroll
        for (int j = 0; j < 4; ++j) dma16<true>(QA_OFF + slot * QA_SLOT + (j * 4 + lw) * 1024, va[j], b);
        if (HALO) { if (lane < 32) dma16<true>(QA_OFF + slot * QA_SLOT + 16384 + lw * 512, va[4], b); }
    };
    auto issueW = [&](int s, int slot, int j0 = 0, int j1 = NWI) {
        const char* b = W + (unsigned)(tile(s) * (64 * 6144));
#pragma unroll
        for (int j = j0; j < j1; ++j) dma16<CPY && !FIRST>(qw_slot(slot) + (j * 4 + lw) * 1024, vw[j], b);
    };
    auto ahead = [&]() {
        const int r3 = (lane * 43) >> 7;
        const unsigned o = (unsigned)((((rot - (c.pq & 7) + 10) * 64 + (c.pq & 7) * 48 + lw * 12 + r3) & 1023) * 6144 + (lane - 3 * r3) * 2048 + c.jq * 128);
        if (lane < 36 && (WTC != 2 || (c.jq & 7) == 3)) asm volatile("s_mov_b32 m0, %0\n\ts_nop 3\n\tglobal_load_lds_dword %1, %2" ::"s"(M_TCH), "v"(o), "s"(W) : "memory");
    };
    if (ST0 && FIRST && lw == 1) STAMP_AT(47);
    if (FIRST && ERL) {
        SCHED();
        issueA(1, 1); issueW(1, 1);
        SCHED();
    }
    const bool docs = FIRST || (HALO && l == 1);
    const int t = lw * 64 + lane;
    static_assert(MBOX & 1, "residual flag");
    unsigned foff, ftg, so, ho, fo_, co, l3, l4, vwo, wt[3], cso, xt, xt1, xso, tp, tl;
    u32x4 on;
    auto addr1 = [&]() {
        int lane = c.lane;
        asm volatile("" : "+v"(lane));
        const int fr = lane & 15, fq = lane >> 4;
        const int ph = c.pq > 0 ? c.pq - 1 : 0;
        foff = pick(lane != 32, (unsigned)WS_FLAGQ + pick(lane < 16, c.pq * 16 + lane, pick(lane < 32, fr * 16 + c.jq, ph * 16 + 14 + (lane & 1))) * FQS, (unsigned)WS_XBOX + (c.pw * 16 + c.jw) * (2 * FQS) + c.jw * 8);
        ftg = lane != 32 ? (unsigned)(4 * l + 1) + (PRT && HALO && (lane >> 4) != 1 ? 1u : 0u) : (unsigned)(l + 1);
        so = FIRST ? (unsigned)((c.pq * 16 + fq * 4) * 1024 + (lw * 32 + fr * 2) * 8) : (unsigned)((c.pq * 16 + (lane >> 2)) * 1024 + (c.jq * 8 + lw * 2 + (lane & 1)) * 8);
        ho = FDM ? (unsigned)(262144 + ((lw * 36 + lane < 16 ? ph * 128 + 128 : c.pq * 128) + lw * 36 + lane - 16) * 8)
                 : (unsigned)(262144 + pick(lane < 32, c.pq * 128 + lw * 32 + lane, ph * 128 + 112 + lw * 4 + (lane & 3)) * 8);
        fo_ = pick(lane < 2, (unsigned)(262144 + (c.pq * 128 + c.jq * 8 + lw * 2 + lane) * 8), 0x80000000u);
        const int cq = lw * 12 + (fr < 12 ? fr : 11);
        co = (unsigned)((fq * 4 * 3072 + (cq >> 4) * 1024 + c.jq * 64 + (cq & 15) * 4) * 4);
        l3 = (unsigned)(M_CS + (HALO ? 768 : 0) + (cq * 4 + fq) * 4);
        l4 = (unsigned)(M_MEAN + pick(lane < 32, 16 + lw * 32 + (FIRST ? fr * 2 + fq : lane), lw * 4 + (lane & 3)) * 4);
        asm volatile("" : "+v"(foff), "+v"(ftg), "+v"(so), "+v"(co), "+v"(l3), "+v"(l4));
        if (!FIRST) asm volatile("" : "+v"(ho), "+v"(fo_));
        SCHED();
    };
    auto addr2 = [&]() {
        int lane = c.lane;
        asm volatile("" : "+v"(lane));
        const int fr = lane & 15, fq = lane >> 4;
        const int q4 = fr >> 2, p4 = lane & 3, fb = (q4 & 2) | ((fq & 1) << 2);
#pragma unroll
        for (int i = 0; i < 3; ++i) {
            const int T = 3 * lw + i;
            wt[i] = (unsigned)((OW && diag ? 0 : qw_slot(SP % NQW)) + (T >> 2) * 8192 + (8 * fq + q4) * 128 + (((2 * (T & 3) + (p4 >> 1)) ^ fb) << 4) + (p4 & 1) * 8);
        }
        const int T = 3 * lw + fq;
        cso = (unsigned)((((HALO ? 16 : 0) + rot) * 3072 + (T >> 2) * 1024 + c.jq * 64 + (T & 3) * 16 + fr) * 4);
        xt = (unsigned)(QA_OFF + (SP % NQA) * QA_SLOT + (lw * 32 + fr) * 128 + ((fq ^ (fr & 7)) << 4));
        xt1 = xt ^ 64;
        xso = (unsigned)hl<HS>((c.pq * 16 + rot) * 1024 + (lw * 32 + fr) * 8);
        on = (u32x4){0x3f803f80u, 0x3f803f80u, 0x3f803f80u, 0x3f803f80u};
        if (FIRST || HALO) asm volatile("" : "+v"(wt[0]), "+v"(wt[1]), "+v"(wt[2]), "+v"(cso), "+v"(on));
        if (FIRST) asm volatile("" : "+v"(xt), "+v"(xt1), "+v"(xso));
        SCHED();
    };
    auto addr3 = [&]() {
        Ctx c2 = c;
        asm volatile("" : "+v"(c2.lane));
        asm volatile("" : "+s"(c2.bid));
        vwo = wo_w_off(c2);
        tp = (unsigned)(pick(c2.lane < 30, (c2.bid >> 3) * 30 + c2.lane, (c2.bid >> 3) * 16 + c2.lane - 30 + (int)(WS_O >> 16)) * 65536 + 65536 - 128);
        tl = (unsigned)(lw == 1 ? ((c2.bid >> 3) * 2 + c2.lane) * 65536 : c2.bid * 8192 + (TWO && !(EXS && (c.jq & 7) == 3) ? c2.lane * 128 : 0));
        asm volatile("" : "+v"(vwo));
        if (FIRST) asm volatile("" : "+v"(tp), "+v"(tl));
        SCHED();
    };
    if (!FIRST || AD1 < 0) addr1();
    if (FIRST && ERL >= 5) { issueA(2, 2); SCHED(); }
    if (FIRST && (ERL == 2 || ERL == 5)) { issueW(2, 2); SCHED(); }
    if (!FIRST || AD2 < 0) addr2();
    if (!FIRST || AD3 < 0) addr3();
#pragma unroll
    for (int j = 0; j < NAI; ++j) asm volatile("" : "+v"(va[j]));
#pragma unroll
    for (int j = 0; j < NWI; ++j) asm volatile("" : "+v"(vw[j]));
    SCHED();
    int tv = 0; unsigned long long lt = 0;
    u64 fv = 0;
    unsigned xfo;
    if (FIRST) {
        LSTAMP(55);
    } else {
        static_assert(CPY == 1 && RS0 > 0 && RN0 == NWI && WPRE >= 3 && (FIRST || SC.w[0] == NAI), "order");
        unsigned vx[2], go = M_GO;
        xfo = (unsigned)WS_XBOX + (c.pq * 16 + c.jq) * (2 * FQS) + (HALO && c.pq > 0 ? (lane & 16) * (FQS / 16) : 0) + fr * 8;
        u64 xv = lds_epoch(smem) + (unsigned)(l + 1);
#pragma unroll
        for (int i = 0; i < 2; ++i) vx[i] = pick(cq_, (unsigned)(((2 * lw + i) * 4 + 3) * 1024 + lane * 16), wq_src((2 * lw + i) * 4 + 3, lane, c.jq));
        asm volatile("" : "+v"(vx[0]), "+v"(vx[1]), "+v"(xfo), "+v"(xv), "+v"(go));
        fv = (((u64)(unsigned)__builtin_amdgcn_readfirstlane((unsigned)(xv >> 32)) << 32) | (unsigned)__builtin_amdgcn_readfirstlane((unsigned)xv)) + (unsigned)(3 * l);
        asm volatile("" : "+s"(fv));
        BAR();
        issueW(0, 0);
        if (OW) asm volatile("s_mov_b64 exec, 1\n\ts_waitcnt vmcnt(0)\n\tds_add_u32 %0, %1 offset:4\n\ts_mov_b64 exec, -1" :: "v"(go), "v"(1u) : "memory");
        auto word = [&](int off) { return __builtin_amdgcn_readfirstlane(*(volatile LAS int*)(lds(go) + off)); };
        auto until = [&](int off, int v) {
#pragma unroll 1
            for (unsigned spins = 0; spins < (1u << 24); ++spins) { if (word(off) == v) break; }
            FENCE();
        };
        static_assert(WPRE == 3, "stages");
        constexpr int WP = OW && CUP == 3 ? 4 : 3;
        int t1 = tile(1), t2 = tile(2);
        if (!OW) asm volatile("" : "+s"(t1), "+s"(t2));
        auto stage = [&](int s) {
            const char* b = W + (unsigned)((OW ? tile(s) : (s == 1 ? t1 : t2)) * (64 * 6144));
            unsigned d = (unsigned)((s == 3 ? QW_TOP : s * QW_SLOT) + lw * 1024), dx = d + (unsigned)(lw * 7168 + 3072);
            asm volatile("" : "+s"(d), "+s"(dx), "+s"(b));
#pragma unroll
            for (int j = 0; j < NWI; ++j) dma16<true>(d + j * 4096, vw[j], b);
#pragma unroll
            for (int i = 0; i < 2; ++i) dma16<true>(dx + i * 4096, vx[i], b);
        };
        auto first2 = [&]() {
            __builtin_amdgcn_s_setprio(LPRIO);
            if (OW) { issueA(1, 1); if (HALO && lane < 32) dma16<true>(M_H0 + lw * 512, va[4], xin + (unsigned)(c.jq * 16384)); }
            else { issueA(0, 0); issueA(1, 1); }
        };
        if (OW && docs && diag) {
            until(M_W0 - M_GO, (OWA ? 4 * l : 2 * l + 2));
            bf16x8 wf[3][2];
            f32x4 d[3];
#pragma unroll
            for (int i = 0; i < 3; ++i)
#pragma unroll
                for (int ks = 0; ks < 2; ++ks) {
                    const s16x4 lo = tr_read(lds(wt[i]) + ks * 4096), hi = tr_read(lds(wt[i]) + ks * 4096 + 512);
                    wf[i][ks] = __builtin_shufflevector(lo, hi, 0, 1, 2, 3, 4, 5, 6, 7);
                }
#pragma unroll
            for (int i = 0; i < 3; ++i) d[i] = MFMA(__builtin_bit_cast(bf16x8, on), wf[i][1], MFMA(__builtin_bit_cast(bf16x8, on), wf[i][0], ((f32x4){0.f, 0.f, 0.f, 0.f})));
            float d0 = d[0][0], d1 = d[1][0], d2 = d[2][0];
            asm volatile("" : "+v"(d0), "+v"(d1), "+v"(d2));
            const float v = fq == 0 ? d0 : (fq == 1 ? d1 : d2);
            auto rc = __builtin_amdgcn_make_buffer_rsrc(c.ws + WS_CSUM, 0, 2 * 16 * 3072 * 4, 0x00020000);
            if (lane < 48) __builtin_amdgcn_raw_buffer_store_b32(__float_as_uint(v), rc, cso, 0, 16);
        }
        if (lw == 3) {
            if (!EPOLL) until(M_CNT - M_GO, 8 * l);
            Ask t;
            poll_rmw(t, c.ws, xfo, lane < (HALO && c.pq > 0 ? 32 : 16) && (!PRT || l == 1 || ((lane ^ c.jq) & (HALO ? 8 : 12)) == 0), xv, 63 - l);
            *(volatile LAS int*)lds(go) = l;
            until(M_CNT - M_GO, 8 * l);
            first2();
            STAMP_AT(32 + l * 8 + 0);
            wait_v<OW && HALO ? NAI + 1 : NAI>();
            keep(t);
        } else {
            until(M_CNT - M_GO, 8 * l);
            if (RSLP) __builtin_amdgcn_s_sleep(RSLP);
            int ns = 1;
#pragma unroll 1
            for (; ns < WP; ++ns) {
                if (word(0) == l) break;
                stage(ns);
            }
            until(0, l);
            first2();
            const int left = WPRE - ns;
#pragma unroll 1
            for (; ns < WP; ++ns) stage(ns);
            if (OW) { }
            else if (left == 0) wait_v<NAI>();
            else if (left == 1) wait_v<NAI + 8>();
            else if (left == 2) wait_v<NAI + 16>();
            else wait_v<NAI + 24>();
        }
        FENCE();
    }
    if (FIRST && ERL == 3) issueW(2, 2);
    if (ST0 && FIRST && lw == 2) STAMP_AT(57);
    if (FIRST && !ERL) BAR();
    __builtin_amdgcn_s_setprio(LPRIO);
    if (FIRST && !ERL) { LSTAMP(57); issueA(1, 1); issueW(1, 1); LSTAMP(58); }
    u32x4 st[4], cs[4]; u32x2 sh, fl; float rs_, rq_, hs;
    asm volatile("" : "=v"(fl));
    f32x4 d[3], sm[2], g[2];
    bf16x8 wf[3][2], xf[2][2];
    const bool fact = FIRST ? lane < 32 : ((lane < 16 || lane >= (docs ? 16 : 32)) && lane <= (HALO ? 34 : 32));
    static_assert(XS + 3 <= (FIRST ? 12 : 13) && SPR == 1, "aux schedule");
    auto side = [&](auto ktc) {
        constexpr int kt = decltype(ktc)::value;
        {
            constexpr int C0 = FIRST ? 12 : XS + 2, C1 = FIRST ? 13 : XS + 3, C2 = 13;
            auto acc4 = [](u32x4& a, const u32x4& b) {
#pragma unroll
                for (int i = 0; i < 4; ++i) a[i] = __float_as_uint(addf(__uint_as_float(a[i]), __uint_as_float(b[i])));
                asm volatile("" : "+v"(a));
            };
            auto fin = [&]() {
                rs_ *= -1.f / 1024.f;
                asm volatile("" : "+v"(rs_));
                rq_ = __builtin_amdgcn_rsqf(fmaxf(rq_ * (1.f / 1024.f) - rs_ * rs_, 0.f) + LN_EPS);
                asm volatile("" : "+v"(rq_));
            };
            if constexpr (FIRST && kt == XS + 2) {
                asm volatile("" : "+v"(st[0]), "+v"(st[1]), "+v"(st[2]));
                acc4(st[0], st[1]); acc4(st[0], st[2]);
            }
            if constexpr (FIRST && kt == XS + 3) { asm volatile("" : "+v"(st[3])); acc4(st[0], st[3]); }
            if constexpr (kt == C0) {
                if (docs) { asm volatile("" : "+v"(cs[0]), "+v"(cs[1])); acc4(cs[0], cs[1]); }
            }
            if constexpr (FIRST && kt == 12) {
                asm volatile("s_nop 1" : "+v"(st[0]));
                rs_ = fold16(__uint_as_float(st[0][0]), __uint_as_float(st[0][2])); rq_ = fold16(__uint_as_float(st[0][1]), __uint_as_float(st[0][3]));
                rs_ = fold32(rs_, rs_); rq_ = fold32(rq_, rq_);
            }
            if constexpr (FIRST && kt == 13) fin();
            if constexpr (!FIRST && kt == T0 + 2) {
                asm volatile("" : "+v"(sh));
                hs = fold16(__uint_as_float(sh[0]), __uint_as_float(sh[1]));
                hs = fold32(hs, hs);
                hs += __uint_as_float(__builtin_amdgcn_update_dpp(0u, __float_as_uint(hs), 0x124, 0xf, 0xf, false));
                hs += __uint_as_float(__builtin_amdgcn_update_dpp(0u, __float_as_uint(hs), 0x128, 0xf, 0xf, false));
                asm volatile("" : "+v"(hs));
            }
            if constexpr (!FIRST && kt == T0 + 2 + TSP) {
                auto hh = __builtin_amdgcn_permlane16_swap(__float_as_uint(hs), __float_as_uint(hs), false, false);
                rs_ = __uint_as_float(hh[0]); rq_ = __uint_as_float(hh[1]);
                asm volatile("" : "+v"(rs_), "+v"(rq_));
                fin();
                auto rf = __builtin_amdgcn_make_buffer_rsrc(c.ws + WS_STATS + (size_t)l * 524288, 0, 524288, 0x00020000);
                __builtin_amdgcn_raw_buffer_store_b64((u32x2){__float_as_uint(rs_), __float_as_uint(rq_)}, rf, fo_, 0, 16);
            }
            if constexpr (kt == C1) {
                if (docs) { asm volatile("" : "+v"(cs[2]), "+v"(cs[3])); acc4(cs[2], cs[3]); acc4(cs[0], cs[2]); }
            }
            if constexpr (kt == C2) {
                if (docs) {
                    asm volatile("s_nop 1" : "+v"(cs[0]));
                    hs = fold32(fold16(__uint_as_float(cs[0][0]), __uint_as_float(cs[0][1])), fold16(__uint_as_float(cs[0][2]), __uint_as_float(cs[0][3])));
                }
            }
            if constexpr (kt == 13) {
                static_assert(WV0 == 1, "wait");
                if (FIRST || !FDM || docs) WAIT_V0();
                if (!FIRST && !FDM) { asm volatile("" : "+v"(sh)); rs_ = __uint_as_float(sh[0]); rq_ = __uint_as_float(sh[1]); }
                if ((FIRST || !FDM) && lane < (HALO ? 36 : 32)) {
                    *(LAS float*)lds(l4) = rs_;
                    *(LAS float*)(lds(l4) + (M_RSTD - M_MEAN)) = rq_;
                }
                if (docs) *(LAS float*)lds(l3) = hs;
                FENCE();
            }
        }
    };
    static_for<0, KT>([&](auto ktc) {
        constexpr int kt = decltype(ktc)::value;
        LSTAMP(3 * kt);
        if constexpr (PRO && !FIRST && kt == 1) { if (lw == 1) STAMP_AT(l == 1 ? 44 : (l == 2 ? 57 : 58)); }
        if constexpr (FIRST || kt > 0) wait_v<SC.w[kt] + (!FIRST && kt == T0 + 1)>();
        LSTAMP(3 * kt + 1);
        constexpr bool P2 = PRT && !FIRST;
        if constexpr (kt == XS || (P2 && kt == 12)) {
            if (lw == 0 && (!P2 || kt == 12 || docs)) {
                asm volatile("" : "+v"(fl));
                const u64 ep = fv - (unsigned)(4 * l + 1);
                const bool fa = fact && (!P2 || (kt == 12) == ((lane >> 4) != 1));
                if (!__all(!fa || reached(__builtin_bit_cast(u64, fl), ep, ftg))) poll_flags(c.ws, ep, foff, fa, ftg);
            }
        }
        if constexpr (P2 && kt == 2) {
            static_assert(!P2 || (SC.w[2] <= NAI + ow_w(2) && TL1 == 12 && XS == 10), "covered");
            if (lw == 3) {
                asm volatile("" : "+v"(fl));
                const u64 ep = fv - (unsigned)(4 * l + 1);
                if (!__all(reached(__builtin_bit_cast(u64, fl), ep, (unsigned)(l + 1)))) poll_flags(c.ws, ep, xfo, true, (unsigned)(l + 1));
            }
        }
        if constexpr (kt == 14) asm volatile("s_waitcnt lgkmcnt(0)" ::: "memory");
        if constexpr (!(OW && kt == 0)) BAR();
        FENCE();
        LMARK();
        LSTAMP(3 * kt + 2);
        if constexpr (TOPJ & (1 << kt)) side(ktc);
        if constexpr (kt == (FIRST ? SP + DS : T0 + 2 + TSP) + 3 || (P2 && HALO && kt == SP + DS + 3)) {
            constexpr bool st2 = P2 && HALO && kt == T0 + 2 + TSP + 3;
            if (lw == 0 && lane == 0 && (!P2 || !HALO || st2 || docs))
                __hip_atomic_store((GAS u64*)(c.ws + WS_FLAGQ + (c.pq * 16 + c.jq) * FQS), fv + (st2 ? 1u : 0u), __ATOMIC_RELAXED, __HIP_MEMORY_SCOPE_AGENT);
        }
        if constexpr (kt == XS + 1 - NQA || (P2 && kt == 10)) {
            if (lw == 0 && (!P2 || kt == 10 || docs)) { if (fact) aload2p(fl, foff, c.ws); }
        }
        if constexpr (P2 && kt == 1) { if (lw == 3) aload2p(fl, xfo, c.ws); }
        {
            const char* sb = c.ws + WS_STATS + (size_t)l * 524288;
            const char* cb = c.ws + WS_CSUM + (HALO ? 16 * 3072 * 4 : 0);
            constexpr int sa = 3 * (kt - XS), ca = FIRST ? 2 * (kt - XS - 1) : 2 * (kt - XS), cn = 2;
            if constexpr (FIRST) {
#pragma unroll
                for (int i = sa; i < sa + 3; ++i) if (i >= 0 && i < 4) aload4c(st[i], so, sb + i * 1024);
            }
            if constexpr (!FIRST && kt == T0) aload2c(sh, so, sb);
            if constexpr (!FIRST && kt == TL1) {
                static_assert(TL1 <= 12, "covered by the wait of step 14");
                if (FDM) { if (lane < 36) { touch(M_MEAN + lw * 144, ho, sb); touch(M_RSTD + lw * 144, ho, sb + 4); } }
                else aload2c(sh, ho, sb);
            }
            if constexpr (ca >= 0 && ca < 4) {
                if (docs) {
#pragma unroll
                    for (int i = ca; i < ca + cn; ++i) aload4c(cs[i], co, cb + i * 12288);
                }
            }
        }
        if constexpr (kt == SP) {
            if (docs && !diag) {
#pragma unroll
                for (int i = 0; i < 3; ++i)
#pragma unroll
                    for (int ks = 0; ks < 2; ++ks) {
                        const s16x4 lo = tr_read(lds(wt[i]) + ks * 4096), hi = tr_read(lds(wt[i]) + ks * 4096 + 512);
                        wf[i][ks] = __builtin_shufflevector(lo, hi, 0, 1, 2, 3, 4, 5, 6, 7);
                    }
                if (FIRST) {
#pragma unroll
                    for (int mt = 0; mt < 2; ++mt)
#pragma unroll
                        for (int ks = 0; ks < 2; ++ks) xf[mt][ks] = *(const LAS bf16x8*)(lds(ks ? xt1 : xt) + mt * 2048);
                }
            }
            SCHED();
        }
        if constexpr (kt + 2 < KT && !(FIRST && ERL >= 5 && kt == 0)) issueA(kt + 2, (kt + 2) % NQA);
        LPUT(3 * kt + 2);
        if constexpr (kt == SP + DM) {
            SCHED();
            if (docs && !diag) {
                const bf16x8 ones = __builtin_bit_cast(bf16x8, on);
                const f32x4 zero = {0.f, 0.f, 0.f, 0.f};
#pragma unroll
                for (int ks = 0; ks < 2; ++ks) {
#pragma unroll
                    for (int i = 0; i < 3; ++i) d[i] = MFMA(ones, wf[i][ks], ks ? d[i] : zero);
                    if (FIRST) {
#pragma unroll
                        for (int mt = 0; mt < 2; ++mt) { sm[mt] = MFMA(ones, xf[mt][ks], ks ? sm[mt] : zero); g[mt] = MFMA(xf[mt][ks], xf[mt][ks], ks ? g[mt] : zero); }
                    }
                }
            }
            SCHED();
        }
        if constexpr (OW && kt < 5) {
            static_assert(ow_w(kt + 1) <= 6 * (kt + 1) && ow_w(5) == 30, "free slots");
#pragma unroll
            for (int s = 3; s < 8; ++s) {
                const int j0 = ow_w(kt) - 6 * (s - 3), j1 = ow_w(kt + 1) - 6 * (s - 3);
                if (j1 > 0 && j0 < 6) issueW(s, s % NQW, j0 < 0 ? 0 : j0, j1 > 6 ? 6 : j1);
            }
        } else if constexpr (FIRST && ERL < 2 && kt < 5) {
            if constexpr (kt < 4) issueW(kt + 2, (kt + 2) % NQW);
            if constexpr (kt == 3) issueW(6, 2, 0, NWI / 2);
            if constexpr (kt == 4) { issueW(6, 2, NWI / 2, NWI); issueW(7, 3); }
        } else {
            if constexpr (WPRE == 2 && kt < 2) issueW(kt + 2, kt + 2);
            if constexpr (FIRST && (ERL == 4 || ERL == 6) && kt == 0) issueW(2, 2);
            if constexpr ((FIRST || WPRE == 3) && kt == 0) issueW(3, 3);
            if constexpr (kt >= 1 && kt + 3 < KT) issueW(kt + 3, (kt + 3) % NQW);
        }
        if constexpr (FIRST && kt == WT0) ahead();
        if constexpr (FIRST && kt == TSTEP) {
            if (lw == 1 && lane < 46) touch(M_TCH, tp, c.ws);
        }
        if constexpr (FIRST && kt == TW0) {
            if (lw == 0) touch(M_TCH, tl, Wo);
            else if (lw == 1) { asm volatile("" : "+s"(kp)); const char* outp = (const char*)kp->out; if (lane < 2) touch(M_TCH, tl, outp); }
        }
        if constexpr (kt == SP && DM > 0) {
            SCHED();
            if (docs) {
                asm volatile("" : "+v"(wf[0][0]), "+v"(wf[0][1]), "+v"(wf[1][0]), "+v"(wf[1][1]), "+v"(wf[2][0]), "+v"(wf[2][1]));
                if (FIRST) asm volatile("" : "+v"(xf[0][0]), "+v"(xf[0][1]), "+v"(xf[1][0]), "+v"(xf[1][1]));
            }
        }
        if constexpr (kt == SP + DS) {
            SCHED();
            if (docs && !diag) {
                if (FIRST) {
                    auto rst = __builtin_amdgcn_make_buffer_rsrc(c.ws + WS_STATS, 0, 524288, 0x00020000);
                    const bool b0 = fr & 1, b1 = fr & 2;
                    u32x2 sv[2];
#pragma unroll
                    for (int mt = 0; mt < 2; ++mt) {
                        float qa = b0 ? g[mt][1] : g[mt][0], qb = b0 ? g[mt][3] : g[mt][2];
                        asm volatile("" : "+v"(qa), "+v"(qb));
                        sv[mt] = (u32x2){__float_as_uint(sm[mt][0]), __float_as_uint(b1 ? qb : qa)};
                    }
                    if (fq == (fr >> 2)) {
                        __builtin_amdgcn_raw_buffer_store_b64(sv[0], rst, xso, 0, 16);
                        __builtin_amdgcn_raw_buffer_store_b64(sv[1], rst, xso + hl<HS>(128), 0, 16);
                    }
                }
                float d0 = d[0][0], d1 = d[1][0], d2 = d[2][0];
                asm volatile("" : "+v"(d0), "+v"(d1), "+v"(d2));
                const float v = fq == 0 ? d0 : (fq == 1 ? d1 : d2);
                auto rc = __builtin_amdgcn_make_buffer_rsrc(c.ws + WS_CSUM, 0, 2 * 16 * 3072 * 4, 0x00020000);
                if (lane < 48) __builtin_amdgcn_raw_buffer_store_b32(__float_as_uint(v), rc, cso, 0, 16);
                asm volatile("s_waitcnt lgkmcnt(0)" ::: "memory");
            }
        }
        if constexpr (FIRST && kt == 1) {
            if (lw == 0) {
                const u32x2 e = *(const LAS u32x2*)(smem + M_EP);
                const u64 es = ((u64)(unsigned)__builtin_amdgcn_readfirstlane(e[1]) << 32) | (unsigned)__builtin_amdgcn_readfirstlane(e[0]);
                fv = es + (unsigned)(4 * l + 1);
                asm volatile("" : "+s"(fv));
            }
        }
        if constexpr (FIRST && kt == AD1) addr1();
        if constexpr (FIRST && kt == AD2) addr2();
        if constexpr (FIRST && kt == AD3) addr3();
        if constexpr (!(TOPJ & (1 << kt))) side(ktc);
        if constexpr (kt == 13) wo_w_issue<0, WT13>(c, Wo, vwo);
        if constexpr (kt == 14) { wo_w_issue<WT13, (HALO || WA0 > 12) ? 12 : WA0>(c, Wo, vwo); if (HALO) wo_w_issue<23, 23 + WB14>(c, Wo, vwo); }
        if constexpr (kt == 15) {
            if (HALO ? WBE < 2 : WA0 > 12) wo_w_issue<12, HALO ? 18 : WA0>(c, Wo, vwo);
            if (HALO) wo_w_issue<23 + WB14, 23 + WB14 + WB15>(c, Wo, vwo);
        }
    });
    __builtin_amdgcn_s_setprio(0);
    BAR();
    LMARK(); LPUT(48);
    LSTAMP(48);
    LSTORE(0);
    if (HALO ? WBE < 1 : WAE < 1) wo_w_issue<18, HALO ? 23 : 20>(c, Wo, vwo);
    if (HALO) {
        static_assert(!WBS || (WBE == 1 && WB14 == 0 && WRG <= 12), "Wo weights of the first twelve steps are older than the last 6 + WB15");
        if (WBS) wait_v<6 + WB15>();
        BAR();
        if (WBE) wo_w_issue<WBE < 2 ? 18 : 12, 23>(c, Wo, vwo);
        wo_w_issue<23 + WB14 + WB15, 32>(c, Wo, vwo);
    }
}

DI float max3(float a, float b, float c) { return fmaxf(fmaxf(a, b), c); }
#define SGB(m, n) __builtin_amdgcn_sched_group_barrier(m, n, 0)
template <bool HALO> struct Tail {
    int f[26], n[4], t[4][26], d[4][27], v[4][27], h3[32], s3[32], o3[33], q3[112];
    constexpr Tail() : f{}, n{}, t{}, d{}, v{}, h3{}, s3{}, o3{}, q3{} {
        const int FB[26] = {0, 1, 2, 3, 4, 5, 6, 7, 8, 9, 10, 11, 12, 13, 14, 15, 24, 25, 19, 23, 18, 22, 17, 21, 16, 20};
        const int FA[24] = {0, 1, 2, 3, 4, 5, 6, 7, 8, 9, 10, 11, 12, 13, 14, 15, 16, 17, 20, 21, 18, 19, 22, 23};
        const int TA[4][26] = {{0, 1, 2, 3, 7, 6, 5, 4, 8, 9, 10, 11, 15, 14, 13, 12, 16, 17, 18, 19, 23, 22, 21, 20, 0, 0}, {4, 8, 9, 10, 11, 15, 14, 13, 12, 16, 17, 18, 19, 23, 22, 21, 20, 0, 1, 2, 3, 7, 6, 5, 0, 0}, {16, 17, 18, 19, 23, 22, 21, 20, 0, 1, 2, 3, 7, 6, 5, 4, 8, 9, 10, 11, 15, 14, 13, 12, 0, 0}, {17, 18, 19, 23, 22, 21, 20, 8, 9, 10, 11, 15, 14, 13, 12, 16, 0, 0, 0, 0, 0, 0, 0, 0, 0, 0}};
        const int DA[4][26] = {{1, 1, 1, 1, 1, 1, 1, 1, 1, 1, 1, 1, 1, 1, 1, 1, 1, 1, 1, 1, 1, 1, 0, 0, 0, 0}, {1, 1, 1, 1, 1, 1, 1, 1, 1, 1, 1, 1, 1, 1, 1, 1, 1, 1, 1, 1, 1, 1, 0, 0, 0, 0}, {1, 1, 1, 1, 1, 1, 1, 1, 1, 1, 1, 1, 1, 1, 1, 1, 0, 0, 0, 0, 0, 0, 0, 0, 0, 0}, {2, 2, 2, 2, 1, 1, 1, 1, 1, 1, 1, 1, 0, 0, 0, 0, 0, 0, 0, 0, 0, 0, 0, 0, 0, 0}};
        const int VA[4][26] = {{1, 1, 0, 0, 0, 0, 1, 1, 1, 1, 1, 1, 1, 1, 1, 1, 1, 1, 1, 1, 1, 1, 2, 2, 0, 0}, {1, 1, 1, 1, 1, 1, 1, 1, 1, 1, 1, 1, 1, 1, 1, 1, 1, 1, 1, 1, 1, 1, 2, 2, 0, 0}, {1, 1, 1, 1, 1, 1, 1, 1, 1, 1, 1, 1, 1, 1, 1, 1, 2, 2, 2, 2, 2, 3, 3, 2, 0, 0}, {0, 0, 0, 0, 1, 1, 1, 1, 1, 1, 1, 1, 2, 2, 2, 2, 0, 0, 0, 0, 0, 0, 0, 0, 0, 0}};
        const int TB[4][26] = {{0, 1, 2, 3, 7, 6, 5, 4, 24, 8, 9, 10, 11, 15, 14, 13, 12, 25, 16, 17, 18, 19, 23, 22, 21, 20}, {24, 8, 9, 10, 11, 15, 14, 13, 12, 25, 16, 17, 18, 19, 23, 22, 21, 20, 0, 1, 2, 3, 7, 6, 5, 4}, {25, 16, 17, 18, 19, 23, 22, 21, 20, 0, 1, 2, 3, 7, 6, 5, 4, 24, 8, 9, 10, 11, 15, 14, 13, 12}, {0, 1, 2, 3, 7, 6, 5, 4, 24, 8, 9, 10, 11, 15, 14, 13, 12, 25, 19, 23, 0, 0, 0, 0, 0, 0}};
        const int DB[4][26] = {{1, 1, 1, 1, 1, 1, 1, 1, 1, 1, 1, 1, 1, 1, 1, 1, 1, 1, 1, 1, 1, 1, 1, 0, 0, 0}, {1, 1, 1, 1, 1, 1, 1, 1, 1, 1, 1, 1, 1, 1, 1, 1, 1, 1, 1, 1, 1, 1, 0, 0, 0, 0}, {1, 1, 1, 1, 1, 1, 1, 1, 1, 1, 1, 1, 1, 1, 1, 1, 1, 1, 1, 1, 1, 1, 1, 0, 0, 0}, {2, 2, 1, 1, 1, 1, 1, 1, 1, 1, 1, 1, 1, 1, 1, 1, 1, 1, 1, 0, 0, 0, 0, 0, 0, 0}};
        const int VB[4][26] = {{1, 1, 1, 1, 1, 1, 1, 1, 1, 1, 1, 1, 1, 1, 1, 1, 1, 1, 1, 1, 1, 1, 1, 2, 2, 2}, {1, 1, 1, 1, 1, 1, 1, 1, 1, 1, 1, 1, 1, 1, 1, 1, 1, 1, 1, 1, 1, 1, 3, 3, 3, 2}, {1, 1, 1, 1, 1, 1, 1, 1, 1, 1, 1, 1, 1, 1, 1, 1, 1, 1, 1, 1, 1, 1, 1, 3, 3, 2}, {0, 0, 1, 1, 1, 1, 1, 1, 1, 1, 1, 1, 1, 1, 1, 1, 1, 1, 1, 2, 0, 0, 0, 0, 0, 0}};
        const int G[32] = {8, 12, 9, 13, 10, 14, 11, 15, 16, 17, 20, 21, 18, 19, 22, 23, 0, 4, 1, 5, 2, 6, 3, 7, 0, 4, 1, 5, 2, 6, 3, 7};
        const int NM = HALO ? 26 : 24;
        n[0] = n[1] = n[2] = NM; n[3] = HALO ? 20 : 16;
        for (int i = 0; i < NM; ++i) f[i] = HALO ? FB[i] : FA[i];
        int vv = 0;
        for (int h = 0; h < 4; ++h) {
            v[h][0] = vv;
            for (int i = 0; i < n[h]; ++i) {
                t[h][i] = HALO ? TB[h][i] : TA[h][i];
                d[h][i + 1] = d[h][i] + (HALO ? DB[h][i] : DA[h][i]);
                vv += HALO ? VB[h][i] : VA[h][i];
                v[h][i + 1] = vv;
            }
        }
        for (int i = 0; i < 32; ++i) { h3[i] = G[i]; s3[i] = i >= 16 && i < 24 ? 0 : 1; }
        for (int i = 3; i < 32; ++i) o3[i + 1] = o3[i] + TOPS;
        int nv = 0, ns = 0;
        for (int k = 0; k < 8 * 14; ++k) {
            const bool top = k % TOPS < 2 || nv == 96;
            if (top && ns < 16 && nv >= 12 * (ns / 2 + 1) && (nv == 96 || k % TOPS == (ns & 1))) { q3[k] = (ns / 2) * 14 + 13 - (ns & 1); ++ns; }
            else { q3[k] = (nv / 12) * 14 + nv % 12; ++nv; }
        }
    }
};
template <bool HALO> DI void qkv_consume(const Ctx& c0, int l, KP kp, unsigned va0 = 0, unsigned vw0 = 0) {
    const Ctx c = fresh(c0);
    if (ST0 && l == 0 && c.wid == 2) STAMP_AT(59);
    LAS char* smem = c.smem;
    const int lane = c.lane, wr = c.wid >> 1, wc = c.wid & 1, fr = lane & 15, fq = lane >> 4;
    const int sw = HALO ? wr : 0;
    f32x4 acc[4][6], ah[2];
#pragma unroll
    for (int mt = 0; mt < 4; ++mt)
#pragma unroll
        for (int nt = 0; nt < 6; ++nt) { acc[mt][nt] = (f32x4){0.f, 0.f, 0.f, 0.f}; if (!(OWN && (HALO || OWA))) asm volatile("" : "+v"(acc[mt][nt])); }
    ah[0] = ah[1] = (f32x4){0.f, 0.f, 0.f, 0.f};
    if (HALO && !OWN) asm volatile("" : "+v"(ah[0]), "+v"(ah[1]));
    const unsigned aoff0 = (unsigned)(QA_OFF + wr * 8192 + (HALO || l ? lane * 16 : fr * 128 + ((fq ^ (fr & 7)) << 4)));
    const unsigned hoff0 = (unsigned)(QA_OFF + 16384 + lane * 16);
    unsigned xm = HALO || l ? 1024u : 64u;
    asm volatile("" : "+s"(xm));
    static_assert(((QA_OFF | QA_SLOT) & 2047) == 0, "halves");
    const int q4 = fr >> 2, p4 = lane & 3;
    const int fb = (q4 & 2) | ((fq & 1) << 2);
    unsigned boff[2];
#pragma unroll
    for (int cc = 0; cc < 2; ++cc)
        boff[cc] = (unsigned)((8 * fq + q4) * 128 + (((2 * (2 * wc + (cc ^ sw)) + (p4 >> 1)) ^ fb) << 4) + (p4 & 1) * 8);
    bf16x8 xa[2][4], xh[2], wb[2][6];
    auto ldfrag = [&](int set, unsigned pa, unsigned ph, unsigned pb0, unsigned pb1, int ks) {
#pragma unroll
        for (int mt = 0; mt < 4; ++mt) xa[set][mt] = *(const LAS bf16x8*)(lds(pa) + mt * 2048);
        if (HALO) xh[set] = *(const LAS bf16x8*)lds(ph);
#pragma unroll
        for (int nt = 0; nt < 6; ++nt) {
            const LAS char* pb = lds((nt & 1) ? pb1 : pb0) + (nt >> 1) * 8192 + ks * 4096;
            const s16x4 lo = tr_read(pb), hi = tr_read(pb + 512);
            wb[set][nt] = __builtin_shufflevector(lo, hi, 0, 1, 2, 3, 4, 5, 6, 7);
        }
    };
    constexpr int NM = HALO ? 26 : 24, NH = HALO ? 17 : 16;
    auto mm = [&](int set, int i0, int i1) {
#pragma unroll
        for (int ii = i0; ii < i1; ++ii) {
            const int i = !HALO || ii < 8 ? ii : (!SNK ? (ii < 10 ? ii + 16 : ii - 2) : (ii == 8 ? 24 : (ii < 17 ? ii - 1 : (ii == 17 ? 25 : ii - 2))));
            const int nt = i >> 2, mt = SNK && (nt & 1) ? 3 - (i & 3) : i & 3;
            if (i < 16) acc[mt][nt] = MFMA(wb[set][nt], xa[set][mt], acc[mt][nt]);
            else if (i < 24) acc[mt][nt] = MFMA(xa[set][mt], wb[set][nt], acc[mt][nt]);
            else if (i == 24) ah[0] = MFMA(wb[set][2], xh[set], ah[0]);
            else ah[1] = MFMA(xh[set], wb[set][4], ah[1]);
        }
    };
    auto interleave = [&]() {
#pragma unroll
        for (int i = 0; i < NH; ++i) { SGB(0x008, 1); SGB(0x100, 1); }
        SCHED();
    };
    unsigned sa = 0, sb = 0;
    unsigned pa0 = aoff0, pa1 = aoff0 ^ xm, ph0 = hoff0, ph1 = hoff0 ^ xm, pb0 = boff[0], pb1 = boff[1];
    asm volatile("" : "+v"(pa0), "+v"(pa1), "+v"(pb0), "+v"(pb1));
    if (HALO) asm volatile("" : "+v"(ph0), "+v"(ph1));
    constexpr int M_CSH = M_CS + (HALO ? 768 : 0);
    unsigned sa0 = (unsigned)(M_MEAN + 64 + (wr * 64 + fr) * 4), sa1 = (unsigned)(M_MEAN + 64 + wr * 256 + fq * 16);
    unsigned sa2 = (unsigned)(M_CSH + wc * 128 + sw * 64 + fq * 16), sa6 = (unsigned)(M_CSH + 512 + wc * 128 + sw * 64 + fr * 4);
    unsigned sa3 = sa2 ^ 64, sa7 = sa6 ^ 64, sa4 = (unsigned)(M_MEAN + fr * 4), sa5 = (unsigned)(M_MEAN + fq * 16);
    asm volatile("" : "+v"(sa0), "+v"(sa1), "+v"(sa2), "+v"(sa6), "+v"(sa3), "+v"(sa7));
    if (HALO) asm volatile("" : "+v"(sa4), "+v"(sa5));
    const int head = c.jq * 2 + wc;
    auto rso = __builtin_amdgcn_make_buffer_rsrc(HALO ? c.ws + WS_O + (size_t)l * (8u << 20) + (size_t)((head * 16 + c.pq) * 8 + wr * 4) * (1024 << HO)
                                                      : c.ws + WS_KV + (size_t)(l >> 1) * (16u << 20) + (size_t)((c.pq * 32 + head) * 4 + wr) * (4096 << HK), 0, HALO ? 4096 << HO : 12288 << HK, 0x00020000);
    unsigned xrs = (unsigned)(M_XCH + wc * 6144 + wr * 2048), xhs = (unsigned)(M_XCH + wc * 6144 + 8 - wr * 8);
    unsigned b0m = (c.pq == 0 && wr == 0) ? 15u * 16u : 0u;
    asm volatile("" : "+s"(rso));
    if (HALO) asm volatile("" : "+s"(xrs), "+s"(xhs), "+s"(b0m));
    u32x4 cpy[3];
    unsigned cv, vt, cdst;
    const char *cs_, *tw, *cd_;
    int more = !HALO && l == 0 ? KT - 3 - XST - (c.pq & 7) : 0, three = c.jq & 4;
    if (!HALO) {
        asm volatile("" : "=s"(cs_), "=s"(tw), "=s"(cd_));
        asm volatile("" : "=v"(cv), "=v"(cdst), "=v"(vt));
        asm volatile("" : "+s"(more), "+s"(three));
        asm volatile("" : "=v"(cpy[0]), "=v"(cpy[1]), "=v"(cpy[2]));
    }
    auto pre = [&]() {
        static_assert(CPY + TOUCH == 2 && XSP == 1 && OWN == 1 && OWA == 1 && HT0 == 0, "extra");
        asm volatile("" : "+s"(kp));
        const int g = c.jq >> 2, n = c.pq * 4 + (c.jq & 3), m = n >> 5, kt = (n >> 1) & 15;
        const char* src = *(const char* const __attribute__((address_space(4)))*)((const __attribute__((address_space(4))) char*)kp + (three ? (m ? 8 : 24) : (m ? 32 : 40)));
        const char* tsr = *(const char* const __attribute__((address_space(4)))*)((const __attribute__((address_space(4))) char*)kp + (c.wid < 3 ? 24 : 40));
        int o1 = kt * (64 * 6144) + (n & 1) * (32 * 6144) + (3 + 4 * (g & 2) - c.jq) * 128 + 2048, o2 = (n & 31) * 65536 + (11 - 4 * g) * 128 - c.pq * 128 * (D_ * 2) + 2048;
        int d1 = (int)WS_CQ + kt * (64 * 6144) + (m * 2 + (g >> 1)) * 24576 + (n & 1) * 4096, d2 = (int)WS_CO + (n & 31) * 65536 + (m * 2 + 1 - (g >> 1)) * 4096;
        asm volatile("" : "+s"(o1), "+s"(o2), "+s"(d1), "+s"(d2));
        cs_ = src + (three ? o1 : o2);
        tw = tsr + (unsigned)((c.wid < 3 ? c.wid : 0) * (2u << 20) + c.bid * 8192);
        cd_ = c.ws + (unsigned)(three ? d1 : d2);
        cv = pick(three, vw0, va0 ^ ((lane & 8) << 1));
        cdst = (unsigned)(c.wid * 1024 + lane * 16);
        vt = TWQ ? (unsigned)(((lane & 7) == 3 ? lane - 1 : lane) * 128) : 0u;
        if (EXS) vt &= (c.jq & 7) == 3 ? 0u : ~0u;
        asm volatile("" : "+s"(cs_), "+s"(tw), "+s"(cd_));
        asm volatile("" : "+v"(cv), "+v"(cdst), "+v"(vt));
    };
    unsigned hto = HT0 == 2 ? 0u : 0x80000000u;
    const char* W0 = (const char*)kp->wqkv0;
    if (!HALO && HT0) {
        const int n = ((c.pq & 7) * 3 + (c.jq & 3)) * 64 + c.wid * 16 + fr, r3 = (n * 43691) >> 17;
        const int t0 = rot_first(c.pq & 8, c.jq | 3) + 8;
        if (l == 0 && !(c.jq & 4) && (c.jq & 3) != 3 && lane < 16) hto = (unsigned)(((t0 * 64 + r3) & 1023) * 6144 + (n - 3 * r3) * 2048 + (c.jq | 3) * 128);
        asm volatile("" : "+v"(hto));
        asm volatile("" : "+s"(W0));
    }
    if (!HALO && HT0 == 2) { asm volatile("s_mov_b32 m0, %0\n\ts_nop 3\n\tglobal_load_lds_dword %1, %2" ::"s"(M_TCH), "v"(hto), "s"(W0) : "memory"); }
    if (!HALO && !ERL && l == 0) BAR();
    if (ST0 && l == 0 && c.wid == 1) STAMP_AT(44);
    if (ST0 && l == 0 && c.wid == 1) STAMP_AT(46);
    bf16x8 hk[2], hv_[2];
    if (OWN && (HALO || (OWA && __builtin_expect(l > 0, EXP0)))) {
        if (PRO && c.wid == 1) STAMP_AT(l == 1 ? 41 : (l == 2 ? 46 : 59));
        unsigned cw = M_CNT;
        asm volatile("" : "+v"(cw));
#pragma unroll 1
        for (unsigned spins = 0; spins < (1u << 24); ++spins) {
            const int a = *(volatile LAS int*)lds(cw), b = *(volatile LAS int*)(lds(cw) + (M_W0 - M_CNT));
            if (__builtin_amdgcn_readfirstlane(a) == 8 * l && __builtin_amdgcn_readfirstlane(b) == (OWA ? 4 * l : 2 * l + 2)) break;
        }
        FENCE();
        SCHED();
        ldfrag(0, pa0, ph0, pb0, pb1, 0);
        SCHED();
        ldfrag(1, pa1, ph1, pb0, pb1, 1);
        if (HALO) { mm(0, 0, 8); mm(0, 9, 17); } else mm(0, 0, 16);
#pragma unroll
        for (int i = 0; i < 16; ++i) { SGB(0x008, 1); SGB(0x100, 1); }
        SCHED();
        mm(0, HALO ? 18 : 16, NM);
        SCHED();
        if (HALO) { mm(1, 0, 8); mm(1, 9, 17); mm(1, 18, NM); } else mm(1, 0, NM);
#pragma unroll
        for (int set = 0; set < 2; ++set) { hk[set] = wb[set][2]; hv_[set] = wb[set][4]; }
        static_assert(SNK == 1, "places of the halo products");
        sa = QA_SLOT; sb = QW_SLOT;
        pa0 = aoff0 + QA_SLOT; pa1 = pa0 ^ xm; ph0 = hoff0 + QA_SLOT; ph1 = ph0 ^ xm; pb0 = boff[0] + QW_SLOT; pb1 = boff[1] + QW_SLOT;
        asm volatile("" : "+v"(pa0), "+v"(pa1), "+v"(pb0), "+v"(pb1));
        if (HALO) asm volatile("" : "+v"(ph0), "+v"(ph1));
#pragma unroll
        for (int mt = 0; mt < 4; ++mt)
#pragma unroll
            for (int nt = 0; nt < 6; ++nt) asm volatile("" : "+v"(acc[mt][nt]));
        SCHED();
    } else if (OWN) {
#pragma unroll
        for (int mt = 0; mt < 4; ++mt)
#pragma unroll
            for (int nt = 0; nt < 6; ++nt) asm volatile("" : "+v"(acc[mt][nt]));
        pre();
        WAIT_V0();
    }
    if (PRO && l > 0 && c.wid == 1) STAMP_AT(l == 1 ? 43 : (l == 2 ? 47 : 60));
    BAR();
    FENCE();
    if (!HALO && HT0 == 1) { if (hto < 0x80000000u) asm volatile("s_mov_b32 m0, %0\n\ts_nop 3\n\tglobal_load_lds_dword %1, %2" ::"s"(M_TCH), "v"(hto), "s"(W0) : "memory"); }
    if (c.wid == 0) STAMP_AT(l * 8 + 0);
    bf16x8 h0[2];
    if (OWN && HALO) {
#pragma unroll
        for (int set = 0; set < 2; ++set) h0[set] = *(const LAS bf16x8*)(lds(ph0) + (M_H0 - (QA_OFF + QA_SLOT + 16384)) + set * 1024);
        SCHED();
    }
    ldfrag(0, pa0, ph0, pb0, pb1, 0);
    SCHED();
    if (OWN && HALO) {
#pragma unroll
        for (int set = 0; set < (XH ? 0 : 2); ++set) { ah[0] = MFMA(hk[set], h0[set], ah[0]); ah[1] = MFMA(h0[set], hv_[set], ah[1]); }
        asm volatile("" : "+v"(ah[0]), "+v"(ah[1]));
        SCHED();
        __builtin_amdgcn_s_setprio(0); __builtin_amdgcn_s_setprio(0);
        SCHED();
    }
    if (ALN) WAIT_L0();
    SCHED();
    int cnt = KT - 3 - more - (OWN == 0 ? 0 : (HALO ? 1 : (OWA ? l >> 1 : 0))), tv = 0;
    QSTAMP(0);
    auto half0 = [&]() {
        ldfrag(1, pa1, ph1, pb0, pb1, 1);
        mm(0, 0, NH);
        interleave();
        mm(0, NH, NH + 1); SCHED();
        asm volatile("s_addk_i32 %0, %2\n\ts_addk_i32 %1, %3" : "+s"(sa), "+s"(sb) : "i"(QA_SLOT), "i"(QW_SLOT) : "scc"); SCHED();
        mm(0, NH + 1, NH + 2); SCHED();
        asm volatile("s_cmpk_lg_u32 %0, %2\n\ts_cselect_b32 %0, %0, 0\n\ts_cmp_lg_u32 %1, %3\n\ts_cselect_b32 %1, %1, %4" : "+s"(sa), "+s"(sb) : "i"(NQA * QA_SLOT), "i"(3 * QW_SLOT), "i"(QW_TOP) : "scc"); SCHED();
        mm(0, NH + 2, NH + 3); SCHED();
        asm volatile("s_cmp_lg_u32 %0, %2\n\ts_cselect_b32 %0, %0, 0\n\ts_addk_i32 %1, -1" : "+s"(sb), "+s"(cnt) : "i"(QW_TOP + QW_SLOT) : "scc"); SCHED();
        mm(0, NH + 3, NH + 4); SCHED();
        pa0 = aoff0 + sa; pb0 = boff[0] + sb;
        asm volatile("" : "+v"(pa0), "+v"(pb0)); SCHED();
        mm(0, NH + 4, NH + 5); SCHED();
        pb1 = boff[1] + sb; pa1 = pa0 ^ xm;
        asm volatile("" : "+v"(pa1), "+v"(pb1)); SCHED();
        mm(0, NH + 5, NH + 6); SCHED();
        if (HALO) { ph0 = hoff0 + sa; ph1 = ph0 ^ xm; asm volatile("" : "+v"(ph0), "+v"(ph1)); SCHED(); }
        mm(0, NH + 6, NM);
        SCHED();
        WAIT_L0();
        BAR();
        SCHED();
    };
    asm volatile("" : "+s"(sa), "+s"(sb), "+s"(cnt));
    do {
    ALIGN8();
    do {
        half0();
        ldfrag(0, pa0, ph0, pb0, pb1, 0);
        mm(1, 0, NH);
        interleave();
        mm(1, NH, NM - 1);
        SCHED();
        WAIT_L0();
        asm volatile("s_nop 0");
        SCHED();
        mm(1, NM - 1, NM);
        SCHED();
    } while (cnt != 0);
    if (!HALO) {
        asm volatile("s_cmp_eq_u32 %[more], 0\n\ts_cbranch_scc1 .Lex%=\n\ts_mov_b32 m0, %[tch]\n\ts_nop 3\n\tglobal_load_lds_dword %[vt], %[tw] sc1\n\t"
                     "global_load_dwordx4 %[c0], %[v0], %[cs] offset:-2048\n\ts_cmp_eq_u32 %[three], 0\n\ts_cbranch_scc1 .Lex%=\n\t"
                     "global_load_dwordx4 %[c1], %[v0], %[cs]\n\tglobal_load_dwordx4 %[c2], %[v0], %[cs] offset:2048\n.Lex%=:"
                     : [c0] "+v"(cpy[0]), [c1] "+v"(cpy[1]), [c2] "+v"(cpy[2])
                     : [more] "s"(more), [tch] "i"(M_TCH), [vt] "v"(vt), [tw] "s"(tw), [v0] "v"(cv), [cs] "s"(cs_), [three] "s"(three) : "scc", "memory");
        cnt = more; more = 0;
        asm volatile("" : "+s"(cnt), "+s"(more));
        SCHED();
    }
    } while (cnt != 0);
    if (!HALO) {
        asm volatile("s_cmp_lg_u32 %[l], 0\n\ts_cbranch_scc1 .Lex%=\n\ts_waitcnt vmcnt(0)\n\ts_nop 3\n\tglobal_store_dwordx4 %[d], %[c0], %[ws] sc1\n\ts_cmp_eq_u32 %[three], 0\n\ts_cbranch_scc1 .Lex%=\n\t"
                     "v_add_u32 %[d], 0x2000, %[d]\n\tglobal_store_dwordx4 %[d], %[c1], %[ws] sc1\n\tv_add_u32 %[d], 0x2000, %[d]\n\tglobal_store_dwordx4 %[d], %[c2], %[ws] sc1\n.Lex%=:\n\ts_nop 1"
                     : [d] "+v"(cdst) : [c0] "v"(cpy[0]), [c1] "v"(cpy[1]), [c2] "v"(cpy[2]), [l] "s"(l), [ws] "s"(cd_), [three] "s"(three) : "scc", "memory");
    }
    QSTAMP(1);
    FENCE();
    SCHED();

    f32x4 csq[4], m4[4], mh4; float csv[2], mr[4], rr[4], mh;
    auto r1 = [&](unsigned b, int off) { return *(const LAS float*)(lds(b) + off); };
    auto r4_ = [&](unsigned b, int off) { return *(const LAS f32x4*)(lds(b) + off); };
    auto mf = [&](int set, int i) {
        const int nt = i >> 2, mt = i & 3;
        if (i < 16) acc[mt][nt] = MFMA(wb[set][nt], xa[set][mt], acc[mt][nt]);
        else if (i < 24) acc[mt][nt] = MFMA(xa[set][mt], wb[set][nt], acc[mt][nt]);
        else if (i == 24) ah[0] = MFMA(wb[set][2], xh[set], ah[0]);
        else ah[1] = MFMA(xh[set], wb[set][4], ah[1]);
        SCHED();
    };
    auto fold = [&](int i, int e) {
        const int nt = i >> 2, mt = i & 3;
        float t;
        if (i < 16) t = __builtin_fmaf(mr[mt], csq[nt][e], acc[mt][nt][e]);
        else if (i < 24) t = __builtin_fmaf(m4[mt][e], csv[nt - 4], acc[mt][nt][e]);
        else if (i == 24) t = __builtin_fmaf(mh, csq[2][e], ah[0][e]);
        else t = __builtin_fmaf(mh4[e], csv[0], ah[1][e]);
        asm volatile("" : "+v"(t));
        if (i < 24) acc[mt][nt][e] = t; else ah[i - 24][e] = t;
    };
    constexpr Tail<HALO> TL{};
    constexpr int NP = HALO ? 6 : 2, FN = 4 * NM;
    static_assert(TL.v[3][TL.n[3]] == NP + FN + (HALO ? 2 : 0) && KT % NQA == 1 && KT % NQW == 0, "tail");
    s16x4 wl[6], wh[6];
    auto frd = [&](int set, int k, int ao) {
        if (k < 4) xa[set][k] = *(const LAS bf16x8*)(lds(set ? pa1 : pa0) + ao + k * 2048);
        else if (k == 16) xh[set] = *(const LAS bf16x8*)(lds(set ? ph1 : ph0) + ao);
        else {
            const int nt = (k - 4) >> 1;
            const LAS char* pb = lds((nt & 1) ? pb1 : pb0) + (nt >> 1) * 8192 + set * 4096;
            if (k & 1) { wh[nt] = tr_read(pb + 512); wb[set][nt] = __builtin_shufflevector(wl[nt], wh[nt], 0, 1, 2, 3, 4, 5, 6, 7); }
            else wl[nt] = tr_read(pb);
        }
    };
    constexpr int RS = M_RSTD - M_MEAN;
    unsigned vl, vg, tb, xr, xh_, b0a, qb0, qb1;
    f32x4 bprev, bcur, bprev0; u32x4 ps; u32x2 ko; float ninf;
    auto rdop = [&](int h, int k) {
        const int j = k - NH;
        if (h == 0) {
            if (k < 2) { mr[2 * k] = r1(sa0, k * 128); mr[2 * k + 1] = r1(sa0, k * 128 + 64); }
            else if (k < 4) csq[k - 2] = r4_((k & 1) ? sa3 : sa2, 0);
            else if (j < 4) frd(0, k - 4, 2 * QA_SLOT);
            else csq[j - 2] = r4_((j & 1) ? sa3 : sa2, 256);
        } else if (j < 0) frd(h & 1, k, h == 1 ? 2 * QA_SLOT : 0);
        else if (h == 1) {
            if (j < 2) csv[j] = r1(j ? sa7 : sa6, 0);
            else if (HALO && j == 2) mh = r1(sa4, 0);
            else if (HALO && j == 3) mh4 = r4_(sa5, 0);
            else { const int mt = HALO ? 7 - j : j - 2; m4[mt] = r4_(sa1, mt * 64); }
        } else if (h == 2) {
            if (j == 0) m4[2] = r4_(sa1, 128);
            if (j == 1) ko = *(const LAS u32x2*)(lds(tb) + 3072);
            if (j == 2) { rr[0] = r1(sa0, RS); rr[1] = r1(sa0, 64 + RS); }
            if (j == 3) { rr[2] = r1(sa0, 128 + RS); rr[3] = r1(sa0, 192 + RS); }
            if (j >= 4) m4[5 - j] = r4_(sa1, (5 - j) * 64);
        } else {
            if (j == 0) bprev = r4_(tb, 0);
            if (j == 1) bcur = r4_(tb, 1024);
            if (j == 2) ps = *(const LAS u32x4*)(lds(tb) + 2048);
            if (j == 3) bprev0 = r4_(b0a, 0);
        }
    };
    auto vop = [&](int k) {
        if (k == 0) { asm volatile("" : "+v"(boff[0])); qb0 = boff[0] + QW_TOP; asm volatile("" : "+v"(qb0)); }
        else if (k == 1) { asm volatile("" : "+v"(boff[1])); qb1 = boff[1] + QW_TOP; asm volatile("" : "+v"(qb1)); }
        else if (k < NP) {
            if (k == 2) { vl = (unsigned)(lane * 16); asm volatile("" : "+v"(vl)); }
            if (k == 3) { tb = vl + M_TAB; asm volatile("" : "+v"(tb)); }
            if (k == 4) { b0a = tb | b0m; asm volatile("" : "+v"(b0a)); }
            if (k == 5) { xr = xrs + vl; asm volatile("" : "+v"(xr)); }
        } else if (k < NP + FN) fold(TL.f[(k - NP) >> 2], (k - NP) & 3);
        else {
            if (k == NP + FN) { xh_ = xhs + vl; asm volatile("" : "+v"(xh_)); }
            if (k == NP + FN + 1) { ninf = -INFINITY; asm volatile("" : "+v"(ninf)); }
        }
    };
    unsigned wa, wq;
    int wr2 = wr, wc2 = wc;
#pragma unroll
    for (int i = 0; i < NM; ++i) {
        mf(0, TL.t[0][i]);
        if (TL.t[0][i] < 24) asm volatile("" : "+v"(acc[TL.t[0][i] & 3][TL.t[0][i] >> 2])); else asm volatile("" : "+v"(ah[TL.t[0][i] & 1]));
        FENCE();
        if (i < NH) frd(1, i, 0);
        if (!HALO && i == NH + 3) { asm volatile("" : "+s"(wr2), "+s"(wc2)); vl = (unsigned)(lane * 16); wq = (unsigned)(AT_Q + (wc2 * 8 + wr2 * 4) * 1024) + vl; asm volatile("" : "+v"(vl), "+v"(wq)); }
        if (!HALO && i == NH + 4) { asm volatile("" : "+s"(wr2), "+s"(wc2)); wa = (unsigned)(AT_OWN + wc2 * 16384 + wr2 * 4096) + vl; asm volatile("" : "+v"(wa)); }
        if (i == NH + 1) { asm volatile("" : "+v"(boff[0]), "+v"(boff[1])); pb0 = boff[0] + 2 * QW_SLOT; pb1 = boff[1] + 2 * QW_SLOT; asm volatile("" : "+v"(pb0), "+v"(pb1)); }
        if (i == NH + 2) { pa0 = aoff0; pa1 = aoff0 ^ xm; asm volatile("" : "+v"(pa1)); if (HALO) { ph0 = hoff0; ph1 = hoff0 ^ xm; asm volatile("" : "+v"(ph1)); } }
        SCHED();
    }
    WAIT_L0();
    BAR();
    FENCE();
    SCHED();
    auto frags = [&](int h) {
        static_assert(TL.d[0][NM] == NH + 6 && TL.d[1][NM] == NH + (HALO ? 5 : 6) && TL.d[2][NM] == NH + (HALO ? 6 : 0) && TL.d[3][TL.n[3]] == NH + (HALO ? 4 : 0), "reads");
        if (h == 0) __builtin_amdgcn_s_waitcnt(0xC27F);
        if (h == 1) __builtin_amdgcn_s_waitcnt(HALO ? 0xC57F : 0xC67F);
        if (h == 2) __builtin_amdgcn_s_waitcnt(HALO ? 0xC67F : 0xC07F);
        if (h == 3) __builtin_amdgcn_s_waitcnt(HALO ? 0xC47F : 0xC07F);
    };
#pragma unroll
    for (int h = 0; h < 4; ++h) {
        if (h == 2) { pb0 = qb0; pb1 = qb1; }
#pragma unroll
        for (int i = 0; i < TL.n[h]; ++i) {
            if (!(h & 1) && i == TL.n[h] - 1) { frags(h); SCHED(); if (h) QSTAMP(4); }
            mf(~h & 1, TL.t[h][i]);
#pragma unroll
            for (int k = TL.d[h][i]; k < TL.d[h][i + 1]; ++k) { FENCE(); rdop(h, k); }
            SCHED();
#pragma unroll
            for (int k = TL.v[h][i]; k < TL.v[h][i + 1]; ++k) vop(k);
            SCHED();
        }
        if (h & 1) {
            frags(h);
            if (h == 1) QSTAMP(2); else QSTAMP(5);
            BAR();
            FENCE();
            SCHED();
            if (h == 1) QSTAMP(3); else QSTAMP(6);
        }
    }
    if (!HALO) {
        f32x4 r4[4];
        mf(1, 8);
#pragma unroll
        for (int mt = 0; mt < 4; ++mt) rr[mt] = r1(sa0, mt * 64 + RS);
        SCHED();
        mf(1, 12);
#pragma unroll
        for (int mt = 0; mt < 4; ++mt) r4[mt] = r4_(sa1, mt * 64 + RS);
        SCHED();
        mf(1, 9);
        vg = HK ? (unsigned)hv<HK>(lane) : vl;
        if (HK) asm volatile("" : "+v"(vg));
        SCHED();
        u32x4 pk[8];
        auto op = [&](int g, int k) {
            const int ta = g < 4 ? g : (g - 4) & 2, tb = g < 4 ? g : ta + 1, na = g < 4 ? 2 : 4 + (g & 1), nb = g < 4 ? 3 : na;
            f32x4& A = acc[ta][na]; f32x4& Bv = acc[tb][nb];
            if (k < 12) {
                const int h = k / 6, kk = k % 6;
                f32x4& X = h ? Bv : A;
                const int tx = h ? tb : ta;
                if (kk < 4) { float t = X[kk] * (g < 4 ? rr[tx] : r4[tx][kk]); asm volatile("" : "+v"(t)); X[kk] = t; }
                else { unsigned t = cvt_pk_bf16(X[2 * (kk - 4)], X[2 * (kk - 4) + 1]); asm volatile("" : "+v"(t)); pk[g][2 * h + kk - 4] = t; }
            } else if (k == 12) __builtin_amdgcn_raw_buffer_store_b128(pk[g], rso, vg + hp<HK>(g & 3), (g >> 2) * (8192 << HK), 16);
            else *(LAS u32x4*)(lds(wa) + (g >> 2) * 8192 + (g & 3) * 1024) = pk[g];
            SCHED();
        };
        auto pm = [&](int i) {
            if (i < 16) mf(1, TL.h3[i]);
            else if (i < 32) mf(((i - 16) >> 1) & 1, ((i - 16) >> 2) + 4 * (i & 1));
        };
        pm(3);
#pragma unroll
        for (int g = 0; g < 8; ++g) {
#pragma unroll
            for (int k = 0; k < 6; ++k) op(g, k);
            pm(2 * g + 4);
            if (g > 0) op(g - 1, 12);
#pragma unroll
            for (int k = 6; k < 12; ++k) op(g, k);
            pm(2 * g + 5);
            if (g > 0 && g <= LWA) op(g - 1, 13);
        }
        op(7, 12);
        QSTAMP(7);
        u32x4 qk;
        auto qop = [&](int mt, int k) {
            if (k == 0) { rr[mt] *= QSCALE; asm volatile("" : "+v"(rr[mt])); }
            else if (k < 13) {
                const int h = (k - 1) / 6, kk = (k - 1) % 6;
                f32x4& X = acc[mt][h];
                if (kk < 4) { float t = X[kk] * rr[mt]; asm volatile("" : "+v"(t)); X[kk] = t; }
                else { unsigned t = cvt_pk_bf16(X[2 * (kk - 4)], X[2 * (kk - 4) + 1]); asm volatile("" : "+v"(t)); qk[2 * h + kk - 4] = t; }
            } else *(LAS u32x4*)(lds(wq) + mt * 1024) = qk;
            SCHED();
        };
#pragma unroll
        for (int mt = 0; mt < 3; ++mt) {
            pm(20 + 4 * mt); if (3 * mt + LWA < 8) op(3 * mt + LWA, 13); qop(mt, 0); qop(mt, 1); qop(mt, 2);
            pm(21 + 4 * mt); if (3 * mt + 1 + LWA < 8) op(3 * mt + 1 + LWA, 13); qop(mt, 3); qop(mt, 4); qop(mt, 5);
            pm(22 + 4 * mt); if (3 * mt + 2 + LWA < 8) op(3 * mt + 2 + LWA, 13); qop(mt, 6); qop(mt, 7); qop(mt, 8);
            pm(23 + 4 * mt); qop(mt, 9); qop(mt, 10); qop(mt, 11); qop(mt, 12); qop(mt, 13);
        }
        if (c.wid == 0) STAMP_AT(l * 8 + 1);
        QSTAMP(8);
        {
            const u32x2 q0 = pack4(acc[3][0] * (rr[3] * QSCALE)), q1 = pack4(acc[3][1] * (rr[3] * QSCALE));
            *(LAS u32x4*)(lds(wq) + 3 * 1024) = (u32x4){q0[0], q0[1], q1[0], q1[1]};
        }
        QSTAMP(9);
        QSTORE();
        if (K0E) {
            SCHED();
            WAIT_L0();
            BAR();
            FENCE();
            SCHED();
        }
    } else {
        typedef unsigned u32x10 __attribute__((ext_vector_type(10)));
        u32x4 kp[4], qp[4];
        u32x10 vw[2];
        float ks[2][4], cs[2][4];
        auto pk8 = [&](const f32x4& x, const f32x4& y) { const u32x2 a = pack4(x), b = pack4(y); u32x4 r = {a[0], a[1], b[0], b[1]}; asm volatile("" : "+v"(r)); SCHED(); return r; };
        auto pkv = [&](int mt, int cc) { const u32x2 a = pack4(acc[mt][4 + cc]); vw[cc][2 * mt + 2] = a[0]; vw[cc][2 * mt + 3] = a[1]; };
        auto fr8 = [](const u32x4& x) { return __builtin_bit_cast(bf16x8, x); };
        mf(1, 24);
        unsigned ka0 = ko[0] + wr * 256, ka1 = ko[1] + wr * 256;
        asm volatile("" : "+v"(ka0), "+v"(ka1));
        SCHED();
        mf(1, 25);
#pragma unroll
        for (int mt = 0; mt < 4; ++mt) ks[0][mt] = r1(ka0, mt * 64);
        rr[0] *= QSCALE; asm volatile("" : "+v"(rr[0]));
        SCHED();
        mf(1, 11);
#pragma unroll
        for (int mt = 0; mt < 4; ++mt) ks[1][mt] = r1(ka1, mt * 64);
        rr[1] *= QSCALE; asm volatile("" : "+v"(rr[1]));
        SCHED();
        mf(1, 15);
        rr[2] *= QSCALE; asm volatile("" : "+v"(rr[2]));
        rr[3] *= QSCALE; asm volatile("" : "+v"(rr[3]));
        SCHED();
        mf(1, 19);
        const u32x4 hh = pk8(ah[0], ah[1]);
        mf(1, 23);
        *(LAS u32x2*)lds(xh_) = (u32x2){hh[0], hh[1]}; *(LAS u32x2*)(lds(xh_) + 1024) = (u32x2){hh[2], hh[3]};
        SCHED();
        mf(1, 10);
        kp[3] = pk8(acc[3][2], acc[3][3]);
        mf(1, 14);
        *(LAS u32x4*)(lds(xr) + 2048) = kp[3];
        pkv(3, 0); asm volatile("" : "+v"(vw[0]));
        *(LAS u32x2*)(lds(xr) + 3072) = (u32x2){vw[0][8], vw[0][9]};
        SCHED();
        mf(1, 3);
        pkv(3, 1); asm volatile("" : "+v"(vw[1]));
        *(LAS u32x2*)(lds(xr) + 3072 + 8) = (u32x2){vw[1][8], vw[1][9]};
        SCHED();
        mf(1, 7);
        mf(1, 9);
        kp[2] = pk8(acc[2][2], acc[2][3]);
        mf(1, 13);
        qp[3] = pk8(acc[3][0], acc[3][1]);
        mf(1, 2);
        f32x4 s0[4], s1[4];
        u32x4 pk;
        auto sc = [&](int mt, int h) {
            if (h) { s1[mt] = MFMA(fr8(kp[mt]), fr8(qp[mt]), bcur); asm volatile("" : "+v"(s1[mt])); }
            else { s0[mt] = MFMA(fr8(mt ? kp[mt - (mt > 0)] : pk), fr8(qp[mt]), mt == 0 ? bprev0 : bprev); asm volatile("" : "+v"(s0[mt])); }
            SCHED();
        };
        auto csm = [&](int mt) { cs[0][mt] = ks[0][mt] * rr[mt]; asm volatile("" : "+v"(cs[0][mt])); cs[1][mt] = ks[1][mt] * rr[mt]; asm volatile("" : "+v"(cs[1][mt])); };
        float u[2][4], mx[4], iv[4];
        auto sv = [](float x) { asm volatile("" : "+v"(x)); return x; };
        auto mx2 = [&](float a, float b) { return max3(ninf, a, b); };
        auto pick = [&](int mt, int h) { u[h][mt] = sv(max3(mx2(s0[mt][2 * h], s0[mt][2 * h + 1]), s1[mt][2 * h], s1[mt][2 * h + 1]) * cs[h][mt]); };
        auto vf = [&](int i) { const int mt = 2 - (i >> 2), nt = 4 + (i & 1), set = (i >> 1) & 1; SCHED(); acc[mt][nt] = MFMA(xa[set][mt], wb[set][nt], acc[mt][nt]); asm volatile("" : "+v"(acc[mt][nt])); SCHED(); };
        mf(1, 6);
        kp[1] = pk8(acc[1][2], acc[1][3]);
        mf(1, 8);
        WAIT_L0();
        QSTAMP(7);
        BAR();
        FENCE();
        SCHED();
        QSTAMP(8);
        mf(1, 12);
        {
            const u32x2 k0 = *(const LAS u32x2*)(lds(xr) + 8);
            FENCE(); SCHED();
            const u32x2 k1 = *(const LAS u32x2*)lds(xr);
            FENCE(); SCHED();
            const u32x2 a = *(const LAS u32x2*)(lds(xr) + 1024 + 8);
            FENCE(); SCHED();
            const u32x2 b = *(const LAS u32x2*)(lds(xr) + 1024);
            pk = (u32x4){k0[0], k0[1], k1[0], k1[1]};
            vw[0][0] = a[0]; vw[0][1] = a[1]; vw[1][0] = b[0]; vw[1][1] = b[1];
        }
        SCHED();
        auto sw16 = [](float& a, float& b) { auto r = __builtin_amdgcn_permlane16_swap(__float_as_uint(a), __float_as_uint(b), false, false); a = __uint_as_float(r[0]); b = __uint_as_float(r[1]); };
        auto sw32 = [](float& a, float& b) { auto r = __builtin_amdgcn_permlane32_swap(__float_as_uint(a), __float_as_uint(b), false, false); a = __uint_as_float(r[0]); b = __uint_as_float(r[1]); };
        auto ldr = [&](int i) { };
        auto top = [&](int mt) { mx[mt] = sv(mx2(u[0][mt], u[1][mt])); };
        auto pv1 = [&](int mt, int k) {
            SCHED();
            vw[k >> 1][2 * mt + 2 + (k & 1)] = cvt_pk_bf16(acc[mt][4 + (k >> 1)][2 * (k & 1)], acc[mt][4 + (k >> 1)][2 * (k & 1) + 1]);
            asm volatile("" : "+v"(vw[k >> 1]));
            SCHED();
        };
        auto uk = [&](int mt) { SCHED(); u[0][mt] = sv(u[0][mt] * ks[0][mt]); u[1][mt] = sv(u[1][mt] * ks[1][mt]); SCHED(); };
        mf(1, 1);
        qp[2] = pk8(acc[2][0], acc[2][1]);
        mf(1, 5);
        csm(3); SCHED();
        mf(1, 0);
        kp[0] = pk8(acc[0][2], acc[0][3]);
        mf(1, 4);
        csm(2); SCHED();
        sc(3, 1);
        qp[1] = pk8(acc[1][0], acc[1][1]);
        sc(3, 0);
        csm(1); ldr(0); SCHED();
        sc(2, 1);
        qp[0] = pk8(acc[0][0], acc[0][1]);
        sc(2, 0);
        csm(0); pick(3, 0); SCHED();
        sc(1, 1);
        pick(3, 1); ldr(1); SCHED();
        sc(1, 0);
        pick(2, 0); top(3); SCHED();
        sc(0, 1);
        pick(2, 1); ldr(2); SCHED();
        sc(0, 0);
        if (c.wid == 0) STAMP_AT(l * 8 + 1);
        pick(1, 0); top(2);
        vf(0);
        pick(1, 1); ldr(3);
        vf(1);
        sw16(mx[2], mx[3]);
        pick(0, 0);
        vf(2);
        pick(0, 1); top(1);
        vf(3);
        top(0); mx[2] = sv(mx2(mx[2], mx[3]));
        vf(4);
        sw16(mx[0], mx[1]); mx[0] = sv(mx2(mx[0], mx[1]));
        vf(5); pv1(2, 0);
        sw32(mx[0], mx[2]);
        { const float a = mx[0], b = mx[2]; mx[0] = sv(mx2(a, b)); mx[2] = sv(mx2(b, a)); }
        vf(6); pv1(2, 1);
        sw32(mx[0], mx[2]);
        mx[3] = sv(mx[2]); mx[1] = sv(mx[0]);
        vf(7);
        sw16(mx[2], mx[3]); sw16(mx[0], mx[1]);
        SCHED();
#pragma unroll
        for (int mm = 0; mm < 4; ++mm) { const int mt = 3 - mm; u[0][mt] = sv(u[0][mt] - mx[mt]); u[1][mt] = sv(u[1][mt] - mx[mt]); if (mm == 1) vf(8); }
        vf(9);
#pragma unroll
        for (int mm = 0; mm < 4; ++mm) {
            const int mt = 3 - mm;
            u[0][mt] = sv(__builtin_amdgcn_exp2f(u[0][mt])); u[1][mt] = sv(__builtin_amdgcn_exp2f(u[1][mt]));
            if (mm > 0) iv[mt + 1] = sv(u[0][mt + 1] + u[1][mt + 1]);
            if (mm < 2) vf(10 + mm);
        }
        SCHED();
        sw16(iv[2], iv[3]);
        iv[0] = sv(u[0][0] + u[1][0]);
        iv[2] = sv(iv[2] + iv[3]);
        pv1(2, 2);
        sw16(iv[0], iv[1]); iv[0] = sv(iv[0] + iv[1]);
        uk(3);
        sw32(iv[0], iv[2]);
        iv[0] = sv(iv[0] + iv[2]);
        pv1(2, 3);
        iv[0] = sv(__builtin_amdgcn_rcpf(iv[0]));
        pv1(1, 0);
        iv[2] = sv(iv[0]);
        uk(2);
        sw32(iv[0], iv[2]);
        iv[3] = sv(iv[2]); iv[1] = sv(iv[0]);
        pv1(1, 1);
        sw16(iv[2], iv[3]); sw16(iv[0], iv[1]);
        SCHED();
        f32x4 o[4][2];
        u32x4 pf[4];
        auto nrm = [&](int mt) { u[0][mt] = sv(u[0][mt] * iv[mt]); u[1][mt] = sv(u[1][mt] * iv[mt]); SCHED(); };
        auto prb = [&](int mt) {
            const unsigned p = cvt_pk_bf16(u[0][mt], u[1][mt]);
            pf[mt] = (u32x4){__builtin_amdgcn_perm(0u, p, ps[0]), __builtin_amdgcn_perm(0u, p, ps[1]), __builtin_amdgcn_perm(0u, p, ps[2]), __builtin_amdgcn_perm(0u, p, ps[3])};
            asm volatile("" : "+v"(pf[mt]));
            SCHED();
        };
        auto pvm = [&](int mt, int cc) {
            o[mt][cc] = MFMA(fr8((u32x4){vw[cc][2 * mt], vw[cc][2 * mt + 1], vw[cc][2 * mt + 2], vw[cc][2 * mt + 3]}), fr8(pf[mt]), ((f32x4){0.f, 0.f, 0.f, 0.f}));
            asm volatile("" : "+v"(o[mt][cc]));
            SCHED();
        };
        auto out = [&](int mt) {
            const u32x2 lo = pack4(o[mt][0]), hi = pack4(o[mt][1]);
            __builtin_amdgcn_raw_buffer_store_b128((u32x4){lo[0], lo[1], hi[0], hi[1]}, rso, vg + hp<HO>(mt), 0, 16);
            SCHED();
        };
        vg = (unsigned)hv<HO>(lane);
        asm volatile("" : "+v"(vg));
        nrm(3); nrm(2); prb(3);
        pv1(1, 2); pv1(1, 3);
        pvm(3, 0); prb(2); pvm(3, 1);
        uk(1); uk(0);
        pvm(2, 0); nrm(1); nrm(0); pvm(2, 1);
        prb(1); pv1(0, 0); pv1(0, 1);
        pvm(1, 0); pv1(0, 2); pv1(0, 3); prb(0);
        pvm(1, 1); out(3); pvm(0, 0); out(2); pvm(0, 1); out(1); out(0);
        SCHED();
        QSTAMP(9);
        if (c.wid == 0) STAMP_AT(l * 8 + 2);
        QSTORE();
    }
}

#define MFMA32(a, b, c) __builtin_amdgcn_mfma_f32_32x32x16_bf16(__builtin_bit_cast(bf16x8, a), __builtin_bit_cast(bf16x8, b), (c), 0, 0, 0)
DI float max16(const f32x16& s) {
    float m = max3(s[0], s[1], s[2]), n = max3(s[3], s[4], s[5]);
    m = max3(m, s[6], s[7]); n = max3(n, s[8], s[9]);
    m = max3(m, s[10], s[11]); n = max3(n, s[12], s[13]);
    m = max3(max3(m, s[14], s[15]), n, n);
    auto b = __builtin_amdgcn_permlane32_swap(__float_as_uint(m), __float_as_uint(m), false, false);
    return __builtin_amdgcn_fmed3f(__uint_as_float(b[0]), __uint_as_float(b[1]), INFINITY);
}
template <bool HI> DI void attn_a(const Ctx& c0, int l, const char* Wo, KP kp) {
    const Ctx c = fresh(c0);
    asm volatile("" : "+s"(Wo));
    LAS char* smem = c.smem;
    const int lane = c.lane, w = c.wid;
    constexpr int H = HI ? 0 : 1;
    int qt = HI ? w - 4 : 3 - w;
    asm volatile("" : "+s"(qt));
    const float NEG = -INFINITY;
    int zq = (lane & 31) - 4 * (lane >> 5);
    unsigned lb = (unsigned)(((lane >> 4) & 1) * 1024 + ((lane >> 5) * 16 + (lane & 15)) * 16);
    unsigned qa = (unsigned)(AT_Q + H * 8192 + qt * 2048) + lb, ta = (unsigned)(AT_OWN + H * 16384 - 3 * 2048 + qt * 2048) + lb;
    asm volatile("" : "+v"(lb), "+v"(qa), "+v"(ta));
    auto rd = [&](unsigned base, int off) { return *(const LAS u32x4*)(lds(base) + off); };
    f32x16 ce;
    u32x4 sel;
    unsigned tm = (unsigned)(AT_N + lane * 16);
    auto masks = [&]() {
        if (HI) {
#pragma unroll
            for (int r = 0; r < 16; ++r) ce[r] = 8 * (r >> 2) + (r & 3) <= zq ? 0.f : NEG;
            asm volatile("" : "+v"(ce));
        }
        const unsigned s1 = ((lane >> 4) ^ (lane >> 2)) & 1 ? 0u : 0x3f803f80u;
        sel = (u32x4){s1, s1, s1, s1};
        asm volatile("" : "+v"(sel));
    };
    u64 ep, pa_, kvv, nkv, kact;
    unsigned po, pk_, cnta = M_CNT, one = 1u;
    char* ob;
    const char *kvb, *s1_, *s2_;
    unsigned vk, dst, fo;
    auto setup = [&]() {
        if (HI) {
            const u32x2 e = *(const LAS u32x2*)(smem + M_EP);
            ep = ((u64)(unsigned)__builtin_amdgcn_readfirstlane(e[1]) << 32) | (unsigned)__builtin_amdgcn_readfirstlane(e[0]);
            po = o_post(c);
            pk_ = pick(lane < 2, (unsigned)(WS_KBOX + ((c.pq + 1 + lane) * 16 + c.jq) * 256 + lane * 8), (unsigned)(WS_FLAGQ + (c.pq * 16 + c.jq) * FQS));
            pa_ = c.pq < 14 ? 7ull : (c.pq < 15 ? 5ull : 4ull);
            fo = (unsigned)(WS_KBOX + (c.pq * 16 + c.jq) * 256 + lane * 8);
            asm volatile("" : "+v"(po), "+v"(pk_), "+v"(fo));
            asm volatile("" : "+s"(ep), "+s"(pa_));
        }
        ob = c.ws + WS_O + (size_t)l * (8u << 20) + (size_t)(unsigned)(((c.jq * 2 + H) * 16 + c.pq) * 8192 + qt * 2048);
        kvb = c.ws + WS_KV + (size_t)(unsigned)((l >> 1) * (16u << 20) + c.jq * 32768);
        asm volatile("" : "+s"(ob), "+s"(kvb));
    };
    auto setup2 = [&]() {
        const int p1 = c.pq >= 1 ? c.pq - 1 : c.pq, p2 = c.pq >= 2 ? c.pq - 2 : c.pq;
        s1_ = kvb + (size_t)(unsigned)(p1 * 524288);
        s2_ = kvb + (size_t)(unsigned)(p2 * 524288);
        vk = (unsigned)((w & 3) * 1024 + lane * 16); dst = (unsigned)(AT_N + (w & 3) * 1024);
        asm volatile("" : "+s"(s1_), "+s"(dst));
        if (!HI) asm volatile("" : "+s"(s2_));
        asm volatile("" : "+v"(vk));
    };
    auto arrive = [&](auto cc, unsigned msk, unsigned val) {
        unsigned old = 0;
        if (lane == 0) asm volatile("ds_add_rtn_u32 %0, %1, %2 offset:%3\n\ts_waitcnt lgkmcnt(0)" : "+v"(old) : "v"(cnta), "v"(one), "i"(decltype(cc)::value * 4) : "memory");
        if ((__builtin_amdgcn_readfirstlane(old) & msk) == msk) { if (lane < 16) post(c.ws, po, ep + (4 * l + val)); }
    };
    static_assert((MBOX & 2) && !HO && !HK, "layout");
    int tv = 0;
    auto kvs = [&]() {
        if (!HI) {
            SCHED();
            LSTAMP(2);
            asm volatile("s_mov_b64 exec, 1\n\ts_waitcnt vmcnt(0)\n\tds_add_u32 %0, %1\n\ts_mov_b64 exec, -1" :: "v"(cnta), "v"(one) : "memory");
            LSTAMP(3);
            SCHED();
        }
    };
    asm volatile("" : "+v"(cnta), "+v"(one), "+v"(tm));
    if (HI) {
        masks(); setup(); setup2();
        if (w == 5) {
#pragma unroll
            for (int i = 0; i < 4; ++i) *(LAS f32x4*)(lds(tm) + i * 1024) = (f32x4){ce[4 * i], ce[4 * i + 1], ce[4 * i + 2], ce[4 * i + 3]};
        }
        kvv = ep + (4 * l + 2); nkv = 0 - kvv;
        kact = __builtin_amdgcn_ballot_w64(w == KPW && lane < 2 && lane < c.pq);
        asm volatile("" : "+v"(kvv), "+v"(nkv));
        asm volatile("" : "+s"(kact));
        WAIT_L0();
        WAIT_V0();
        LSTAMP(0);
        if (w == 4) __builtin_amdgcn_s_setprio(HP4);
        BAR();
        FENCE();
    }
    auto kvp = [&]() {
        if (HI && w == 4) {
            __builtin_amdgcn_s_setprio(3);
#pragma unroll 1
            for (unsigned spins = 0; spins < (1u << 24); ++spins) {
                if (__builtin_amdgcn_readfirstlane(*(volatile LAS int*)lds(cnta)) == 8 * l + 4) break;
            }
            if (__builtin_amdgcn_inverse_ballot_w64(pa_)) post(c.ws, pk_, kvv);
            STAMP_AT(l * 8 + 2);
            __builtin_amdgcn_s_setprio(HP4);
        }
        SCHED();
    };
    if (KPP == -1) kvp();
    FENCE();
    SCHED();
    LSTAMP(1);
    u32x4 q0, q1, k0, k1, va, vp, pa, pb;
    f32x16 cm, s0, s1, o;
    f32x4 ls;
    auto stage = [&](f32x16& sc, f32x16& sn, const f32x16& cn, bool pend, bool zero, unsigned kb, int ko, unsigned vb, int vo1, unsigned nb, int vo0) {
        auto ex = [&](int i) { float x = __builtin_amdgcn_exp2f(sc[i]); asm volatile("" : "+v"(x)); sc[i] = x; SCHED(); };
        auto cv = [&](u32x4& p, int j, int i) { unsigned x = cvt_pk_bf16(sc[i], sc[i + 1]); asm volatile("" : "+v"(x)); p[j] = x; SCHED(); };
        sn = MFMA32(k0, q0, cn); SCHED();
        k0 = rd(kb, ko); SCHED();
        ex(0); ex(1); ex(2);
        if (pend) { o = MFMA32(vp, pb, o); SCHED(); }
        ex(3); ex(8); ex(9);
        sn = MFMA32(k1, q1, sn); SCHED();
        k1 = rd(kb, ko + 512); SCHED();
        ex(10); ex(11); cv(pa, 0, 0); cv(pa, 1, 2);
        if (pend) { ls = MFMA(__builtin_bit_cast(bf16x8, sel), __builtin_bit_cast(bf16x8, pb), ls); SCHED(); }
        vp = rd(vb, 8192 + vo1 + 512); SCHED();
        cv(pa, 2, 8); cv(pa, 3, 10); ex(4); ex(5);
        o = MFMA32(va, pa, zero ? (f32x16)(0.f) : o); SCHED();
        va = rd(nb, 8192 + vo0); SCHED();
        ex(6); ex(7); ex(12);
        ls = MFMA(__builtin_bit_cast(bf16x8, sel), __builtin_bit_cast(bf16x8, pa), zero ? (f32x4)(0.f) : ls); SCHED();
        ex(13); ex(14); ex(15); cv(pb, 0, 4); cv(pb, 1, 6); cv(pb, 2, 12); cv(pb, 3, 14);
    };
    auto flush = [&]() {
        ls = MFMA(__builtin_bit_cast(bf16x8, sel), __builtin_bit_cast(bf16x8, pb), ls); SCHED();
        o = MFMA32(vp, pb, o); SCHED();
    };
    float nm;
    {
        if (!HI) {
#pragma unroll
            for (int i = 0; i < 4; ++i) {
                const f32x4 t = *(const LAS f32x4*)(lds(tm) + i * 1024);
                ce[4 * i] = t[0]; ce[4 * i + 1] = t[1]; ce[4 * i + 2] = t[2]; ce[4 * i + 3] = t[3];
            }
        }
        q0 = rd(qa, 0);
        const u32x4 d0 = rd(ta, 6144);
        q1 = rd(qa, 512);
        const u32x4 d1 = rd(ta, 6144 + 512);
        k0 = rd(ta, 4096); k1 = rd(ta, 4096 + 512); va = rd(ta, 8192 + 6144);
        SCHED();
        if (KSP == -1) kvs();
        if (!HI) { masks(); setup(); SCHED(); }
        s0 = MFMA32(d0, q0, ce);
        s0 = MFMA32(d1, q1, s0);
        SCHED();
        if (!HI) { setup2(); SCHED(); }
        if (KSP == 0) kvs();
        if (KPP == 0) kvp();
        nm = -max16(s0);
        asm volatile("" : "+v"(nm));
#pragma unroll
        for (int r = 0; r < 16; ++r) { float t; asm("v_mov_b32 %0, %1" : "=v"(t) : "v"(nm), "i"(r)); cm[r] = t; }
        asm volatile("" : "+v"(cm));
        SCHED();
#pragma unroll
        for (int r = 0; r < 16; ++r) s0[r] = addf(s0[r], nm);
        asm volatile("" : "+v"(s0));
        SCHED();
        if (KSP == 1) kvs();
        if (KPP == 1) kvp();
        stage(s0, s1, cm, false, true, ta, 2048, ta, 6144, ta, 4096);
        if (KSP == 2) kvs();
        if (KPP == 2) kvp();
        if (qt > 0) {
            stage(s1, s0, cm, true, false, ta, 0, ta, 4096, ta, 2048);
            if (KSP == 3) kvs();
            if (qt > 1) {
                stage(s0, s1, cm, true, false, ta, 0, ta, 2048, ta, 0);
                if (qt > 2) stage(s1, s0, cm, true, false, ta, 0, ta, 0, ta, 0);
            }
        } else if (KSP == 3) kvs();
        flush();
    }
    LSTAMP(4);
    f32x16 c2;
    unsigned na, nb;
    auto rso = __builtin_amdgcn_make_buffer_rsrc(ob, 0, 2048, 0x00020000);
    auto starts = [&]() {
        const float n1 = c.pq >= 1 ? nm : NEG, n2 = c.pq >= 2 ? nm : NEG;
        asm volatile("" : "+v"(zq));
#pragma unroll
        for (int r = 0; r < 16; ++r) { cm[r] = n1; c2[r] = n2; ce[r] = 8 * (r >> 2) + (r & 3) <= zq ? NEG : n2; }
        na = (unsigned)(AT_N + H * 32768) + lb; nb = na + (unsigned)(16384 + qt * 2048);
        asm volatile("" : "+v"(cm), "+v"(c2), "+v"(ce), "+v"(na), "+v"(nb));
        asm volatile("" : "+s"(rso));
        SCHED();
    };
    {
        auto piece = [&](int b, int i) { dma16<true>(dst + b * 16384 + i * 4096, vk, ((b & 1) ? s2_ : s1_) + (b >> 1) * 16384 + i * 4096); };
        auto block = [&](int b) {
#pragma unroll
            for (int i = 0; i < 4; ++i) piece(b, i);
        };
        Ask t;
        if (HI) {
            poll_rmw<true>(t, c.ws, fo, __builtin_amdgcn_inverse_ballot_w64(kact), nkv, 62 - 4 * l);
            if (w == KPW) STAMP_AT(32 + l * 8 + 1);
        }
        WAIT_L0();
        LSTAMP(5);
        BAR();
        FENCE();
        LSTAMP(6);
        piece(0, H); piece(0, 2 + H);
        if (HI) {
            starts();
            if (APRIO) __builtin_amdgcn_s_setprio(APRIO);
            WAIT_V0();
            keep(t);
            LSTAMP(7);
            BAR();
            FENCE();
            if (w == 4) STAMP_AT(l * 8 + 3);
        } else {
            if (AB2 >= 0) block(1);
            if (AB2 > 0) block(2);
            starts();
            Ctx c2 = c;
            if (ELO) c2.wid = 0;
            unsigned vw = wo_w_off(c2);
            int qs = c.cw == 3 ? 1024 : 8 * D_ * 2;
            asm volatile("" : "+v"(vw));
            asm volatile("" : "+s"(qs));
            SCHED();
            wait_v<4 * AB2 + 4>();
            LSTAMP(7);
            BAR();
            if (AB2 < 0) block(1);
            if (AB2 <= 0) block(2);
            block(3);
            wait_v<8>();
            BAR();
            if (!ELO) {
                wo_w_issue<WA0, WA1>(c, Wo, vw);
                wait_v<WA1 - WA0>();
            } else WAIT_V0();
            LSTAMP(9);
            BAR();
            FENCE();
            if (!ELO) wo_w_issue<WA1, 26>(c, Wo, vw);
            else {
                const int s0 = w == 0 ? WA0 : (w == 1 ? WA0 + EN0 : WA0 + EN0 + EN1), s1 = w == 0 ? WA0 + EN0 : (w == 1 ? WA0 + EN0 + EN1 : (w == 2 ? 22 : 0));
#pragma unroll 1
                for (int sl = s0; sl < s1; ++sl) {
                    const char* b = Wo + (size_t)(unsigned)((2 * (sl & 15) + (sl >> 4)) * 65536);
#pragma unroll
                    for (int q = 0; q < 4; ++q) dma16<true>(sl * 4096 + q * 1024, vw, b + q * qs);
                }
            }
            if (LSL) { for (int i = 0; i < qt; ++i) __builtin_amdgcn_s_sleep(LSL); }
        }
    }
    SCHED();
    LSTAMP(8);
    {
        const u32x4 d0 = rd(na, 0), d1 = rd(na, 512);
        k0 = rd(na, 2048); k1 = rd(na, 2048 + 512); va = rd(na, 8192);
        SCHED();
        s0 = MFMA32(d0, q0, cm);
        s0 = MFMA32(d1, q1, s0);
        SCHED();
        stage(s0, s1, cm, false, false, na, 4096, na, 0, na, 2048);
        stage(s1, s0, cm, true, false, na, 6144, na, 2048, na, 4096);
        if (HI) { BAR(); FENCE(); SCHED(); }
        stage(s0, s1, cm, true, false, nb, 0, na, 4096, na, 6144);
        if (HI && CPOS == 3) { BAR(); SCHED(); }
        if (!HI && E30) {
            asm volatile("s_mov_b64 exec, 1\n\tds_add_u32 %0, %1 offset:12\n\ts_mov_b64 exec, -1" :: "v"(cnta), "v"(one) : "memory");
            SCHED();
        }
        stage(s1, s0, ce, true, false, nb, 2048, na, 6144, nb, 0);
        if (HI && CPOS == 4) { WAIT_L0(); BAR(); SCHED(); }
        stage(s0, s1, c2, true, false, nb, 4096, nb, 0, nb, 2048);
        if (HI && CPOS == 5) { BAR(); SCHED(); }
        if (qt < 3) {
            stage(s1, s0, c2, true, false, nb, 6144, nb, 2048, nb, 4096);
            if (qt < 2) {
                stage(s0, s1, c2, true, false, nb, 0, nb, 4096, nb, 6144);
                if (qt < 1) stage(s1, s0, c2, true, false, nb, 0, nb, 6144, nb, 0);
            }
        }
        flush();
    }
    LSTAMP(11);
    auto out = [&]() {
        const float iv = __builtin_amdgcn_rcpf(ls[0]);
#pragma unroll
        for (int u = 0; u < 2; ++u) {
            u32x4 t;
#pragma unroll
            for (int i = 0; i < 4; ++i) { const int r = 4 * u + (i >> 1) * 8 + (i & 1) * 2; t[i] = cvt_pk_bf16(o[r] * iv, o[r + 1] * iv); }
            __builtin_amdgcn_raw_buffer_store_b128(t, rso, lb + u * 512, 0, 16);
        }
        SCHED();
    };
    out();
    if (__builtin_expect(__any(!(ls[0] < SAFE_SUM)), 0)) {
        auto rk = __builtin_amdgcn_make_buffer_rsrc((void*)(kvb + H * 16384), 0, 16u << 20, 0x00020000);
        const int g0 = c.pq * 4 + qt - 8;
        float m = NEG;
#pragma unroll 1
        for (int ps = 0; ps < 2; ++ps) {
            o = (f32x16)(0.f); ls = (f32x4)(0.f);
#pragma unroll 1
            for (int j = g0 < 0 ? -g0 : 0; j < 9; ++j) {
                const int so = ((g0 + j) >> 2) * 524288 + ((g0 + j) & 3) * 2048;
                u32x4 f[4];
#pragma unroll
                for (int i = 0; i < 4; ++i) f[i] = __builtin_amdgcn_raw_buffer_load_b128(rk, lb + (i & 1) * 512, so + (i >> 1) * 8192, 16);
                f32x16 s;
                const float sb = ps ? -m : 0.f;
                float sa = j == 0 ? NEG : sb, sz = j == 8 ? NEG : sb;
                asm volatile("" : "+v"(zq), "+v"(sa), "+v"(sz));
#pragma unroll
                for (int r = 0; r < 16; ++r) s[r] = 8 * (r >> 2) + (r & 3) <= zq ? sa : sz;
                s = MFMA32(f[0], q0, s);
                s = MFMA32(f[1], q1, s);
                if (ps == 0) m = fmaxf(m, max16(s));
                else {
                    u32x4 p[2];
#pragma unroll
                    for (int r = 0; r < 16; r += 2) p[(r >> 2) & 1][(r >> 3) * 2 + ((r >> 1) & 1)] = cvt_pk_bf16(__builtin_amdgcn_exp2f(s[r]), __builtin_amdgcn_exp2f(s[r + 1]));
#pragma unroll
                    for (int u = 0; u < 2; ++u) {
                        o = MFMA32(f[2 + u], p[u], o);
                        ls = MFMA(__builtin_bit_cast(bf16x8, sel), __builtin_bit_cast(bf16x8, p[u]), ls);
                    }
                }
            }
        }
        __builtin_amdgcn_s_waitcnt(0x0F70);
        WAIT_V0();
        out();
    }
    SCHED();
    if (w == 3 || w == 4) STAMP_AT(32 + l * 8 + 3 + w);
    LSTAMP(12);
    if (HI) {
        WAIT_V0();
        arrive(std::integral_constant<int, 1>{}, 3u, 3u);
        if (w == 4) STAMP_AT(32 + l * 8 + 4);
        LSTAMP(13);
        if (APRIO) __builtin_amdgcn_s_setprio(0);
        if (lane == 0) asm volatile("ds_add_u32 %0, %1 offset:12" :: "v"(cnta), "v"(one) : "memory");
        WAIT_V0();
        arrive(std::integral_constant<int, 2>{}, 7u, 4u);
        if (w == 5 || w == 6) {
            Ctx c2 = c;
            c2.wid = 2 * w - 10;
            unsigned vw = wo_w_off(c2), vw2 = vw + (c.cw == 3 ? 1024u : 8u * D_ * 2);
            asm volatile("" : "+v"(vw), "+v"(vw2));
#pragma unroll 1
            for (unsigned spins = 0; spins < (1u << 22); ++spins) {
                if (__hip_atomic_load((LAS unsigned*)(smem + M_CNT + 12), __ATOMIC_RELAXED, __HIP_MEMORY_SCOPE_WORKGROUP) == (unsigned)(E30 ? 4 * l + 8 : 2 * l + 4)) break;
                __builtin_amdgcn_s_sleep(1);
            }
            FENCE();
            wo_w_issue<ELO ? 22 : 26, E30 ? 32 : 30>(c2, Wo, vw);
            c2.wid += 1;
            wo_w_issue<ELO ? 22 : 26, E30 ? 32 : 30>(c2, Wo, vw2);
        }
    }
    LSTAMP(23);
    ASTORE();
}

DI void wo_load(const Ctx& c0, int l, const char* Wn, const char* Wo) {
    const Ctx c = fresh(c0);
    asm volatile("" : "+s"(Wn), "+s"(Wo));
    const u64 ep = lds_epoch(c.smem);
    const unsigned fo = o_find(c);
        if (l == 0 && c.wid == 6) {
            const int fr = c.lane & 15, od = fr & 1, j0 = 4 * (c.lane >> 4) + od;
            const bool p0 = j0 > fr, p1 = j0 + 2 > fr;
            const unsigned N = 0xff800000u, Z = 0x0c0c0c0cu, lo = od ? 0x01000c0cu : 0x0c0c0100u, hi = od ? 0x03020c0cu : 0x0c0c0302u;
            LAS u32x4* t = (LAS u32x4*)(c.smem + M_TAB + c.lane * 16);
            t[0] = (u32x4){p0 && !od ? 0u : N, p0 && od ? 0u : N, p1 && !od ? 0u : N, p1 && od ? 0u : N};
            t[64] = (u32x4){!p0 && !od ? 0u : N, !p0 && od ? 0u : N, !p1 && !od ? 0u : N, !p1 && od ? 0u : N};
            t[128] = (u32x4){p0 ? lo : Z, p1 ? hi : Z, p0 ? Z : lo, p1 ? Z : hi};
            t[192] = (u32x4){(unsigned)(M_RSTD + j0 * 4 + (p0 ? 0 : 64)), (unsigned)(M_RSTD + j0 * 4 + 8 + (p1 ? 0 : 64)), 0u, 0u};
        }
    if (CPY && l == 0 && c.wid < 6) {
#pragma unroll 1
        for (int i = 0; i < 2; ++i) {
            const int j = i ? (c.bid & 4) * 2 + (c.bid >> 5) : c.jq, g = c.wid == 5 ? 2 - 2 * (j >> 3) : 1 + 2 * (j >> 3);
            if ((j & 7) == 3) poll_flags(c.ws, ep, (unsigned)WS_FLAGQ + ((c.lane >> 2) * 16 + 4 * g + (c.lane & 3)) * FQS, true, 2u);
        }
    }
    Ask t, t2;
    if (!(l & 1)) {
        Ctx c2 = c;
        c2.wid = 2 * c.wid - 10;
        unsigned vw = wo_w_off(c2), vw2 = vw + (c.cw == 3 ? 1024u : 8u * D_ * 2);
        asm volatile("" : "+v"(vw), "+v"(vw2));
        poll_rmw(t, c.ws, fo, c.wid == 7 && c.lane < 16, ep + (4 * l + 3), 61 - 4 * l);
        if (c.wid == 7) STAMP_AT(32 + l * 8 + 2);
        BAR();
        if (!E30 && (c.wid == 5 || c.wid == 6)) {
            __builtin_amdgcn_s_sleep(W30S);
            wo_w_issue<30, 32>(c2, Wo, vw);
            c2.wid += 1;
            wo_w_issue<30, 32>(c2, Wo, vw2);
        }
        if (c.wid != 7) WAIT_V0();
        else {
            __builtin_amdgcn_s_sleep(OSL);
            if (OPL) poll_flags(c.ws, ep, fo, c.lane < 16, (unsigned)(4 * l + 4));
        }
    } else {
        asm volatile("" : "=v"(t.v[0]), "=v"(t.v[1]), "=v"(t.v[2]), "=v"(t.v[3]));
        WAIT_V0();
    }
    poll_rmw(t2, c.ws, fo, c.wid == 7 && c.lane < 16 && (!OPL || (l & 1)), ep + (4 * l + 4), 60 - 4 * l);
    if (c.wid == 7) STAMP_AT(32 + l * 8 + 2 + !(l & 1));
    BAR();
    WAIT_V0();
    keep(t); keep(t2);
    if (l == 3 && c.bid == 0 && c.wid == 7) {
#pragma unroll 1
        for (int i = 0; i < 4; ++i) poll_flags(c.ws, ep, (unsigned)WS_FLAGQ + (i * 64 + c.lane) * FQS, true, 1u);
        if (c.lane == 0) __hip_atomic_store((GAS u64*)(c.ws + WS_EPOCH), ep + 64, __ATOMIC_RELAXED, __HIP_MEMORY_SCOPE_AGENT);
    }
    if (RS0 && l == 3) BAR();
}

DI void wo_consume(const bool B, const Ctx& c0, const bf16_t* xin, bf16_t* xout, int l) {
    u32x4 res[2][2];
    const Ctx c = fresh(c0);
    LAS char* smem = c.smem;
    const int lane = c.lane, w = c.wid, fr = lane & 15, fq = lane >> 4;
    const char* obase = c.ws + WS_O + (size_t)l * (8u << 20);
    int ovo = (c.pw * 8 + w * 2) * 1024 + lane * 16;
    asm volatile("" : "+v"(ovo));
    constexpr int NP = WNQ, R = 3;
    static_assert(!HO && !HX && !HS && !RLD && BPR == 1, "layout");
    auto ro = __builtin_amdgcn_make_buffer_rsrc((void*)obase, 0, 4u << 20, 0x00020000);
    asm volatile("" : "+s"(ro));
    u32x4 xa[NP][2];
    s16x4 wf[R][8];
    f32x4 acc[2][4];
    unsigned bw[8];
    int coff, rmo, spr;
    u32x4 on = {0x3f803f80u, 0x3f803f80u, 0x3f803f80u, 0x3f803f80u};
    unsigned sto, xpo, coff1, cnta = M_CNT, one = 1;
    u64 xv, b0, b1, am;
    auto rs = __builtin_amdgcn_make_buffer_rsrc(xout, 0, S_ * D_ * 2, 0x00020000);
    auto rst = __builtin_amdgcn_make_buffer_rsrc(c.ws + WS_STATS + (size_t)((l + 1) & 3) * 524288, 0, 524288, 0x00020000);
    auto prep2 = [&]() {
        sto = fq == (fr >> 2) && l < 3 ? (unsigned)((c.pw * 16 + c.jw) * 1024 + (w * 32 + fr) * 8) : 0x80000000u;
        xpo = x_post(c);
        coff = (int)pick(l == 3, (unsigned)rmo, (unsigned)coff);
        coff1 = coff + (l == 3 ? xrow<0>(0, 16) : 2048);
        spr = l == 3 ? 64 : 1024;
        asm volatile("" : "+s"(spr));
        b0 = __builtin_amdgcn_ballot_w64(fr & 1); b1 = __builtin_amdgcn_ballot_w64(fr & 2); am = __builtin_amdgcn_ballot_w64(x_act(c, !B));
        asm volatile("" : "+v"(on), "+v"(sto), "+v"(xpo), "+v"(coff), "+v"(coff1), "+v"(xv), "+v"(cnta), "+v"(one));
        asm volatile("" : "+s"(rs), "+s"(rst), "+s"(b0), "+s"(b1), "+s"(am));
    };
    auto prep = [&]() {
        int ln = lane;
        asm volatile("" : "+v"(ln));
        const int fr = ln & 15, fq = ln >> 4, q4 = fr >> 2, p4 = ln & 3;
        coff = (c.pw * 16 + c.jw) * 16384 + w * 4096 + (((fq & 1) * 2 + (fq >> 1)) * 16 + fr) * 16;
        rmo = xrow<0>(c.pw, w * 32 + fr) + xcol<0>(c.jw) + (fq & 1) * 32 + (fq >> 1) * 16;
        asm volatile("" : "+v"(coff), "+v"(rmo));
        const int fb = (q4 & 2) | ((fq & 1) << 2);
        const unsigned swp = (B && w >= 2) ? 2048u : 0u;
#pragma unroll
        for (int cc = 0; cc < 4; ++cc) {
            const unsigned b = (unsigned)((4 * fq + q4) * 128 + (((2 * cc + (p4 >> 1)) ^ fb) << 4) + (p4 & 1) * 8);
            bw[2 * cc] = b + swp; bw[2 * cc + 1] = b + (swp ^ 2048u);
            asm volatile("" : "+v"(bw[2 * cc]), "+v"(bw[2 * cc + 1]));
        }
    };
    auto start = [&](int mt, int pr) {
            {
                auto r0 = __builtin_amdgcn_permlane16_swap(res[mt][pr][0], res[mt][pr][2], false, false);
                auto r1 = __builtin_amdgcn_permlane16_swap(res[mt][pr][1], res[mt][pr][3], false, false);
#pragma unroll
                for (int h = 0; h < 2; ++h) {
                    acc[mt][2 * pr + h] = (f32x4){bf_lo(r0[h]), bf_hi(r0[h]), bf_lo(r1[h]), bf_hi(r1[h])};
                    asm volatile("" : "+v"(acc[mt][2 * pr + h]));
                }
            }
    };
    auto rdw = [&](auto ic) {
        constexpr int i = decltype(ic)::value, h = i >> 3, k = i & 7;
        wf[h % R][k] = tr_read(lds(bw[k]) + (h & 15) * 4096);
        if constexpr (h == 15) { bw[k] += 65536; asm volatile("" : "+v"(bw[k])); }
    };
    {
        auto resid = [&]() {
            const unsigned roff = pick(l != 0, (unsigned)coff, (unsigned)rmo);
            const char* xp = (const char*)xin;
#pragma unroll
            for (int i = 0; i < 4; ++i) aload4c(res[i >> 1][i & 1], roff, xp + (i >> 1) * (l ? 2048 : xrow<0>(0, 16)) + (i & 1) * (l ? 1024 : 64));
            SCHED();
        };
        unsigned opo = o_post(c), oca = B ? M_CNT : M_CNT + 8, old = 0, msk = B ? 3u : 7u;
        xv = lds_epoch(smem) + (unsigned)(l + 2);
        u64 ov = xv + (unsigned)(3 * l + 2);
        prep();
        asm volatile("" : "+v"(opo), "+v"(oca), "+v"(ov), "+v"(old), "+v"(one));
        asm volatile("" : "+s"(msk));
        SCHED();
        if (RBP) resid();
        static_for<0, 16>([&](auto ic) { rdw(ic); SCHED(); });
        if (!P2A) prep2();
        SCHED();
        wait_v<RBP ? 4 : 0>();
        if (lane == 0) asm volatile("ds_add_rtn_u32 %0, %1, %2\n\ts_waitcnt lgkmcnt(0)" : "+v"(old) : "v"(oca), "v"(one) : "memory");
        if ((__builtin_amdgcn_readfirstlane(old) & msk) == msk) { if (lane < 16) post(c.ws, opo, ov); }
        if (w == 0) STAMP_AT(l * 8 + 4);
        SCHED();
        if (!RBP) resid();
        if (P2A == 1) prep2();
        SCHED();
        if (w == 3) STAMP_AT(32 + l * 8 + 5);
        BAR();
    }
    FENCE();
    SCHED();
    if (w == 0) STAMP_AT(l * 8 + 5);
    int so = 0;
    auto next = [&](int j) { so += j == 15 ? 131072 - 15 * 262144 : 262144; asm volatile("" : "+s"(so)); };
    auto ldo = [&](int j, int mt) { xa[j % NP][mt] = __builtin_amdgcn_raw_buffer_load_b128(ro, ovo + mt * 1024, so, 16); };
#pragma unroll
    for (int h = 0; h < NP; ++h) {
        ldo(h, 0);
        SCHED();
        if (h > SH0 && h <= SH0 + 2) { start(h - SH0 - 1, 0); SCHED(); }
        ldo(h, 1);
        if (!WAL || h + 1 < NP) next(h);
        SCHED();
        if (P2A >= 2 && h == P2A - 2) { prep2(); SCHED(); }
        if (h == SH0) {
            wait_v<2 * SH0 + 2>();
            asm volatile("" : "+v"(res[0][0]), "+v"(res[0][1]), "+v"(res[1][0]), "+v"(res[1][1]));
        }
        if (h > SH0 && h <= SH0 + 2) start(h - SH0 - 1, 1);
        SCHED();
    }
    int d0 = 262144, d1 = 131072 - 15 * 262144;
    asm volatile("" : "+s"(d0), "+s"(d1));
    static_assert(WEP || !WAL, "last step");
    static_for<0, WEP ? 31 : 32>([&](auto jc) {
        constexpr int j = decltype(jc)::value;
        constexpr int c0 = j < 30 ? 16 + 8 * j : 256, c1 = j < 30 ? c0 + 8 : 256;
        if constexpr (j + NP == 16) {
            static_assert(c0 <= 8 * 20, "weights that landed before the attention");
            if (!B) BAR();
            FENCE();
            asm volatile("" : "+v"(ovo));
            SCHED();
        }
        if constexpr (j == 0 || j + NP == 16) ALIGN8();
        constexpr int nv = WAL == 1 || j == 0 || j + NP > 32 ? 2 * (NP - 1 < 31 - j ? NP - 1 : 31 - j) : 2 * NP - 3;
        __builtin_amdgcn_s_waitcnt(0x0870 | nv);
        const int sp = so;
        static_assert(!RS0 || (RS0 + NP >= 32 && RS0 < 31 && QW_SLOT <= (RS0 - 1) * 4096), "R0");
        if constexpr (RS0 && j == RS0) BAR();
        else asm volatile("s_add_i32 %0, %1, %2" : "=&s"(so) : "s"(sp), "s"(j + NP == 16 ? d1 : d0) : "scc");
        SCHED();
        static_for<0, 8>([&](auto mc) {
            constexpr int m = decltype(mc)::value, mt = m >> 2, nt = mt ? 3 - (m & 3) : m & 3;
            const bf16x8 wv = __builtin_shufflevector(wf[j % R][2 * nt], wf[j % R][2 * nt + 1], 0, 1, 2, 3, 4, 5, 6, 7);
            if constexpr (j < 31) asm volatile("v_mfma_f32_16x16x32_bf16 %0, %1, %2, %0" : "+v"(acc[mt][nt]) : "v"(wv), "v"(xa[j % NP][mt]));
            else acc[mt][nt] = MFMA(wv, __builtin_bit_cast(bf16x8, xa[j % NP][mt]), acc[mt][nt]);
            SCHED();
            if constexpr (c0 < c1 && m < 7) { rdw(std::integral_constant<int, c0 + m>{}); SCHED(); }
            if constexpr (c0 < c1 && m == 6) rdw(std::integral_constant<int, c0 + 7>{});
            if constexpr (m == WL1 && j > 0 && j - 1 + NP < 32) xa[(j + NP - 1) % NP][1] = __builtin_amdgcn_raw_buffer_load_b128(ro, ovo + 1024, sp, 16);
            if constexpr (m == WL1 + 4 && j + NP < 32) ldo(j, 0);
            SCHED();
        });
    });
    const bf16x8 ones = __builtin_bit_cast(bf16x8, on);
    u32x4 o[2][2];
    f32x4 sm[2], g[2];
    const f32x4 zero = {0.f, 0.f, 0.f, 0.f};
    auto sel = [&](float a, float b, u64 m) { return __builtin_amdgcn_inverse_ballot_w64(m) ? b : a; };
    auto stats = [&](int mt) {
        float qa = sel(g[mt][0], g[mt][1], b0), qb = sel(g[mt][2], g[mt][3], b0);
        asm volatile("" : "+v"(qa), "+v"(qb));
        SCHED();
        if (WEP) asm volatile("" :: "v"(sm[1][1]), "v"(sm[1][2]), "v"(sm[1][3]), "v"(o[1][0]), "v"(o[1][1]), "v"(on));
        __builtin_amdgcn_raw_buffer_store_b64((u32x2){__float_as_uint(sm[mt][0]), __float_as_uint(sel(qa, qb, b1))}, rst, sto + mt * 128, 0, 16);
        SCHED();
    };
    u32x2 pk[2];
    auto last = [&](int mt, int nt) {
        acc[mt][nt] = MFMA(__builtin_shufflevector(wf[31 % R][2 * nt], wf[31 % R][2 * nt + 1], 0, 1, 2, 3, 4, 5, 6, 7), __builtin_bit_cast(bf16x8, xa[31 % NP][mt]), acc[mt][nt]);
        SCHED();
    };
    auto cv = [&](int mt, int pr) {
        pk[0] = pack4(acc[mt][2 * pr]); pk[1] = pack4(acc[mt][2 * pr + 1]);
        asm volatile("" : "+v"(pk[0]), "+v"(pk[1]));
        SCHED();
    };
    auto st = [&](int mt, int pr) {
        o[mt][pr] = widen(pk[0], pk[1]);
        __builtin_amdgcn_raw_buffer_store_b128(o[mt][pr], rs, mt ? coff1 : coff, pr ? spr : 0, 16);
        SCHED();
    };
    auto gq = [&](int mt, int pr, bool f) { const bf16x8 of = __builtin_bit_cast(bf16x8, o[mt][pr]); g[mt] = MFMA(of, of, f ? zero : g[mt]); SCHED(); };
    auto gs = [&](int mt, int pr, bool f) { sm[mt] = MFMA(ones, __builtin_bit_cast(bf16x8, o[mt][pr]), f ? zero : sm[mt]); SCHED(); };
    WAIT_L0();
    last(0, 0); last(0, 1); last(0, 2); last(0, 3);
    last(1, 3); cv(0, 0);
    last(1, 2); st(0, 0);
    last(1, 1); cv(0, 1);
    last(1, 0); st(0, 1);
    if (w == 0) STAMP_AT(l * 8 + 6);
    gq(0, 0, true); cv(1, 1);
    gs(0, 0, true); st(1, 1);
    gq(0, 1, false); cv(1, 0);
    gs(0, 1, false); st(1, 0);
    gq(1, 1, true);
    gs(1, 1, true);
    gq(1, 0, false);
    gs(1, 0, false);
    stats(0);
    stats(1);
    if (OWN && (OWA ? l < 3 : !B)) {
        const unsigned la = QA_OFF + ((unsigned)coff & 16383u);
#pragma unroll
        for (int i = 0; i < 4; ++i) *(LAS u32x4*)(lds(la) + i * 1024) = o[i >> 1][i & 1];
        FENCE();
    }
    if (l < 3) {
        unsigned old = 0;
        if (!RS0 || RN1 > RN0) BAR();
        wait_v<XE2>();
        if (lane == 0) asm volatile("ds_add_rtn_u32 %0, %1, %2\n\ts_waitcnt lgkmcnt(0)" : "+v"(old) : "v"(cnta), "v"(one) : "memory");
        if ((__builtin_amdgcn_readfirstlane(old) & 3) == 3) {
            if (__builtin_amdgcn_inverse_ballot_w64(am)) post(c.ws, xpo, xv);
        }
    }
    if (w == 0) STAMP_AT(l * 8 + 7);
}

__global__ __launch_bounds__(512, 2) __attribute__((aligned(4096))) void k_main(Params P) {
    extern __shared__ __attribute__((aligned(16))) char smem_[];
    KP kp = (KP)__builtin_amdgcn_kernarg_segment_ptr();
    const char *x0, *w0, *wo;
    Ctx c;
    c.smem = (LAS char*)smem_;
    c.lane = threadIdx.x & 63;
    c.wid = __builtin_amdgcn_readfirstlane(threadIdx.x >> 6);
    c.bid = blockIdx.x;
    const bool loader = c.wid >= 4;
    unsigned va, vw;
    auto enter = [&](const bool LD) {
        if (LD) asm volatile("s_load_dwordx2 %0, %4, 0x0\n\ts_load_dwordx2 %1, %4, 0x8\n\ts_load_dwordx2 %2, %4, 0x10\n\ts_load_dwordx2 %3, %4, 0x20 ; loader" : "=&s"(x0), "=&s"(w0), "=&s"(c.ws), "=&s"(wo) : "s"(kp));
        else asm volatile("s_load_dwordx2 %0, %4, 0x0\n\ts_load_dwordx2 %1, %4, 0x8\n\ts_load_dwordx2 %2, %4, 0x10\n\ts_load_dwordx2 %3, %4, 0x20" : "=&s"(x0), "=&s"(w0), "=&s"(c.ws), "=&s"(wo) : "s"(kp));
        if (LD) asm volatile("; loader" : "+s"(c.bid), "+s"(c.wid)); else asm volatile("; consumer" : "+s"(c.bid), "+s"(c.wid));
        if (LD) asm volatile("; loader" : "+v"(c.lane)); else asm volatile("; consumer" : "+v"(c.lane));
        static_assert(QXM == 8 && WXM == 4 && ALT == 1, "map");
        c.pq = (((c.bid & 1) * 8) ^ PQX) + ((c.bid >> 3) & 7); c.jq = (c.bid & 6) * 2 + (c.bid >> 6);
        const int w = c.wid, lr = c.lane >> 3, lc = c.lane & 7;
        va = (unsigned)((c.pq * 128 + 8 * (w & 3) + lr) * (D_ * 2) + ((lc ^ lr) << 4));
        vw = (unsigned)((8 * (w & 3) + lr) * 6144 + c.jq * 128 + ((lc ^ ((lr & 2) | ((w & 1) << 2))) << 4));
        asm volatile("" : "+v"(va), "+v"(vw));
        c.pw = ((c.pq & 8) ^ 8) + ((c.bid >> 1) & 1) * 4 + ((c.bid >> 3) & 3); c.jw = (c.bid & 4) * 2 + (c.bid >> 5); c.cw = 0;
        if (UNI) { c.pw = c.pq ^ 8; c.jw = c.jq; }
        if (LD) {
            asm volatile("" : "+s"(c.pw), "+s"(c.jw));
            asm volatile("s_waitcnt lgkmcnt(0)" : "+s"(x0), "+s"(w0), "+s"(c.ws), "+s"(wo));
            if (ESL) __builtin_amdgcn_s_sleep(ESL);
        } else {
            int t0 = first_tile(first_order(c.pq, c.jq), 0);
            asm volatile("" : "+s"(t0));
            asm volatile("s_waitcnt lgkmcnt(0) ; c" : "+s"(x0), "+s"(w0), "+s"(c.ws), "+s"(wo));
            if (ST0 && c.wid == 3) STAMP_AT(41);
            const char* xb = x0 + (unsigned)(t0 * 128);
            const char* wb = w0 + (unsigned)(t0 * (64 * 6144));
#pragma unroll
            for (int h = 0; h < 2; ++h) {
#pragma unroll
                for (int j = 0; j < 2; ++j) dma16<false>(QA_OFF + (j * 8 + h * 4 + w) * 1024, va, xb + (j * 64 + h * 32) * (D_ * 2));
#pragma unroll
                for (int j = 0; j < 3; ++j) dma16<false>((j * 8 + h * 4 + w) * 1024, vw, wb + h * (32 * 6144) + j * 2048);
            }
            if (ST0 && c.wid == 1) STAMP_AT(43);
            if (w == 0 && c.lane < 2) touch(M_EP, (unsigned)(WS_EPOCH + c.lane * 4), c.ws);
            if (w == 1 && c.lane < 6) *(LAS unsigned*)(c.smem + (c.lane < 4 ? M_CNT + c.lane * 4 : M_GO - 16 + c.lane * 4)) = 0u;
        }
        asm volatile("" : "+s"(kp), "+s"(c.bid), "+s"(c.ws));
    };
    if (c.wid == CPW) {
        const char* pc; int len;
        asm volatile("s_getpc_b64 %0\n.Lcpf_a:\n\ts_mov_b32 %1, .Lcpf_end-.Lcpf_a-4" : "=s"(pc), "=s"(len));
        const int o = min((int)(((c.bid >> 3) & 7) * 64 + c.lane) * 128, len);
        asm volatile("s_mov_b32 m0, %0\n\ts_nop 3\n\tglobal_load_lds_dword %1, %2" ::"s"(M_TCH), "v"(o), "s"(pc) : "memory");
    }
    const char* xi;
    auto step = [&](int l) {
        asm volatile("" : "+s"(kp), "+s"(c.ws), "+s"(c.jw), "+s"(c.bid));
        if (HYB) {
            const int base = (c.bid & 1) * 8 + (c.bid & 2) * 2 + ((c.bid >> 3) & 3), jb = (c.bid & 4) * 2 + (c.bid >> 5);
            const int pa = ((((c.bid & 1) * 8) ^ PQX) + ((c.bid >> 3) & 7)) ^ MA2, ja = (c.bid & 6) * 2 + (c.bid >> 6);
            c.pw = OWA && l == 0 ? pa : base ^ ((MW >> (4 * l)) & 15);
            c.jw = OWA && l == 0 ? ja : jb;
            c.pq = (l & 1) ? pa : base ^ (l ? MB3 : MB1);
            c.jq = (l & 1) ? ja : jb;
            if (UNI) { c.pq = (l & 1) ? pa : pa ^ 8; c.jq = ja; if (l < 2 || UNI == 2) { c.pw = c.pq ^ 8; c.jw = ja; } }
        } else { c.pq ^= 8; c.pw ^= 8; } c.cw = CPY ? c.jw & 7 : 0;
        xi = c.ws + WS_X + (unsigned)(l * (8u << 20));
        wo = (const char*)((l & 1) ? kp->wo0 : kp->wo1);
        if (c.cw == 3) wo = c.ws + WS_CO + (((l & 1) ? 2 : 0) + (c.jw >> 3)) * 4096;
    };
    if (loader) {
        enter(true);
        xi = x0;
            qkv_load<false, true>(c, xi, w0, wo, 0, kp, va, vw);
#pragma unroll 1
        for (int l = 0;;) {
            asm volatile("" : "+s"(l));
            if (!(l & 1)) attn_a<true>(c, l, wo, kp);
            wo_load(c, l, nullptr, wo);
            if (l == 3) break;
            step(l);
            ++l;
            if (l == 2) { qkv_load<false, false>(c, xi, (const char*)kp->wqkv0, wo, l); }
            else { qkv_load<true, false>(c, xi, (const char*)kp->wqkv1, wo, l); }
        }
    } else {
        enter(false);
        xi = x0;
        bf16_t* xo = (bf16_t*)(c.ws + WS_X);
#pragma unroll 1
        for (int l = 0; l < 4; ++l) {
            if (l & 1) { qkv_consume<true>(c, l, kp); }
            else {
                qkv_consume<false>(c, l, kp, va, vw);
                attn_a<false>(c, l, wo, kp);
            }
            wo_consume(l & 1, c, (const bf16_t*)xi, xo, l);
            step(l);
            xo = l == 2 ? kp->out : (bf16_t*)(c.ws + WS_X + (unsigned)((l + 1) * (8u << 20)));
            asm volatile("" : "=v"(va), "=v"(vw));
        }
    }
    asm volatile(".Lcpf_end:");
}

extern "C" void kernel_launch(void* const* d_in, const int* in_sizes, int n_in, void* d_out, int out_size, void* d_ws, size_t ws_size,
                              hipStream_t stream) {
    Params P;
    P.x0 = (const bf16_t*)d_in[0];
    P.wqkv0 = (const bf16_t*)d_in[1];
    P.wo0 = (const bf16_t*)d_in[2];
    P.wqkv1 = (const bf16_t*)d_in[4];
    P.wo1 = (const bf16_t*)d_in[5];
    P.out = (bf16_t*)d_out;
    P.ws = (char*)d_ws;
    (void)hipFuncSetAttribute((const void*)k_main, hipFuncAttributeMaxDynamicSharedMemorySize, LDS_TOTAL);
    k_main<<<256, 512, LDS_TOTAL, stream>>>(P);
}
```

```cpp
#include <hip/hip_runtime.h>
#include <stdint.h>
#include <type_traits>

typedef unsigned short bf16_t;
typedef short bf16x8 __attribute__((ext_vector_type(8)));
typedef short s16x4 __attribute__((ext_vector_type(4)));
typedef float f32x4 __attribute__((ext_vector_type(4)));
typedef float f32x2 __attribute__((ext_vector_type(2)));
typedef float f32x16 __attribute__((ext_vector_type(16)));
typedef unsigned u32x4 __attribute__((ext_vector_type(4)));
typedef unsigned u32x2 __attribute__((ext_vector_type(2)));
#define DI __device__ __forceinline__
#define LAS __attribute__((address_space(3)))
#define GAS __attribute__((address_space(1)))
#define STAMP 0

constexpr int S_ = 2048, D_ = 1024, KT = 16;
constexpr float LN_EPS = 1e-5f;
constexpr float QSCALE = 0.17677669529663687f * 1.4426950408889634f;

#define FQO 0
#define XBO 524288
#define HX 0
#define HO 0
#define HK 0
#define HS 0
#define HM 1
constexpr size_t WS_FLAGQ = FQO;
constexpr size_t WS_FLAGW = 98304;
constexpr size_t WS_EPOCH = 65536;
constexpr size_t WS_CSUM = 131072;
constexpr size_t WS_XBOX = XBO;
constexpr size_t WS_OBOX = 655360;
constexpr size_t WS_KBOX = 720896;
constexpr size_t WS_STATS = 1u << 20;
constexpr size_t WS_X = 4u << 20;
constexpr size_t WS_O = 96u << 20;
constexpr size_t WS_KV = 28u << 20;
constexpr size_t WS_CQ = 36u << 20;
constexpr size_t WS_CO = 42u << 20;
constexpr size_t WS_STAMP = 64u << 20;

template <int H> DI constexpr int hp(int c) { return !H ? c * 1024 : (c >> 2) * 8192 + (HM == 1 ? (c & 3) * 256 : (c & 1) * 128 + (c & 2) * 256); }
template <int H> DI constexpr int hl(int u) { return !H ? u : (HM == 1 ? u + (u & ~127) : u + (u & ~255)); }
template <int H> DI constexpr int hv(int lane) { return !H ? lane * 16 : (lane >> 3) * 1024 + (lane & 7) * 16; }
template <int H> DI constexpr int xrow(int p, int r) { return H == 0 ? (p * 128 + r) * 2048 : (H == 1 ? (p * 128 + r) * 4096 : (H == 2 ? p * 524288 + r * 256 : p * 262144 + r * 128)); }
template <int H> DI constexpr int xcol(int t) { return H == 0 ? t * 128 : (H == 1 ? hl<1>(t * 128) : (H == 2 ? t * 32768 : t * 16384)); }
#define WTC 2
#define WT0 1
#define SPR 1
#define TOPJ 0x2000
#define WV0 1
#define FQS 256
#define SNK 1
#define WBE 1
#define WAE 1
#define KSP 0
#define K0E 1
#define TOPS 4
#define BPR 1
#define RLD 0
#define CPF 2
#define WB14 0
#define WB15 5
#define WT13 2
#define ENT 1
#define MBOX 3
#define OBS 256
#define PQX 8
#define ALT 1
#define ALN 1
#define NQA_ 3
#define NQW_ 4
#define LPRIO 3
#define TOUCH 1
#define TSTEP 13
#define XS_ 9
#define ROT0 0
#define WPRE 3
#define PSLEEP 1
#define TW0 5
#define APRIO 1
#define QXM 8
#define WNQ 5
#define WRG 2
#define WBS 1
#define WEP 1
#define WAL 2
#define WL1 0
#define RS0 27
#define RN0 6
#define RN1 6
#define WXM 4
#define CPOS 5
#define LSL 0
#define SAFE_SUM 0x1p80f
#define AB2 0
#define W30S 8
#define CPY 1
#define XST 2
#define XSP 1
#define EPOLL 1
#define RSLP 1
#define APOST 1
#define ASLP 2
#define APIPE 4
#define RBP 0
#define P2A 1
#define SH0 2
#define KPW 4
#define LWA 7
#define WA0 12
#define WA1 22
#define OSL 14
#define OPL 1
#define E30 1
#define HYB 1
#define MW 0x808
#define MB1 0
#define MB3 0
#define MA2 0
#define OWN 1
#define CUP 1
#define TL0B 0
#define OWA 1
#define PRO 0
#define EXP0 0
#define KPP 1
#define HP4 1
#define TWQ 1
#define ERL 1
#define ESL 0
#define DM 0
#define DS 0
#define ELO 0
#define EN0 3
#define EN1 2
#define ST0 0
#define NC 1
#define NCM 1
#define NCB 4
#define AD1 3
#define AD2 0
#define AD3 4
#define TWO 1
#define XSB 10
#define TL0 0
#define TL1 12
#define FDM 1
#define TSP 0
#define CPW 4
#define EXS 1
#define NCH 2
#define XE2 0
constexpr int QA_SLOT = 18432, NQA = NQA_, QW_SLOT = 24576, NQW = NQW_, QA_OFF = 3 * QW_SLOT, QW_TOP = 131072;
DI constexpr int qw_slot(int s) { return s == 3 ? QW_TOP : s * QW_SLOT; }
constexpr int AT_N = 90112, AT_OWN = AT_N + 32768, AT_Q = 73728;
constexpr int MISC = 155648, M_MEAN = MISC, M_RSTD = MISC + 640, M_CS = MISC + 1280, M_CNT = MISC + 2816, M_EP = MISC + 2832, M_GO = MISC + 2848, M_W0 = MISC + 2852, M_H0 = QA_OFF + NQA * QA_SLOT, M_TCH = MISC + 3072, M_TAB = MISC + 4096, M_XCH = 131072;
constexpr int LDS_TOTAL = 163840;
static_assert(M_H0 + 2048 <= QW_TOP && QW_TOP + QW_SLOT <= MISC && AT_N + 65536 <= MISC && AT_Q + 16384 <= AT_N, "lds");

DI unsigned cvt_pk_bf16(float lo, float hi) {
    typedef __bf16 bf2 __attribute__((ext_vector_type(2)));
    bf2 r; r[0] = (__bf16)lo; r[1] = (__bf16)hi;
    return __builtin_bit_cast(unsigned, r);
}
DI u32x2 pack4(const f32x4& v) { return (u32x2){cvt_pk_bf16(v[0], v[1]), cvt_pk_bf16(v[2], v[3])}; }
DI float bf_lo(unsigned u) { return __uint_as_float(u << 16); }
DI float bf_hi(unsigned u) { return __uint_as_float(u & 0xffff0000u); }
DI u32x4 widen(const u32x2& a, const u32x2& b) {
    auto r0 = __builtin_amdgcn_permlane16_swap(a[0], b[0], false, false);
    auto r1 = __builtin_amdgcn_permlane16_swap(a[1], b[1], false, false);
    return (u32x4){r0[0], r1[0], r0[1], r1[1]};
}
#define MFMA(a, b, c) __builtin_amdgcn_mfma_f32_16x16x32_bf16((a), (b), (c), 0, 0, 0)
#define WAIT_L0() __builtin_amdgcn_s_waitcnt(0xC07F)
#define WAIT_V0() asm volatile("s_waitcnt vmcnt(0)" ::: "memory")
#define BAR() __builtin_amdgcn_s_barrier()
#define SCHED() __builtin_amdgcn_sched_barrier(0)
template <int N> DI void wait_v() { asm volatile("s_waitcnt vmcnt(%0)" ::"i"(N) : "memory"); }
template <int I, int N, class F> DI void static_for(F&& f) { if constexpr (I < N) { f(std::integral_constant<int, I>{}); static_for<I + 1, N>(f); } }

template <bool SC1> DI void dma16(unsigned lds, unsigned voff, const char* sbase) {
    if (SC1) asm volatile("s_mov_b32 m0, %0\n\ts_nop 3\n\tglobal_load_lds_dwordx4 %1, %2 sc1" ::"s"(lds), "v"(voff), "s"(sbase) : "memory");
    else asm volatile("s_mov_b32 m0, %0\n\ts_nop 3\n\tglobal_load_lds_dwordx4 %1, %2" ::"s"(lds), "v"(voff), "s"(sbase) : "memory");
}
DI void touch(unsigned lds, unsigned voff, const char* sbase) {
    asm volatile("s_mov_b32 m0, %0\n\ts_nop 3\n\tglobal_load_lds_dword %1, %2 sc1" ::"s"(lds), "v"(voff), "s"(sbase) : "memory");
}
DI s16x4 tr_read(const LAS char* p) {
    typedef short v4i16_t __attribute__((ext_vector_type(4)));
    return __builtin_bit_cast(s16x4, __builtin_amdgcn_ds_read_tr16_b64_v4i16((LAS v4i16_t*)p));
}
DI LAS char* lds(unsigned off) { return (LAS char*)(uintptr_t)off; }
DI unsigned long long rstamp() { unsigned long long t; asm volatile("s_memrealtime %0\n\ts_waitcnt lgkmcnt(0)" : "=s"(t)::"memory"); return t; }

constexpr int ow_w(int kt) { return CUP == 3 ? (kt < 1 ? 6 : 6 * kt) : kt < 2 ? 0 : (CUP == 0 ? (kt < 5 ? 8 * (kt - 1) : 30) : (CUP == 1 ? 8 * kt - 10 : (kt == 2 ? 6 : (kt == 3 ? 13 : (kt == 4 ? 21 : 30))))); }
template <int NAI, int NWI, bool FIRST> struct Sched {
    int w[KT];
    constexpr Sched() : w{} {
        int endA[KT] = {}, endW[KT] = {};
        int n = 0;
        if (FIRST) {
            if (ENT == 0) { n += 2; endA[0] = n; n += 3; endW[0] = n; }
            n += NAI; endA[1] = n; n += NWI; endW[1] = n;
            if (ERL >= 5) { n += NAI; endA[2] = n; }
            if (ERL == 2 || ERL == 3 || ERL == 5) { n += NWI; endW[2] = n; }
        } else {
            for (int s = 0; s < WPRE; ++s) { n += NWI; endW[s] = n; }
            for (int s = 0; s < 2; ++s) { n += NAI; endA[s] = n; }
        }
        for (int kt = 0; kt < KT; ++kt) {
            const int need = endA[kt] > endW[kt] ? endA[kt] : endW[kt];
            w[kt] = (n - need) > 63 ? 63 : (n - need);
            if (kt + 2 < KT && !(FIRST && ERL >= 5 && kt == 0)) { n += NAI; endA[kt + 2] = n; }
            if (FIRST && (ERL == 4 || ERL == 6) && kt == 0) { n += NWI; endW[2] = n; }
            if (OWN && !FIRST && (NAI == 5 || OWA) && kt < 5) {
                for (int p = ow_w(kt); p < ow_w(kt + 1); ++p) { ++n; endW[3 + p / 6] = n; }
                continue;
            }
            if (FIRST && ERL < 2 && kt < 5) {
                if (kt < 4) { n += NWI; endW[kt + 2] = n; }
                if (kt >= 3) n += NWI / 2;
                if (kt == 4) { endW[6] = n; n += NWI; endW[7] = n; }
                continue;
            }
            if (!FIRST && WPRE == 2 && kt < 2) { n += NWI; endW[kt + 2] = n; }
            if ((FIRST || WPRE == 3) && kt == 0) { n += NWI; endW[3] = n; }
            if (kt >= 1 && kt + 3 < KT) { n += NWI; endW[kt + 3] = n; }
            if (kt == 13) n += WT13;
            if (kt == 14) n += (NAI == 5 || WA0 > 12 ? 12 : WA0) - WT13 + (NAI == 5 ? WB14 : 0);
        }
    }
};

struct Params {
    const bf16_t* x0;
    const bf16_t* wqkv0;
    char* ws;
    const bf16_t* wqkv1;
    const bf16_t* wo0;
    const bf16_t* wo1;
    bf16_t* out;
};
typedef const __attribute__((address_space(4))) Params* KP;
DI int rot_first(int pq, int jq) {
    const int pr = 2 * (pq & 7) + (pq >> 3);
    if (ROT0 == 4) return (pq + (jq ^ 3)) & 15;
    if (ROT0 == 5) return (pr + (jq ^ 3)) & 15;
    return (ROT0 == 0 ? pq + jq : (ROT0 == 1 ? pr + jq : (ROT0 == 2 ? pr + 4 * (jq & 3) + (jq >> 2) : pq + 4 * (jq & 3) + (jq >> 2)))) & 15;
}

struct Order { int base, r, re; };
template <int N> DI Order make_order(int base, int rot) { Order o; o.base = base; o.r = (rot - base) & 15; o.re = o.r > N ? o.r : N; return o; }
DI Order first_order(int pq, int jq) { return make_order<NC>((pq & NCM) * (16 / (NCM + 1)) + NCB, rot_first(pq, jq)); }
template <int N = NC> DI int first_tile(const Order& o, int s) {
    const int v = o.re + s - N;
    if (s < N) { int a = N + o.base, b = s + o.base; if (N - s == 1) asm volatile("" : "+s"(a)); return (o.r == s ? a : b) & 15; }
    return ((s == N ? o.r : (v > 15 ? v - (16 - N) : v)) + o.base) & 15;
}

typedef unsigned long long u64;
DI u64 lds_epoch(LAS char* smem) { return *(const LAS u64*)(smem + M_EP); }
DI bool reached(u64 v, u64 ep, unsigned target) { return (v - ep - target) < (u64)(64u - target); }
DI void poll_flags(const char* ws, u64 ep, unsigned off, bool active, unsigned target) {
    const GAS u64* f = (const GAS u64*)(ws + off);
#pragma unroll 1
    for (unsigned spins = 0; spins < (1u << 22); ++spins) {
        const u64 v = __hip_atomic_load(f, __ATOMIC_RELAXED, __HIP_MEMORY_SCOPE_AGENT);
        if (__all(!active || reached(v, ep, target))) break;
        __builtin_amdgcn_s_sleep(PSLEEP);
    }
    asm volatile("" ::: "memory");
}
struct Ask { u64 v[4]; };
DI void keep(Ask& t) { asm volatile("" : "+v"(t.v[0]), "+v"(t.v[1]), "+v"(t.v[2]), "+v"(t.v[3])); }
#define ASK(r) "global_atomic_or_x2 " r ", %[off], %[z], %[ws] sc0 sc1\n\t"
#define CHK(r) "s_waitcnt vmcnt(%[n])\n\tv_lshl_add_u64 %[d], " r ", 0, %[nb]\n\tv_cmp_gt_u64 vcc, %[span], %[d]\n\ts_cmp_eq_u64 vcc, exec\n\ts_cbranch_scc1 .Lpe%=\n\ts_sleep %[sl]\n\t"
template <bool NEG = false> DI void poll_rmw(Ask& t, const char* ws, unsigned off, bool act, u64 base, unsigned span) {
    u64 z, d, nb = NEG ? base : 0 - base, sp = span;
    unsigned cnt;
    asm volatile("" : "=v"(t.v[0]), "=v"(t.v[1]), "=v"(t.v[2]), "=v"(t.v[3]));
    if (act) {
        if (APIPE == 4)
            asm volatile("v_mov_b64 %[z], 0\n\ts_mov_b32 %[cnt], 0x80000\n\ts_nop 3\n\t" ASK("%[a]") "s_sleep %[sl]\n\t" ASK("%[b]") "s_sleep %[sl]\n\t" ASK("%[c]") "s_sleep %[sl]\n"
                         ".Lpl%=:\n\t" ASK("%[e]") CHK("%[a]") ASK("%[a]") CHK("%[b]") ASK("%[b]") CHK("%[c]") ASK("%[c]") CHK("%[e]")
                         "s_sub_u32 %[cnt], %[cnt], 1\n\ts_cbranch_scc0 .Lpl%=\n.Lpe%=:"
                         : [a] "+v"(t.v[0]), [b] "+v"(t.v[1]), [c] "+v"(t.v[2]), [e] "+v"(t.v[3]), [d] "=&v"(d), [z] "=&v"(z), [cnt] "=&s"(cnt)
                         : [off] "v"(off), [ws] "s"(ws), [nb] "v"(nb), [span] "s"(sp), [sl] "i"(ASLP), [n] "i"(3) : "vcc", "scc", "memory");
        else if (APIPE == 2)
            asm volatile("v_mov_b64 %[z], 0\n\ts_mov_b32 %[cnt], 0x100000\n\ts_nop 3\n\t" ASK("%[a]") "s_sleep %[sl]\n"
                         ".Lpl%=:\n\t" ASK("%[b]") CHK("%[a]") ASK("%[a]") CHK("%[b]")
                         "s_sub_u32 %[cnt], %[cnt], 1\n\ts_cbranch_scc0 .Lpl%=\n.Lpe%=:"
                         : [a] "+v"(t.v[0]), [b] "+v"(t.v[1]), [d] "=&v"(d), [z] "=&v"(z), [cnt] "=&s"(cnt)
                         : [off] "v"(off), [ws] "s"(ws), [nb] "v"(nb), [span] "s"(sp), [sl] "i"(ASLP), [n] "i"(1) : "vcc", "scc", "memory");
        else
            asm volatile("v_mov_b64 %[z], 0\n\ts_mov_b32 %[cnt], 0x200000\n\ts_nop 3\n"
                         ".Lpl%=:\n\t" ASK("%[a]") CHK("%[a]")
                         "s_sub_u32 %[cnt], %[cnt], 1\n\ts_cbranch_scc0 .Lpl%=\n.Lpe%=:"
                         : [a] "+v"(t.v[0]), [d] "=&v"(d), [z] "=&v"(z), [cnt] "=&s"(cnt)
                         : [off] "v"(off), [ws] "s"(ws), [nb] "v"(nb), [span] "s"(sp), [sl] "i"(ASLP), [n] "i"(0) : "vcc", "scc", "memory");
    }
}
DI void post(char* ws, unsigned off, u64 v) {
    if (APOST) asm volatile("s_nop 4\n\tglobal_atomic_swap_x2 %0, %1, %2 sc1" :: "v"(off), "v"(v), "s"(ws) : "memory");
    else __hip_atomic_store((GAS u64*)(ws + off), v, __ATOMIC_RELAXED, __HIP_MEMORY_SCOPE_AGENT);
}
struct Ctx {
    LAS char* smem;
    char* ws;
    int lane, wid;
    int pq, jq;
    int pw, jw;
    int bid;
    int cw;
};
DI Ctx fresh(const Ctx& c0) {
    Ctx c = c0;
    asm volatile("" : "+v"(c.lane));
    asm volatile("" : "+s"(c.wid), "+s"(c.pq), "+s"(c.jq), "+s"(c.pw), "+s"(c.jw), "+s"(c.ws), "+s"(c.cw));
    return c;
}
DI unsigned o_post(const Ctx& c) { return (MBOX & 2) ? (unsigned)WS_OBOX + (c.pq * 16 + (c.lane & 15)) * OBS + c.jq * 8 : (unsigned)WS_FLAGQ + (c.pq * 16 + c.jq) * FQS; }
DI unsigned o_find(const Ctx& c) { return (MBOX & 2) ? (unsigned)WS_OBOX + (c.pw * 16 + c.jw) * OBS + (c.lane & 15) * 8 : (unsigned)WS_FLAGQ + (c.pw * 16 + (c.lane & 15)) * FQS; }
DI unsigned x_post(const Ctx& c) { return (MBOX & 1) ? (unsigned)WS_XBOX + (c.pw * 16 + (c.lane & 31)) * (2 * FQS) + (c.lane & 16) * (FQS / 16) + c.jw * 8 : (unsigned)WS_FLAGW + (c.pw * 16 + c.jw) * 128; }
DI bool x_act(const Ctx& c, bool halo) { return c.lane < ((MBOX & 1) ? (halo && c.pw < 15 ? 32 : 16) : 1); }
#define STAMP_AT(slot) do { } while (0)

#define LSTAMP(i) do { } while (0)
#define LSTORE(ph) do { } while (0)
#define LMARK() do { } while (0)
#define LPUT(i) do { } while (0)
#define ASTORE() do { } while (0)
#define QSTAMP(i) do { } while (0)
#define QSTORE() do { } while (0)
#define FENCE() asm volatile("" ::: "memory")
#define ALIGN8() do { if (ALN) { asm volatile(".p2align 3"); SCHED(); } } while (0)
DI float addf(float a, float b) { float r; asm("v_add_f32 %0, %1, %2" : "=v"(r) : "v"(a), "v"(b)); return r; }
DI float fold16(float a, float b) {
    auto r = __builtin_amdgcn_permlane16_swap(__float_as_uint(a), __float_as_uint(b), false, false);
    float x = __uint_as_float(r[0]) + __uint_as_float(r[1]);
    asm volatile("" : "+v"(x));
    return x;
}
DI float fold32(float a, float b) {
    auto r = __builtin_amdgcn_permlane32_swap(__float_as_uint(a), __float_as_uint(b), false, false);
    float x = __uint_as_float(r[0]) + __uint_as_float(r[1]);
    asm volatile("" : "+v"(x));
    return x;
}
DI void aload4c(u32x4& d, unsigned voff, const char* sb) { asm volatile("s_nop 4\n\tglobal_load_dwordx4 %0, %1, %2 sc1" : "=v"(d) : "v"(voff), "s"(sb) : "memory"); }
DI void aload2p(u32x2& d, unsigned voff, const char* sb) { asm volatile("s_nop 4\n\tglobal_load_dwordx2 %0, %1, %2 sc1" : "+v"(d) : "v"(voff), "s"(sb) : "memory"); }
DI void aload2c(u32x2& d, unsigned voff, const char* sb) { asm volatile("s_nop 4\n\tglobal_load_dwordx2 %0, %1, %2 sc1" : "=v"(d) : "v"(voff), "s"(sb) : "memory"); }
DI unsigned wo_src(int q, int lane, int jw) { return (unsigned)((8 * q + (lane >> 3)) * (D_ * 2) + jw * 128 + (((lane & 7) ^ ((lane >> 3) & 6)) << 4)); }
DI unsigned pick(bool f, unsigned a, unsigned b) { asm volatile("" : "+v"(a), "+v"(b)); return f ? a : b; }
DI unsigned wo_w_off(const Ctx& c) { return pick(c.cw == 3, (unsigned)((c.wid & 3) * 1024 + c.lane * 16), wo_src(c.wid & 3, c.lane, c.jw)); }
DI unsigned wq_src(int ib, int lane, int jq) {
    const int seg = ib >> 3, ii = ib & 7, lr = lane >> 3;
    return (unsigned)((8 * ii + lr) * 6144 + seg * 2048 + jq * 128 + (((lane & 7) ^ ((lr & 2) | ((ii & 1) << 2))) << 4));
}
template <int S0, int S1> DI void wo_w_issue(const Ctx& c, const char* W, unsigned vw) {
    const int lw = c.wid & 3;
#pragma unroll
    for (int s = S0; s < S1; ++s) dma16<CPY != 0>(s * 4096 + lw * 1024, vw, W + (size_t)((2 * (s & 15) + (s >> 4)) * 32) * (D_ * 2));
}

template <bool HALO, bool FIRST> DI void qkv_load(const Ctx& c0, const char* xin, const char* W, const char* Wo, int l, KP kp = nullptr, unsigned va0 = 0, unsigned vw0 = 0) {
    const Ctx c = fresh(c0);
    asm volatile("" : "+s"(xin), "+s"(W), "+s"(Wo));
    LAS char* smem = c.smem;
    constexpr int NAI = HALO ? 5 : 4, NWI = 6, XS = FIRST ? XS_ : XSB, NR = HALO ? 144 : 128;
    constexpr Sched<NAI, NWI, FIRST> SC{};
    static_assert(NQA == 3 && NQW == 4, "schedule");
    const int lw = c.wid - 4, lane = c.lane, lr = lane >> 3, lc = lane & 7, fr = lane & 15, fq = lane >> 4;
    const int row0 = c.pq * 128;
    const bool cq_ = CPY && !FIRST && (c.jq & 7) == 3;
    if (cq_) W = c.ws + WS_CQ + ((HALO ? 0 : 2) + (c.jq >> 3)) * 24576;
    asm volatile("" : "+s"(W));
    constexpr int SP = FIRST ? NC : (HALO ? NCH : 0), T0 = OWN && HALO ? TL0B : TL0;
    static_assert(FIRST || SP + DS <= T0 + 2 + TSP, "one flag for both kinds of sums");
    constexpr bool OW = OWN && !FIRST && (HALO || OWA);
    const int rot = FIRST ? rot_first(c.pq, c.jq) : (l == 1 ? c.pq : SP);
    const bool diag = OW && rot == c.jq;
    Order od = FIRST ? first_order(c.pq, c.jq) : make_order<SP>(0, rot);
    if (FIRST) asm volatile("" : "+s"(od.base));
    if (FIRST || (HALO && !OW)) asm volatile("" : "+s"(od.r), "+s"(od.re));
    u64 tab = 0;
    if (OW) {
        static_assert(!OW || ((!HALO || SP == 2) && HYB == 1 && MB1 == 0 && MB3 == 0 && MW == 0x808), "order");
        auto drop = [](u64 x, int p) { const u64 m = (1ull << (4 * p)) - 1; return (x & m) | ((x >> 4) & ~m); };
        tab = drop(0xfedcba9876543210ull, c.jq);
        if (l == 1 && !diag) {
            const int cc = __builtin_ctz(~((1u << c.jq) | (1u << rot))), k = 4 * (rot - (rot > c.jq ? 1 : 0) - (rot > cc ? 1 : 0));
            tab = drop(tab, cc - (cc > c.jq ? 1 : 0));
            tab = (((tab >> k) | (tab << (56 - k))) << 4) | (u64)cc;
        }
        tab = (tab << 4) | (u64)c.jq;
        asm volatile("" : "+s"(tab));
    }
    auto tile = [&](int s) { return OW ? (int)((tab >> (4 * s)) & 15) : (FIRST || HALO ? first_tile<SP>(od, s) : s); };
    unsigned va[5], vw[NWI];
#pragma unroll
    for (int j = 0; j < 4; ++j) va[j] = (unsigned)(FIRST ? va0 + j * 32 * (D_ * 2) : c.pq * 262144 + (j * 4 + lw) * 1024 + lane * 16);
    if (HALO) va[4] = (unsigned)((c.pq > 0 ? (c.pq - 1) * 262144 + 14336 : 0) + lw * 512 + lane * 16);
#pragma unroll
    for (int j = 0; j < NWI; ++j) vw[j] = FIRST ? vw0 + (j & 1) * (32 * 6144) + (j >> 1) * 2048 : pick(cq_, (unsigned)((j * 4 + lw) * 1024 + lane * 16), wq_src(j * 4 + lw, lane, c.jq));
    auto issueA = [&](int s, int slot) {
        const char* b = xin + (unsigned)(tile(s) * (FIRST ? 128 : 16384));
#pragma unroll
        for (int j = 0; j < 4; ++j) dma16<true>(QA_OFF + slot * QA_SLOT + (j * 4 + lw) * 1024, va[j], b);
        if (HALO) { if (lane < 32) dma16<true>(QA_OFF + slot * QA_SLOT + 16384 + lw * 512, va[4], b); }
    };
    auto issueW = [&](int s, int slot, int j0 = 0, int j1 = NWI) {
        const char* b = W + (unsigned)(tile(s) * (64 * 6144));
#pragma unroll
        for (int j = j0; j < j1; ++j) dma16<CPY && !FIRST>(qw_slot(slot) + (j * 4 + lw) * 1024, vw[j], b);
    };
    auto ahead = [&]() {
        const int r3 = (lane * 43) >> 7;
        const unsigned o = (unsigned)((((rot - (c.pq & 7) + 10) * 64 + (c.pq & 7) * 48 + lw * 12 + r3) & 1023) * 6144 + (lane - 3 * r3) * 2048 + c.jq * 128);
        if (lane < 36 && (WTC != 2 || (c.jq & 7) == 3)) asm volatile("s_mov_b32 m0, %0\n\ts_nop 3\n\tglobal_load_lds_dword %1, %2" ::"s"(M_TCH), "v"(o), "s"(W) : "memory");
    };
    if (ST0 && FIRST && lw == 1) STAMP_AT(47);
    if (FIRST && ERL) {
        SCHED();
        issueA(1, 1); issueW(1, 1);
        SCHED();
    }
    const bool docs = FIRST || (HALO && l == 1);
    const int t = lw * 64 + lane;
    static_assert(MBOX & 1, "residual flag");
    unsigned foff, ftg, so, ho, fo_, co, l3, l4, vwo, wt[3], cso, xt, xt1, xso, tp, tl;
    u32x4 on;
    auto addr1 = [&]() {
        int lane = c.lane;
        asm volatile("" : "+v"(lane));
        const int fr = lane & 15, fq = lane >> 4;
        const int ph = c.pq > 0 ? c.pq - 1 : 0;
        foff = pick(lane != 32, (unsigned)WS_FLAGQ + pick(lane < 16, c.pq * 16 + lane, pick(lane < 32, fr * 16 + c.jq, ph * 16 + 14 + (lane & 1))) * FQS, (unsigned)WS_XBOX + (c.pw * 16 + c.jw) * (2 * FQS) + c.jw * 8);
        ftg = lane != 32 ? (unsigned)(4 * l + 1) : (unsigned)(l + 1);
        so = FIRST ? (unsigned)((c.pq * 16 + fq * 4) * 1024 + (lw * 32 + fr * 2) * 8) : (unsigned)((c.pq * 16 + (lane >> 2)) * 1024 + (c.jq * 8 + lw * 2 + (lane & 1)) * 8);
        ho = FDM ? (unsigned)(262144 + ((lw * 36 + lane < 16 ? ph * 128 + 128 : c.pq * 128) + lw * 36 + lane - 16) * 8)
                 : (unsigned)(262144 + pick(lane < 32, c.pq * 128 + lw * 32 + lane, ph * 128 + 112 + lw * 4 + (lane & 3)) * 8);
        fo_ = pick(lane < 2, (unsigned)(262144 + (c.pq * 128 + c.jq * 8 + lw * 2 + lane) * 8), 0x80000000u);
        const int cq = lw * 12 + (fr < 12 ? fr : 11);
        co = (unsigned)((fq * 4 * 3072 + (cq >> 4) * 1024 + c.jq * 64 + (cq & 15) * 4) * 4);
        l3 = (unsigned)(M_CS + (HALO ? 768 : 0) + (cq * 4 + fq) * 4);
        l4 = (unsigned)(M_MEAN + pick(lane < 32, 16 + lw * 32 + (FIRST ? fr * 2 + fq : lane), lw * 4 + (lane & 3)) * 4);
        asm volatile("" : "+v"(foff), "+v"(ftg), "+v"(so), "+v"(co), "+v"(l3), "+v"(l4));
        if (!FIRST) asm volatile("" : "+v"(ho), "+v"(fo_));
        SCHED();
    };
    auto addr2 = [&]() {
        int lane = c.lane;
        asm volatile("" : "+v"(lane));
        const int fr = lane & 15, fq = lane >> 4;
        const int q4 = fr >> 2, p4 = lane & 3, fb = (q4 & 2) | ((fq & 1) << 2);
#pragma unroll
        for (int i = 0; i < 3; ++i) {
            const int T = 3 * lw + i;
            wt[i] = (unsigned)((OW && diag ? 0 : qw_slot(SP % NQW)) + (T >> 2) * 8192 + (8 * fq + q4) * 128 + (((2 * (T & 3) + (p4 >> 1)) ^ fb) << 4) + (p4 & 1) * 8);
        }
        const int T = 3 * lw + fq;
        cso = (unsigned)((((HALO ? 16 : 0) + rot) * 3072 + (T >> 2) * 1024 + c.jq * 64 + (T & 3) * 16 + fr) * 4);
        xt = (unsigned)(QA_OFF + (SP % NQA) * QA_SLOT + (lw * 32 + fr) * 128 + ((fq ^ (fr & 7)) << 4));
        xt1 = xt ^ 64;
        xso = (unsigned)hl<HS>((c.pq * 16 + rot) * 1024 + (lw * 32 + fr) * 8);
        on = (u32x4){0x3f803f80u, 0x3f803f80u, 0x3f803f80u, 0x3f803f80u};
        if (FIRST || HALO) asm volatile("" : "+v"(wt[0]), "+v"(wt[1]), "+v"(wt[2]), "+v"(cso), "+v"(on));
        if (FIRST) asm volatile("" : "+v"(xt), "+v"(xt1), "+v"(xso));
        SCHED();
    };
    auto addr3 = [&]() {
        Ctx c2 = c;
        asm volatile("" : "+v"(c2.lane));
        asm volatile("" : "+s"(c2.bid));
        vwo = wo_w_off(c2);
        tp = (unsigned)(pick(c2.lane < 30, (c2.bid >> 3) * 30 + c2.lane, (c2.bid >> 3) * 16 + c2.lane - 30 + (int)(WS_O >> 16)) * 65536 + 65536 - 128);
        tl = (unsigned)(lw == 1 ? ((c2.bid >> 3) * 2 + c2.lane) * 65536 : c2.bid * 8192 + (TWO && !(EXS && (c.jq & 7) == 3) ? c2.lane * 128 : 0));
        asm volatile("" : "+v"(vwo));
        if (FIRST) asm volatile("" : "+v"(tp), "+v"(tl));
        SCHED();
    };
    if (!FIRST || AD1 < 0) addr1();
    if (FIRST && ERL >= 5) { issueA(2, 2); SCHED(); }
    if (FIRST && (ERL == 2 || ERL == 5)) { issueW(2, 2); SCHED(); }
    if (!FIRST || AD2 < 0) addr2();
    if (!FIRST || AD3 < 0) addr3();
#pragma unroll
    for (int j = 0; j < NAI; ++j) asm volatile("" : "+v"(va[j]));
#pragma unroll
    for (int j = 0; j < NWI; ++j) asm volatile("" : "+v"(vw[j]));
    SCHED();
    int tv = 0; unsigned long long lt = 0;
    u64 fv = 0;
    unsigned xfo;
    if (FIRST) {
        LSTAMP(55);
    } else {
        static_assert(CPY == 1 && RS0 > 0 && RN0 == NWI && WPRE >= 3 && (FIRST || SC.w[0] == NAI), "order");
        unsigned vx[2], go = M_GO;
        xfo = (unsigned)WS_XBOX + (c.pq * 16 + c.jq) * (2 * FQS) + (HALO && c.pq > 0 ? (lane & 16) * (FQS / 16) : 0) + fr * 8;
        u64 xv = lds_epoch(smem) + (unsigned)(l + 1);
#pragma unroll
        for (int i = 0; i < 2; ++i) vx[i] = pick(cq_, (unsigned)(((2 * lw + i) * 4 + 3) * 1024 + lane * 16), wq_src((2 * lw + i) * 4 + 3, lane, c.jq));
        asm volatile("" : "+v"(vx[0]), "+v"(vx[1]), "+v"(xfo), "+v"(xv), "+v"(go));
        fv = (((u64)(unsigned)__builtin_amdgcn_readfirstlane((unsigned)(xv >> 32)) << 32) | (unsigned)__builtin_amdgcn_readfirstlane((unsigned)xv)) + (unsigned)(3 * l);
        asm volatile("" : "+s"(fv));
        BAR();
        issueW(0, 0);
        if (OW) asm volatile("s_mov_b64 exec, 1\n\ts_waitcnt vmcnt(0)\n\tds_add_u32 %0, %1 offset:4\n\ts_mov_b64 exec, -1" :: "v"(go), "v"(1u) : "memory");
        auto word = [&](int off) { return __builtin_amdgcn_readfirstlane(*(volatile LAS int*)(lds(go) + off)); };
        auto until = [&](int off, int v) {
#pragma unroll 1
            for (unsigned spins = 0; spins < (1u << 24); ++spins) { if (word(off) == v) break; }
            FENCE();
        };
        static_assert(WPRE == 3, "stages");
        constexpr int WP = OW && CUP == 3 ? 4 : 3;
        int t1 = tile(1), t2 = tile(2);
        if (!OW) asm volatile("" : "+s"(t1), "+s"(t2));
        auto stage = [&](int s) {
            const char* b = W + (unsigned)((OW ? tile(s) : (s == 1 ? t1 : t2)) * (64 * 6144));
            unsigned d = (unsigned)((s == 3 ? QW_TOP : s * QW_SLOT) + lw * 1024), dx = d + (unsigned)(lw * 7168 + 3072);
            asm volatile("" : "+s"(d), "+s"(dx), "+s"(b));
#pragma unroll
            for (int j = 0; j < NWI; ++j) dma16<true>(d + j * 4096, vw[j], b);
#pragma unroll
            for (int i = 0; i < 2; ++i) dma16<true>(dx + i * 4096, vx[i], b);
        };
        auto first2 = [&]() {
            __builtin_amdgcn_s_setprio(LPRIO);
            if (OW) { issueA(1, 1); if (HALO && lane < 32) dma16<true>(M_H0 + lw * 512, va[4], xin + (unsigned)(c.jq * 16384)); }
            else { issueA(0, 0); issueA(1, 1); }
        };
        if (OW && docs && diag) {
            until(M_W0 - M_GO, (OWA ? 4 * l : 2 * l + 2));
            bf16x8 wf[3][2];
            f32x4 d[3];
#pragma unroll
            for (int i = 0; i < 3; ++i)
#pragma unroll
                for (int ks = 0; ks < 2; ++ks) {
                    const s16x4 lo = tr_read(lds(wt[i]) + ks * 4096), hi = tr_read(lds(wt[i]) + ks * 4096 + 512);
                    wf[i][ks] = __builtin_shufflevector(lo, hi, 0, 1, 2, 3, 4, 5, 6, 7);
                }
#pragma unroll
            for (int i = 0; i < 3; ++i) d[i] = MFMA(__builtin_bit_cast(bf16x8, on), wf[i][1], MFMA(__builtin_bit_cast(bf16x8, on), wf[i][0], ((f32x4){0.f, 0.f, 0.f, 0.f})));
            float d0 = d[0][0], d1 = d[1][0], d2 = d[2][0];
            asm volatile("" : "+v"(d0), "+v"(d1), "+v"(d2));
            const float v = fq == 0 ? d0 : (fq == 1 ? d1 : d2);
            auto rc = __builtin_amdgcn_make_buffer_rsrc(c.ws + WS_CSUM, 0, 2 * 16 * 3072 * 4, 0x00020000);
            if (lane < 48) __builtin_amdgcn_raw_buffer_store_b32(__float_as_uint(v), rc, cso, 0, 16);
        }
        if (lw == 3) {
            if (!EPOLL) until(M_CNT - M_GO, 8 * l);
            Ask t;
            poll_rmw(t, c.ws, xfo, lane < (HALO && c.pq > 0 ? 32 : 16), xv, 63 - l);
            *(volatile LAS int*)lds(go) = l;
            until(M_CNT - M_GO, 8 * l);
            first2();
            STAMP_AT(32 + l * 8 + 0);
            wait_v<OW && HALO ? NAI + 1 : NAI>();
            keep(t);
        } else {
            until(M_CNT - M_GO, 8 * l);
            if (RSLP) __builtin_amdgcn_s_sleep(RSLP);
            int ns = 1;
#pragma unroll 1
            for (; ns < WP; ++ns) {
                if (word(0) == l) break;
                stage(ns);
            }
            until(0, l);
            first2();
            const int left = WPRE - ns;
#pragma unroll 1
            for (; ns < WP; ++ns) stage(ns);
            if (OW) { }
            else if (left == 0) wait_v<NAI>();
            else if (left == 1) wait_v<NAI + 8>();
            else if (left == 2) wait_v<NAI + 16>();
            else wait_v<NAI + 24>();
        }
        FENCE();
    }
    if (FIRST && ERL == 3) issueW(2, 2);
    if (ST0 && FIRST && lw == 2) STAMP_AT(57);
    if (FIRST && !ERL) BAR();
    __builtin_amdgcn_s_setprio(LPRIO);
    if (FIRST && !ERL) { LSTAMP(57); issueA(1, 1); issueW(1, 1); LSTAMP(58); }
    u32x4 st[4], cs[4]; u32x2 sh, fl; float rs_, rq_, hs;
    asm volatile("" : "=v"(fl));
    f32x4 d[3], sm[2], g[2];
    bf16x8 wf[3][2], xf[2][2];
    const bool fact = FIRST ? lane < 32 : ((lane < 16 || lane >= (docs ? 16 : 32)) && lane <= (HALO ? 34 : 32));
    static_assert(XS + 3 <= (FIRST ? 12 : 13) && SPR == 1, "aux schedule");
    auto side = [&](auto ktc) {
        constexpr int kt = decltype(ktc)::value;
        {
            constexpr int C0 = FIRST ? 12 : XS + 2, C1 = FIRST ? 13 : XS + 3, C2 = 13;
            auto acc4 = [](u32x4& a, const u32x4& b) {
#pragma unroll
                for (int i = 0; i < 4; ++i) a[i] = __float_as_uint(addf(__uint_as_float(a[i]), __uint_as_float(b[i])));
                asm volatile("" : "+v"(a));
            };
            auto fin = [&]() {
                rs_ *= -1.f / 1024.f;
                asm volatile("" : "+v"(rs_));
                rq_ = __builtin_amdgcn_rsqf(fmaxf(rq_ * (1.f / 1024.f) - rs_ * rs_, 0.f) + LN_EPS);
                asm volatile("" : "+v"(rq_));
            };
            if constexpr (FIRST && kt == XS + 2) {
                asm volatile("" : "+v"(st[0]), "+v"(st[1]), "+v"(st[2]));
                acc4(st[0], st[1]); acc4(st[0], st[2]);
            }
            if constexpr (FIRST && kt == XS + 3) { asm volatile("" : "+v"(st[3])); acc4(st[0], st[3]); }
            if constexpr (kt == C0) {
                if (docs) { asm volatile("" : "+v"(cs[0]), "+v"(cs[1])); acc4(cs[0], cs[1]); }
            }
            if constexpr (FIRST && kt == 12) {
                asm volatile("s_nop 1" : "+v"(st[0]));
                rs_ = fold16(__uint_as_float(st[0][0]), __uint_as_float(st[0][2])); rq_ = fold16(__uint_as_float(st[0][1]), __uint_as_float(st[0][3]));
                rs_ = fold32(rs_, rs_); rq_ = fold32(rq_, rq_);
            }
            if constexpr (FIRST && kt == 13) fin();
            if constexpr (!FIRST && kt == T0 + 2) {
                asm volatile("" : "+v"(sh));
                hs = fold16(__uint_as_float(sh[0]), __uint_as_float(sh[1]));
                hs = fold32(hs, hs);
                hs += __uint_as_float(__builtin_amdgcn_update_dpp(0u, __float_as_uint(hs), 0x124, 0xf, 0xf, false));
                hs += __uint_as_float(__builtin_amdgcn_update_dpp(0u, __float_as_uint(hs), 0x128, 0xf, 0xf, false));
                asm volatile("" : "+v"(hs));
            }
            if constexpr (!FIRST && kt == T0 + 2 + TSP) {
                auto hh = __builtin_amdgcn_permlane16_swap(__float_as_uint(hs), __float_as_uint(hs), false, false);
                rs_ = __uint_as_float(hh[0]); rq_ = __uint_as_float(hh[1]);
                asm volatile("" : "+v"(rs_), "+v"(rq_));
                fin();
                auto rf = __builtin_amdgcn_make_buffer_rsrc(c.ws + WS_STATS + (size_t)l * 524288, 0, 524288, 0x00020000);
                __builtin_amdgcn_raw_buffer_store_b64((u32x2){__float_as_uint(rs_), __float_as_uint(rq_)}, rf, fo_, 0, 16);
            }
            if constexpr (kt == C1) {
                if (docs) { asm volatile("" : "+v"(cs[2]), "+v"(cs[3])); acc4(cs[2], cs[3]); acc4(cs[0], cs[2]); }
            }
            if constexpr (kt == C2) {
                if (docs) {
                    asm volatile("s_nop 1" : "+v"(cs[0]));
                    hs = fold32(fold16(__uint_as_float(cs[0][0]), __uint_as_float(cs[0][1])), fold16(__uint_as_float(cs[0][2]), __uint_as_float(cs[0][3])));
                }
            }
            if constexpr (kt == 13) {
                static_assert(WV0 == 1, "wait");
                if (FIRST || !FDM || docs) WAIT_V0();
                if (!FIRST && !FDM) { asm volatile("" : "+v"(sh)); rs_ = __uint_as_float(sh[0]); rq_ = __uint_as_float(sh[1]); }
                if ((FIRST || !FDM) && lane < (HALO ? 36 : 32)) {
                    *(LAS float*)lds(l4) = rs_;
                    *(LAS float*)(lds(l4) + (M_RSTD - M_MEAN)) = rq_;
                }
                if (docs) *(LAS float*)lds(l3) = hs;
                FENCE();
            }
        }
    };
    static_for<0, KT>([&](auto ktc) {
        constexpr int kt = decltype(ktc)::value;
        LSTAMP(3 * kt);
        if constexpr (PRO && !FIRST && kt == 1) { if (lw == 1) STAMP_AT(l == 1 ? 44 : (l == 2 ? 57 : 58)); }
        if constexpr (FIRST || kt > 0) wait_v<SC.w[kt] + (!FIRST && kt == T0 + 1)>();
        LSTAMP(3 * kt + 1);
        if constexpr (kt == XS) {
            if (lw == 0) {
                asm volatile("" : "+v"(fl));
                const u64 ep = fv - (unsigned)(4 * l + 1);
                if (!__all(!fact || reached(__builtin_bit_cast(u64, fl), ep, ftg))) poll_flags(c.ws, ep, foff, fact, ftg);
            }
        }
        if constexpr (kt == 14) asm volatile("s_waitcnt lgkmcnt(0)" ::: "memory");
        if constexpr (!(OW && kt == 0)) BAR();
        FENCE();
        LMARK();
        LSTAMP(3 * kt + 2);
        if constexpr (TOPJ & (1 << kt)) side(ktc);
        if constexpr (kt == (FIRST ? SP + DS : T0 + 2 + TSP) + 3) {
            if (lw == 0 && lane == 0) __hip_atomic_store((GAS u64*)(c.ws + WS_FLAGQ + (c.pq * 16 + c.jq) * FQS), fv, __ATOMIC_RELAXED, __HIP_MEMORY_SCOPE_AGENT);
        }
        if constexpr (kt == XS + 1 - NQA) {
            if (lw == 0) { if (fact) aload2p(fl, foff, c.ws); }
        }
        {
            const char* sb = c.ws + WS_STATS + (size_t)l * 524288;
            const char* cb = c.ws + WS_CSUM + (HALO ? 16 * 3072 * 4 : 0);
            constexpr int sa = 3 * (kt - XS), ca = FIRST ? 2 * (kt - XS - 1) : 2 * (kt - XS), cn = 2;
            if constexpr (FIRST) {
#pragma unroll
                for (int i = sa; i < sa + 3; ++i) if (i >= 0 && i < 4) aload4c(st[i], so, sb + i * 1024);
            }
            if constexpr (!FIRST && kt == T0) aload2c(sh, so, sb);
            if constexpr (!FIRST && kt == TL1) {
                static_assert(TL1 <= 12, "covered by the wait of step 14");
                if (FDM) { if (lane < 36) { touch(M_MEAN + lw * 144, ho, sb); touch(M_RSTD + lw * 144, ho, sb + 4); } }
                else aload2c(sh, ho, sb);
            }
            if constexpr (ca >= 0 && ca < 4) {
                if (docs) {
#pragma unroll
                    for (int i = ca; i < ca + cn; ++i) aload4c(cs[i], co, cb + i * 12288);
                }
            }
        }
        if constexpr (kt == SP) {
            if (docs && !diag) {
#pragma unroll
                for (int i = 0; i < 3; ++i)
#pragma unroll
                    for (int ks = 0; ks < 2; ++ks) {
                        const s16x4 lo = tr_read(lds(wt[i]) + ks * 4096), hi = tr_read(lds(wt[i]) + ks * 4096 + 512);
                        wf[i][ks] = __builtin_shufflevector(lo, hi, 0, 1, 2, 3, 4, 5, 6, 7);
                    }
                if (FIRST) {
#pragma unroll
                    for (int mt = 0; mt < 2; ++mt)
#pragma unroll
                        for (int ks = 0; ks < 2; ++ks) xf[mt][ks] = *(const LAS bf16x8*)(lds(ks ? xt1 : xt) + mt * 2048);
                }
            }
            SCHED();
        }
        if constexpr (kt + 2 < KT && !(FIRST && ERL >= 5 && kt == 0)) issueA(kt + 2, (kt + 2) % NQA);
        LPUT(3 * kt + 2);
        if constexpr (kt == SP + DM) {
            SCHED();
            if (docs && !diag) {
                const bf16x8 ones = __builtin_bit_cast(bf16x8, on);
                const f32x4 zero = {0.f, 0.f, 0.f, 0.f};
#pragma unroll
                for (int ks = 0; ks < 2; ++ks) {
#pragma unroll
                    for (int i = 0; i < 3; ++i) d[i] = MFMA(ones, wf[i][ks], ks ? d[i] : zero);
                    if (FIRST) {
#pragma unroll
                        for (int mt = 0; mt < 2; ++mt) { sm[mt] = MFMA(ones, xf[mt][ks], ks ? sm[mt] : zero); g[mt] = MFMA(xf[mt][ks], xf[mt][ks], ks ? g[mt] : zero); }
                    }
                }
            }
            SCHED();
        }
        if constexpr (OW && kt < 5) {
            static_assert(ow_w(kt + 1) <= 6 * (kt + 1) && ow_w(5) == 30, "free slots");
#pragma unroll
            for (int s = 3; s < 8; ++s) {
                const int j0 = ow_w(kt) - 6 * (s - 3), j1 = ow_w(kt + 1) - 6 * (s - 3);
                if (j1 > 0 && j0 < 6) issueW(s, s % NQW, j0 < 0 ? 0 : j0, j1 > 6 ? 6 : j1);
            }
        } else if constexpr (FIRST && ERL < 2 && kt < 5) {
            if constexpr (kt < 4) issueW(kt + 2, (kt + 2) % NQW);
            if constexpr (kt == 3) issueW(6, 2, 0, NWI / 2);
            if constexpr (kt == 4) { issueW(6, 2, NWI / 2, NWI); issueW(7, 3); }
        } else {
            if constexpr (WPRE == 2 && kt < 2) issueW(kt + 2, kt + 2);
            if constexpr (FIRST && (ERL == 4 || ERL == 6) && kt == 0) issueW(2, 2);
            if constexpr ((FIRST || WPRE == 3) && kt == 0) issueW(3, 3);
            if constexpr (kt >= 1 && kt + 3 < KT) issueW(kt + 3, (kt + 3) % NQW);
        }
        if constexpr (FIRST && kt == WT0) ahead();
        if constexpr (FIRST && kt == TSTEP) {
            if (lw == 1 && lane < 46) touch(M_TCH, tp, c.ws);
        }
        if constexpr (FIRST && kt == TW0) {
            if (lw == 0) touch(M_TCH, tl, Wo);
            else if (lw == 1) { asm volatile("" : "+s"(kp)); const char* outp = (const char*)kp->out; if (lane < 2) touch(M_TCH, tl, outp); }
        }
        if constexpr (kt == SP && DM > 0) {
            SCHED();
            if (docs) {
                asm volatile("" : "+v"(wf[0][0]), "+v"(wf[0][1]), "+v"(wf[1][0]), "+v"(wf[1][1]), "+v"(wf[2][0]), "+v"(wf[2][1]));
                if (FIRST) asm volatile("" : "+v"(xf[0][0]), "+v"(xf[0][1]), "+v"(xf[1][0]), "+v"(xf[1][1]));
            }
        }
        if constexpr (kt == SP + DS) {
            SCHED();
            if (docs && !diag) {
                if (FIRST) {
                    auto rst = __builtin_amdgcn_make_buffer_rsrc(c.ws + WS_STATS, 0, 524288, 0x00020000);
                    const bool b0 = fr & 1, b1 = fr & 2;
                    u32x2 sv[2];
#pragma unroll
                    for (int mt = 0; mt < 2; ++mt) {
                        float qa = b0 ? g[mt][1] : g[mt][0], qb = b0 ? g[mt][3] : g[mt][2];
                        asm volatile("" : "+v"(qa), "+v"(qb));
                        sv[mt] = (u32x2){__float_as_uint(sm[mt][0]), __float_as_uint(b1 ? qb : qa)};
                    }
                    if (fq == (fr >> 2)) {
                        __builtin_amdgcn_raw_buffer_store_b64(sv[0], rst, xso, 0, 16);
                        __builtin_amdgcn_raw_buffer_store_b64(sv[1], rst, xso + hl<HS>(128), 0, 16);
                    }
                }
                float d0 = d[0][0], d1 = d[1][0], d2 = d[2][0];
                asm volatile("" : "+v"(d0), "+v"(d1), "+v"(d2));
                const float v = fq == 0 ? d0 : (fq == 1 ? d1 : d2);
                auto rc = __builtin_amdgcn_make_buffer_rsrc(c.ws + WS_CSUM, 0, 2 * 16 * 3072 * 4, 0x00020000);
                if (lane < 48) __builtin_amdgcn_raw_buffer_store_b32(__float_as_uint(v), rc, cso, 0, 16);
                asm volatile("s_waitcnt lgkmcnt(0)" ::: "memory");
            }
        }
        if constexpr (FIRST && kt == 1) {
            if (lw == 0) {
                const u32x2 e = *(const LAS u32x2*)(smem + M_EP);
                const u64 es = ((u64)(unsigned)__builtin_amdgcn_readfirstlane(e[1]) << 32) | (unsigned)__builtin_amdgcn_readfirstlane(e[0]);
                fv = es + (unsigned)(4 * l + 1);
                asm volatile("" : "+s"(fv));
            }
        }
        if constexpr (FIRST && kt == AD1) addr1();
        if constexpr (FIRST && kt == AD2) addr2();
        if constexpr (FIRST && kt == AD3) addr3();
        if constexpr (!(TOPJ & (1 << kt))) side(ktc);
        if constexpr (kt == 13) wo_w_issue<0, WT13>(c, Wo, vwo);
        if constexpr (kt == 14) { wo_w_issue<WT13, (HALO || WA0 > 12) ? 12 : WA0>(c, Wo, vwo); if (HALO) wo_w_issue<23, 23 + WB14>(c, Wo, vwo); }
        if constexpr (kt == 15) {
            if (HALO ? WBE < 2 : WA0 > 12) wo_w_issue<12, HALO ? 18 : WA0>(c, Wo, vwo);
            if (HALO) wo_w_issue<23 + WB14, 23 + WB14 + WB15>(c, Wo, vwo);
        }
    });
    __builtin_amdgcn_s_setprio(0);
    BAR();
    LMARK(); LPUT(48);
    LSTAMP(48);
    LSTORE(0);
    if (HALO ? WBE < 1 : WAE < 1) wo_w_issue<18, HALO ? 23 : 20>(c, Wo, vwo);
    if (HALO) {
        static_assert(!WBS || (WBE == 1 && WB14 == 0 && WRG <= 12), "Wo weights of the first twelve steps are older than the last 6 + WB15");
        if (WBS) wait_v<6 + WB15>();
        BAR();
        if (WBE) wo_w_issue<WBE < 2 ? 18 : 12, 23>(c, Wo, vwo);
        wo_w_issue<23 + WB14 + WB15, 32>(c, Wo, vwo);
    }
}

DI float max3(float a, float b, float c) { return fmaxf(fmaxf(a, b), c); }
#define SGB(m, n) __builtin_amdgcn_sched_group_barrier(m, n, 0)
template <bool HALO> struct Tail {
    int f[26], n[4], t[4][26], d[4][27], v[4][27], h3[32], s3[32], o3[33], q3[112];
    constexpr Tail() : f{}, n{}, t{}, d{}, v{}, h3{}, s3{}, o3{}, q3{} {
        const int FB[26] = {0, 1, 2, 3, 4, 5, 6, 7, 8, 9, 10, 11, 12, 13, 14, 15, 24, 25, 19, 23, 18, 22, 17, 21, 16, 20};
        const int FA[24] = {0, 1, 2, 3, 4, 5, 6, 7, 8, 9, 10, 11, 12, 13, 14, 15, 16, 17, 20, 21, 18, 19, 22, 23};
        const int TA[4][26] = {{0, 1, 2, 3, 7, 6, 5, 4, 8, 9, 10, 11, 15, 14, 13, 12, 16, 17, 18, 19, 23, 22, 21, 20, 0, 0}, {4, 8, 9, 10, 11, 15, 14, 13, 12, 16, 17, 18, 19, 23, 22, 21, 20, 0, 1, 2, 3, 7, 6, 5, 0, 0}, {16, 17, 18, 19, 23, 22, 21, 20, 0, 1, 2, 3, 7, 6, 5, 4, 8, 9, 10, 11, 15, 14, 13, 12, 0, 0}, {17, 18, 19, 23, 22, 21, 20, 8, 9, 10, 11, 15, 14, 13, 12, 16, 0, 0, 0, 0, 0, 0, 0, 0, 0, 0}};
        const int DA[4][26] = {{1, 1, 1, 1, 1, 1, 1, 1, 1, 1, 1, 1, 1, 1, 1, 1, 1, 1, 1, 1, 1, 1, 0, 0, 0, 0}, {1, 1, 1, 1, 1, 1, 1, 1, 1, 1, 1, 1, 1, 1, 1, 1, 1, 1, 1, 1, 1, 1, 0, 0, 0, 0}, {1, 1, 1, 1, 1, 1, 1, 1, 1, 1, 1, 1, 1, 1, 1, 1, 0, 0, 0, 0, 0, 0, 0, 0, 0, 0}, {2, 2, 2, 2, 1, 1, 1, 1, 1, 1, 1, 1, 0, 0, 0, 0, 0, 0, 0, 0, 0, 0, 0, 0, 0, 0}};
        const int VA[4][26] = {{1, 1, 0, 0, 0, 0, 1, 1, 1, 1, 1, 1, 1, 1, 1, 1, 1, 1, 1, 1, 1, 1, 2, 2, 0, 0}, {1, 1, 1, 1, 1, 1, 1, 1, 1, 1, 1, 1, 1, 1, 1, 1, 1, 1, 1, 1, 1, 1, 2, 2, 0, 0}, {1, 1, 1, 1, 1, 1, 1, 1, 1, 1, 1, 1, 1, 1, 1, 1, 2, 2, 2, 2, 2, 3, 3, 2, 0, 0}, {0, 0, 0, 0, 1, 1, 1, 1, 1, 1, 1, 1, 2, 2, 2, 2, 0, 0, 0, 0, 0, 0, 0, 0, 0, 0}};
        const int TB[4][26] = {{0, 1, 2, 3, 7, 6, 5, 4, 24, 8, 9, 10, 11, 15, 14, 13, 12, 25, 16, 17, 18, 19, 23, 22, 21, 20}, {24, 8, 9, 10, 11, 15, 14, 13, 12, 25, 16, 17, 18, 19, 23, 22, 21, 20, 0, 1, 2, 3, 7, 6, 5, 4}, {25, 16, 17, 18, 19, 23, 22, 21, 20, 0, 1, 2, 3, 7, 6, 5, 4, 24, 8, 9, 10, 11, 15, 14, 13, 12}, {0, 1, 2, 3, 7, 6, 5, 4, 24, 8, 9, 10, 11, 15, 14, 13, 12, 25, 19, 23, 0, 0, 0, 0, 0, 0}};
        const int DB[4][26] = {{1, 1, 1, 1, 1, 1, 1, 1, 1, 1, 1, 1, 1, 1, 1, 1, 1, 1, 1, 1, 1, 1, 1, 0, 0, 0}, {1, 1, 1, 1, 1, 1, 1, 1, 1, 1, 1, 1, 1, 1, 1, 1, 1, 1, 1, 1, 1, 1, 0, 0, 0, 0}, {1, 1, 1, 1, 1, 1, 1, 1, 1, 1, 1, 1, 1, 1, 1, 1, 1, 1, 1, 1, 1, 1, 1, 0, 0, 0}, {2, 2, 1, 1, 1, 1, 1, 1, 1, 1, 1, 1, 1, 1, 1, 1, 1, 1, 1, 0, 0, 0, 0, 0, 0, 0}};
        const int VB[4][26] = {{1, 1, 1, 1, 1, 1, 1, 1, 1, 1, 1, 1, 1, 1, 1, 1, 1, 1, 1, 1, 1, 1, 1, 2, 2, 2}, {1, 1, 1, 1, 1, 1, 1, 1, 1, 1, 1, 1, 1, 1, 1, 1, 1, 1, 1, 1, 1, 1, 3, 3, 3, 2}, {1, 1, 1, 1, 1, 1, 1, 1, 1, 1, 1, 1, 1, 1, 1, 1, 1, 1, 1, 1, 1, 1, 1, 3, 3, 2}, {0, 0, 1, 1, 1, 1, 1, 1, 1, 1, 1, 1, 1, 1, 1, 1, 1, 1, 1, 2, 0, 0, 0, 0, 0, 0}};
        const int G[32] = {8, 12, 9, 13, 10, 14, 11, 15, 16, 17, 20, 21, 18, 19, 22, 23, 0, 4, 1, 5, 2, 6, 3, 7, 0, 4, 1, 5, 2, 6, 3, 7};
        const int NM = HALO ? 26 : 24;
        n[0] = n[1] = n[2] = NM; n[3] = HALO ? 20 : 16;
        for (int i = 0; i < NM; ++i) f[i] = HALO ? FB[i] : FA[i];
        int vv = 0;
        for (int h = 0; h < 4; ++h) {
            v[h][0] = vv;
            for (int i = 0; i < n[h]; ++i) {
                t[h][i] = HALO ? TB[h][i] : TA[h][i];
                d[h][i + 1] = d[h][i] + (HALO ? DB[h][i] : DA[h][i]);
                vv += HALO ? VB[h][i] : VA[h][i];
                v[h][i + 1] = vv;
            }
        }
        for (int i = 0; i < 32; ++i) { h3[i] = G[i]; s3[i] = i >= 16 && i < 24 ? 0 : 1; }
        for (int i = 3; i < 32; ++i) o3[i + 1] = o3[i] + TOPS;
        int nv = 0, ns = 0;
        for (int k = 0; k < 8 * 14; ++k) {
            const bool top = k % TOPS < 2 || nv == 96;
            if (top && ns < 16 && nv >= 12 * (ns / 2 + 1) && (nv == 96 || k % TOPS == (ns & 1))) { q3[k] = (ns / 2) * 14 + 13 - (ns & 1); ++ns; }
            else { q3[k] = (nv / 12) * 14 + nv % 12; ++nv; }
        }
    }
};
template <bool HALO> DI void qkv_consume(const Ctx& c0, int l, KP kp, unsigned va0 = 0, unsigned vw0 = 0) {
    const Ctx c = fresh(c0);
    if (ST0 && l == 0 && c.wid == 2) STAMP_AT(59);
    LAS char* smem = c.smem;
    const int lane = c.lane, wr = c.wid >> 1, wc = c.wid & 1, fr = lane & 15, fq = lane >> 4;
    const int sw = HALO ? wr : 0;
    f32x4 acc[4][6], ah[2];
#pragma unroll
    for (int mt = 0; mt < 4; ++mt)
#pragma unroll
        for (int nt = 0; nt < 6; ++nt) { acc[mt][nt] = (f32x4){0.f, 0.f, 0.f, 0.f}; if (!(OWN && (HALO || OWA))) asm volatile("" : "+v"(acc[mt][nt])); }
    ah[0] = ah[1] = (f32x4){0.f, 0.f, 0.f, 0.f};
    if (HALO && !OWN) asm volatile("" : "+v"(ah[0]), "+v"(ah[1]));
    const unsigned aoff0 = (unsigned)(QA_OFF + wr * 8192 + (HALO || l ? lane * 16 : fr * 128 + ((fq ^ (fr & 7)) << 4)));
    const unsigned hoff0 = (unsigned)(QA_OFF + 16384 + lane * 16);
    unsigned xm = HALO || l ? 1024u : 64u;
    asm volatile("" : "+s"(xm));
    static_assert(((QA_OFF | QA_SLOT) & 2047) == 0, "halves");
    const int q4 = fr >> 2, p4 = lane & 3;
    const int fb = (q4 & 2) | ((fq & 1) << 2);
    unsigned boff[2];
#pragma unroll
    for (int cc = 0; cc < 2; ++cc)
        boff[cc] = (unsigned)((8 * fq + q4) * 128 + (((2 * (2 * wc + (cc ^ sw)) + (p4 >> 1)) ^ fb) << 4) + (p4 & 1) * 8);
    bf16x8 xa[2][4], xh[2], wb[2][6];
    auto ldfrag = [&](int set, unsigned pa, unsigned ph, unsigned pb0, unsigned pb1, int ks) {
#pragma unroll
        for (int mt = 0; mt < 4; ++mt) xa[set][mt] = *(const LAS bf16x8*)(lds(pa) + mt * 2048);
        if (HALO) xh[set] = *(const LAS bf16x8*)lds(ph);
#pragma unroll
        for (int nt = 0; nt < 6; ++nt) {
            const LAS char* pb = lds((nt & 1) ? pb1 : pb0) + (nt >> 1) * 8192 + ks * 4096;
            const s16x4 lo = tr_read(pb), hi = tr_read(pb + 512);
            wb[set][nt] = __builtin_shufflevector(lo, hi, 0, 1, 2, 3, 4, 5, 6, 7);
        }
    };
    constexpr int NM = HALO ? 26 : 24, NH = HALO ? 17 : 16;
    auto mm = [&](int set, int i0, int i1) {
#pragma unroll
        for (int ii = i0; ii < i1; ++ii) {
            const int i = !HALO || ii < 8 ? ii : (!SNK ? (ii < 10 ? ii + 16 : ii - 2) : (ii == 8 ? 24 : (ii < 17 ? ii - 1 : (ii == 17 ? 25 : ii - 2))));
            const int nt = i >> 2, mt = SNK && (nt & 1) ? 3 - (i & 3) : i & 3;
            if (i < 16) acc[mt][nt] = MFMA(wb[set][nt], xa[set][mt], acc[mt][nt]);
            else if (i < 24) acc[mt][nt] = MFMA(xa[set][mt], wb[set][nt], acc[mt][nt]);
            else if (i == 24) ah[0] = MFMA(wb[set][2], xh[set], ah[0]);
            else ah[1] = MFMA(xh[set], wb[set][4], ah[1]);
        }
    };
    auto interleave = [&]() {
#pragma unroll
        for (int i = 0; i < NH; ++i) { SGB(0x008, 1); SGB(0x100, 1); }
        SCHED();
    };
    unsigned sa = 0, sb = 0;
    unsigned pa0 = aoff0, pa1 = aoff0 ^ xm, ph0 = hoff0, ph1 = hoff0 ^ xm, pb0 = boff[0], pb1 = boff[1];
    asm volatile("" : "+v"(pa0), "+v"(pa1), "+v"(pb0), "+v"(pb1));
    if (HALO) asm volatile("" : "+v"(ph0), "+v"(ph1));
    constexpr int M_CSH = M_CS + (HALO ? 768 : 0);
    unsigned sa0 = (unsigned)(M_MEAN + 64 + (wr * 64 + fr) * 4), sa1 = (unsigned)(M_MEAN + 64 + wr * 256 + fq * 16);
    unsigned sa2 = (unsigned)(M_CSH + wc * 128 + sw * 64 + fq * 16), sa6 = (unsigned)(M_CSH + 512 + wc * 128 + sw * 64 + fr * 4);
    unsigned sa3 = sa2 ^ 64, sa7 = sa6 ^ 64, sa4 = (unsigned)(M_MEAN + fr * 4), sa5 = (unsigned)(M_MEAN + fq * 16);
    asm volatile("" : "+v"(sa0), "+v"(sa1), "+v"(sa2), "+v"(sa6), "+v"(sa3), "+v"(sa7));
    if (HALO) asm volatile("" : "+v"(sa4), "+v"(sa5));
    const int head = c.jq * 2 + wc;
    auto rso = __builtin_amdgcn_make_buffer_rsrc(HALO ? c.ws + WS_O + (size_t)l * (8u << 20) + (size_t)((head * 16 + c.pq) * 8 + wr * 4) * (1024 << HO)
                                                      : c.ws + WS_KV + (size_t)(l >> 1) * (16u << 20) + (size_t)((c.pq * 32 + head) * 4 + wr) * (4096 << HK), 0, HALO ? 4096 << HO : 12288 << HK, 0x00020000);
    unsigned xrs = (unsigned)(M_XCH + wc * 6144 + wr * 2048), xhs = (unsigned)(M_XCH + wc * 6144 + 8 - wr * 8);
    unsigned b0m = (c.pq == 0 && wr == 0) ? 15u * 16u : 0u;
    asm volatile("" : "+s"(rso));
    if (HALO) asm volatile("" : "+s"(xrs), "+s"(xhs), "+s"(b0m));
    u32x4 cpy[3];
    unsigned cv, vt, cdst;
    const char *cs_, *tw, *cd_;
    int more = !HALO && l == 0 ? KT - 3 - XST - (c.pq & 7) : 0, three = c.jq & 4;
    if (!HALO) {
        asm volatile("" : "=s"(cs_), "=s"(tw), "=s"(cd_));
        asm volatile("" : "=v"(cv), "=v"(cdst), "=v"(vt));
        asm volatile("" : "+s"(more), "+s"(three));
        asm volatile("" : "=v"(cpy[0]), "=v"(cpy[1]), "=v"(cpy[2]));
    }
    auto pre = [&]() {
        static_assert(CPY + TOUCH == 2 && XSP == 1 && OWN == 1 && OWA == 1, "extra");
        asm volatile("" : "+s"(kp));
        const int g = c.jq >> 2, n = c.pq * 4 + (c.jq & 3), m = n >> 5, kt = (n >> 1) & 15;
        const char* src = *(const char* const __attribute__((address_space(4)))*)((const __attribute__((address_space(4))) char*)kp + (three ? (m ? 8 : 24) : (m ? 32 : 40)));
        const char* tsr = *(const char* const __attribute__((address_space(4)))*)((const __attribute__((address_space(4))) char*)kp + (c.wid < 3 ? 24 : 40));
        int o1 = kt * (64 * 6144) + (n & 1) * (32 * 6144) + (3 + 4 * (g & 2) - c.jq) * 128 + 2048, o2 = (n & 31) * 65536 + (11 - 4 * g) * 128 - c.pq * 128 * (D_ * 2) + 2048;
        int d1 = (int)WS_CQ + kt * (64 * 6144) + (m * 2 + (g >> 1)) * 24576 + (n & 1) * 4096, d2 = (int)WS_CO + (n & 31) * 65536 + (m * 2 + 1 - (g >> 1)) * 4096;
        asm volatile("" : "+s"(o1), "+s"(o2), "+s"(d1), "+s"(d2));
        cs_ = src + (three ? o1 : o2);
        tw = tsr + (unsigned)((c.wid < 3 ? c.wid : 0) * (2u << 20) + c.bid * 8192);
        cd_ = c.ws + (unsigned)(three ? d1 : d2);
        cv = pick(three, vw0, va0 ^ ((lane & 8) << 1));
        cdst = (unsigned)(c.wid * 1024 + lane * 16);
        vt = TWQ ? (unsigned)(((lane & 7) == 3 ? lane - 1 : lane) * 128) : 0u;
        if (EXS) vt &= (c.jq & 7) == 3 ? 0u : ~0u;
        asm volatile("" : "+s"(cs_), "+s"(tw), "+s"(cd_));
        asm volatile("" : "+v"(cv), "+v"(cdst), "+v"(vt));
    };
    if (!HALO && !ERL && l == 0) BAR();
    if (ST0 && l == 0 && c.wid == 1) STAMP_AT(44);
    if (ST0 && l == 0 && c.wid == 1) STAMP_AT(46);
    bf16x8 hk[2], hv_[2];
    if (OWN && (HALO || (OWA && __builtin_expect(l > 0, EXP0)))) {
        if (PRO && c.wid == 1) STAMP_AT(l == 1 ? 41 : (l == 2 ? 46 : 59));
        unsigned cw = M_CNT;
        asm volatile("" : "+v"(cw));
#pragma unroll 1
        for (unsigned spins = 0; spins < (1u << 24); ++spins) {
            const int a = *(volatile LAS int*)lds(cw), b = *(volatile LAS int*)(lds(cw) + (M_W0 - M_CNT));
            if (__builtin_amdgcn_readfirstlane(a) == 8 * l && __builtin_amdgcn_readfirstlane(b) == (OWA ? 4 * l : 2 * l + 2)) break;
        }
        FENCE();
        SCHED();
        ldfrag(0, pa0, ph0, pb0, pb1, 0);
        SCHED();
        ldfrag(1, pa1, ph1, pb0, pb1, 1);
        if (HALO) { mm(0, 0, 8); mm(0, 9, 17); } else mm(0, 0, 16);
#pragma unroll
        for (int i = 0; i < 16; ++i) { SGB(0x008, 1); SGB(0x100, 1); }
        SCHED();
        mm(0, HALO ? 18 : 16, NM);
        SCHED();
        if (HALO) { mm(1, 0, 8); mm(1, 9, 17); mm(1, 18, NM); } else mm(1, 0, NM);
#pragma unroll
        for (int set = 0; set < 2; ++set) { hk[set] = wb[set][2]; hv_[set] = wb[set][4]; }
        static_assert(SNK == 1, "places of the halo products");
        sa = QA_SLOT; sb = QW_SLOT;
        pa0 = aoff0 + QA_SLOT; pa1 = pa0 ^ xm; ph0 = hoff0 + QA_SLOT; ph1 = ph0 ^ xm; pb0 = boff[0] + QW_SLOT; pb1 = boff[1] + QW_SLOT;
        asm volatile("" : "+v"(pa0), "+v"(pa1), "+v"(pb0), "+v"(pb1));
        if (HALO) asm volatile("" : "+v"(ph0), "+v"(ph1));
#pragma unroll
        for (int mt = 0; mt < 4; ++mt)
#pragma unroll
            for (int nt = 0; nt < 6; ++nt) asm volatile("" : "+v"(acc[mt][nt]));
        SCHED();
    } else if (OWN) {
#pragma unroll
        for (int mt = 0; mt < 4; ++mt)
#pragma unroll
            for (int nt = 0; nt < 6; ++nt) asm volatile("" : "+v"(acc[mt][nt]));
        pre();
        WAIT_V0();
    }
    if (PRO && l > 0 && c.wid == 1) STAMP_AT(l == 1 ? 43 : (l == 2 ? 47 : 60));
    BAR();
    FENCE();
    if (c.wid == 0) STAMP_AT(l * 8 + 0);
    bf16x8 h0[2];
    if (OWN && HALO) {
#pragma unroll
        for (int set = 0; set < 2; ++set) h0[set] = *(const LAS bf16x8*)(lds(ph0) + (M_H0 - (QA_OFF + QA_SLOT + 16384)) + set * 1024);
        SCHED();
    }
    ldfrag(0, pa0, ph0, pb0, pb1, 0);
    SCHED();
    if (OWN && HALO) {
#pragma unroll
        for (int set = 0; set < 2; ++set) { ah[0] = MFMA(hk[set], h0[set], ah[0]); ah[1] = MFMA(h0[set], hv_[set], ah[1]); }
        asm volatile("" : "+v"(ah[0]), "+v"(ah[1]));
        SCHED();
        __builtin_amdgcn_s_setprio(0); __builtin_amdgcn_s_setprio(0);
        SCHED();
    }
    if (ALN) WAIT_L0();
    SCHED();
    int cnt = KT - 3 - more - (OWN == 0 ? 0 : (HALO ? 1 : (OWA ? l >> 1 : 0))), tv = 0;
    QSTAMP(0);
    auto half0 = [&]() {
        ldfrag(1, pa1, ph1, pb0, pb1, 1);
        mm(0, 0, NH);
        interleave();
        mm(0, NH, NH + 1); SCHED();
        asm volatile("s_addk_i32 %0, %2\n\ts_addk_i32 %1, %3" : "+s"(sa), "+s"(sb) : "i"(QA_SLOT), "i"(QW_SLOT) : "scc"); SCHED();
        mm(0, NH + 1, NH + 2); SCHED();
        asm volatile("s_cmpk_lg_u32 %0, %2\n\ts_cselect_b32 %0, %0, 0\n\ts_cmp_lg_u32 %1, %3\n\ts_cselect_b32 %1, %1, %4" : "+s"(sa), "+s"(sb) : "i"(NQA * QA_SLOT), "i"(3 * QW_SLOT), "i"(QW_TOP) : "scc"); SCHED();
        mm(0, NH + 2, NH + 3); SCHED();
        asm volatile("s_cmp_lg_u32 %0, %2\n\ts_cselect_b32 %0, %0, 0\n\ts_addk_i32 %1, -1" : "+s"(sb), "+s"(cnt) : "i"(QW_TOP + QW_SLOT) : "scc"); SCHED();
        mm(0, NH + 3, NH + 4); SCHED();
        pa0 = aoff0 + sa; pb0 = boff[0] + sb;
        asm volatile("" : "+v"(pa0), "+v"(pb0)); SCHED();
        mm(0, NH + 4, NH + 5); SCHED();
        pb1 = boff[1] + sb; pa1 = pa0 ^ xm;
        asm volatile("" : "+v"(pa1), "+v"(pb1)); SCHED();
        mm(0, NH + 5, NH + 6); SCHED();
        if (HALO) { ph0 = hoff0 + sa; ph1 = ph0 ^ xm; asm volatile("" : "+v"(ph0), "+v"(ph1)); SCHED(); }
        mm(0, NH + 6, NM);
        SCHED();
        WAIT_L0();
        BAR();
        SCHED();
    };
    asm volatile("" : "+s"(sa), "+s"(sb), "+s"(cnt));
    do {
    ALIGN8();
    do {
        half0();
        ldfrag(0, pa0, ph0, pb0, pb1, 0);
        mm(1, 0, NH);
        interleave();
        mm(1, NH, NM - 1);
        SCHED();
        WAIT_L0();
        asm volatile("s_nop 0");
        SCHED();
        mm(1, NM - 1, NM);
        SCHED();
    } while (cnt != 0);
    if (!HALO) {
        asm volatile("s_cmp_eq_u32 %[more], 0\n\ts_cbranch_scc1 .Lex%=\n\ts_mov_b32 m0, %[tch]\n\ts_nop 3\n\tglobal_load_lds_dword %[vt], %[tw] sc1\n\t"
                     "global_load_dwordx4 %[c0], %[v0], %[cs] offset:-2048\n\ts_cmp_eq_u32 %[three], 0\n\ts_cbranch_scc1 .Lex%=\n\t"
                     "global_load_dwordx4 %[c1], %[v0], %[cs]\n\tglobal_load_dwordx4 %[c2], %[v0], %[cs] offset:2048\n.Lex%=:"
                     : [c0] "+v"(cpy[0]), [c1] "+v"(cpy[1]), [c2] "+v"(cpy[2])
                     : [more] "s"(more), [tch] "i"(M_TCH), [vt] "v"(vt), [tw] "s"(tw), [v0] "v"(cv), [cs] "s"(cs_), [three] "s"(three) : "scc", "memory");
        cnt = more; more = 0;
        asm volatile("" : "+s"(cnt), "+s"(more));
        SCHED();
    }
    } while (cnt != 0);
    if (!HALO) {
        asm volatile("s_cmp_lg_u32 %[l], 0\n\ts_cbranch_scc1 .Lex%=\n\ts_waitcnt vmcnt(0)\n\ts_nop 3\n\tglobal_store_dwordx4 %[d], %[c0], %[ws] sc1\n\ts_cmp_eq_u32 %[three], 0\n\ts_cbranch_scc1 .Lex%=\n\t"
                     "v_add_u32 %[d], 0x2000, %[d]\n\tglobal_store_dwordx4 %[d], %[c1], %[ws] sc1\n\tv_add_u32 %[d], 0x2000, %[d]\n\tglobal_store_dwordx4 %[d], %[c2], %[ws] sc1\n.Lex%=:\n\ts_nop 1"
                     : [d] "+v"(cdst) : [c0] "v"(cpy[0]), [c1] "v"(cpy[1]), [c2] "v"(cpy[2]), [l] "s"(l), [ws] "s"(cd_), [three] "s"(three) : "scc", "memory");
    }
    QSTAMP(1);
    FENCE();
    SCHED();

    f32x4 csq[4], m4[4], mh4; float csv[2], mr[4], rr[4], mh;
    auto r1 = [&](unsigned b, int off) { return *(const LAS float*)(lds(b) + off); };
    auto r4_ = [&](unsigned b, int off) { return *(const LAS f32x4*)(lds(b) + off); };
    auto mf = [&](int set, int i) {
        const int nt = i >> 2, mt = i & 3;
        if (i < 16) acc[mt][nt] = MFMA(wb[set][nt], xa[set][mt], acc[mt][nt]);
        else if (i < 24) acc[mt][nt] = MFMA(xa[set][mt], wb[set][nt], acc[mt][nt]);
        else if (i == 24) ah[0] = MFMA(wb[set][2], xh[set], ah[0]);
        else ah[1] = MFMA(xh[set], wb[set][4], ah[1]);
        SCHED();
    };
    auto fold = [&](int i, int e) {
        const int nt = i >> 2, mt = i & 3;
        float t;
        if (i < 16) t = __builtin_fmaf(mr[mt], csq[nt][e], acc[mt][nt][e]);
        else if (i < 24) t = __builtin_fmaf(m4[mt][e], csv[nt - 4], acc[mt][nt][e]);
        else if (i == 24) t = __builtin_fmaf(mh, csq[2][e], ah[0][e]);
        else t = __builtin_fmaf(mh4[e], csv[0], ah[1][e]);
        asm volatile("" : "+v"(t));
        if (i < 24) acc[mt][nt][e] = t; else ah[i - 24][e] = t;
    };
    constexpr Tail<HALO> TL{};
    constexpr int NP = HALO ? 6 : 2, FN = 4 * NM;
    static_assert(TL.v[3][TL.n[3]] == NP + FN + (HALO ? 2 : 0) && KT % NQA == 1 && KT % NQW == 0, "tail");
    s16x4 wl[6], wh[6];
    auto frd = [&](int set, int k, int ao) {
        if (k < 4) xa[set][k] = *(const LAS bf16x8*)(lds(set ? pa1 : pa0) + ao + k * 2048);
        else if (k == 16) xh[set] = *(const LAS bf16x8*)(lds(set ? ph1 : ph0) + ao);
        else {
            const int nt = (k - 4) >> 1;
            const LAS char* pb = lds((nt & 1) ? pb1 : pb0) + (nt >> 1) * 8192 + set * 4096;
            if (k & 1) { wh[nt] = tr_read(pb + 512); wb[set][nt] = __builtin_shufflevector(wl[nt], wh[nt], 0, 1, 2, 3, 4, 5, 6, 7); }
            else wl[nt] = tr_read(pb);
        }
    };
    constexpr int RS = M_RSTD - M_MEAN;
    unsigned vl, vg, tb, xr, xh_, b0a, qb0, qb1;
    f32x4 bprev, bcur, bprev0; u32x4 ps; u32x2 ko; float ninf;
    auto rdop = [&](int h, int k) {
        const int j = k - NH;
        if (h == 0) {
            if (k < 2) { mr[2 * k] = r1(sa0, k * 128); mr[2 * k + 1] = r1(sa0, k * 128 + 64); }
            else if (k < 4) csq[k - 2] = r4_((k & 1) ? sa3 : sa2, 0);
            else if (j < 4) frd(0, k - 4, 2 * QA_SLOT);
            else csq[j - 2] = r4_((j & 1) ? sa3 : sa2, 256);
        } else if (j < 0) frd(h & 1, k, h == 1 ? 2 * QA_SLOT : 0);
        else if (h == 1) {
            if (j < 2) csv[j] = r1(j ? sa7 : sa6, 0);
            else if (HALO && j == 2) mh = r1(sa4, 0);
            else if (HALO && j == 3) mh4 = r4_(sa5, 0);
            else { const int mt = HALO ? 7 - j : j - 2; m4[mt] = r4_(sa1, mt * 64); }
        } else if (h == 2) {
            if (j == 0) m4[2] = r4_(sa1, 128);
            if (j == 1) ko = *(const LAS u32x2*)(lds(tb) + 3072);
            if (j == 2) { rr[0] = r1(sa0, RS); rr[1] = r1(sa0, 64 + RS); }
            if (j == 3) { rr[2] = r1(sa0, 128 + RS); rr[3] = r1(sa0, 192 + RS); }
            if (j >= 4) m4[5 - j] = r4_(sa1, (5 - j) * 64);
        } else {
            if (j == 0) bprev = r4_(tb, 0);
            if (j == 1) bcur = r4_(tb, 1024);
            if (j == 2) ps = *(const LAS u32x4*)(lds(tb) + 2048);
            if (j == 3) bprev0 = r4_(b0a, 0);
        }
    };
    auto vop = [&](int k) {
        if (k == 0) { asm volatile("" : "+v"(boff[0])); qb0 = boff[0] + QW_TOP; asm volatile("" : "+v"(qb0)); }
        else if (k == 1) { asm volatile("" : "+v"(boff[1])); qb1 = boff[1] + QW_TOP; asm volatile("" : "+v"(qb1)); }
        else if (k < NP) {
            if (k == 2) { vl = (unsigned)(lane * 16); asm volatile("" : "+v"(vl)); }
            if (k == 3) { tb = vl + M_TAB; asm volatile("" : "+v"(tb)); }
            if (k == 4) { b0a = tb | b0m; asm volatile("" : "+v"(b0a)); }
            if (k == 5) { xr = xrs + vl; asm volatile("" : "+v"(xr)); }
        } else if (k < NP + FN) fold(TL.f[(k - NP) >> 2], (k - NP) & 3);
        else {
            if (k == NP + FN) { xh_ = xhs + vl; asm volatile("" : "+v"(xh_)); }
            if (k == NP + FN + 1) { ninf = -INFINITY; asm volatile("" : "+v"(ninf)); }
        }
    };
    unsigned wa, wq;
    int wr2 = wr, wc2 = wc;
#pragma unroll
    for (int i = 0; i < NM; ++i) {
        mf(0, TL.t[0][i]);
        if (TL.t[0][i] < 24) asm volatile("" : "+v"(acc[TL.t[0][i] & 3][TL.t[0][i] >> 2])); else asm volatile("" : "+v"(ah[TL.t[0][i] & 1]));
        FENCE();
        if (i < NH) frd(1, i, 0);
        if (!HALO && i == NH + 3) { asm volatile("" : "+s"(wr2), "+s"(wc2)); vl = (unsigned)(lane * 16); wq = (unsigned)(AT_Q + (wc2 * 8 + wr2 * 4) * 1024) + vl; asm volatile("" : "+v"(vl), "+v"(wq)); }
        if (!HALO && i == NH + 4) { asm volatile("" : "+s"(wr2), "+s"(wc2)); wa = (unsigned)(AT_OWN + wc2 * 16384 + wr2 * 4096) + vl; asm volatile("" : "+v"(wa)); }
        if (i == NH + 1) { asm volatile("" : "+v"(boff[0]), "+v"(boff[1])); pb0 = boff[0] + 2 * QW_SLOT; pb1 = boff[1] + 2 * QW_SLOT; asm volatile("" : "+v"(pb0), "+v"(pb1)); }
        if (i == NH + 2) { pa0 = aoff0; pa1 = aoff0 ^ xm; asm volatile("" : "+v"(pa1)); if (HALO) { ph0 = hoff0; ph1 = hoff0 ^ xm; asm volatile("" : "+v"(ph1)); } }
        SCHED();
    }
    WAIT_L0();
    BAR();
    FENCE();
    SCHED();
    auto frags = [&](int h) {
        static_assert(TL.d[0][NM] == NH + 6 && TL.d[1][NM] == NH + (HALO ? 5 : 6) && TL.d[2][NM] == NH + (HALO ? 6 : 0) && TL.d[3][TL.n[3]] == NH + (HALO ? 4 : 0), "reads");
        if (h == 0) __builtin_amdgcn_s_waitcnt(0xC27F);
        if (h == 1) __builtin_amdgcn_s_waitcnt(HALO ? 0xC57F : 0xC67F);
        if (h == 2) __builtin_amdgcn_s_waitcnt(HALO ? 0xC67F : 0xC07F);
        if (h == 3) __builtin_amdgcn_s_waitcnt(HALO ? 0xC47F : 0xC07F);
    };
#pragma unroll
    for (int h = 0; h < 4; ++h) {
        if (h == 2) { pb0 = qb0; pb1 = qb1; }
#pragma unroll
        for (int i = 0; i < TL.n[h]; ++i) {
            if (!(h & 1) && i == TL.n[h] - 1) { frags(h); SCHED(); if (h) QSTAMP(4); }
            mf(~h & 1, TL.t[h][i]);
#pragma unroll
            for (int k = TL.d[h][i]; k < TL.d[h][i + 1]; ++k) { FENCE(); rdop(h, k); }
            SCHED();
#pragma unroll
            for (int k = TL.v[h][i]; k < TL.v[h][i + 1]; ++k) vop(k);
            SCHED();
        }
        if (h & 1) {
            frags(h);
            if (h == 1) QSTAMP(2); else QSTAMP(5);
            BAR();
            FENCE();
            SCHED();
            if (h == 1) QSTAMP(3); else QSTAMP(6);
        }
    }
    if (!HALO) {
        f32x4 r4[4];
        mf(1, 8);
#pragma unroll
        for (int mt = 0; mt < 4; ++mt) rr[mt] = r1(sa0, mt * 64 + RS);
        SCHED();
        mf(1, 12);
#pragma unroll
        for (int mt = 0; mt < 4; ++mt) r4[mt] = r4_(sa1, mt * 64 + RS);
        SCHED();
        mf(1, 9);
        vg = HK ? (unsigned)hv<HK>(lane) : vl;
        if (HK) asm volatile("" : "+v"(vg));
        SCHED();
        u32x4 pk[8];
        auto op = [&](int g, int k) {
            const int ta = g < 4 ? g : (g - 4) & 2, tb = g < 4 ? g : ta + 1, na = g < 4 ? 2 : 4 + (g & 1), nb = g < 4 ? 3 : na;
            f32x4& A = acc[ta][na]; f32x4& Bv = acc[tb][nb];
            if (k < 12) {
                const int h = k / 6, kk = k % 6;
                f32x4& X = h ? Bv : A;
                const int tx = h ? tb : ta;
                if (kk < 4) { float t = X[kk] * (g < 4 ? rr[tx] : r4[tx][kk]); asm volatile("" : "+v"(t)); X[kk] = t; }
                else { unsigned t = cvt_pk_bf16(X[2 * (kk - 4)], X[2 * (kk - 4) + 1]); asm volatile("" : "+v"(t)); pk[g][2 * h + kk - 4] = t; }
            } else if (k == 12) __builtin_amdgcn_raw_buffer_store_b128(pk[g], rso, vg + hp<HK>(g & 3), (g >> 2) * (8192 << HK), 16);
            else *(LAS u32x4*)(lds(wa) + (g >> 2) * 8192 + (g & 3) * 1024) = pk[g];
            SCHED();
        };
        auto pm = [&](int i) {
            if (i < 16) mf(1, TL.h3[i]);
            else if (i < 32) mf(((i - 16) >> 1) & 1, ((i - 16) >> 2) + 4 * (i & 1));
        };
        pm(3);
#pragma unroll
        for (int g = 0; g < 8; ++g) {
#pragma unroll
            for (int k = 0; k < 6; ++k) op(g, k);
            pm(2 * g + 4);
            if (g > 0) op(g - 1, 12);
#pragma unroll
            for (int k = 6; k < 12; ++k) op(g, k);
            pm(2 * g + 5);
            if (g > 0 && g <= LWA) op(g - 1, 13);
        }
        op(7, 12);
        QSTAMP(7);
        u32x4 qk;
        auto qop = [&](int mt, int k) {
            if (k == 0) { rr[mt] *= QSCALE; asm volatile("" : "+v"(rr[mt])); }
            else if (k < 13) {
                const int h = (k - 1) / 6, kk = (k - 1) % 6;
                f32x4& X = acc[mt][h];
                if (kk < 4) { float t = X[kk] * rr[mt]; asm volatile("" : "+v"(t)); X[kk] = t; }
                else { unsigned t = cvt_pk_bf16(X[2 * (kk - 4)], X[2 * (kk - 4) + 1]); asm volatile("" : "+v"(t)); qk[2 * h + kk - 4] = t; }
            } else *(LAS u32x4*)(lds(wq) + mt * 1024) = qk;
            SCHED();
        };
#pragma unroll
        for (int mt = 0; mt < 3; ++mt) {
            pm(20 + 4 * mt); if (3 * mt + LWA < 8) op(3 * mt + LWA, 13); qop(mt, 0); qop(mt, 1); qop(mt, 2);
            pm(21 + 4 * mt); if (3 * mt + 1 + LWA < 8) op(3 * mt + 1 + LWA, 13); qop(mt, 3); qop(mt, 4); qop(mt, 5);
            pm(22 + 4 * mt); if (3 * mt + 2 + LWA < 8) op(3 * mt + 2 + LWA, 13); qop(mt, 6); qop(mt, 7); qop(mt, 8);
            pm(23 + 4 * mt); qop(mt, 9); qop(mt, 10); qop(mt, 11); qop(mt, 12); qop(mt, 13);
        }
        if (c.wid == 0) STAMP_AT(l * 8 + 1);
        QSTAMP(8);
        {
            const u32x2 q0 = pack4(acc[3][0] * (rr[3] * QSCALE)), q1 = pack4(acc[3][1] * (rr[3] * QSCALE));
            *(LAS u32x4*)(lds(wq) + 3 * 1024) = (u32x4){q0[0], q0[1], q1[0], q1[1]};
        }
        QSTAMP(9);
        QSTORE();
        if (K0E) {
            SCHED();
            WAIT_L0();
            BAR();
            FENCE();
            SCHED();
        }
    } else {
        typedef unsigned u32x10 __attribute__((ext_vector_type(10)));
        u32x4 kp[4], qp[4];
        u32x10 vw[2];
        float ks[2][4], cs[2][4];
        auto pk8 = [&](const f32x4& x, const f32x4& y) { const u32x2 a = pack4(x), b = pack4(y); u32x4 r = {a[0], a[1], b[0], b[1]}; asm volatile("" : "+v"(r)); SCHED(); return r; };
        auto pkv = [&](int mt, int cc) { const u32x2 a = pack4(acc[mt][4 + cc]); vw[cc][2 * mt + 2] = a[0]; vw[cc][2 * mt + 3] = a[1]; };
        auto fr8 = [](const u32x4& x) { return __builtin_bit_cast(bf16x8, x); };
        mf(1, 24);
        unsigned ka0 = ko[0] + wr * 256, ka1 = ko[1] + wr * 256;
        asm volatile("" : "+v"(ka0), "+v"(ka1));
        SCHED();
        mf(1, 25);
#pragma unroll
        for (int mt = 0; mt < 4; ++mt) ks[0][mt] = r1(ka0, mt * 64);
        rr[0] *= QSCALE; asm volatile("" : "+v"(rr[0]));
        SCHED();
        mf(1, 11);
#pragma unroll
        for (int mt = 0; mt < 4; ++mt) ks[1][mt] = r1(ka1, mt * 64);
        rr[1] *= QSCALE; asm volatile("" : "+v"(rr[1]));
        SCHED();
        mf(1, 15);
        rr[2] *= QSCALE; asm volatile("" : "+v"(rr[2]));
        rr[3] *= QSCALE; asm volatile("" : "+v"(rr[3]));
        SCHED();
        mf(1, 19);
        const u32x4 hh = pk8(ah[0], ah[1]);
        mf(1, 23);
        *(LAS u32x2*)lds(xh_) = (u32x2){hh[0], hh[1]}; *(LAS u32x2*)(lds(xh_) + 1024) = (u32x2){hh[2], hh[3]};
        SCHED();
        mf(1, 10);
        kp[3] = pk8(acc[3][2], acc[3][3]);
        mf(1, 14);
        *(LAS u32x4*)(lds(xr) + 2048) = kp[3];
        pkv(3, 0); asm volatile("" : "+v"(vw[0]));
        *(LAS u32x2*)(lds(xr) + 3072) = (u32x2){vw[0][8], vw[0][9]};
        SCHED();
        mf(1, 3);
        pkv(3, 1); asm volatile("" : "+v"(vw[1]));
        *(LAS u32x2*)(lds(xr) + 3072 + 8) = (u32x2){vw[1][8], vw[1][9]};
        SCHED();
        mf(1, 7);
        mf(1, 9);
        kp[2] = pk8(acc[2][2], acc[2][3]);
        mf(1, 13);
        qp[3] = pk8(acc[3][0], acc[3][1]);
        mf(1, 2);
        f32x4 s0[4], s1[4];
        u32x4 pk;
        auto sc = [&](int mt, int h) {
            if (h) { s1[mt] = MFMA(fr8(kp[mt]), fr8(qp[mt]), bcur); asm volatile("" : "+v"(s1[mt])); }
            else { s0[mt] = MFMA(fr8(mt ? kp[mt - (mt > 0)] : pk), fr8(qp[mt]), mt == 0 ? bprev0 : bprev); asm volatile("" : "+v"(s0[mt])); }
            SCHED();
        };
        auto csm = [&](int mt) { cs[0][mt] = ks[0][mt] * rr[mt]; asm volatile("" : "+v"(cs[0][mt])); cs[1][mt] = ks[1][mt] * rr[mt]; asm volatile("" : "+v"(cs[1][mt])); };
        float u[2][4], mx[4], iv[4];
        auto sv = [](float x) { asm volatile("" : "+v"(x)); return x; };
        auto mx2 = [&](float a, float b) { return max3(ninf, a, b); };
        auto pick = [&](int mt, int h) { u[h][mt] = sv(max3(mx2(s0[mt][2 * h], s0[mt][2 * h + 1]), s1[mt][2 * h], s1[mt][2 * h + 1]) * cs[h][mt]); };
        auto vf = [&](int i) { const int mt = 2 - (i >> 2), nt = 4 + (i & 1), set = (i >> 1) & 1; SCHED(); acc[mt][nt] = MFMA(xa[set][mt], wb[set][nt], acc[mt][nt]); asm volatile("" : "+v"(acc[mt][nt])); SCHED(); };
        mf(1, 6);
        kp[1] = pk8(acc[1][2], acc[1][3]);
        mf(1, 8);
        WAIT_L0();
        QSTAMP(7);
        BAR();
        FENCE();
        SCHED();
        QSTAMP(8);
        mf(1, 12);
        {
            const u32x2 k0 = *(const LAS u32x2*)(lds(xr) + 8);
            FENCE(); SCHED();
            const u32x2 k1 = *(const LAS u32x2*)lds(xr);
            FENCE(); SCHED();
            const u32x2 a = *(const LAS u32x2*)(lds(xr) + 1024 + 8);
            FENCE(); SCHED();
            const u32x2 b = *(const LAS u32x2*)(lds(xr) + 1024);
            pk = (u32x4){k0[0], k0[1], k1[0], k1[1]};
            vw[0][0] = a[0]; vw[0][1] = a[1]; vw[1][0] = b[0]; vw[1][1] = b[1];
        }
        SCHED();
        auto sw16 = [](float& a, float& b) { auto r = __builtin_amdgcn_permlane16_swap(__float_as_uint(a), __float_as_uint(b), false, false); a = __uint_as_float(r[0]); b = __uint_as_float(r[1]); };
        auto sw32 = [](float& a, float& b) { auto r = __builtin_amdgcn_permlane32_swap(__float_as_uint(a), __float_as_uint(b), false, false); a = __uint_as_float(r[0]); b = __uint_as_float(r[1]); };
        auto ldr = [&](int i) { };
        auto top = [&](int mt) { mx[mt] = sv(mx2(u[0][mt], u[1][mt])); };
        auto pv1 = [&](int mt, int k) {
            SCHED();
            vw[k >> 1][2 * mt + 2 + (k & 1)] = cvt_pk_bf16(acc[mt][4 + (k >> 1)][2 * (k & 1)], acc[mt][4 + (k >> 1)][2 * (k & 1) + 1]);
            asm volatile("" : "+v"(vw[k >> 1]));
            SCHED();
        };
        auto uk = [&](int mt) { SCHED(); u[0][mt] = sv(u[0][mt] * ks[0][mt]); u[1][mt] = sv(u[1][mt] * ks[1][mt]); SCHED(); };
        mf(1, 1);
        qp[2] = pk8(acc[2][0], acc[2][1]);
        mf(1, 5);
        csm(3); SCHED();
        mf(1, 0);
        kp[0] = pk8(acc[0][2], acc[0][3]);
        mf(1, 4);
        csm(2); SCHED();
        sc(3, 1);
        qp[1] = pk8(acc[1][0], acc[1][1]);
        sc(3, 0);
        csm(1); ldr(0); SCHED();
        sc(2, 1);
        qp[0] = pk8(acc[0][0], acc[0][1]);
        sc(2, 0);
        csm(0); pick(3, 0); SCHED();
        sc(1, 1);
        pick(3, 1); ldr(1); SCHED();
        sc(1, 0);
        pick(2, 0); top(3); SCHED();
        sc(0, 1);
        pick(2, 1); ldr(2); SCHED();
        sc(0, 0);
        if (c.wid == 0) STAMP_AT(l * 8 + 1);
        pick(1, 0); top(2);
        vf(0);
        pick(1, 1); ldr(3);
        vf(1);
        sw16(mx[2], mx[3]);
        pick(0, 0);
        vf(2);
        pick(0, 1); top(1);
        vf(3);
        top(0); mx[2] = sv(mx2(mx[2], mx[3]));
        vf(4);
        sw16(mx[0], mx[1]); mx[0] = sv(mx2(mx[0], mx[1]));
        vf(5); pv1(2, 0);
        sw32(mx[0], mx[2]);
        { const float a = mx[0], b = mx[2]; mx[0] = sv(mx2(a, b)); mx[2] = sv(mx2(b, a)); }
        vf(6); pv1(2, 1);
        sw32(mx[0], mx[2]);
        mx[3] = sv(mx[2]); mx[1] = sv(mx[0]);
        vf(7);
        sw16(mx[2], mx[3]); sw16(mx[0], mx[1]);
        SCHED();
#pragma unroll
        for (int mm = 0; mm < 4; ++mm) { const int mt = 3 - mm; u[0][mt] = sv(u[0][mt] - mx[mt]); u[1][mt] = sv(u[1][mt] - mx[mt]); if (mm == 1) vf(8); }
        vf(9);
#pragma unroll
        for (int mm = 0; mm < 4; ++mm) {
            const int mt = 3 - mm;
            u[0][mt] = sv(__builtin_amdgcn_exp2f(u[0][mt])); u[1][mt] = sv(__builtin_amdgcn_exp2f(u[1][mt]));
            if (mm > 0) iv[mt + 1] = sv(u[0][mt + 1] + u[1][mt + 1]);
            if (mm < 2) vf(10 + mm);
        }
        SCHED();
        sw16(iv[2], iv[3]);
        iv[0] = sv(u[0][0] + u[1][0]);
        iv[2] = sv(iv[2] + iv[3]);
        pv1(2, 2);
        sw16(iv[0], iv[1]); iv[0] = sv(iv[0] + iv[1]);
        uk(3);
        sw32(iv[0], iv[2]);
        iv[0] = sv(iv[0] + iv[2]);
        pv1(2, 3);
        iv[0] = sv(__builtin_amdgcn_rcpf(iv[0]));
        pv1(1, 0);
        iv[2] = sv(iv[0]);
        uk(2);
        sw32(iv[0], iv[2]);
        iv[3] = sv(iv[2]); iv[1] = sv(iv[0]);
        pv1(1, 1);
        sw16(iv[2], iv[3]); sw16(iv[0], iv[1]);
        SCHED();
        f32x4 o[4][2];
        u32x4 pf[4];
        auto nrm = [&](int mt) { u[0][mt] = sv(u[0][mt] * iv[mt]); u[1][mt] = sv(u[1][mt] * iv[mt]); SCHED(); };
        auto prb = [&](int mt) {
            const unsigned p = cvt_pk_bf16(u[0][mt], u[1][mt]);
            pf[mt] = (u32x4){__builtin_amdgcn_perm(0u, p, ps[0]), __builtin_amdgcn_perm(0u, p, ps[1]), __builtin_amdgcn_perm(0u, p, ps[2]), __builtin_amdgcn_perm(0u, p, ps[3])};
            asm volatile("" : "+v"(pf[mt]));
            SCHED();
        };
        auto pvm = [&](int mt, int cc) {
            o[mt][cc] = MFMA(fr8((u32x4){vw[cc][2 * mt], vw[cc][2 * mt + 1], vw[cc][2 * mt + 2], vw[cc][2 * mt + 3]}), fr8(pf[mt]), ((f32x4){0.f, 0.f, 0.f, 0.f}));
            asm volatile("" : "+v"(o[mt][cc]));
            SCHED();
        };
        auto out = [&](int mt) {
            const u32x2 lo = pack4(o[mt][0]), hi = pack4(o[mt][1]);
            __builtin_amdgcn_raw_buffer_store_b128((u32x4){lo[0], lo[1], hi[0], hi[1]}, rso, vg + hp<HO>(mt), 0, 16);
            SCHED();
        };
        vg = (unsigned)hv<HO>(lane);
        asm volatile("" : "+v"(vg));
        nrm(3); nrm(2); prb(3);
        pv1(1, 2); pv1(1, 3);
        pvm(3, 0); prb(2); pvm(3, 1);
        uk(1); uk(0);
        pvm(2, 0); nrm(1); nrm(0); pvm(2, 1);
        prb(1); pv1(0, 0); pv1(0, 1);
        pvm(1, 0); pv1(0, 2); pv1(0, 3); prb(0);
        pvm(1, 1); out(3); pvm(0, 0); out(2); pvm(0, 1); out(1); out(0);
        SCHED();
        QSTAMP(9);
        if (c.wid == 0) STAMP_AT(l * 8 + 2);
        QSTORE();
    }
}

#define MFMA32(a, b, c) __builtin_amdgcn_mfma_f32_32x32x16_bf16(__builtin_bit_cast(bf16x8, a), __builtin_bit_cast(bf16x8, b), (c), 0, 0, 0)
DI float max16(const f32x16& s) {
    float m = max3(s[0], s[1], s[2]), n = max3(s[3], s[4], s[5]);
    m = max3(m, s[6], s[7]); n = max3(n, s[8], s[9]);
    m = max3(m, s[10], s[11]); n = max3(n, s[12], s[13]);
    m = max3(max3(m, s[14], s[15]), n, n);
    auto b = __builtin_amdgcn_permlane32_swap(__float_as_uint(m), __float_as_uint(m), false, false);
    return __builtin_amdgcn_fmed3f(__uint_as_float(b[0]), __uint_as_float(b[1]), INFINITY);
}
template <bool HI> DI void attn_a(const Ctx& c0, int l, const char* Wo, KP kp) {
    const Ctx c = fresh(c0);
    asm volatile("" : "+s"(Wo));
    LAS char* smem = c.smem;
    const int lane = c.lane, w = c.wid;
    constexpr int H = HI ? 0 : 1;
    int qt = HI ? w - 4 : 3 - w;
    asm volatile("" : "+s"(qt));
    const float NEG = -INFINITY;
    int zq = (lane & 31) - 4 * (lane >> 5);
    unsigned lb = (unsigned)(((lane >> 4) & 1) * 1024 + ((lane >> 5) * 16 + (lane & 15)) * 16);
    unsigned qa = (unsigned)(AT_Q + H * 8192 + qt * 2048) + lb, ta = (unsigned)(AT_OWN + H * 16384 - 3 * 2048 + qt * 2048) + lb;
    asm volatile("" : "+v"(lb), "+v"(qa), "+v"(ta));
    auto rd = [&](unsigned base, int off) { return *(const LAS u32x4*)(lds(base) + off); };
    f32x16 ce;
    u32x4 sel;
    unsigned tm = (unsigned)(AT_N + lane * 16);
    auto masks = [&]() {
        if (HI) {
#pragma unroll
            for (int r = 0; r < 16; ++r) ce[r] = 8 * (r >> 2) + (r & 3) <= zq ? 0.f : NEG;
            asm volatile("" : "+v"(ce));
        }
        const unsigned s1 = ((lane >> 4) ^ (lane >> 2)) & 1 ? 0u : 0x3f803f80u;
        sel = (u32x4){s1, s1, s1, s1};
        asm volatile("" : "+v"(sel));
    };
    u64 ep, pa_, kvv, nkv, kact;
    unsigned po, pk_, cnta = M_CNT, one = 1u;
    char* ob;
    const char *kvb, *s1_, *s2_;
    unsigned vk, dst, fo;
    auto setup = [&]() {
        if (HI) {
            const u32x2 e = *(const LAS u32x2*)(smem + M_EP);
            ep = ((u64)(unsigned)__builtin_amdgcn_readfirstlane(e[1]) << 32) | (unsigned)__builtin_amdgcn_readfirstlane(e[0]);
            po = o_post(c);
            pk_ = pick(lane < 2, (unsigned)(WS_KBOX + ((c.pq + 1 + lane) * 16 + c.jq) * 256 + lane * 8), (unsigned)(WS_FLAGQ + (c.pq * 16 + c.jq) * FQS));
            pa_ = c.pq < 14 ? 7ull : (c.pq < 15 ? 5ull : 4ull);
            fo = (unsigned)(WS_KBOX + (c.pq * 16 + c.jq) * 256 + lane * 8);
            asm volatile("" : "+v"(po), "+v"(pk_), "+v"(fo));
            asm volatile("" : "+s"(ep), "+s"(pa_));
        }
        ob = c.ws + WS_O + (size_t)l * (8u << 20) + (size_t)(unsigned)(((c.jq * 2 + H) * 16 + c.pq) * 8192 + qt * 2048);
        kvb = c.ws + WS_KV + (size_t)(unsigned)((l >> 1) * (16u << 20) + c.jq * 32768);
        asm volatile("" : "+s"(ob), "+s"(kvb));
    };
    auto setup2 = [&]() {
        const int p1 = c.pq >= 1 ? c.pq - 1 : c.pq, p2 = c.pq >= 2 ? c.pq - 2 : c.pq;
        s1_ = kvb + (size_t)(unsigned)(p1 * 524288);
        s2_ = kvb + (size_t)(unsigned)(p2 * 524288);
        vk = (unsigned)((w & 3) * 1024 + lane * 16); dst = (unsigned)(AT_N + (w & 3) * 1024);
        asm volatile("" : "+s"(s1_), "+s"(dst));
        if (!HI) asm volatile("" : "+s"(s2_));
        asm volatile("" : "+v"(vk));
    };
    auto arrive = [&](auto cc, unsigned msk, unsigned val) {
        unsigned old = 0;
        if (lane == 0) asm volatile("ds_add_rtn_u32 %0, %1, %2 offset:%3\n\ts_waitcnt lgkmcnt(0)" : "+v"(old) : "v"(cnta), "v"(one), "i"(decltype(cc)::value * 4) : "memory");
        if ((__builtin_amdgcn_readfirstlane(old) & msk) == msk) { if (lane < 16) post(c.ws, po, ep + (4 * l + val)); }
    };
    static_assert((MBOX & 2) && !HO && !HK, "layout");
    int tv = 0;
    auto kvs = [&]() {
        if (!HI) {
            SCHED();
            LSTAMP(2);
            asm volatile("s_mov_b64 exec, 1\n\ts_waitcnt vmcnt(0)\n\tds_add_u32 %0, %1\n\ts_mov_b64 exec, -1" :: "v"(cnta), "v"(one) : "memory");
            LSTAMP(3);
            SCHED();
        }
    };
    asm volatile("" : "+v"(cnta), "+v"(one), "+v"(tm));
    if (HI) {
        masks(); setup(); setup2();
        if (w == 5) {
#pragma unroll
            for (int i = 0; i < 4; ++i) *(LAS f32x4*)(lds(tm) + i * 1024) = (f32x4){ce[4 * i], ce[4 * i + 1], ce[4 * i + 2], ce[4 * i + 3]};
        }
        kvv = ep + (4 * l + 2); nkv = 0 - kvv;
        kact = __builtin_amdgcn_ballot_w64(w == KPW && lane < 2 && lane < c.pq);
        asm volatile("" : "+v"(kvv), "+v"(nkv));
        asm volatile("" : "+s"(kact));
        WAIT_L0();
        WAIT_V0();
        LSTAMP(0);
        if (w == 4) __builtin_amdgcn_s_setprio(HP4);
        BAR();
        FENCE();
    }
    auto kvp = [&]() {
        if (HI && w == 4) {
            __builtin_amdgcn_s_setprio(3);
#pragma unroll 1
            for (unsigned spins = 0; spins < (1u << 24); ++spins) {
                if (__builtin_amdgcn_readfirstlane(*(volatile LAS int*)lds(cnta)) == 8 * l + 4) break;
            }
            if (__builtin_amdgcn_inverse_ballot_w64(pa_)) post(c.ws, pk_, kvv);
            STAMP_AT(l * 8 + 2);
            __builtin_amdgcn_s_setprio(HP4);
        }
        SCHED();
    };
    if (KPP == -1) kvp();
    FENCE();
    SCHED();
    LSTAMP(1);
    u32x4 q0, q1, k0, k1, va, vp, pa, pb;
    f32x16 cm, s0, s1, o;
    f32x4 ls;
    auto stage = [&](f32x16& sc, f32x16& sn, const f32x16& cn, bool pend, bool zero, unsigned kb, int ko, unsigned vb, int vo1, unsigned nb, int vo0) {
        auto ex = [&](int i) { float x = __builtin_amdgcn_exp2f(sc[i]); asm volatile("" : "+v"(x)); sc[i] = x; SCHED(); };
        auto cv = [&](u32x4& p, int j, int i) { unsigned x = cvt_pk_bf16(sc[i], sc[i + 1]); asm volatile("" : "+v"(x)); p[j] = x; SCHED(); };
        sn = MFMA32(k0, q0, cn); SCHED();
        k0 = rd(kb, ko); SCHED();
        ex(0); ex(1); ex(2);
        if (pend) { o = MFMA32(vp, pb, o); SCHED(); }
        ex(3); ex(8); ex(9);
        sn = MFMA32(k1, q1, sn); SCHED();
        k1 = rd(kb, ko + 512); SCHED();
        ex(10); ex(11); cv(pa, 0, 0); cv(pa, 1, 2);
        if (pend) { ls = MFMA(__builtin_bit_cast(bf16x8, sel), __builtin_bit_cast(bf16x8, pb), ls); SCHED(); }
        vp = rd(vb, 8192 + vo1 + 512); SCHED();
        cv(pa, 2, 8); cv(pa, 3, 10); ex(4); ex(5);
        o = MFMA32(va, pa, zero ? (f32x16)(0.f) : o); SCHED();
        va = rd(nb, 8192 + vo0); SCHED();
        ex(6); ex(7); ex(12);
        ls = MFMA(__builtin_bit_cast(bf16x8, sel), __builtin_bit_cast(bf16x8, pa), zero ? (f32x4)(0.f) : ls); SCHED();
        ex(13); ex(14); ex(15); cv(pb, 0, 4); cv(pb, 1, 6); cv(pb, 2, 12); cv(pb, 3, 14);
    };
    auto flush = [&]() {
        ls = MFMA(__builtin_bit_cast(bf16x8, sel), __builtin_bit_cast(bf16x8, pb), ls); SCHED();
        o = MFMA32(vp, pb, o); SCHED();
    };
    float nm;
    {
        if (!HI) {
#pragma unroll
            for (int i = 0; i < 4; ++i) {
                const f32x4 t = *(const LAS f32x4*)(lds(tm) + i * 1024);
                ce[4 * i] = t[0]; ce[4 * i + 1] = t[1]; ce[4 * i + 2] = t[2]; ce[4 * i + 3] = t[3];
            }
        }
        q0 = rd(qa, 0);
        const u32x4 d0 = rd(ta, 6144);
        q1 = rd(qa, 512);
        const u32x4 d1 = rd(ta, 6144 + 512);
        k0 = rd(ta, 4096); k1 = rd(ta, 4096 + 512); va = rd(ta, 8192 + 6144);
        SCHED();
        if (KSP == -1) kvs();
        if (!HI) { masks(); setup(); SCHED(); }
        s0 = MFMA32(d0, q0, ce);
        s0 = MFMA32(d1, q1, s0);
        SCHED();
        if (!HI) { setup2(); SCHED(); }
        if (KSP == 0) kvs();
        if (KPP == 0) kvp();
        nm = -max16(s0);
        asm volatile("" : "+v"(nm));
#pragma unroll
        for (int r = 0; r < 16; ++r) { float t; asm("v_mov_b32 %0, %1" : "=v"(t) : "v"(nm), "i"(r)); cm[r] = t; }
        asm volatile("" : "+v"(cm));
        SCHED();
#pragma unroll
        for (int r = 0; r < 16; ++r) s0[r] = addf(s0[r], nm);
        asm volatile("" : "+v"(s0));
        SCHED();
        if (KSP == 1) kvs();
        if (KPP == 1) kvp();
        stage(s0, s1, cm, false, true, ta, 2048, ta, 6144, ta, 4096);
        if (KSP == 2) kvs();
        if (KPP == 2) kvp();
        if (qt > 0) {
            stage(s1, s0, cm, true, false, ta, 0, ta, 4096, ta, 2048);
            if (KSP == 3) kvs();
            if (qt > 1) {
                stage(s0, s1, cm, true, false, ta, 0, ta, 2048, ta, 0);
                if (qt > 2) stage(s1, s0, cm, true, false, ta, 0, ta, 0, ta, 0);
            }
        } else if (KSP == 3) kvs();
        flush();
    }
    LSTAMP(4);
    f32x16 c2;
    unsigned na, nb;
    auto rso = __builtin_amdgcn_make_buffer_rsrc(ob, 0, 2048, 0x00020000);
    auto starts = [&]() {
        const float n1 = c.pq >= 1 ? nm : NEG, n2 = c.pq >= 2 ? nm : NEG;
        asm volatile("" : "+v"(zq));
#pragma unroll
        for (int r = 0; r < 16; ++r) { cm[r] = n1; c2[r] = n2; ce[r] = 8 * (r >> 2) + (r & 3) <= zq ? NEG : n2; }
        na = (unsigned)(AT_N + H * 32768) + lb; nb = na + (unsigned)(16384 + qt * 2048);
        asm volatile("" : "+v"(cm), "+v"(c2), "+v"(ce), "+v"(na), "+v"(nb));
        asm volatile("" : "+s"(rso));
        SCHED();
    };
    {
        auto piece = [&](int b, int i) { dma16<true>(dst + b * 16384 + i * 4096, vk, ((b & 1) ? s2_ : s1_) + (b >> 1) * 16384 + i * 4096); };
        auto block = [&](int b) {
#pragma unroll
            for (int i = 0; i < 4; ++i) piece(b, i);
        };
        Ask t;
        if (HI) {
            poll_rmw<true>(t, c.ws, fo, __builtin_amdgcn_inverse_ballot_w64(kact), nkv, 62 - 4 * l);
            if (w == KPW) STAMP_AT(32 + l * 8 + 1);
        }
        WAIT_L0();
        LSTAMP(5);
        BAR();
        FENCE();
        LSTAMP(6);
        piece(0, H); piece(0, 2 + H);
        if (HI) {
            starts();
            if (APRIO) __builtin_amdgcn_s_setprio(APRIO);
            WAIT_V0();
            keep(t);
            LSTAMP(7);
            BAR();
            FENCE();
            if (w == 4) STAMP_AT(l * 8 + 3);
        } else {
            if (AB2 >= 0) block(1);
            if (AB2 > 0) block(2);
            starts();
            Ctx c2 = c;
            if (ELO) c2.wid = 0;
            unsigned vw = wo_w_off(c2);
            int qs = c.cw == 3 ? 1024 : 8 * D_ * 2;
            asm volatile("" : "+v"(vw));
            asm volatile("" : "+s"(qs));
            SCHED();
            wait_v<4 * AB2 + 4>();
            LSTAMP(7);
            BAR();
            if (AB2 < 0) block(1);
            if (AB2 <= 0) block(2);
            block(3);
            wait_v<8>();
            BAR();
            if (!ELO) {
                wo_w_issue<WA0, WA1>(c, Wo, vw);
                wait_v<WA1 - WA0>();
            } else WAIT_V0();
            LSTAMP(9);
            BAR();
            FENCE();
            if (!ELO) wo_w_issue<WA1, 26>(c, Wo, vw);
            else {
                const int s0 = w == 0 ? WA0 : (w == 1 ? WA0 + EN0 : WA0 + EN0 + EN1), s1 = w == 0 ? WA0 + EN0 : (w == 1 ? WA0 + EN0 + EN1 : (w == 2 ? 22 : 0));
#pragma unroll 1
                for (int sl = s0; sl < s1; ++sl) {
                    const char* b = Wo + (size_t)(unsigned)((2 * (sl & 15) + (sl >> 4)) * 65536);
#pragma unroll
                    for (int q = 0; q < 4; ++q) dma16<true>(sl * 4096 + q * 1024, vw, b + q * qs);
                }
            }
            if (LSL) { for (int i = 0; i < qt; ++i) __builtin_amdgcn_s_sleep(LSL); }
        }
    }
    SCHED();
    LSTAMP(8);
    {
        const u32x4 d0 = rd(na, 0), d1 = rd(na, 512);
        k0 = rd(na, 2048); k1 = rd(na, 2048 + 512); va = rd(na, 8192);
        SCHED();
        s0 = MFMA32(d0, q0, cm);
        s0 = MFMA32(d1, q1, s0);
        SCHED();
        stage(s0, s1, cm, false, false, na, 4096, na, 0, na, 2048);
        stage(s1, s0, cm, true, false, na, 6144, na, 2048, na, 4096);
        if (HI) { BAR(); FENCE(); SCHED(); }
        stage(s0, s1, cm, true, false, nb, 0, na, 4096, na, 6144);
        if (HI && CPOS == 3) { BAR(); SCHED(); }
        if (!HI && E30) {
            asm volatile("s_mov_b64 exec, 1\n\tds_add_u32 %0, %1 offset:12\n\ts_mov_b64 exec, -1" :: "v"(cnta), "v"(one) : "memory");
            SCHED();
        }
        stage(s1, s0, ce, true, false, nb, 2048, na, 6144, nb, 0);
        if (HI && CPOS == 4) { WAIT_L0(); BAR(); SCHED(); }
        stage(s0, s1, c2, true, false, nb, 4096, nb, 0, nb, 2048);
        if (HI && CPOS == 5) { BAR(); SCHED(); }
        if (qt < 3) {
            stage(s1, s0, c2, true, false, nb, 6144, nb, 2048, nb, 4096);
            if (qt < 2) {
                stage(s0, s1, c2, true, false, nb, 0, nb, 4096, nb, 6144);
                if (qt < 1) stage(s1, s0, c2, true, false, nb, 0, nb, 6144, nb, 0);
            }
        }
        flush();
    }
    LSTAMP(11);
    auto out = [&]() {
        const float iv = __builtin_amdgcn_rcpf(ls[0]);
#pragma unroll
        for (int u = 0; u < 2; ++u) {
            u32x4 t;
#pragma unroll
            for (int i = 0; i < 4; ++i) { const int r = 4 * u + (i >> 1) * 8 + (i & 1) * 2; t[i] = cvt_pk_bf16(o[r] * iv, o[r + 1] * iv); }
            __builtin_amdgcn_raw_buffer_store_b128(t, rso, lb + u * 512, 0, 16);
        }
        SCHED();
    };
    out();
    if (__builtin_expect(__any(!(ls[0] < SAFE_SUM)), 0)) {
        auto rk = __builtin_amdgcn_make_buffer_rsrc((void*)(kvb + H * 16384), 0, 16u << 20, 0x00020000);
        const int g0 = c.pq * 4 + qt - 8;
        float m = NEG;
#pragma unroll 1
        for (int ps = 0; ps < 2; ++ps) {
            o = (f32x16)(0.f); ls = (f32x4)(0.f);
#pragma unroll 1
            for (int j = g0 < 0 ? -g0 : 0; j < 9; ++j) {
                const int so = ((g0 + j) >> 2) * 524288 + ((g0 + j) & 3) * 2048;
                u32x4 f[4];
#pragma unroll
                for (int i = 0; i < 4; ++i) f[i] = __builtin_amdgcn_raw_buffer_load_b128(rk, lb + (i & 1) * 512, so + (i >> 1) * 8192, 16);
                f32x16 s;
                const float sb = ps ? -m : 0.f;
                float sa = j == 0 ? NEG : sb, sz = j == 8 ? NEG : sb;
                asm volatile("" : "+v"(zq), "+v"(sa), "+v"(sz));
#pragma unroll
                for (int r = 0; r < 16; ++r) s[r] = 8 * (r >> 2) + (r & 3) <= zq ? sa : sz;
                s = MFMA32(f[0], q0, s);
                s = MFMA32(f[1], q1, s);
                if (ps == 0) m = fmaxf(m, max16(s));
                else {
                    u32x4 p[2];
#pragma unroll
                    for (int r = 0; r < 16; r += 2) p[(r >> 2) & 1][(r >> 3) * 2 + ((r >> 1) & 1)] = cvt_pk_bf16(__builtin_amdgcn_exp2f(s[r]), __builtin_amdgcn_exp2f(s[r + 1]));
#pragma unroll
                    for (int u = 0; u < 2; ++u) {
                        o = MFMA32(f[2 + u], p[u], o);
                        ls = MFMA(__builtin_bit_cast(bf16x8, sel), __builtin_bit_cast(bf16x8, p[u]), ls);
                    }
                }
            }
        }
        __builtin_amdgcn_s_waitcnt(0x0F70);
        WAIT_V0();
        out();
    }
    SCHED();
    if (w == 3 || w == 4) STAMP_AT(32 + l * 8 + 3 + w);
    LSTAMP(12);
    if (HI) {
        WAIT_V0();
        arrive(std::integral_constant<int, 1>{}, 3u, 3u);
        if (w == 4) STAMP_AT(32 + l * 8 + 4);
        LSTAMP(13);
        if (APRIO) __builtin_amdgcn_s_setprio(0);
        if (lane == 0) asm volatile("ds_add_u32 %0, %1 offset:12" :: "v"(cnta), "v"(one) : "memory");
        WAIT_V0();
        arrive(std::integral_constant<int, 2>{}, 7u, 4u);
        if (w == 5 || w == 6) {
            Ctx c2 = c;
            c2.wid = 2 * w - 10;
            unsigned vw = wo_w_off(c2), vw2 = vw + (c.cw == 3 ? 1024u : 8u * D_ * 2);
            asm volatile("" : "+v"(vw), "+v"(vw2));
#pragma unroll 1
            for (unsigned spins = 0; spins < (1u << 22); ++spins) {
                if (__hip_atomic_load((LAS unsigned*)(smem + M_CNT + 12), __ATOMIC_RELAXED, __HIP_MEMORY_SCOPE_WORKGROUP) == (unsigned)(E30 ? 4 * l + 8 : 2 * l + 4)) break;
                __builtin_amdgcn_s_sleep(1);
            }
            FENCE();
            wo_w_issue<ELO ? 22 : 26, E30 ? 32 : 30>(c2, Wo, vw);
            c2.wid += 1;
            wo_w_issue<ELO ? 22 : 26, E30 ? 32 : 30>(c2, Wo, vw2);
        }
    }
    LSTAMP(23);
    ASTORE();
}

DI void wo_load(const Ctx& c0, int l, const char* Wn, const char* Wo) {
    const Ctx c = fresh(c0);
    asm volatile("" : "+s"(Wn), "+s"(Wo));
    const u64 ep = lds_epoch(c.smem);
    const unsigned fo = o_find(c);
        if (l == 0 && c.wid == 6) {
            const int fr = c.lane & 15, od = fr & 1, j0 = 4 * (c.lane >> 4) + od;
            const bool p0 = j0 > fr, p1 = j0 + 2 > fr;
            const unsigned N = 0xff800000u, Z = 0x0c0c0c0cu, lo = od ? 0x01000c0cu : 0x0c0c0100u, hi = od ? 0x03020c0cu : 0x0c0c0302u;
            LAS u32x4* t = (LAS u32x4*)(c.smem + M_TAB + c.lane * 16);
            t[0] = (u32x4){p0 && !od ? 0u : N, p0 && od ? 0u : N, p1 && !od ? 0u : N, p1 && od ? 0u : N};
            t[64] = (u32x4){!p0 && !od ? 0u : N, !p0 && od ? 0u : N, !p1 && !od ? 0u : N, !p1 && od ? 0u : N};
            t[128] = (u32x4){p0 ? lo : Z, p1 ? hi : Z, p0 ? Z : lo, p1 ? Z : hi};
            t[192] = (u32x4){(unsigned)(M_RSTD + j0 * 4 + (p0 ? 0 : 64)), (unsigned)(M_RSTD + j0 * 4 + 8 + (p1 ? 0 : 64)), 0u, 0u};
        }
    if (CPY && l == 0 && c.wid < 6) {
#pragma unroll 1
        for (int i = 0; i < 2; ++i) {
            const int j = i ? (c.bid & 4) * 2 + (c.bid >> 5) : c.jq, g = c.wid == 5 ? 2 - 2 * (j >> 3) : 1 + 2 * (j >> 3);
            if ((j & 7) == 3) poll_flags(c.ws, ep, (unsigned)WS_FLAGQ + ((c.lane >> 2) * 16 + 4 * g + (c.lane & 3)) * FQS, true, 2u);
        }
    }
    Ask t, t2;
    if (!(l & 1)) {
        Ctx c2 = c;
        c2.wid = 2 * c.wid - 10;
        unsigned vw = wo_w_off(c2), vw2 = vw + (c.cw == 3 ? 1024u : 8u * D_ * 2);
        asm volatile("" : "+v"(vw), "+v"(vw2));
        poll_rmw(t, c.ws, fo, c.wid == 7 && c.lane < 16, ep + (4 * l + 3), 61 - 4 * l);
        if (c.wid == 7) STAMP_AT(32 + l * 8 + 2);
        BAR();
        if (!E30 && (c.wid == 5 || c.wid == 6)) {
            __builtin_amdgcn_s_sleep(W30S);
            wo_w_issue<30, 32>(c2, Wo, vw);
            c2.wid += 1;
            wo_w_issue<30, 32>(c2, Wo, vw2);
        }
        if (c.wid != 7) WAIT_V0();
        else {
            __builtin_amdgcn_s_sleep(OSL);
            if (OPL) poll_flags(c.ws, ep, fo, c.lane < 16, (unsigned)(4 * l + 4));
        }
    } else {
        asm volatile("" : "=v"(t.v[0]), "=v"(t.v[1]), "=v"(t.v[2]), "=v"(t.v[3]));
        WAIT_V0();
    }
    poll_rmw(t2, c.ws, fo, c.wid == 7 && c.lane < 16 && (!OPL || (l & 1)), ep + (4 * l + 4), 60 - 4 * l);
    if (c.wid == 7) STAMP_AT(32 + l * 8 + 2 + !(l & 1));
    BAR();
    WAIT_V0();
    keep(t); keep(t2);
    if (l == 3 && c.bid == 0 && c.wid == 7) {
#pragma unroll 1
        for (int i = 0; i < 4; ++i) poll_flags(c.ws, ep, (unsigned)WS_FLAGQ + (i * 64 + c.lane) * FQS, true, 1u);
        if (c.lane == 0) __hip_atomic_store((GAS u64*)(c.ws + WS_EPOCH), ep + 64, __ATOMIC_RELAXED, __HIP_MEMORY_SCOPE_AGENT);
    }
    if (RS0 && l == 3) BAR();
}

DI void wo_consume(const bool B, const Ctx& c0, const bf16_t* xin, bf16_t* xout, int l) {
    u32x4 res[2][2];
    const Ctx c = fresh(c0);
    LAS char* smem = c.smem;
    const int lane = c.lane, w = c.wid, fr = lane & 15, fq = lane >> 4;
    const char* obase = c.ws + WS_O + (size_t)l * (8u << 20);
    int ovo = (c.pw * 8 + w * 2) * 1024 + lane * 16;
    asm volatile("" : "+v"(ovo));
    constexpr int NP = WNQ, R = 3;
    static_assert(!HO && !HX && !HS && !RLD && BPR == 1, "layout");
    auto ro = __builtin_amdgcn_make_buffer_rsrc((void*)obase, 0, 4u << 20, 0x00020000);
    asm volatile("" : "+s"(ro));
    u32x4 xa[NP][2];
    s16x4 wf[R][8];
    f32x4 acc[2][4];
    unsigned bw[8];
    int coff, rmo, spr;
    u32x4 on = {0x3f803f80u, 0x3f803f80u, 0x3f803f80u, 0x3f803f80u};
    unsigned sto, xpo, coff1, cnta = M_CNT, one = 1;
    u64 xv, b0, b1, am;
    auto rs = __builtin_amdgcn_make_buffer_rsrc(xout, 0, S_ * D_ * 2, 0x00020000);
    auto rst = __builtin_amdgcn_make_buffer_rsrc(c.ws + WS_STATS + (size_t)((l + 1) & 3) * 524288, 0, 524288, 0x00020000);
    auto prep2 = [&]() {
        sto = fq == (fr >> 2) && l < 3 ? (unsigned)((c.pw * 16 + c.jw) * 1024 + (w * 32 + fr) * 8) : 0x80000000u;
        xpo = x_post(c);
        coff = (int)pick(l == 3, (unsigned)rmo, (unsigned)coff);
        coff1 = coff + (l == 3 ? xrow<0>(0, 16) : 2048);
        spr = l == 3 ? 64 : 1024;
        asm volatile("" : "+s"(spr));
        b0 = __builtin_amdgcn_ballot_w64(fr & 1); b1 = __builtin_amdgcn_ballot_w64(fr & 2); am = __builtin_amdgcn_ballot_w64(x_act(c, !B));
        asm volatile("" : "+v"(on), "+v"(sto), "+v"(xpo), "+v"(coff), "+v"(coff1), "+v"(xv), "+v"(cnta), "+v"(one));
        asm volatile("" : "+s"(rs), "+s"(rst), "+s"(b0), "+s"(b1), "+s"(am));
    };
    auto prep = [&]() {
        int ln = lane;
        asm volatile("" : "+v"(ln));
        const int fr = ln & 15, fq = ln >> 4, q4 = fr >> 2, p4 = ln & 3;
        coff = (c.pw * 16 + c.jw) * 16384 + w * 4096 + (((fq & 1) * 2 + (fq >> 1)) * 16 + fr) * 16;
        rmo = xrow<0>(c.pw, w * 32 + fr) + xcol<0>(c.jw) + (fq & 1) * 32 + (fq >> 1) * 16;
        asm volatile("" : "+v"(coff), "+v"(rmo));
        const int fb = (q4 & 2) | ((fq & 1) << 2);
        const unsigned swp = (B && w >= 2) ? 2048u : 0u;
#pragma unroll
        for (int cc = 0; cc < 4; ++cc) {
            const unsigned b = (unsigned)((4 * fq + q4) * 128 + (((2 * cc + (p4 >> 1)) ^ fb) << 4) + (p4 & 1) * 8);
            bw[2 * cc] = b + swp; bw[2 * cc + 1] = b + (swp ^ 2048u);
            asm volatile("" : "+v"(bw[2 * cc]), "+v"(bw[2 * cc + 1]));
        }
    };
    auto start = [&](int mt, int pr) {
            {
                auto r0 = __builtin_amdgcn_permlane16_swap(res[mt][pr][0], res[mt][pr][2], false, false);
                auto r1 = __builtin_amdgcn_permlane16_swap(res[mt][pr][1], res[mt][pr][3], false, false);
#pragma unroll
                for (int h = 0; h < 2; ++h) {
                    acc[mt][2 * pr + h] = (f32x4){bf_lo(r0[h]), bf_hi(r0[h]), bf_lo(r1[h]), bf_hi(r1[h])};
                    asm volatile("" : "+v"(acc[mt][2 * pr + h]));
                }
            }
    };
    auto rdw = [&](auto ic) {
        constexpr int i = decltype(ic)::value, h = i >> 3, k = i & 7;
        wf[h % R][k] = tr_read(lds(bw[k]) + (h & 15) * 4096);
        if constexpr (h == 15) { bw[k] += 65536; asm volatile("" : "+v"(bw[k])); }
    };
    {
        auto resid = [&]() {
            const unsigned roff = pick(l != 0, (unsigned)coff, (unsigned)rmo);
            const char* xp = (const char*)xin;
#pragma unroll
            for (int i = 0; i < 4; ++i) aload4c(res[i >> 1][i & 1], roff, xp + (i >> 1) * (l ? 2048 : xrow<0>(0, 16)) + (i & 1) * (l ? 1024 : 64));
            SCHED();
        };
        unsigned opo = o_post(c), oca = B ? M_CNT : M_CNT + 8, old = 0, msk = B ? 3u : 7u;
        xv = lds_epoch(smem) + (unsigned)(l + 2);
        u64 ov = xv + (unsigned)(3 * l + 2);
        prep();
        asm volatile("" : "+v"(opo), "+v"(oca), "+v"(ov), "+v"(old), "+v"(one));
        asm volatile("" : "+s"(msk));
        SCHED();
        if (RBP) resid();
        static_for<0, 16>([&](auto ic) { rdw(ic); SCHED(); });
        if (!P2A) prep2();
        SCHED();
        wait_v<RBP ? 4 : 0>();
        if (lane == 0) asm volatile("ds_add_rtn_u32 %0, %1, %2\n\ts_waitcnt lgkmcnt(0)" : "+v"(old) : "v"(oca), "v"(one) : "memory");
        if ((__builtin_amdgcn_readfirstlane(old) & msk) == msk) { if (lane < 16) post(c.ws, opo, ov); }
        if (w == 0) STAMP_AT(l * 8 + 4);
        SCHED();
        if (!RBP) resid();
        if (P2A == 1) prep2();
        SCHED();
        if (w == 3) STAMP_AT(32 + l * 8 + 5);
        BAR();
    }
    FENCE();
    SCHED();
    if (w == 0) STAMP_AT(l * 8 + 5);
    int so = 0;
    auto next = [&](int j) { so += j == 15 ? 131072 - 15 * 262144 : 262144; asm volatile("" : "+s"(so)); };
    auto ldo = [&](int j, int mt) { xa[j % NP][mt] = __builtin_amdgcn_raw_buffer_load_b128(ro, ovo + mt * 1024, so, 16); };
#pragma unroll
    for (int h = 0; h < NP; ++h) {
        ldo(h, 0);
        SCHED();
        if (h > SH0 && h <= SH0 + 2) { start(h - SH0 - 1, 0); SCHED(); }
        ldo(h, 1);
        if (!WAL || h + 1 < NP) next(h);
        SCHED();
        if (P2A >= 2 && h == P2A - 2) { prep2(); SCHED(); }
        if (h == SH0) {
            wait_v<2 * SH0 + 2>();
            asm volatile("" : "+v"(res[0][0]), "+v"(res[0][1]), "+v"(res[1][0]), "+v"(res[1][1]));
        }
        if (h > SH0 && h <= SH0 + 2) start(h - SH0 - 1, 1);
        SCHED();
    }
    int d0 = 262144, d1 = 131072 - 15 * 262144;
    asm volatile("" : "+s"(d0), "+s"(d1));
    static_assert(WEP || !WAL, "last step");
    static_for<0, WEP ? 31 : 32>([&](auto jc) {
        constexpr int j = decltype(jc)::value;
        constexpr int c0 = j < 30 ? 16 + 8 * j : 256, c1 = j < 30 ? c0 + 8 : 256;
        if constexpr (j + NP == 16) {
            static_assert(c0 <= 8 * 20, "weights that landed before the attention");
            if (!B) BAR();
            FENCE();
            asm volatile("" : "+v"(ovo));
            SCHED();
        }
        if constexpr (j == 0 || j + NP == 16) ALIGN8();
        constexpr int nv = WAL == 1 || j == 0 || j + NP > 32 ? 2 * (NP - 1 < 31 - j ? NP - 1 : 31 - j) : 2 * NP - 3;
        __builtin_amdgcn_s_waitcnt(0x0870 | nv);
        const int sp = so;
        static_assert(!RS0 || (RS0 + NP >= 32 && RS0 < 31 && QW_SLOT <= (RS0 - 1) * 4096), "R0");
        if constexpr (RS0 && j == RS0) BAR();
        else asm volatile("s_add_i32 %0, %1, %2" : "=&s"(so) : "s"(sp), "s"(j + NP == 16 ? d1 : d0) : "scc");
        SCHED();
        static_for<0, 8>([&](auto mc) {
            constexpr int m = decltype(mc)::value, mt = m >> 2, nt = mt ? 3 - (m & 3) : m & 3;
            const bf16x8 wv = __builtin_shufflevector(wf[j % R][2 * nt], wf[j % R][2 * nt + 1], 0, 1, 2, 3, 4, 5, 6, 7);
            if constexpr (j < 31) asm volatile("v_mfma_f32_16x16x32_bf16 %0, %1, %2, %0" : "+v"(acc[mt][nt]) : "v"(wv), "v"(xa[j % NP][mt]));
            else acc[mt][nt] = MFMA(wv, __builtin_bit_cast(bf16x8, xa[j % NP][mt]), acc[mt][nt]);
            SCHED();
            if constexpr (c0 < c1 && m < 7) { rdw(std::integral_constant<int, c0 + m>{}); SCHED(); }
            if constexpr (c0 < c1 && m == 6) rdw(std::integral_constant<int, c0 + 7>{});
            if constexpr (m == WL1 && j > 0 && j - 1 + NP < 32) xa[(j + NP - 1) % NP][1] = __builtin_amdgcn_raw_buffer_load_b128(ro, ovo + 1024, sp, 16);
            if constexpr (m == WL1 + 4 && j + NP < 32) ldo(j, 0);
            SCHED();
        });
    });
    const bf16x8 ones = __builtin_bit_cast(bf16x8, on);
    u32x4 o[2][2];
    f32x4 sm[2], g[2];
    const f32x4 zero = {0.f, 0.f, 0.f, 0.f};
    auto sel = [&](float a, float b, u64 m) { return __builtin_amdgcn_inverse_ballot_w64(m) ? b : a; };
    auto stats = [&](int mt) {
        float qa = sel(g[mt][0], g[mt][1], b0), qb = sel(g[mt][2], g[mt][3], b0);
        asm volatile("" : "+v"(qa), "+v"(qb));
        SCHED();
        if (WEP) asm volatile("" :: "v"(sm[1][1]), "v"(sm[1][2]), "v"(sm[1][3]), "v"(o[1][0]), "v"(o[1][1]), "v"(on));
        __builtin_amdgcn_raw_buffer_store_b64((u32x2){__float_as_uint(sm[mt][0]), __float_as_uint(sel(qa, qb, b1))}, rst, sto + mt * 128, 0, 16);
        SCHED();
    };
    u32x2 pk[2];
    auto last = [&](int mt, int nt) {
        acc[mt][nt] = MFMA(__builtin_shufflevector(wf[31 % R][2 * nt], wf[31 % R][2 * nt + 1], 0, 1, 2, 3, 4, 5, 6, 7), __builtin_bit_cast(bf16x8, xa[31 % NP][mt]), acc[mt][nt]);
        SCHED();
    };
    auto cv = [&](int mt, int pr) {
        pk[0] = pack4(acc[mt][2 * pr]); pk[1] = pack4(acc[mt][2 * pr + 1]);
        asm volatile("" : "+v"(pk[0]), "+v"(pk[1]));
        SCHED();
    };
    auto st = [&](int mt, int pr) {
        o[mt][pr] = widen(pk[0], pk[1]);
        __builtin_amdgcn_raw_buffer_store_b128(o[mt][pr], rs, mt ? coff1 : coff, pr ? spr : 0, 16);
        SCHED();
    };
    auto gq = [&](int mt, int pr, bool f) { const bf16x8 of = __builtin_bit_cast(bf16x8, o[mt][pr]); g[mt] = MFMA(of, of, f ? zero : g[mt]); SCHED(); };
    auto gs = [&](int mt, int pr, bool f) { sm[mt] = MFMA(ones, __builtin_bit_cast(bf16x8, o[mt][pr]), f ? zero : sm[mt]); SCHED(); };
    WAIT_L0();
    last(0, 0); last(0, 1); last(0, 2); last(0, 3);
    last(1, 3); cv(0, 0);
    last(1, 2); st(0, 0);
    last(1, 1); cv(0, 1);
    last(1, 0); st(0, 1);
    if (w == 0) STAMP_AT(l * 8 + 6);
    gq(0, 0, true); cv(1, 1);
    gs(0, 0, true); st(1, 1);
    gq(0, 1, false); cv(1, 0);
    gs(0, 1, false); st(1, 0);
    gq(1, 1, true);
    gs(1, 1, true);
    gq(1, 0, false);
    gs(1, 0, false);
    stats(0);
    stats(1);
    if (OWN && (OWA ? l < 3 : !B)) {
        const unsigned la = QA_OFF + ((unsigned)coff & 16383u);
#pragma unroll
        for (int i = 0; i < 4; ++i) *(LAS u32x4*)(lds(la) + i * 1024) = o[i >> 1][i & 1];
        FENCE();
    }
    if (l < 3) {
        unsigned old = 0;
        if (!RS0 || RN1 > RN0) BAR();
        wait_v<XE2>();
        if (lane == 0) asm volatile("ds_add_rtn_u32 %0, %1, %2\n\ts_waitcnt lgkmcnt(0)" : "+v"(old) : "v"(cnta), "v"(one) : "memory");
        if ((__builtin_amdgcn_readfirstlane(old) & 3) == 3) {
            if (__builtin_amdgcn_inverse_ballot_w64(am)) post(c.ws, xpo, xv);
        }
    }
    if (w == 0) STAMP_AT(l * 8 + 7);
}

__global__ __launch_bounds__(512, 2) __attribute__((aligned(4096))) void k_main(Params P) {
    extern __shared__ __attribute__((aligned(16))) char smem_[];
    KP kp = (KP)__builtin_amdgcn_kernarg_segment_ptr();
    const char *x0, *w0, *wo;
    Ctx c;
    c.smem = (LAS char*)smem_;
    c.lane = threadIdx.x & 63;
    c.wid = __builtin_amdgcn_readfirstlane(threadIdx.x >> 6);
    c.bid = blockIdx.x;
    const bool loader = c.wid >= 4;
    unsigned va, vw;
    auto enter = [&](const bool LD) {
        if (LD) asm volatile("s_load_dwordx2 %0, %4, 0x0\n\ts_load_dwordx2 %1, %4, 0x8\n\ts_load_dwordx2 %2, %4, 0x10\n\ts_load_dwordx2 %3, %4, 0x20 ; loader" : "=&s"(x0), "=&s"(w0), "=&s"(c.ws), "=&s"(wo) : "s"(kp));
        else asm volatile("s_load_dwordx2 %0, %4, 0x0\n\ts_load_dwordx2 %1, %4, 0x8\n\ts_load_dwordx2 %2, %4, 0x10\n\ts_load_dwordx2 %3, %4, 0x20" : "=&s"(x0), "=&s"(w0), "=&s"(c.ws), "=&s"(wo) : "s"(kp));
        if (LD) asm volatile("; loader" : "+s"(c.bid), "+s"(c.wid)); else asm volatile("; consumer" : "+s"(c.bid), "+s"(c.wid));
        if (LD) asm volatile("; loader" : "+v"(c.lane)); else asm volatile("; consumer" : "+v"(c.lane));
        static_assert(QXM == 8 && WXM == 4 && ALT == 1, "map");
        c.pq = (((c.bid & 1) * 8) ^ PQX) + ((c.bid >> 3) & 7); c.jq = (c.bid & 6) * 2 + (c.bid >> 6);
        const int w = c.wid, lr = c.lane >> 3, lc = c.lane & 7;
        va = (unsigned)((c.pq * 128 + 8 * (w & 3) + lr) * (D_ * 2) + ((lc ^ lr) << 4));
        vw = (unsigned)((8 * (w & 3) + lr) * 6144 + c.jq * 128 + ((lc ^ ((lr & 2) | ((w & 1) << 2))) << 4));
        asm volatile("" : "+v"(va), "+v"(vw));
        c.pw = ((c.pq & 8) ^ 8) + ((c.bid >> 1) & 1) * 4 + ((c.bid >> 3) & 3); c.jw = (c.bid & 4) * 2 + (c.bid >> 5); c.cw = 0;
        if (LD) {
            asm volatile("" : "+s"(c.pw), "+s"(c.jw));
            asm volatile("s_waitcnt lgkmcnt(0)" : "+s"(x0), "+s"(w0), "+s"(c.ws), "+s"(wo));
            if (ESL) __builtin_amdgcn_s_sleep(ESL);
        } else {
            int t0 = first_tile(first_order(c.pq, c.jq), 0);
            asm volatile("" : "+s"(t0));
            asm volatile("s_waitcnt lgkmcnt(0) ; c" : "+s"(x0), "+s"(w0), "+s"(c.ws), "+s"(wo));
            if (ST0 && c.wid == 3) STAMP_AT(41);
            const char* xb = x0 + (unsigned)(t0 * 128);
            const char* wb = w0 + (unsigned)(t0 * (64 * 6144));
#pragma unroll
            for (int h = 0; h < 2; ++h) {
#pragma unroll
                for (int j = 0; j < 2; ++j) dma16<false>(QA_OFF + (j * 8 + h * 4 + w) * 1024, va, xb + (j * 64 + h * 32) * (D_ * 2));
#pragma unroll
                for (int j = 0; j < 3; ++j) dma16<false>((j * 8 + h * 4 + w) * 1024, vw, wb + h * (32 * 6144) + j * 2048);
            }
            if (ST0 && c.wid == 1) STAMP_AT(43);
            if (w == 0 && c.lane < 2) touch(M_EP, (unsigned)(WS_EPOCH + c.lane * 4), c.ws);
            if (w == 1 && c.lane < 6) *(LAS unsigned*)(c.smem + (c.lane < 4 ? M_CNT + c.lane * 4 : M_GO - 16 + c.lane * 4)) = 0u;
        }
        asm volatile("" : "+s"(kp), "+s"(c.bid), "+s"(c.ws));
    };
    if (c.wid == CPW) {
        const char* pc; int len;
        asm volatile("s_getpc_b64 %0\n.Lcpf_a:\n\ts_mov_b32 %1, .Lcpf_end-.Lcpf_a-4" : "=s"(pc), "=s"(len));
        const int o = min((int)(((c.bid >> 3) & 7) * 64 + c.lane) * 128, len);
        asm volatile("s_mov_b32 m0, %0\n\ts_nop 3\n\tglobal_load_lds_dword %1, %2" ::"s"(M_TCH), "v"(o), "s"(pc) : "memory");
    }
    const char* xi;
    auto step = [&](int l) {
        asm volatile("" : "+s"(kp), "+s"(c.ws), "+s"(c.jw), "+s"(c.bid));
        if (HYB) {
            const int base = (c.bid & 1) * 8 + (c.bid & 2) * 2 + ((c.bid >> 3) & 3), jb = (c.bid & 4) * 2 + (c.bid >> 5);
            const int pa = ((((c.bid & 1) * 8) ^ PQX) + ((c.bid >> 3) & 7)) ^ MA2, ja = (c.bid & 6) * 2 + (c.bid >> 6);
            c.pw = OWA && l == 0 ? pa : base ^ ((MW >> (4 * l)) & 15);
            c.jw = OWA && l == 0 ? ja : jb;
            c.pq = (l & 1) ? pa : base ^ (l ? MB3 : MB1);
            c.jq = (l & 1) ? ja : jb;
        } else { c.pq ^= 8; c.pw ^= 8; } c.cw = CPY ? c.jw & 7 : 0;
        xi = c.ws + WS_X + (unsigned)(l * (8u << 20));
        wo = (const char*)((l & 1) ? kp->wo0 : kp->wo1);
        if (c.cw == 3) wo = c.ws + WS_CO + (((l & 1) ? 2 : 0) + (c.jw >> 3)) * 4096;
    };
    if (loader) {
        enter(true);
        xi = x0;
            qkv_load<false, true>(c, xi, w0, wo, 0, kp, va, vw);
#pragma unroll 1
        for (int l = 0;;) {
            asm volatile("" : "+s"(l));
            if (!(l & 1)) attn_a<true>(c, l, wo, kp);
            wo_load(c, l, nullptr, wo);
            if (l == 3) break;
            step(l);
            ++l;
            if (l == 2) { qkv_load<false, false>(c, xi, (const char*)kp->wqkv0, wo, l); }
            else { qkv_load<true, false>(c, xi, (const char*)kp->wqkv1, wo, l); }
        }
    } else {
        enter(false);
        xi = x0;
        bf16_t* xo = (bf16_t*)(c.ws + WS_X);
#pragma unroll 1
        for (int l = 0; l < 4; ++l) {
            if (l & 1) { qkv_consume<true>(c, l, kp); }
            else {
                qkv_consume<false>(c, l, kp, va, vw);
                attn_a<false>(c, l, wo, kp);
            }
            wo_consume(l & 1, c, (const bf16_t*)xi, xo, l);
            step(l);
            xo = l == 2 ? kp->out : (bf16_t*)(c.ws + WS_X + (unsigned)((l + 1) * (8u << 20)));
            asm volatile("" : "=v"(va), "=v"(vw));
        }
    }
    asm volatile(".Lcpf_end:");
}

extern "C" void kernel_launch(void* const* d_in, const int* in_sizes, int n_in, void* d_out, int out_size, void* d_ws, size_t ws_size,
                              hipStream_t stream) {
    Params P;
    P.x0 = (const bf16_t*)d_in[0];
    P.wqkv0 = (const bf16_t*)d_in[1];
    P.wo0 = (const bf16_t*)d_in[2];
    P.wqkv1 = (const bf16_t*)d_in[4];
    P.wo1 = (const bf16_t*)d_in[5];
    P.out = (bf16_t*)d_out;
    P.ws = (char*)d_ws;
    (void)hipFuncSetAttribute((const void*)k_main, hipFuncAttributeMaxDynamicSharedMemorySize, LDS_TOTAL);
    k_main<<<256, 512, LDS_TOTAL, stream>>>(P);
}
```
